```python
import torch

def _softmax(x, axis=-1):
    return torch.softmax(x, dim=axis)
def _gelu(x):
    return torch.nn.functional.gelu(x, approximate="tanh")
def _silu(x):
    return torch.nn.functional.silu(x)
def _sigmoid(x):
    return torch.sigmoid(x)
def _layernorm(x, axis=-1, eps=1e-5):
    m = x.mean(dim=axis, keepdim=True)
    v = x.var(dim=axis, keepdim=True, unbiased=False)
    return (x - m) / torch.sqrt(v + eps)
def _scatter_add(tgt, idx, src, axis=0):
    return tgt.clone().index_add_(axis, idx, src)
def _segment_sum(src, seg, n):
    out = torch.zeros((n,) + src.shape[1:], dtype=src.dtype)
    return out.index_add_(0, seg, src)
def _pad(x, pw):
    flat = tuple(v for pair in reversed(pw) for v in pair)
    return torch.nn.functional.pad(x, flat)

kernel_name = 'recurrent_scan__mha_block__mixer_channel_mlp__prompt'

def setup_inputs():

    _w = (lambda a: (lambda t: t.to(torch.bfloat16) if t.is_floating_point() else t)(torch.as_tensor(a)))
    def _A_setup():
        B, S, D, H = 1, 2048, 640, 32
        return {
            "x": _w((lambda *s: torch.randn(*s, dtype=torch.bfloat16) * 0.05)(B, S, D)),
            "wqkv": _w((lambda *s: torch.randn(*s, dtype=torch.bfloat16) * 0.05)(D, 3 * D)),
            "wo": _w((lambda *s: torch.randn(*s, dtype=torch.bfloat16) * 0.05)(D, D)),
        }
    _A_in = _A_setup()

    def _B_setup():
        B, S, D, F = 1, 2048, 640, 1728
        return {
            "x": _w((lambda *s: torch.randn(*s, dtype=torch.bfloat16) * 0.05)(B, S, D)),
            "w1": _w((lambda *s: torch.randn(*s, dtype=torch.bfloat16) * 3.2)(D, F) * (D ** -0.5)),
            "b1": _w((lambda *s: torch.randn(*s, dtype=torch.bfloat16) * 1.6)(F)),
            "w2": _w((lambda *s: torch.randn(*s, dtype=torch.bfloat16) * 0.05)(F, D) * (F ** -0.5)),
            "b2": _w((lambda *s: torch.randn(*s, dtype=torch.bfloat16) * 0.05)(D)),
        }
    _B_in = _B_setup()

    return {
        "A_x": _A_in["x"],
        "A_wqkv": _A_in["wqkv"],
        "A_wo": _A_in["wo"],
        "B_x": _B_in["x"],
        "B_w1": _B_in["w1"],
        "B_b1": _B_in["b1"],
        "B_w2": _B_in["w2"],
        "B_b2": _B_in["b2"],
    }

def reference(A_x, A_wqkv, A_wo, B_x, B_w1, B_b1, B_w2, B_b2):
    def _A_ref(x, wqkv, wo):
        B, S, D = x.shape
        H = 32
        Dh = D // H
        qkv = (lambda x, *d: x.permute(*d))((x @ wqkv).reshape(B, S, 3, H, Dh), 2, 0, 3, 1, 4)
        q, k, v = qkv[0], qkv[1], qkv[2]
        scores = torch.einsum("bhqd,bhkd->bhqk", q, k) * (Dh ** -0.5)
        p = _softmax(scores, axis=-1)
        o = torch.einsum("bhqk,bhkd->bhqd", p, v)
        return (lambda x, *d: x.permute(*d))(o, 0, 2, 1, 3).reshape(B, S, D) @ wo

    def _B_ref(x, w1, b1, w2, b2):
        y = _layernorm(x, axis=-1)
        h = _gelu(torch.einsum("bsd,df->bsf", y, w1) + b1)
        return torch.einsum("bsf,fd->bsd", h, w2) + b2

    x = A_x
    xn = _layernorm(x, axis=-1)
    s = _A_ref(xn, A_wqkv, A_wo)
    g = _silu(_B_ref(xn, B_w1, B_b1, B_w2, B_b2))
    return x + s * g
```

```cpp
#include <hip/hip_runtime.h>

__global__ __launch_bounds__(256, 1) void fused(const void*, const void*, const void*, void*, const void*, const void*, const void*, const void*, void*) {
    auto ka = __builtin_amdgcn_kernarg_segment_ptr();
    unsigned bid = blockIdx.x, tid = threadIdx.x;
    asm volatile(
"s_load_dwordx16 s[60:75], s[0:1], 0\n"
"s_load_dwordx2 s[76:77], s[0:1], 64\n"
"s_getpc_b64 s[36:37]\n"
"98:\n"
"s_lshr_b32 s38, s2, 3\n"
"v_lshrrev_b32 v34, 3, v0\n"
"v_lshl_add_u32 v34, s38, 5, v34\n"
"v_lshlrev_b32 v34, 7, v34\n"
"v_min_u32 v34, 99f-98b-4, v34\n"
"global_load_dword v34, v34, s[36:37]\n"
"s_and_b32 s4, s2, 3\n"
"s_and_b32 s5, s2, 4\n"
"s_bfe_u32 s6, s2, 0x20003\n"
"s_or_b32 s5, s6, s5\n"
"s_lshl_b32 s4, s4, 3\n"
"s_bfe_u32 s6, s2, 0x10001\n"
"s_sub_i32 s4, s4, s6\n"
"s_lshr_b32 s6, s2, 5\n"
"s_lshl_b32 s7, s2, 2\n"
"s_lshr_b32 s3, s2, 3\n"
"s_add_i32 s4, s4, s6\n"
"s_lshl_b32 s6, s2, 1\n"
"s_and_b32 s7, s7, 4\n"
"s_and_b32 s6, s6, 12\n"
"s_add_i32 s7, s7, s3\n"
"s_add_i32 s6, s3, s6\n"
"s_and_b32 s3, s7, 32\n"
"s_lshr_b32 s7, s2, 1\n"
"s_and_b32 s7, s7, 2\n"
"v_readfirstlane_b32 s0, v0\n"
"s_lshr_b32 s30, s0, 6\n"
"s_add_i32 s7, s7, s2\n"
"s_bfe_u32 s8, s2, 0x10003\n"
"s_add_i32 s7, s7, s8\n"
"s_lshl_b32 s8, s5, 1\n"
"s_lshl_b32 s5, s5, 3\n"
"s_lshl_b32 s10, s30, 1\n"
"v_bfe_u32 v2, v0, 3, 3\n"
"v_and_b32_e32 v3, 7, v0\n"
"s_and_b32 s1, s2, 7\n"
"s_add_i32 s5, s5, s10\n"
"v_mul_u32_u24_e32 v4, 0x500, v2\n"
"v_lshlrev_b32_e32 v5, 4, v3\n"
"v_and_b32_e32 v6, 48, v0\n"
"v_bitop3_b32 v3, v2, v3, 2 bitop3:0x6c\n"
"v_lshl_or_b32 v2, s30, 4, v2\n"
"s_movk_i32 s10, 0xf00\n"
"s_or_b32 s87, s4, s3\n"
"s_lshr_b32 s3, s2, 6\n"
"s_lshl_b32 s1, s1, 2\n"
"v_bitop3_b32 v7, v5, v0, 48 bitop3:0x78\n"
"v_bitop3_b32 v76, v4, v5, v6 bitop3:0xf6\n"
"v_mul_lo_u32 v5, v2, s10\n"
"s_movk_i32 s10, 0xd80\n"
"s_add_i32 s1, s1, s3\n"
"v_mul_lo_u32 v2, v2, s10\n"
"v_lshlrev_b32_e32 v3, 4, v3\n"
"s_mulk_i32 s1, 0x140\n"
"v_or_b32_e32 v84, v3, v5\n"
"v_or_b32_e32 v86, v3, v2\n"
"v_mov_b32_e32 v2, 0x7400\n"
"s_and_b32 s3, s1, 0xfffffc00\n"
"s_addk_i32 s1, 0x130\n"
"v_xad_u32 v85, v84, 64, v2\n"
"v_mov_b32_e32 v2, 0x6800\n"
"s_and_b32 s1, s1, 0xfffffc00\n"
"v_bitop3_b32 v4, v4, 64, v7 bitop3:0x36\n"
"v_xad_u32 v87, v86, 64, v2\n"
"v_and_b32_e32 v2, 4, v0\n"
"s_and_b32 s7, s7, 3\n"
"v_add_u32_e32 v77, 0x2400, v4\n"
"v_add_u32_e32 v79, 0x6c00, v4\n"
"v_add_u32_e32 v81, 0xc400, v4\n"
"v_add_u32_e32 v83, 0x10c00, v4\n"
"v_mov_b32_e32 v3, s1\n"
"v_mov_b32_e32 v4, s3\n"
"v_cmp_eq_u32_e32 vcc, 0, v2\n"
"v_and_b32_e32 v5, 8, v0\n"
"s_mul_i32 s7, s7, 5\n"
"s_sub_i32 s6, s6, 28\n"
"v_cndmask_b32_e32 v2, v3, v4, vcc\n"
"v_mov_b32_e32 v6, 0x5000\n"
"v_mov_b32_e32 v7, 0x2800\n"
"v_cmp_eq_u32_e32 vcc, 0, v5\n"
"s_lshr_b32 s0, s0, 7\n"
"s_lshr_b32 s86, s7, 1\n"
"s_lshl_b32 s7, s2, 4\n"
"s_lshl_b32 s9, s6, 2\n"
"v_cndmask_b32_e32 v5, v6, v7, vcc\n"
"v_lshlrev_b32_e32 v6, 7, v0\n"
"s_add_i32 s8, s8, s0\n"
"s_and_b32 s7, s7, 0x380\n"
"s_add_i32 s9, s9, s30\n"
"v_and_b32_e32 v6, 0x180, v6\n"
"s_cmp_gt_u32 s87, 31\n"
"v_lshl_or_b32 v6, s0, 9, v6\n"
"s_cselect_b32 s6, s6, s8\n"
"v_add3_u32 v2, v6, v5, v2\n"
"v_and_b32_e32 v5, 1, v0\n"
"v_cmp_eq_u32_e32 vcc, 0, v5\n"
"s_cselect_b32 s0, s9, s5\n"
"s_lshl_b32 s1, s6, 7\n"
"v_and_b32_e32 v1, 63, v0\n"
"v_cndmask_b32_e32 v3, v3, v4, vcc\n"
"s_addk_i32 s1, 0x7800\n"
"v_or_b32_e32 v3, s7, v3\n"
"v_mov_b32_e32 v4, s1\n"
"v_cmp_gt_u32_e32 vcc, 18, v1\n"
"s_mul_i32 s80, s0, 0xa000\n"
"s_min_u32 s0, s4, 26\n"
"v_cndmask_b32_e32 v3, v4, v3, vcc\n"
"v_cmp_gt_u32_e32 vcc, 16, v1\n"
"s_lshl_b32 s84, s0, 7\n"
"s_lshl_b32 s0, s30, 12\n"
"v_add_u32_e32 v78, 0x4800, v76\n"
"v_add_u32_e32 v80, 0xa000, v76\n"
"v_add_u32_e32 v82, 0xe800, v76\n"
"v_cndmask_b32_e32 v1, v3, v2, vcc\n"
"s_lshl_b32 s81, s86, 7\n"
"s_lshl_b32 s82, s4, 7\n"
"s_mul_i32 s83, s86, 0x3c000\n"
"s_mul_i32 s85, s86, 0x36000\n"
"s_mul_i32 s88, s30, 0x6000\n"
"s_add_i32 s89, s0, 0x18000\n"
"s_mov_b32 m0, s89\n"
"s_cmp_ge_u32 s87, 32\n"
"s_waitcnt lgkmcnt(0)\n"
"s_cbranch_scc1 70f\n"
"s_add_u32 s82, s62, s82\n"
"s_addc_u32 s41, s63, 0\n"
"s_add_u32 s40, s82, s83\n"
"s_mov_b32 s83, s41\n"
"s_addc_u32 s41, s41, 0\n"
"global_load_lds_dwordx4 v84, s[40:41] offset:0\n"
"global_load_lds_dwordx4 v85, s[40:41] offset:1024\n"
"s_add_u32 m0, s89, 2048\n"
"s_add_u32 s84, s64, s84\n"
"s_addc_u32 s43, s65, 0\n"
"s_add_u32 s42, s84, s85\n"
"s_mov_b32 s85, s43\n"
"s_addc_u32 s43, s43, 0\n"
"global_load_lds_dwordx4 v86, s[42:43] offset:0\n"
"global_load_lds_dwordx4 v87, s[42:43] offset:1024\n"
"s_mov_b32 m0, s88\n"
"s_add_u32 s80, s60, s80\n"
"s_addc_u32 s39, s61, 0\n"
"s_add_u32 s38, s80, s81\n"
"s_mov_b32 s81, s39\n"
"s_addc_u32 s39, s39, 0\n"
"global_load_lds_dwordx4 v76, s[38:39] offset:0\n"
"global_load_lds_dwordx4 v77, s[38:39] offset:1024\n"
"global_load_dword v35, v1, s[66:67] sc1\n"
"s_mul_hi_u32 s0, s87, 0xcccccccd\n"
"s_lshr_b32 s0, s0, 2\n"
"s_mul_i32 s0, s0, 5\n"
"s_sub_i32 s0, s87, s0\n"
"s_cmp_lt_u32 s87, 20\n"
"s_cselect_b32 s3, s0, 5\n"
"s_and_b32 s0, s30, 1\n"
"s_lshl_b32 s1, s0, 2\n"
"v_bfe_u32 v2, v0, 4, 2\n"
"v_bfe_u32 v3, v0, 1, 3\n"
"v_bitop3_b32 v2, s1, v3, v2 bitop3:0x36\n"
"s_mul_i32 s1, s30, 0x6000\n"
"v_and_b32_e32 v3, 15, v0\n"
"v_lshl_or_b32 v3, v3, 7, s1\n"
"v_lshlrev_b32_e32 v2, 4, v2\n"
"v_or_b32_e32 v52, v2, v3\n"
"v_bitop3_b32 v53, v2, 64, v3 bitop3:0x36\n"
"global_load_lds_dwordx4 v78, s[38:39] offset:2048\n"
"v_lshrrev_b32_e32 v2, 1, v0\n"
"v_lshlrev_b32_e32 v4, 5, v0\n"
"v_and_b32_e32 v2, 4, v2\n"
"v_bfe_u32 v3, v0, 4, 1\n"
"v_and_b32_e32 v4, 0x180, v4\n"
"v_lshl_or_b32 v2, v3, 3, v2\n"
"v_lshl_or_b32 v3, v3, 10, v4\n"
"v_lshlrev_b32_e32 v4, 7, v0\n"
"s_lshl_b32 s0, s0, 13\n"
"v_and_b32_e32 v4, 0x1000, v4\n"
"v_or3_b32 v3, v4, s0, v3\n"
"s_mov_b32 s0, 0x76543210\n"
"s_mov_b32 s1, 0xfedcba98\n"
"s_mov_b32 s12, 0\n"
"s_cmp_eq_u32 s3, 0\n"
"s_cmov_b32 s0, 0xec97db86\n"
"s_cmov_b32 s1, 0xf542a031\n"
"s_cmov_b32 s12, 0x2a\n"
"global_load_lds_dwordx4 v79, s[38:39] offset:3072\n"
"s_add_u32 m0, s88, 4096\n"
"s_cmp_eq_u32 s3, 1\n"
"s_cmov_b32 s0, 0xfd86ec75\n"
"s_cmov_b32 s1, 0x4a319b20\n"
"s_cmov_b32 s12, 0x26\n"
"s_cmp_eq_u32 s3, 2\n"
"s_cmov_b32 s0, 0xfca5eb94\n"
"s_cmov_b32 s1, 0xd0728361\n"
"s_cmov_b32 s12, 0x34\n"
"s_cmp_eq_u32 s3, 3\n"
"s_cmov_b32 s0, 0xecb9fda8\n"
"s_cmov_b32 s1, 0x76412530\n"
"s_cmov_b32 s12, 0x5a\n"
"s_cmp_eq_u32 s3, 4\n"
"s_cmov_b32 s0, 0xfda8ec97\n"
"s_cmov_b32 s1, 0x6053b142\n"
"s_cmov_b32 s12, 0x2a\n"
"s_and_b32 s3, s30, 2\n"
"global_load_lds_dwordx4 v80, s[38:39] offset:0\n"
"s_mov_b64 exec, 0\n"
"s_cmp_eq_u32 s30, 0\n"
"s_cbranch_scc1 9000f\n"
"s_cmp_eq_u32 s30, 1\n"
"s_cbranch_scc1 9100f\n"
"s_cmp_eq_u32 s30, 2\n"
"s_cbranch_scc1 9200f\n"
"s_cmp_ge_u32 s87, 27\n"
"s_cbranch_scc1 9500f\n"
"s_cmp_ge_u32 s87, 20\n"
"s_cbranch_scc1 9400f\n"
"s_branch 9300f\n"
"97:\n"
"s_mov_b64 exec, -1\n"
"s_lshl_b32 s4, s3, 2\n"
"v_and_b32_e32 v4, 3, v0\n"
"s_lshl_b32 s3, s3, 4\n"
"v_bitop3_b32 v5, s4, v2, v4 bitop3:0x36\n"
"v_lshlrev_b32_e32 v4, 2, v4\n"
"s_lshr_b64 s[4:5], s[0:1], s3\n"
"v_lshrrev_b32_e64 v6, v4, s4\n"
"v_bitop3_b32 v6, v6, v2, 15 bitop3:0x6c\n"
"s_or_b32 s4, s3, 16\n"
"v_lshlrev_b32_e32 v6, 3, v6\n"
"s_mov_b32 s6, 0x1a000\n"
"s_lshr_b64 s[4:5], s[0:1], s4\n"
"v_or_b32_e32 v8, v6, v3\n"
"v_bitop3_b32 v40, v6, s6, v3 bitop3:0x36\n"
"v_lshrrev_b32_e64 v6, v4, s4\n"
"v_lshlrev_b32_e32 v5, 3, v5\n"
"v_bitop3_b32 v6, v6, v2, 15 bitop3:0x6c\n"
"global_load_lds_dwordx4 v81, s[38:39] offset:1024\n"
"s_xor_b32 s4, s3, 32\n"
"v_or_b32_e32 v7, v3, v5\n"
"v_lshlrev_b32_e32 v6, 3, v6\n"
"s_lshr_b64 s[4:5], s[0:1], s4\n"
"s_xor_b32 s3, s3, 48\n"
"v_or_b32_e32 v44, 0x18800, v7\n"
"v_or_b32_e32 v7, v6, v3\n"
"v_bitop3_b32 v41, v6, s6, v3 bitop3:0x36\n"
"v_lshrrev_b32_e64 v6, v4, s4\n"
"s_lshr_b64 s[4:5], s[0:1], s3\n"
"s_mov_b32 s3, 0x1a800\n"
"v_bitop3_b32 v48, v3, s3, v5 bitop3:0x36\n"
"s_mov_b32 s3, 0x18820\n"
"v_bitop3_b32 v45, v3, s3, v5 bitop3:0x36\n"
"s_mov_b32 s3, 0x1a820\n"
"v_lshrrev_b32_e64 v4, v4, s4\n"
"v_bitop3_b32 v49, v3, s3, v5 bitop3:0x36\n"
"global_load_lds_dwordx4 v82, s[38:39] offset:2048\n"
"s_mov_b32 s3, 0x18840\n"
"v_bitop3_b32 v6, v6, v2, 15 bitop3:0x6c\n"
"v_bitop3_b32 v2, v4, v2, 15 bitop3:0x6c\n"
"v_bitop3_b32 v46, v3, s3, v5 bitop3:0x36\n"
"s_mov_b32 s3, 0x1a840\n"
"v_lshlrev_b32_e32 v6, 3, v6\n"
"v_bitop3_b32 v50, v3, s3, v5 bitop3:0x36\n"
"s_mov_b32 s3, 0x18860\n"
"v_lshlrev_b32_e32 v2, 3, v2\n"
"v_or_b32_e32 v37, 0x18000, v7\n"
"v_or_b32_e32 v7, v6, v3\n"
"v_bitop3_b32 v47, v3, s3, v5 bitop3:0x36\n"
"s_mov_b32 s3, 0x1a860\n"
"v_bitop3_b32 v43, v2, s6, v3 bitop3:0x36\n"
"v_or_b32_e32 v2, v2, v3\n"
"v_or_b32_e32 v36, 0x18000, v8\n"
"v_or_b32_e32 v38, 0x18000, v7\n"
"global_load_lds_dwordx4 v83, s[38:39] offset:3072\n"
"v_bitop3_b32 v42, v6, s6, v3 bitop3:0x36\n"
"v_bitop3_b32 v51, v3, s3, v5 bitop3:0x36\n"
"v_or_b32_e32 v39, 0x18000, v2\n"
"s_cmp_ge_u32 s87, 27\n"
"s_cbranch_scc1 60f\n"
"s_cmp_ge_u32 s87, 20\n"
"s_cbranch_scc1 4f\n"
"s_mov_b32 s36, s86\n"
"s_add_u32 s36, s36, 1\n"
"s_cmp_eq_u32 s36, 10\n"
"s_cselect_b32 s36, 0, s36\n"
"s_mul_i32 s37, s36, 245760\n"
"s_add_u32 s40, s82, s37\n"
"s_addc_u32 s41, s83, 0\n"
"s_mul_i32 s37, s36, 221184\n"
"s_add_u32 s42, s84, s37\n"
"s_addc_u32 s43, s85, 0\n"
"s_lshl_b32 s37, s36, 7\n"
"s_add_u32 s38, s80, s37\n"
"s_addc_u32 s39, s81, 0\n"
"v_mov_b32 v56, 0x3f803f80\n"
"v_mov_b32 v57, 0x3f803f80\n"
"v_mov_b32 v58, 0x3f803f80\n"
"v_mov_b32 v59, 0x3f803f80\n"
"s_add_u32 m0, s89, 16384\n"
"s_nop 0\n"
"global_load_lds_dwordx4 v84, s[40:41] offset:0\n"
"s_and_b32 s3, s2, 3\n"
"s_lshl_b32 s3, s3, 3\n"
"s_bfe_u32 s4, s2, 0x10001\n"
"s_lshl_b32 s9, s2, 2\n"
"s_sub_i32 s10, s3, s4\n"
"s_lshr_b32 s14, s2, 3\n"
"s_and_b32 s3, s9, 4\n"
"s_lshr_b32 s18, s2, 5\n"
"s_add_i32 s3, s3, s14\n"
"s_add_i32 s10, s10, s18\n"
"s_and_b32 s3, s3, 32\n"
"s_or_b32 s4, s10, s3\n"
"s_lshl_b32 s3, s2, 1\n"
"s_and_b32 s3, s3, 12\n"
"s_bfe_u32 s7, s2, 0x20003\n"
"s_add_i32 s6, s3, s14\n"
"global_load_lds_dwordx4 v85, s[40:41] offset:1024\n"
"s_add_u32 m0, s89, 18432\n"
"s_and_b32 s3, s2, 4\n"
"s_lshr_b32 s13, s30, 1\n"
"s_sub_i32 s11, s6, 28\n"
"s_or_b32 s8, s7, s3\n"
"s_lshl_b32 s5, s8, 2\n"
"s_and_b32 s22, s30, 1\n"
"s_lshl_b32 s3, s11, 11\n"
"s_lshl_b32 s16, s13, 10\n"
"s_add_i32 s5, s5, s30\n"
"s_add_i32 s3, s3, s16\n"
"s_lshl_b32 s16, s22, 7\n"
"s_lshl_b32 s15, s10, 3\n"
"s_or_b32 s3, s3, s16\n"
"s_lshl_b32 s16, s5, 10\n"
"s_cmp_gt_u32 s4, 31\n"
"s_cselect_b32 s3, s3, s16\n"
"global_load_lds_dwordx4 v86, s[42:43] offset:0\n"
"s_addk_i32 s6, 0xd4\n"
"s_or_b32 s15, s15, s8\n"
"s_cmp_gt_u32 s4, 31\n"
"s_cselect_b32 s6, s6, s15\n"
"s_add_i32 s8, s15, 0x1fffff38\n"
"s_cmp_gt_u32 s4, 31\n"
"s_cselect_b32 s8, s11, s8\n"
"s_addk_i32 s15, 0xff58\n"
"s_cmp_gt_u32 s4, 31\n"
"s_mul_hi_u32 s16, s10, 0xcccccccd\n"
"s_cselect_b32 s31, s11, s15\n"
"s_lshr_b32 s11, s16, 2\n"
"s_lshr_b32 s15, s16, 3\n"
"s_sub_i32 s11, s10, s11\n"
"s_mul_i32 s15, s15, 10\n"
"s_and_b32 s11, s11, 1\n"
"global_load_lds_dwordx4 v87, s[42:43] offset:1024\n"
"s_add_u32 m0, s88, 8192\n"
"s_sub_i32 s16, s10, s15\n"
"s_or_b32 s17, s11, 10\n"
"s_lshl_b32 s15, s16, 4\n"
"s_cmp_lt_u32 s10, 10\n"
"s_cselect_b64 vcc, -1, 0\n"
"s_movk_i32 s19, 0xa00\n"
"s_and_b64 s[20:21], vcc, exec\n"
"s_cselect_b32 s54, 0x500, s19\n"
"s_mov_b32 s19, 0x58000\n"
"s_cselect_b32 s21, s19, 0x2d8000\n"
"s_lshl_b32 s55, s54, 6\n"
"s_and_b32 s25, s30, 2\n"
"v_mov_b32_e32 v2, 0x3ea52b5b\n"
"s_cmp_lt_u32 s10, 20\n"
"v_cndmask_b32_e32 v2, 1.0, v2, vcc\n"
"s_cselect_b64 vcc, -1, 0\n"
"global_load_lds_dwordx4 v76, s[38:39] offset:0\n"
"s_add_u32 s36, s36, 1\n"
"s_cmp_eq_u32 s36, 10\n"
"s_cselect_b32 s36, 0, s36\n"
"s_xor_b32 s20, s30, 2\n"
"s_lshr_b32 s58, s20, 1\n"
"s_lshl_b32 s19, s13, 3\n"
"s_lshl_b32 s23, s58, 3\n"
"s_sub_i32 s56, s17, s19\n"
"s_lshl_b32 s57, s25, 4\n"
"s_xor_b32 s19, s30, 1\n"
"s_sub_i32 s59, s17, s23\n"
"s_cmp_gt_u32 s20, 1\n"
"s_cselect_b32 s62, s1, s0\n"
"s_xor_b32 s24, s30, 3\n"
"s_lshr_b32 s63, s24, 1\n"
"s_lshl_b32 s23, s63, 3\n"
"s_sub_i32 s64, s17, s23\n"
"s_cmp_gt_u32 s24, 1\n"
"s_cselect_b32 s65, s1, s0\n"
"global_load_lds_dwordx4 v77, s[38:39] offset:1024\n"
"s_mul_i32 s37, s36, 245760\n"
"s_add_u32 s40, s82, s37\n"
"s_addc_u32 s41, s83, 0\n"
"s_and_b32 s17, s9, 28\n"
"s_mul_i32 s9, s5, s54\n"
"s_or_b32 s23, s17, s7\n"
"s_lshr_b32 s7, s2, 6\n"
"s_lshl_b32 s78, s25, 8\n"
"s_lshl_b32 s33, s30, 10\n"
"s_lshl_b32 s34, s30, 8\n"
"s_lshl_b32 s9, s9, 1\n"
"s_add_i32 s17, s17, s7\n"
"s_or_b32 s35, s78, 0x25000\n"
"s_add_i32 s51, s33, 0x24000\n"
"s_add_i32 s52, s34, 0x25000\n"
"s_xor_b32 s53, s78, 0x25200\n"
"s_add_i32 s9, s9, s21\n"
"s_cmp_eq_u32 s11, 0\n"
"s_cselect_b32 s50, -1, 0xffff0000\n"
"global_load_lds_dwordx4 v78, s[38:39] offset:2048\n"
"s_mul_i32 s37, s36, 221184\n"
"s_add_u32 s42, s84, s37\n"
"s_addc_u32 s43, s85, 0\n"
"s_lshr_b32 s11, s2, 1\n"
"s_lshl_b32 s21, s22, 1\n"
"s_and_b32 s11, s11, 28\n"
"s_add_i32 s27, s21, s13\n"
"s_add_i32 s27, s27, s11\n"
"s_lshl_b32 s11, s23, 6\n"
"s_lshl_b32 s21, s30, 4\n"
"s_add_i32 s11, s11, s21\n"
"s_lshl_b32 s21, s2, 15\n"
"s_lshl_b32 s26, s30, 13\n"
"s_add_i32 s21, s26, s21\n"
"s_mul_i32 s26, s5, 0x36000\n"
"s_lshl_b32 s28, s10, 13\n"
"s_add_i32 s29, s28, s26\n"
"s_mul_i32 s26, s30, 40\n"
"s_add_i32 s8, s26, s8\n"
"global_load_lds_dwordx4 v79, s[38:39] offset:3072\n"
"s_add_u32 m0, s88, 12288\n"
"s_mul_i32 s26, s11, 0x500\n"
"s_mul_i32 s28, s18, 0xa0\n"
"s_lshl_b32 s5, s5, 5\n"
"s_add_i32 s26, s26, s28\n"
"s_add_i32 s21, s21, 0x13fc000\n"
"s_add_i32 s29, s29, 0x7d8000\n"
"s_addk_i32 s8, 0x400\n"
"s_add_i32 s5, s5, s10\n"
"s_cmp_gt_u32 s4, 26\n"
"s_cselect_b32 s4, s21, s29\n"
"s_cselect_b32 s8, s8, s5\n"
"s_add_u32 s4, s66, s4\n"
"s_addc_u32 s5, s67, 0\n"
"s_lshl_b32 s21, s27, 5\n"
"s_mul_i32 s11, s7, 40\n"
"s_add_i32 s86, s7, 0xfffffee0\n"
"s_waitcnt vmcnt(15)\n"
"s_barrier\n"
"ds_read_b128 a[0:3], v52 offset:0\n"
"ds_read_b64_tr_b16 a[16:17], v36 offset:0\n"
"ds_read_b64_tr_b16 a[18:19], v36 offset:512\n"
"ds_read_b64_tr_b16 a[20:21], v37 offset:0\n"
"ds_read_b64_tr_b16 a[22:23], v37 offset:512\n"
"ds_read_b64_tr_b16 a[24:25], v38 offset:0\n"
"ds_read_b64_tr_b16 a[26:27], v38 offset:512\n"
"ds_read_b64_tr_b16 a[28:29], v39 offset:0\n"
"ds_read_b64_tr_b16 a[30:31], v39 offset:512\n"
"ds_read_b64_tr_b16 a[32:33], v44 offset:0\n"
"ds_read_b64_tr_b16 a[34:35], v44 offset:512\n"
"ds_read_b64_tr_b16 a[36:37], v45 offset:0\n"
"ds_read_b64_tr_b16 a[38:39], v45 offset:512\n"
"ds_read_b64_tr_b16 a[40:41], v46 offset:0\n"
"ds_read_b64_tr_b16 a[42:43], v46 offset:512\n"
"ds_read_b64_tr_b16 a[44:45], v47 offset:0\n"
"ds_read_b64_tr_b16 a[46:47], v47 offset:512\n"
"s_waitcnt lgkmcnt(14)\n"
"v_mfma_f32_16x16x32_bf16 v[112:115], a[16:19], v[56:59], 0\n"
"global_load_lds_dwordx4 v80, s[38:39] offset:0\n"
"s_waitcnt lgkmcnt(12)\n"
"v_mfma_f32_16x16x32_bf16 v[116:119], a[20:23], v[56:59], 0\n"
"ds_read_b128 a[48:51], v53 offset:0\n"
"s_lshl_b32 s87, s23, 5\n"
"s_lshl_b32 s7, s22, 4\n"
"s_waitcnt lgkmcnt(7)\n"
"v_mfma_f32_16x16x32_bf16 v[120:123], a[32:35], v[56:59], 0\n"
"ds_read_b64_tr_b16 a[64:65], v40 offset:0\n"
"s_add_i32 s21, s21, s17\n"
"s_or_b32 s90, s87, s7\n"
"s_waitcnt lgkmcnt(6)\n"
"v_mfma_f32_16x16x32_bf16 v[124:127], a[36:39], v[56:59], 0\n"
"ds_read_b64_tr_b16 a[66:67], v40 offset:512\n"
"s_lshl_b32 s7, s21, 4\n"
"s_add_i32 s91, s7, 0x2f000\n"
"v_mfma_f32_16x16x32_bf16 v[128:131], a[16:19], a[0:3], 0\n"
"ds_read_b64_tr_b16 a[68:69], v41 offset:0\n"
"s_lshl_b32 s7, s8, 3\n"
"s_lshl_b32 s2, s2, 3\n"
"v_mfma_f32_16x16x32_bf16 v[144:147], a[20:23], a[0:3], 0\n"
"s_waitcnt vmcnt(13)\n"
"ds_read_b128 a[4:7], v52 offset:2048\n"
"ds_read_b64_tr_b16 a[70:71], v41 offset:512\n"
"v_mfma_f32_16x16x32_bf16 v[160:163], a[24:27], a[0:3], 0\n"
"global_load_lds_dwordx4 v81, s[38:39] offset:1024\n"
"v_mfma_f32_16x16x32_bf16 v[176:179], a[28:31], a[0:3], 0\n"
"ds_read_b64_tr_b16 a[72:73], v42 offset:0\n"
"s_mul_i32 s29, s30, 13\n"
"s_add_i32 s79, s11, 0x400\n"
"v_mfma_f32_16x16x32_bf16 v[192:195], a[32:35], a[0:3], 0\n"
"ds_read_b64_tr_b16 a[74:75], v42 offset:512\n"
"s_add_i32 s92, s7, 0xa000\n"
"s_lshl_b32 s93, s6, 7\n"
"v_mfma_f32_16x16x32_bf16 v[208:211], a[36:39], a[0:3], 0\n"
"ds_read_b64_tr_b16 a[76:77], v43 offset:0\n"
"s_add_i32 s94, s2, 0x8000\n"
"s_add_i32 s3, s3, 0x50000\n"
"s_waitcnt lgkmcnt(11)\n"
"v_mfma_f32_16x16x32_bf16 v[224:227], a[40:43], a[0:3], 0\n"
"ds_read_b64_tr_b16 a[78:79], v43 offset:512\n"
"s_or_b32 s95, s22, 52\n"
"s_lshr_b32 s96, s29, 1\n"
"s_waitcnt lgkmcnt(10)\n"
"v_mfma_f32_16x16x32_bf16 v[240:243], a[44:47], a[0:3], 0\n"
"global_load_lds_dwordx4 v82, s[38:39] offset:2048\n"
"s_waitcnt lgkmcnt(5)\n"
"v_mfma_f32_16x16x32_bf16 v[132:135], a[16:19], a[4:7], 0\n"
"ds_read_b64_tr_b16 a[80:81], v48 offset:0\n"
"s_add_u32 s2, s66, s3\n"
"s_addc_u32 s3, s67, 0\n"
"v_mfma_f32_16x16x32_bf16 v[148:151], a[20:23], a[4:7], 0\n"
"s_waitcnt vmcnt(13)\n"
"ds_read_b128 a[8:11], v52 offset:4096\n"
"ds_read_b64_tr_b16 a[82:83], v48 offset:512\n"
"v_mfma_f32_16x16x32_bf16 v[164:167], a[24:27], a[4:7], 0\n"
"ds_read_b64_tr_b16 a[84:85], v49 offset:0\n"
"s_add_u32 s6, s66, s9\n"
"s_addc_u32 s7, s67, 0\n"
"v_mfma_f32_16x16x32_bf16 v[180:183], a[28:31], a[4:7], 0\n"
"ds_read_b64_tr_b16 a[86:87], v49 offset:512\n"
"s_add_i32 s9, s9, s54\n"
"v_mfma_f32_16x16x32_bf16 v[196:199], a[32:35], a[4:7], 0\n"
"global_load_lds_dwordx4 v83, s[38:39] offset:3072\n"
"s_lshl_b32 s37, s36, 7\n"
"s_add_u32 s38, s80, s37\n"
"s_addc_u32 s39, s81, 0\n"
"s_add_u32 m0, s89, 32768\n"
"v_mfma_f32_16x16x32_bf16 v[212:215], a[36:39], a[4:7], 0\n"
"ds_read_b64_tr_b16 a[88:89], v50 offset:0\n"
"s_add_u32 s8, s66, s9\n"
"s_addc_u32 s9, s67, 0\n"
"v_mfma_f32_16x16x32_bf16 v[228:231], a[40:43], a[4:7], 0\n"
"ds_read_b64_tr_b16 a[90:91], v50 offset:512\n"
"s_min_u32 s10, s10, 26\n"
"s_lshl_b32 s10, s10, 7\n"
"v_mfma_f32_16x16x32_bf16 v[244:247], a[44:47], a[4:7], 0\n"
"ds_read_b64_tr_b16 a[92:93], v51 offset:0\n"
"v_bfe_u32 v3, v0, 4, 2\n"
"s_waitcnt lgkmcnt(6)\n"
"v_mfma_f32_16x16x32_bf16 v[136:139], a[16:19], a[8:11], 0\n"
"ds_read_b64_tr_b16 a[94:95], v51 offset:512\n"
"v_mfma_f32_16x16x32_bf16 v[152:155], a[20:23], a[8:11], 0\n"
"s_waitcnt vmcnt(12)\n"
"ds_read_b128 a[12:15], v52 offset:6144\n"
"ds_read_b128 a[52:55], v53 offset:2048\n"
"v_mfma_f32_16x16x32_bf16 v[168:171], a[24:27], a[8:11], 0\n"
"global_load_lds_dwordx4 v84, s[40:41] offset:0\n"
"v_mfma_f32_16x16x32_bf16 v[184:187], a[28:31], a[8:11], 0\n"
"ds_read_b128 a[56:59], v53 offset:4096\n"
"v_mfma_f32_16x16x32_bf16 v[200:203], a[32:35], a[8:11], 0\n"
"ds_read_b128 a[60:63], v53 offset:6144\n"
"v_mfma_f32_16x16x32_bf16 v[216:219], a[36:39], a[8:11], 0\n"
"s_add_u32 s10, s70, s10\n"
"v_lshl_or_b32 v4, s58, 2, v3\n"
"s_addc_u32 s11, s71, 0\n"
"v_mfma_f32_16x16x32_bf16 v[232:235], a[40:43], a[8:11], 0\n"
"v_lshrrev_b32_e64 v4, v4, s12\n"
"v_mfma_f32_16x16x32_bf16 v[248:251], a[44:47], a[8:11], 0\n"
"global_load_lds_dwordx4 v85, s[40:41] offset:1024\n"
"s_add_u32 m0, s89, 34816\n"
"s_waitcnt lgkmcnt(3)\n"
"v_mfma_f32_16x16x32_bf16 v[140:143], a[16:19], a[12:15], 0\n"
"s_cmp_gt_u32 s30, 1\n"
"v_lshlrev_b32_e32 v4, 9, v4\n"
"s_cselect_b32 s54, s1, s0\n"
"v_mfma_f32_16x16x32_bf16 v[156:159], a[20:23], a[12:15], 0\n"
"v_and_b32_e32 v4, 0x200, v4\n"
"v_mov_b32_e32 v5, 0x400\n"
"v_mfma_f32_16x16x32_bf16 v[172:175], a[24:27], a[12:15], 0\n"
"v_cmp_gt_u32_e64 s[0:1], s59, v3\n"
"v_lshlrev_b32_e32 v10, 2, v3\n"
"v_mfma_f32_16x16x32_bf16 v[188:191], a[28:31], a[12:15], 0\n"
"v_lshl_or_b32 v6, s13, 2, v3\n"
"v_cndmask_b32_e64 v4, v5, v4, s[0:1]\n"
"v_mfma_f32_16x16x32_bf16 v[204:207], a[32:35], a[12:15], 0\n"
"global_load_lds_dwordx4 v86, s[42:43] offset:0\n"
"v_mfma_f32_16x16x32_bf16 v[220:223], a[36:39], a[12:15], 0\n"
"v_cmp_gt_u32_e64 s[0:1], s56, v3\n"
"v_lshrrev_b32_e64 v11, v10, s54\n"
"v_mfma_f32_16x16x32_bf16 v[236:239], a[40:43], a[12:15], 0\n"
"v_lshrrev_b32_e64 v6, v6, s12\n"
"v_cndmask_b32_e64 v8, 4, 0, s[0:1]\n"
"v_mfma_f32_16x16x32_bf16 v[252:255], a[44:47], a[12:15], 0\n"
"v_and_or_b32 v11, v11, 15, s15\n"
"v_lshlrev_b32_e32 v6, 9, v6\n"
"s_waitcnt vmcnt(11) lgkmcnt(0)\n"
"s_barrier\n"
"v_mfma_f32_16x16x32_bf16 v[128:131], a[64:67], a[48:51], v[128:131]\n"
"s_waitcnt vmcnt(9)\n"
"ds_read_b128 a[0:3], v52 offset:8192\n"
"v_or_b32_e32 v8, v8, v3\n"
"v_mfma_f32_16x16x32_bf16 v[144:147], a[68:71], a[48:51], v[144:147]\n"
"global_load_lds_dwordx4 v87, s[42:43] offset:1024\n"
"s_add_u32 m0, s88, 16384\n"
"v_mfma_f32_16x16x32_bf16 v[160:163], a[72:75], a[48:51], v[160:163]\n"
"ds_read_b64_tr_b16 a[16:17], v36 offset:16384\n"
"v_mul_lo_u16_e32 v11, 0xcd, v11\n"
"v_mfma_f32_16x16x32_bf16 v[176:179], a[76:79], a[48:51], v[176:179]\n"
"ds_read_b64_tr_b16 a[18:19], v36 offset:16896\n"
"v_and_b32_e32 v6, 0x200, v6\n"
"v_mfma_f32_16x16x32_bf16 v[192:195], a[80:83], a[48:51], v[192:195]\n"
"ds_read_b64_tr_b16 a[20:21], v37 offset:16384\n"
"v_lshlrev_b32_e32 v8, 2, v8\n"
"v_mfma_f32_16x16x32_bf16 v[208:211], a[84:87], a[48:51], v[208:211]\n"
"ds_read_b64_tr_b16 a[22:23], v37 offset:16896\n"
"v_lshrrev_b16_e32 v11, 10, v11\n"
"v_mfma_f32_16x16x32_bf16 v[224:227], a[88:91], a[48:51], v[224:227]\n"
"global_load_lds_dwordx4 v76, s[38:39] offset:0\n"
"s_add_u32 s36, s36, 1\n"
"s_cmp_eq_u32 s36, 10\n"
"s_cselect_b32 s36, 0, s36\n"
"v_mfma_f32_16x16x32_bf16 v[240:243], a[92:95], a[48:51], v[240:243]\n"
"ds_read_b64_tr_b16 a[24:25], v38 offset:16384\n"
"v_and_b32_e32 v13, 15, v0\n"
"v_mfma_f32_16x16x32_bf16 v[132:135], a[64:67], a[52:55], v[132:135]\n"
"ds_read_b64_tr_b16 a[26:27], v38 offset:16896\n"
"v_cndmask_b32_e64 v7, v5, v6, s[0:1]\n"
"v_mfma_f32_16x16x32_bf16 v[148:151], a[68:71], a[52:55], v[148:151]\n"
"ds_read_b64_tr_b16 a[28:29], v39 offset:16384\n"
"v_lshrrev_b32_e64 v8, v8, s54\n"
"v_mfma_f32_16x16x32_bf16 v[164:167], a[72:75], a[52:55], v[164:167]\n"
"ds_read_b64_tr_b16 a[30:31], v39 offset:16896\n"
"v_mul_u32_u24_e32 v11, s55, v11\n"
"v_mfma_f32_16x16x32_bf16 v[180:183], a[76:79], a[52:55], v[180:183]\n"
"global_load_lds_dwordx4 v77, s[38:39] offset:1024\n"
"s_mul_i32 s37, s36, 245760\n"
"s_add_u32 s40, s82, s37\n"
"s_addc_u32 s41, s83, 0\n"
"v_mfma_f32_16x16x32_bf16 v[196:199], a[80:83], a[52:55], v[196:199]\n"
"ds_read_b64_tr_b16 a[32:33], v44 offset:16384\n"
"v_lshlrev_b32_e32 v14, 4, v13\n"
"v_mfma_f32_16x16x32_bf16 v[212:215], a[84:87], a[52:55], v[212:215]\n"
"ds_read_b64_tr_b16 a[34:35], v44 offset:16896\n"
"v_or3_b32 v7, v11, v7, v14\n"
"v_mfma_f32_16x16x32_bf16 v[228:231], a[88:91], a[52:55], v[228:231]\n"
"ds_read_b64_tr_b16 a[36:37], v45 offset:16384\n"
"v_lshl_or_b32 v11, s16, 6, v13\n"
"v_mfma_f32_16x16x32_bf16 v[244:247], a[92:95], a[52:55], v[244:247]\n"
"ds_read_b64_tr_b16 a[38:39], v45 offset:16896\n"
"v_and_or_b32 v8, v8, 15, s15\n"
"v_mfma_f32_16x16x32_bf16 v[136:139], a[64:67], a[56:59], v[136:139]\n"
"global_load_lds_dwordx4 v78, s[38:39] offset:2048\n"
"s_mul_i32 s37, s36, 221184\n"
"s_add_u32 s42, s84, s37\n"
"s_addc_u32 s43, s85, 0\n"
"v_mfma_f32_16x16x32_bf16 v[152:155], a[68:71], a[56:59], v[152:155]\n"
"ds_read_b64_tr_b16 a[40:41], v46 offset:16384\n"
"v_lshl_or_b32 v12, s63, 2, v3\n"
"v_mfma_f32_16x16x32_bf16 v[168:171], a[72:75], a[56:59], v[168:171]\n"
"ds_read_b64_tr_b16 a[42:43], v46 offset:16896\n"
"v_or_b32_e32 v15, s57, v11\n"
"v_mfma_f32_16x16x32_bf16 v[184:187], a[76:79], a[56:59], v[184:187]\n"
"ds_read_b64_tr_b16 a[44:45], v47 offset:16384\n"
"v_mul_lo_u16_e32 v8, 0xcd, v8\n"
"v_mfma_f32_16x16x32_bf16 v[200:203], a[80:83], a[56:59], v[200:203]\n"
"ds_read_b64_tr_b16 a[46:47], v47 offset:16896\n"
"v_or_b32_e32 v6, 0x100, v6\n"
"v_mfma_f32_16x16x32_bf16 v[216:219], a[84:87], a[56:59], v[216:219]\n"
"global_load_lds_dwordx4 v79, s[38:39] offset:3072\n"
"s_add_u32 m0, s88, 20480\n"
"v_mfma_f32_16x16x32_bf16 v[232:235], a[88:91], a[56:59], v[232:235]\n"
"v_lshrrev_b32_e64 v12, v12, s12\n"
"v_mul_u32_u24_e32 v16, 0xccd, v15\n"
"s_mov_b32 s12, 0x28000\n"
"v_mfma_f32_16x16x32_bf16 v[248:251], a[92:95], a[56:59], v[248:251]\n"
"v_mov_b32_e32 v18, 20\n"
"v_lshrrev_b16_e32 v8, 10, v8\n"
"v_mfma_f32_16x16x32_bf16 v[140:143], a[64:67], a[60:63], v[140:143]\n"
"v_cndmask_b32_e64 v6, v5, v6, s[0:1]\n"
"v_mov_b32_e32 v9, 0x500\n"
"s_movk_i32 s0, 0x140\n"
"v_mfma_f32_16x16x32_bf16 v[156:159], a[68:71], a[60:63], v[156:159]\n"
"s_waitcnt vmcnt(12)\n"
"ds_read_b128 a[4:7], v52 offset:10240\n"
"v_mul_u32_u24_sdwa v17, v16, s12 dst_sel:DWORD dst_unused:UNUSED_PAD src0_sel:WORD_1 src1_sel:DWORD\n"
"v_mfma_f32_16x16x32_bf16 v[172:175], a[72:75], a[60:63], v[172:175]\n"
"global_load_lds_dwordx4 v80, s[38:39] offset:0\n"
"v_mfma_f32_16x16x32_bf16 v[188:191], a[76:79], a[60:63], v[188:191]\n"
"s_waitcnt vmcnt(11)\n"
"ds_read_b128 a[8:11], v52 offset:12288\n"
"v_mul_lo_u16_sdwa v16, v16, v18 dst_sel:DWORD dst_unused:UNUSED_PAD src0_sel:WORD_1 src1_sel:DWORD\n"
"v_mfma_f32_16x16x32_bf16 v[204:207], a[80:83], a[60:63], v[204:207]\n"
"s_waitcnt vmcnt(9)\n"
"ds_read_b128 a[12:15], v52 offset:14336\n"
"v_mul_u32_u24_e32 v8, s55, v8\n"
"v_mfma_f32_16x16x32_bf16 v[220:223], a[84:87], a[60:63], v[220:223]\n"
"v_mad_u32_u24 v9, v3, s0, v9\n"
"v_sub_u16_e32 v16, v15, v16\n"
"v_mfma_f32_16x16x32_bf16 v[236:239], a[88:91], a[60:63], v[236:239]\n"
"v_or3_b32 v6, v8, v6, v14\n"
"v_or_b32_e32 v8, 16, v15\n"
"v_mfma_f32_16x16x32_bf16 v[252:255], a[92:95], a[60:63], v[252:255]\n"
"global_load_lds_dwordx4 v81, s[38:39] offset:1024\n"
"s_waitcnt lgkmcnt(0)\n"
"v_mfma_f32_16x16x32_bf16 v[112:115], a[16:19], v[56:59], v[112:115]\n"
"ds_read_b128 a[48:51], v53 offset:8192\n"
"v_lshlrev_b16_e32 v16, 4, v16\n"
"v_mfma_f32_16x16x32_bf16 v[116:119], a[20:23], v[56:59], v[116:119]\n"
"ds_read_b64_tr_b16 a[64:65], v40 offset:16384\n"
"v_or_b32_e32 v17, v9, v17\n"
"v_mfma_f32_16x16x32_bf16 v[120:123], a[32:35], v[56:59], v[120:123]\n"
"ds_read_b64_tr_b16 a[66:67], v40 offset:16896\n"
"v_mul_u32_u24_e32 v15, 0xccd, v8\n"
"v_mfma_f32_16x16x32_bf16 v[124:127], a[36:39], v[56:59], v[124:127]\n"
"ds_read_b64_tr_b16 a[68:69], v41 offset:16384\n"
"v_add_u32_e32 v16, v17, v16\n"
"v_mfma_f32_16x16x32_bf16 v[128:131], a[16:19], a[0:3], v[128:131]\n"
"global_load_lds_dwordx4 v82, s[38:39] offset:2048\n"
"v_mfma_f32_16x16x32_bf16 v[144:147], a[20:23], a[0:3], v[144:147]\n"
"ds_read_b64_tr_b16 a[70:71], v41 offset:16896\n"
"v_mul_lo_u16_sdwa v17, v15, v18 dst_sel:DWORD dst_unused:UNUSED_PAD src0_sel:WORD_1 src1_sel:DWORD\n"
"v_mfma_f32_16x16x32_bf16 v[160:163], a[24:27], a[0:3], v[160:163]\n"
"ds_read_b64_tr_b16 a[72:73], v42 offset:16384\n"
"v_sub_u16_e32 v8, v8, v17\n"
"v_mfma_f32_16x16x32_bf16 v[176:179], a[28:31], a[0:3], v[176:179]\n"
"ds_read_b64_tr_b16 a[74:75], v42 offset:16896\n"
"v_mul_u32_u24_sdwa v15, v15, s12 dst_sel:DWORD dst_unused:UNUSED_PAD src0_sel:WORD_1 src1_sel:DWORD\n"
"v_mfma_f32_16x16x32_bf16 v[192:195], a[32:35], a[0:3], v[192:195]\n"
"ds_read_b64_tr_b16 a[76:77], v43 offset:16384\n"
"v_lshlrev_b16_e32 v8, 4, v8\n"
"v_mfma_f32_16x16x32_bf16 v[208:211], a[36:39], a[0:3], v[208:211]\n"
"global_load_lds_dwordx4 v83, s[38:39] offset:3072\n"
"s_lshl_b32 s37, s36, 7\n"
"s_add_u32 s38, s80, s37\n"
"s_addc_u32 s39, s81, 0\n"
"s_add_u32 m0, s89, 0\n"
"v_mfma_f32_16x16x32_bf16 v[224:227], a[40:43], a[0:3], v[224:227]\n"
"ds_read_b64_tr_b16 a[78:79], v43 offset:16896\n"
"v_or_b32_e32 v15, v9, v15\n"
"v_mfma_f32_16x16x32_bf16 v[240:243], a[44:47], a[0:3], v[240:243]\n"
"ds_read_b64_tr_b16 a[80:81], v48 offset:16384\n"
"v_lshrrev_b32_e64 v10, v10, s62\n"
"v_mfma_f32_16x16x32_bf16 v[132:135], a[16:19], a[4:7], v[132:135]\n"
"ds_read_b64_tr_b16 a[82:83], v48 offset:16896\n"
"v_add_u32_e32 v8, v15, v8\n"
"v_mfma_f32_16x16x32_bf16 v[148:151], a[20:23], a[4:7], v[148:151]\n"
"ds_read_b64_tr_b16 a[84:85], v49 offset:16384\n"
"v_cndmask_b32_e32 v61, v8, v6, vcc\n"
"v_mfma_f32_16x16x32_bf16 v[164:167], a[24:27], a[4:7], v[164:167]\n"
"global_load_lds_dwordx4 v84, s[40:41] offset:0\n"
"v_mfma_f32_16x16x32_bf16 v[180:183], a[28:31], a[4:7], v[180:183]\n"
"ds_read_b64_tr_b16 a[86:87], v49 offset:16896\n"
"v_and_or_b32 v6, v10, 15, s15\n"
"v_mfma_f32_16x16x32_bf16 v[196:199], a[32:35], a[4:7], v[196:199]\n"
"ds_read_b64_tr_b16 a[88:89], v50 offset:16384\n"
"v_mul_lo_u16_e32 v6, 0xcd, v6\n"
"v_mfma_f32_16x16x32_bf16 v[212:215], a[36:39], a[4:7], v[212:215]\n"
"ds_read_b64_tr_b16 a[90:91], v50 offset:16896\n"
"v_lshrrev_b16_e32 v6, 10, v6\n"
"v_mfma_f32_16x16x32_bf16 v[228:231], a[40:43], a[4:7], v[228:231]\n"
"ds_read_b64_tr_b16 a[92:93], v51 offset:16384\n"
"v_mul_u32_u24_e32 v6, s55, v6\n"
"v_mfma_f32_16x16x32_bf16 v[244:247], a[44:47], a[4:7], v[244:247]\n"
"global_load_lds_dwordx4 v85, s[40:41] offset:1024\n"
"s_add_u32 m0, s89, 2048\n"
"v_mfma_f32_16x16x32_bf16 v[136:139], a[16:19], a[8:11], v[136:139]\n"
"ds_read_b64_tr_b16 a[94:95], v51 offset:16896\n"
"v_or3_b32 v4, v6, v4, v14\n"
"v_mfma_f32_16x16x32_bf16 v[152:155], a[20:23], a[8:11], v[152:155]\n"
"ds_read_b128 a[52:55], v53 offset:10240\n"
"v_bitop3_b32 v6, s57, 32, v11 bitop3:0x36\n"
"v_mfma_f32_16x16x32_bf16 v[168:171], a[24:27], a[8:11], v[168:171]\n"
"ds_read_b128 a[56:59], v53 offset:12288\n"
"v_cndmask_b32_e32 v60, v16, v7, vcc\n"
"v_mfma_f32_16x16x32_bf16 v[184:187], a[28:31], a[8:11], v[184:187]\n"
"ds_read_b128 a[60:63], v53 offset:14336\n"
"v_mul_u32_u24_e32 v7, 0xccd, v6\n"
"v_mfma_f32_16x16x32_bf16 v[200:203], a[32:35], a[8:11], v[200:203]\n"
"global_load_lds_dwordx4 v86, s[42:43] offset:0\n"
"v_mfma_f32_16x16x32_bf16 v[216:219], a[36:39], a[8:11], v[216:219]\n"
"v_mul_lo_u16_sdwa v8, v7, v18 dst_sel:DWORD dst_unused:UNUSED_PAD src0_sel:WORD_1 src1_sel:DWORD\n"
"v_sub_u16_e32 v6, v6, v8\n"
"v_mfma_f32_16x16x32_bf16 v[232:235], a[40:43], a[8:11], v[232:235]\n"
"v_mul_u32_u24_sdwa v7, v7, s12 dst_sel:DWORD dst_unused:UNUSED_PAD src0_sel:WORD_1 src1_sel:DWORD\n"
"v_lshlrev_b16_e32 v6, 4, v6\n"
"v_mfma_f32_16x16x32_bf16 v[248:251], a[44:47], a[8:11], v[248:251]\n"
"v_or_b32_e32 v7, v9, v7\n"
"v_add_u32_e32 v6, v7, v6\n"
"v_mfma_f32_16x16x32_bf16 v[140:143], a[16:19], a[12:15], v[140:143]\n"
"v_cndmask_b32_e32 v62, v6, v4, vcc\n"
"v_lshlrev_b32_e32 v4, 9, v12\n"
"v_mfma_f32_16x16x32_bf16 v[156:159], a[20:23], a[12:15], v[156:159]\n"
"global_load_lds_dwordx4 v87, s[42:43] offset:1024\n"
"s_add_u32 m0, s88, 0\n"
"v_mfma_f32_16x16x32_bf16 v[172:175], a[24:27], a[12:15], v[172:175]\n"
"v_and_b32_e32 v4, 0x200, v4\n"
"v_or_b32_e32 v4, 0x100, v4\n"
"v_mfma_f32_16x16x32_bf16 v[188:191], a[28:31], a[12:15], v[188:191]\n"
"v_cmp_gt_u32_e64 s[0:1], s64, v3\n"
"v_mov_b32_e32 v15, s95\n"
"v_mfma_f32_16x16x32_bf16 v[204:207], a[32:35], a[12:15], v[204:207]\n"
"s_cmp_gt_u32 s30, 1\n"
"s_cselect_b32 s58, 0, s50\n"
"v_cndmask_b32_e64 v4, v5, v4, s[0:1]\n"
"v_mfma_f32_16x16x32_bf16 v[220:223], a[36:39], a[12:15], v[220:223]\n"
"v_cndmask_b32_e64 v5, 4, 0, s[0:1]\n"
"v_or_b32_e32 v5, v5, v3\n"
"v_mfma_f32_16x16x32_bf16 v[236:239], a[40:43], a[12:15], v[236:239]\n"
"global_load_lds_dwordx4 v76, s[38:39] offset:0\n"
"s_add_u32 s36, s36, 1\n"
"s_cmp_eq_u32 s36, 10\n"
"s_cselect_b32 s36, 0, s36\n"
"v_mfma_f32_16x16x32_bf16 v[252:255], a[44:47], a[12:15], v[252:255]\n"
"v_lshlrev_b32_e32 v5, 2, v5\n"
"v_lshrrev_b32_e64 v5, v5, s65\n"
"s_waitcnt vmcnt(13) lgkmcnt(0)\n"
"s_barrier\n"
"v_mfma_f32_16x16x32_bf16 v[128:131], a[64:67], a[48:51], v[128:131]\n"
"s_waitcnt vmcnt(11)\n"
"ds_read_b128 a[0:3], v52 offset:16384\n"
"v_and_or_b32 v5, v5, 15, s15\n"
"v_mfma_f32_16x16x32_bf16 v[144:147], a[68:71], a[48:51], v[144:147]\n"
"ds_read_b64_tr_b16 a[16:17], v36 offset:32768\n"
"v_mul_lo_u16_e32 v5, 0xcd, v5\n"
"v_mfma_f32_16x16x32_bf16 v[160:163], a[72:75], a[48:51], v[160:163]\n"
"ds_read_b64_tr_b16 a[18:19], v36 offset:33280\n"
"v_lshrrev_b16_e32 v5, 10, v5\n"
"v_mfma_f32_16x16x32_bf16 v[176:179], a[76:79], a[48:51], v[176:179]\n"
"global_load_lds_dwordx4 v77, s[38:39] offset:1024\n"
"s_mul_i32 s37, s36, 245760\n"
"s_add_u32 s40, s82, s37\n"
"s_addc_u32 s41, s83, 0\n"
"v_mfma_f32_16x16x32_bf16 v[192:195], a[80:83], a[48:51], v[192:195]\n"
"ds_read_b64_tr_b16 a[20:21], v37 offset:32768\n"
"v_mul_u32_u24_e32 v5, s55, v5\n"
"v_mfma_f32_16x16x32_bf16 v[208:211], a[84:87], a[48:51], v[208:211]\n"
"ds_read_b64_tr_b16 a[22:23], v37 offset:33280\n"
"v_or3_b32 v4, v5, v4, v14\n"
"v_mfma_f32_16x16x32_bf16 v[224:227], a[88:91], a[48:51], v[224:227]\n"
"ds_read_b64_tr_b16 a[24:25], v38 offset:32768\n"
"v_bitop3_b32 v5, s57, 48, v11 bitop3:0x36\n"
"v_mfma_f32_16x16x32_bf16 v[240:243], a[92:95], a[48:51], v[240:243]\n"
"ds_read_b64_tr_b16 a[26:27], v38 offset:33280\n"
"v_mul_u32_u24_e32 v6, 0xccd, v5\n"
"v_mfma_f32_16x16x32_bf16 v[132:135], a[64:67], a[52:55], v[132:135]\n"
"global_load_lds_dwordx4 v78, s[38:39] offset:2048\n"
"s_mul_i32 s37, s36, 221184\n"
"s_add_u32 s42, s84, s37\n"
"s_addc_u32 s43, s85, 0\n"
"v_mfma_f32_16x16x32_bf16 v[148:151], a[68:71], a[52:55], v[148:151]\n"
"ds_read_b64_tr_b16 a[28:29], v39 offset:32768\n"
"v_mul_lo_u16_sdwa v7, v6, v18 dst_sel:DWORD dst_unused:UNUSED_PAD src0_sel:WORD_1 src1_sel:DWORD\n"
"v_mfma_f32_16x16x32_bf16 v[164:167], a[72:75], a[52:55], v[164:167]\n"
"ds_read_b64_tr_b16 a[30:31], v39 offset:33280\n"
"v_sub_u16_e32 v5, v5, v7\n"
"v_mfma_f32_16x16x32_bf16 v[180:183], a[76:79], a[52:55], v[180:183]\n"
"ds_read_b64_tr_b16 a[32:33], v44 offset:32768\n"
"v_mul_u32_u24_sdwa v6, v6, s12 dst_sel:DWORD dst_unused:UNUSED_PAD src0_sel:WORD_1 src1_sel:DWORD\n"
"v_mfma_f32_16x16x32_bf16 v[196:199], a[80:83], a[52:55], v[196:199]\n"
"ds_read_b64_tr_b16 a[34:35], v44 offset:33280\n"
"v_or_b32_e32 v6, v9, v6\n"
"v_mfma_f32_16x16x32_bf16 v[212:215], a[84:87], a[52:55], v[212:215]\n"
"global_load_lds_dwordx4 v79, s[38:39] offset:3072\n"
"s_add_u32 m0, s88, 4096\n"
"v_mfma_f32_16x16x32_bf16 v[228:231], a[88:91], a[52:55], v[228:231]\n"
"ds_read_b64_tr_b16 a[36:37], v45 offset:32768\n"
"v_lshlrev_b16_e32 v5, 4, v5\n"
"v_mfma_f32_16x16x32_bf16 v[244:247], a[92:95], a[52:55], v[244:247]\n"
"ds_read_b64_tr_b16 a[38:39], v45 offset:33280\n"
"v_add_u32_e32 v5, v6, v5\n"
"v_mfma_f32_16x16x32_bf16 v[136:139], a[64:67], a[56:59], v[136:139]\n"
"ds_read_b64_tr_b16 a[40:41], v46 offset:32768\n"
"v_cndmask_b32_e32 v63, v5, v4, vcc\n"
"v_mfma_f32_16x16x32_bf16 v[152:155], a[68:71], a[56:59], v[152:155]\n"
"ds_read_b64_tr_b16 a[42:43], v46 offset:33280\n"
"v_lshrrev_b32_e32 v4, 1, v0\n"
"v_mfma_f32_16x16x32_bf16 v[168:171], a[72:75], a[56:59], v[168:171]\n"
"global_load_lds_dwordx4 v80, s[38:39] offset:0\n"
"v_mfma_f32_16x16x32_bf16 v[184:187], a[76:79], a[56:59], v[184:187]\n"
"ds_read_b64_tr_b16 a[44:45], v47 offset:32768\n"
"v_and_b32_e32 v5, 3, v0\n"
"v_mfma_f32_16x16x32_bf16 v[200:203], a[80:83], a[56:59], v[200:203]\n"
"ds_read_b64_tr_b16 a[46:47], v47 offset:33280\n"
"v_and_or_b32 v4, v4, 12, v5\n"
"v_mfma_f32_16x16x32_bf16 v[216:219], a[84:87], a[56:59], v[216:219]\n"
"v_bfe_u32 v5, v0, 5, 1\n"
"v_bfe_u32 v6, v0, 2, 1\n"
"v_mfma_f32_16x16x32_bf16 v[232:235], a[88:91], a[56:59], v[232:235]\n"
"v_lshlrev_b32_e32 v4, 4, v4\n"
"v_lshlrev_b32_e32 v7, 9, v5\n"
"v_mfma_f32_16x16x32_bf16 v[248:251], a[92:95], a[56:59], v[248:251]\n"
"global_load_lds_dwordx4 v81, s[38:39] offset:1024\n"
"v_mfma_f32_16x16x32_bf16 v[140:143], a[64:67], a[60:63], v[140:143]\n"
"v_lshlrev_b32_e32 v8, 8, v6\n"
"v_or3_b32 v7, v7, v8, v4\n"
"v_mfma_f32_16x16x32_bf16 v[156:159], a[68:71], a[60:63], v[156:159]\n"
"s_waitcnt vmcnt(14)\n"
"ds_read_b128 a[4:7], v52 offset:18432\n"
"v_lshlrev_b32_e32 v6, 3, v6\n"
"v_mfma_f32_16x16x32_bf16 v[172:175], a[72:75], a[60:63], v[172:175]\n"
"s_waitcnt vmcnt(12)\n"
"ds_read_b128 a[8:11], v52 offset:20480\n"
"v_mul_u32_u24_e32 v8, 0xa00, v5\n"
"v_mfma_f32_16x16x32_bf16 v[188:191], a[76:79], a[60:63], v[188:191]\n"
"s_waitcnt vmcnt(10)\n"
"ds_read_b128 a[12:15], v52 offset:22528\n"
"v_or3_b32 v4, v4, v6, v8\n"
"v_mfma_f32_16x16x32_bf16 v[204:207], a[80:83], a[60:63], v[204:207]\n"
"global_load_lds_dwordx4 v82, s[38:39] offset:2048\n"
"v_mfma_f32_16x16x32_bf16 v[220:223], a[84:87], a[60:63], v[220:223]\n"
"v_and_b32_e32 v6, 63, v0\n"
"v_lshlrev_b32_e32 v9, 4, v6\n"
"v_mfma_f32_16x16x32_bf16 v[236:239], a[88:91], a[60:63], v[236:239]\n"
"v_and_b32_e32 v10, 0x100, v9\n"
"v_bfe_u32 v11, v0, 4, 1\n"
"v_mfma_f32_16x16x32_bf16 v[252:255], a[92:95], a[60:63], v[252:255]\n"
"v_or3_b32 v8, v14, v10, v8\n"
"v_mul_u32_u24_e32 v10, 0x500, v5\n"
"s_waitcnt lgkmcnt(0)\n"
"v_mfma_f32_16x16x32_bf16 v[112:115], a[16:19], v[56:59], v[112:115]\n"
"ds_read_b128 a[48:51], v53 offset:16384\n"
"v_lshlrev_b32_e32 v12, 3, v11\n"
"v_mfma_f32_16x16x32_bf16 v[116:119], a[20:23], v[56:59], v[116:119]\n"
"global_load_lds_dwordx4 v83, s[38:39] offset:3072\n"
"s_lshl_b32 s37, s36, 7\n"
"s_add_u32 s38, s80, s37\n"
"s_addc_u32 s39, s81, 0\n"
"s_add_u32 m0, s89, 16384\n"
"v_mfma_f32_16x16x32_bf16 v[120:123], a[32:35], v[56:59], v[120:123]\n"
"ds_read_b64_tr_b16 a[64:65], v40 offset:32768\n"
"v_or3_b32 v10, v12, v10, v14\n"
"v_mfma_f32_16x16x32_bf16 v[124:127], a[36:39], v[56:59], v[124:127]\n"
"ds_read_b64_tr_b16 a[66:67], v40 offset:33280\n"
"v_lshlrev_b32_e32 v11, 9, v11\n"
"v_mfma_f32_16x16x32_bf16 v[128:131], a[16:19], a[0:3], v[128:131]\n"
"ds_read_b64_tr_b16 a[68:69], v41 offset:32768\n"
"v_lshlrev_b32_e32 v12, 8, v5\n"
"v_mfma_f32_16x16x32_bf16 v[144:147], a[20:23], a[0:3], v[144:147]\n"
"ds_read_b64_tr_b16 a[70:71], v41 offset:33280\n"
"v_or3_b32 v11, v11, v12, v14\n"
"v_mfma_f32_16x16x32_bf16 v[160:163], a[24:27], a[0:3], v[160:163]\n"
"global_load_lds_dwordx4 v84, s[40:41] offset:0\n"
"v_mfma_f32_16x16x32_bf16 v[176:179], a[28:31], a[0:3], v[176:179]\n"
"ds_read_b64_tr_b16 a[72:73], v42 offset:32768\n"
"v_lshlrev_b32_e32 v12, 2, v13\n"
"v_mfma_f32_16x16x32_bf16 v[192:195], a[32:35], a[0:3], v[192:195]\n"
"ds_read_b64_tr_b16 a[74:75], v42 offset:33280\n"
"v_mov_b32_e32 v14, 0x25200\n"
"v_mfma_f32_16x16x32_bf16 v[208:211], a[36:39], a[0:3], v[208:211]\n"
"ds_read_b64_tr_b16 a[76:77], v43 offset:32768\n"
"v_bitop3_b32 v70, s78, v12, v14 bitop3:0xde\n"
"v_mfma_f32_16x16x32_bf16 v[224:227], a[40:43], a[0:3], v[224:227]\n"
"ds_read_b64_tr_b16 a[78:79], v43 offset:33280\n"
"v_or_b32_e32 v64, s51, v12\n"
"v_mfma_f32_16x16x32_bf16 v[240:243], a[44:47], a[0:3], v[240:243]\n"
"global_load_lds_dwordx4 v85, s[40:41] offset:1024\n"
"s_add_u32 m0, s89, 18432\n"
"v_mfma_f32_16x16x32_bf16 v[132:135], a[16:19], a[4:7], v[132:135]\n"
"ds_read_b64_tr_b16 a[80:81], v48 offset:32768\n"
"v_or_b32_e32 v69, s35, v12\n"
"v_mfma_f32_16x16x32_bf16 v[148:151], a[20:23], a[4:7], v[148:151]\n"
"ds_read_b64_tr_b16 a[82:83], v48 offset:33280\n"
"v_and_b32_e32 v12, 31, v0\n"
"s_mul_i32 s0, s30, 5\n"
"v_mfma_f32_16x16x32_bf16 v[164:167], a[24:27], a[4:7], v[164:167]\n"
"ds_read_b64_tr_b16 a[84:85], v49 offset:32768\n"
"v_min_u32_e32 v14, 26, v12\n"
"v_mfma_f32_16x16x32_bf16 v[180:183], a[28:31], a[4:7], v[180:183]\n"
"ds_read_b64_tr_b16 a[86:87], v49 offset:33280\n"
"v_add3_u32 v14, s0, 32, v14\n"
"v_mfma_f32_16x16x32_bf16 v[196:199], a[32:35], a[4:7], v[196:199]\n"
"global_load_lds_dwordx4 v86, s[42:43] offset:0\n"
"v_mfma_f32_16x16x32_bf16 v[212:215], a[36:39], a[4:7], v[212:215]\n"
"ds_read_b64_tr_b16 a[88:89], v50 offset:32768\n"
"v_cmp_eq_u32_e32 vcc, 21, v12\n"
"v_mfma_f32_16x16x32_bf16 v[228:231], a[40:43], a[4:7], v[228:231]\n"
"ds_read_b64_tr_b16 a[90:91], v50 offset:33280\n"
"v_cmp_gt_u32_e64 s[0:1], 21, v12\n"
"v_mfma_f32_16x16x32_bf16 v[244:247], a[44:47], a[4:7], v[244:247]\n"
"ds_read_b64_tr_b16 a[92:93], v51 offset:32768\n"
"v_mul_u32_u24_e32 v5, 20, v5\n"
"v_mfma_f32_16x16x32_bf16 v[136:139], a[16:19], a[8:11], v[136:139]\n"
"ds_read_b64_tr_b16 a[94:95], v51 offset:33280\n"
"v_cndmask_b32_e32 v14, v14, v15, vcc\n"
"v_mfma_f32_16x16x32_bf16 v[152:155], a[20:23], a[8:11], v[152:155]\n"
"global_load_lds_dwordx4 v87, s[42:43] offset:1024\n"
"s_add_u32 m0, s88, 8192\n"
"v_mfma_f32_16x16x32_bf16 v[168:171], a[24:27], a[8:11], v[168:171]\n"
"ds_read_b128 a[52:55], v53 offset:18432\n"
"v_add_u32_e32 v15, s96, v12\n"
"v_mfma_f32_16x16x32_bf16 v[184:187], a[28:31], a[8:11], v[184:187]\n"
"ds_read_b128 a[56:59], v53 offset:20480\n"
"v_cmp_gt_u32_e32 vcc, 7, v12\n"
"v_mfma_f32_16x16x32_bf16 v[200:203], a[32:35], a[8:11], v[200:203]\n"
"ds_read_b128 a[60:63], v53 offset:22528\n"
"v_mov_b32_e32 v19, 0x3f803f80\n"
"v_mfma_f32_16x16x32_bf16 v[216:219], a[36:39], a[8:11], v[216:219]\n"
"v_add_lshl_u32 v5, v5, v12, 4\n"
"v_cndmask_b32_e32 v15, 26, v15, vcc\n"
"v_mfma_f32_16x16x32_bf16 v[232:235], a[40:43], a[8:11], v[232:235]\n"
"global_load_lds_dwordx4 v76, s[38:39] offset:0\n"
"s_add_u32 s36, s36, 1\n"
"s_cmp_eq_u32 s36, 10\n"
"s_cselect_b32 s36, 0, s36\n"
"v_mfma_f32_16x16x32_bf16 v[248:251], a[44:47], a[8:11], v[248:251]\n"
"v_subrev_co_u32_e32 v16, vcc, 8, v12\n"
"v_add_u32_e32 v16, s29, v16\n"
"v_mfma_f32_16x16x32_bf16 v[140:143], a[16:19], a[12:15], v[140:143]\n"
"v_cndmask_b32_e64 v14, v14, v16, s[0:1]\n"
"s_movk_i32 s0, 0x48\n"
"v_add_u32_e32 v16, 0x1fffffe0, v14\n"
"v_mfma_f32_16x16x32_bf16 v[156:159], a[20:23], a[12:15], v[156:159]\n"
"v_lshl_add_u32 v17, v14, 2, s86\n"
"v_cmp_gt_u32_e64 s[0:1], s0, v14\n"
"v_mfma_f32_16x16x32_bf16 v[172:175], a[24:27], a[12:15], v[172:175]\n"
"v_add_u32_e32 v15, s87, v15\n"
"s_mul_i32 s16, s18, 54\n"
"v_cndmask_b32_e64 v16, v17, v16, s[0:1]\n"
"v_mfma_f32_16x16x32_bf16 v[188:191], a[28:31], a[12:15], v[188:191]\n"
"global_load_lds_dwordx4 v77, s[38:39] offset:1024\n"
"s_mul_i32 s37, s36, 245760\n"
"s_add_u32 s40, s82, s37\n"
"s_addc_u32 s41, s83, 0\n"
"v_mfma_f32_16x16x32_bf16 v[204:207], a[32:35], a[12:15], v[204:207]\n"
"v_and_b32_e32 v17, 15, v14\n"
"v_cmp_gt_u32_e64 s[0:1], 48, v14\n"
"v_mfma_f32_16x16x32_bf16 v[220:223], a[36:39], a[12:15], v[220:223]\n"
"v_lshl_add_u32 v24, s30, 9, v8\n"
"v_lshl_add_u32 v8, s19, 9, v8\n"
"v_mfma_f32_16x16x32_bf16 v[236:239], a[40:43], a[12:15], v[236:239]\n"
"v_cndmask_b32_e64 v14, v16, v17, s[0:1]\n"
"v_add_u32_e32 v14, s79, v14\n"
"v_mfma_f32_16x16x32_bf16 v[252:255], a[44:47], a[12:15], v[252:255]\n"
"v_cndmask_b32_e32 v14, v14, v15, vcc\n"
"v_or_b32_e32 v15, s90, v13\n"
"s_waitcnt vmcnt(14) lgkmcnt(0)\n"
"s_barrier\n"
"v_mfma_f32_16x16x32_bf16 v[128:131], a[64:67], a[48:51], v[128:131]\n"
"global_load_lds_dwordx4 v78, s[38:39] offset:2048\n"
"s_mul_i32 s37, s36, 221184\n"
"s_add_u32 s42, s84, s37\n"
"s_addc_u32 s43, s85, 0\n"
"v_mfma_f32_16x16x32_bf16 v[144:147], a[68:71], a[48:51], v[144:147]\n"
"s_waitcnt vmcnt(13)\n"
"ds_read_b128 a[0:3], v52 offset:0\n"
"v_mov_b32_e32 v16, 0x2f000\n"
"v_mfma_f32_16x16x32_bf16 v[160:163], a[72:75], a[48:51], v[160:163]\n"
"ds_read_b64_tr_b16 a[16:17], v36 offset:0\n"
"v_lshl_add_u32 v15, v15, 4, v16\n"
"v_mfma_f32_16x16x32_bf16 v[176:179], a[76:79], a[48:51], v[176:179]\n"
"ds_read_b64_tr_b16 a[18:19], v36 offset:512\n"
"v_mov_b32_e32 v16, 0xa000\n"
"v_mfma_f32_16x16x32_bf16 v[192:195], a[80:83], a[48:51], v[192:195]\n"
"ds_read_b64_tr_b16 a[20:21], v37 offset:0\n"
"v_lshl_add_u32 v14, v14, 3, v16\n"
"v_mfma_f32_16x16x32_bf16 v[208:211], a[84:87], a[48:51], v[208:211]\n"
"global_load_lds_dwordx4 v79, s[38:39] offset:3072\n"
"s_add_u32 m0, s88, 12288\n"
"v_mfma_f32_16x16x32_bf16 v[224:227], a[88:91], a[48:51], v[224:227]\n"
"ds_read_b64_tr_b16 a[22:23], v37 offset:512\n"
"v_cmp_gt_u32_e32 vcc, 32, v6\n"
"v_mfma_f32_16x16x32_bf16 v[240:243], a[92:95], a[48:51], v[240:243]\n"
"ds_read_b64_tr_b16 a[24:25], v38 offset:0\n"
"v_mov_b32_e32 v16, s94\n"
"v_mfma_f32_16x16x32_bf16 v[132:135], a[64:67], a[52:55], v[132:135]\n"
"ds_read_b64_tr_b16 a[26:27], v38 offset:512\n"
"v_mov_b32_e32 v17, s93\n"
"v_mfma_f32_16x16x32_bf16 v[148:151], a[68:71], a[52:55], v[148:151]\n"
"ds_read_b64_tr_b16 a[28:29], v39 offset:0\n"
"v_cndmask_b32_e32 v14, v14, v15, vcc\n"
"v_mfma_f32_16x16x32_bf16 v[164:167], a[72:75], a[52:55], v[164:167]\n"
"global_load_lds_dwordx4 v80, s[38:39] offset:0\n"
"v_mfma_f32_16x16x32_bf16 v[180:183], a[76:79], a[52:55], v[180:183]\n"
"ds_read_b64_tr_b16 a[30:31], v39 offset:512\n"
"v_and_b32_e32 v15, 1, v0\n"
"v_mfma_f32_16x16x32_bf16 v[196:199], a[80:83], a[52:55], v[196:199]\n"
"ds_read_b64_tr_b16 a[32:33], v44 offset:0\n"
"v_cmp_eq_u32_e32 vcc, 0, v15\n"
"s_mul_i32 s1, s19, 0x3c00\n"
"v_mfma_f32_16x16x32_bf16 v[212:215], a[84:87], a[52:55], v[212:215]\n"
"ds_read_b64_tr_b16 a[34:35], v44 offset:512\n"
"s_mul_i32 s18, s18, 0x19000\n"
"v_cndmask_b32_e32 v15, v16, v17, vcc\n"
"v_mfma_f32_16x16x32_bf16 v[228:231], a[88:91], a[52:55], v[228:231]\n"
"ds_read_b64_tr_b16 a[36:37], v45 offset:0\n"
"v_mov_b32_e32 v16, s92\n"
"v_mfma_f32_16x16x32_bf16 v[244:247], a[92:95], a[52:55], v[244:247]\n"
"global_load_lds_dwordx4 v81, s[38:39] offset:1024\n"
"v_mfma_f32_16x16x32_bf16 v[136:139], a[64:67], a[56:59], v[136:139]\n"
"ds_read_b64_tr_b16 a[38:39], v45 offset:512\n"
"v_mov_b32_e32 v17, s91\n"
"v_mfma_f32_16x16x32_bf16 v[152:155], a[68:71], a[56:59], v[152:155]\n"
"ds_read_b64_tr_b16 a[40:41], v46 offset:0\n"
"v_cndmask_b32_e32 v16, v16, v17, vcc\n"
"v_mfma_f32_16x16x32_bf16 v[168:171], a[72:75], a[56:59], v[168:171]\n"
"ds_read_b64_tr_b16 a[42:43], v46 offset:512\n"
"v_and_b32_e32 v17, 2, v0\n"
"v_mfma_f32_16x16x32_bf16 v[184:187], a[76:79], a[56:59], v[184:187]\n"
"ds_read_b64_tr_b16 a[44:45], v47 offset:0\n"
"v_cmp_eq_u32_e32 vcc, 0, v17\n"
"v_mfma_f32_16x16x32_bf16 v[200:203], a[80:83], a[56:59], v[200:203]\n"
"global_load_lds_dwordx4 v82, s[38:39] offset:2048\n"
"v_mfma_f32_16x16x32_bf16 v[216:219], a[84:87], a[56:59], v[216:219]\n"
"ds_read_b64_tr_b16 a[46:47], v47 offset:512\n"
"v_lshlrev_b32_e32 v17, 3, v3\n"
"v_mfma_f32_16x16x32_bf16 v[232:235], a[88:91], a[56:59], v[232:235]\n"
"v_mul_u32_u24_e32 v3, 0x1400, v3\n"
"v_cndmask_b32_e32 v15, v15, v16, vcc\n"
"v_mfma_f32_16x16x32_bf16 v[248:251], a[92:95], a[56:59], v[248:251]\n"
"v_mov_b32_e32 v16, 0x8000\n"
"v_lshl_add_u32 v16, v0, 3, v16\n"
"v_mfma_f32_16x16x32_bf16 v[140:143], a[64:67], a[60:63], v[140:143]\n"
"v_and_b32_e32 v0, 48, v0\n"
"v_or_b32_e32 v67, s35, v0\n"
"v_mfma_f32_16x16x32_bf16 v[156:159], a[68:71], a[60:63], v[156:159]\n"
"global_load_lds_dwordx4 v83, s[38:39] offset:3072\n"
"s_lshl_b32 s37, s36, 7\n"
"s_add_u32 s38, s80, s37\n"
"s_addc_u32 s39, s81, 0\n"
"s_add_u32 m0, s89, 32768\n"
"v_mfma_f32_16x16x32_bf16 v[172:175], a[72:75], a[60:63], v[172:175]\n"
"s_waitcnt vmcnt(16)\n"
"ds_read_b128 a[4:7], v52 offset:2048\n"
"s_cmp_gt_u32 s30, 1\n"
"s_cselect_b32 s35, s50, 0\n"
"v_mfma_f32_16x16x32_bf16 v[188:191], a[76:79], a[60:63], v[188:191]\n"
"s_waitcnt vmcnt(14)\n"
"ds_read_b128 a[8:11], v52 offset:4096\n"
"s_lshl_b32 s0, s14, 10\n"
"v_cmp_eq_u32_e32 vcc, 20, v12\n"
"v_mfma_f32_16x16x32_bf16 v[204:207], a[80:83], a[60:63], v[204:207]\n"
"s_waitcnt vmcnt(12)\n"
"ds_read_b128 a[12:15], v52 offset:6144\n"
"s_add_i32 s34, s0, s34\n"
"s_mul_i32 s0, s30, 0x3c00\n"
"v_mfma_f32_16x16x32_bf16 v[220:223], a[84:87], a[60:63], v[220:223]\n"
"v_cndmask_b32_e32 v12, 0, v19, vcc\n"
"v_or_b32_e32 v19, 0x19000, v9\n"
"v_mfma_f32_16x16x32_bf16 v[236:239], a[88:91], a[60:63], v[236:239]\n"
"global_load_lds_dwordx4 v84, s[40:41] offset:0\n"
"v_mfma_f32_16x16x32_bf16 v[252:255], a[92:95], a[60:63], v[252:255]\n"
"v_add_u32_e32 v20, s0, v19\n"
"s_mul_i32 s0, s22, 0x2800\n"
"v_or_b32_e32 v23, s0, v9\n"
"s_waitcnt lgkmcnt(0)\n"
"v_mfma_f32_16x16x32_bf16 v[112:115], a[16:19], v[56:59], v[112:115]\n"
"ds_read_b128 a[48:51], v53 offset:0\n"
"s_lshl_b32 s0, s17, 5\n"
"s_add_i32 s0, s27, s0\n"
"v_mfma_f32_16x16x32_bf16 v[116:119], a[20:23], v[56:59], v[116:119]\n"
"ds_read_b64_tr_b16 a[64:65], v40 offset:0\n"
"s_mulk_i32 s0, 0xa00\n"
"s_lshl_b32 s50, s13, 12\n"
"v_mfma_f32_16x16x32_bf16 v[120:123], a[32:35], v[56:59], v[120:123]\n"
"ds_read_b64_tr_b16 a[66:67], v40 offset:512\n"
"s_add_i32 s0, s0, 0x58000\n"
"v_mfma_f32_16x16x32_bf16 v[124:127], a[36:39], v[56:59], v[124:127]\n"
"global_load_lds_dwordx4 v85, s[40:41] offset:1024\n"
"s_add_u32 m0, s89, 34816\n"
"v_mfma_f32_16x16x32_bf16 v[128:131], a[16:19], a[0:3], v[128:131]\n"
"ds_read_b64_tr_b16 a[68:69], v41 offset:0\n"
"v_mfma_f32_16x16x32_bf16 v[144:147], a[20:23], a[0:3], v[144:147]\n"
"ds_read_b64_tr_b16 a[70:71], v41 offset:512\n"
"v_mfma_f32_16x16x32_bf16 v[160:163], a[24:27], a[0:3], v[160:163]\n"
"ds_read_b64_tr_b16 a[72:73], v42 offset:0\n"
"v_mfma_f32_16x16x32_bf16 v[176:179], a[28:31], a[0:3], v[176:179]\n"
"ds_read_b64_tr_b16 a[74:75], v42 offset:512\n"
"v_mfma_f32_16x16x32_bf16 v[192:195], a[32:35], a[0:3], v[192:195]\n"
"global_load_lds_dwordx4 v86, s[42:43] offset:0\n"
"v_mfma_f32_16x16x32_bf16 v[208:211], a[36:39], a[0:3], v[208:211]\n"
"ds_read_b64_tr_b16 a[76:77], v43 offset:0\n"
"v_mfma_f32_16x16x32_bf16 v[224:227], a[40:43], a[0:3], v[224:227]\n"
"ds_read_b64_tr_b16 a[78:79], v43 offset:512\n"
"v_mfma_f32_16x16x32_bf16 v[240:243], a[44:47], a[0:3], v[240:243]\n"
"ds_read_b64_tr_b16 a[80:81], v48 offset:0\n"
"v_mfma_f32_16x16x32_bf16 v[132:135], a[16:19], a[4:7], v[132:135]\n"
"ds_read_b64_tr_b16 a[82:83], v48 offset:512\n"
"v_mfma_f32_16x16x32_bf16 v[148:151], a[20:23], a[4:7], v[148:151]\n"
"global_load_lds_dwordx4 v87, s[42:43] offset:1024\n"
"s_add_u32 m0, s88, 16384\n"
"v_mfma_f32_16x16x32_bf16 v[164:167], a[24:27], a[4:7], v[164:167]\n"
"ds_read_b64_tr_b16 a[84:85], v49 offset:0\n"
"v_mfma_f32_16x16x32_bf16 v[180:183], a[28:31], a[4:7], v[180:183]\n"
"ds_read_b64_tr_b16 a[86:87], v49 offset:512\n"
"v_mfma_f32_16x16x32_bf16 v[196:199], a[32:35], a[4:7], v[196:199]\n"
"ds_read_b64_tr_b16 a[88:89], v50 offset:0\n"
"v_mfma_f32_16x16x32_bf16 v[212:215], a[36:39], a[4:7], v[212:215]\n"
"ds_read_b64_tr_b16 a[90:91], v50 offset:512\n"
"v_mfma_f32_16x16x32_bf16 v[228:231], a[40:43], a[4:7], v[228:231]\n"
"global_load_lds_dwordx4 v76, s[38:39] offset:0\n"
"s_add_u32 s36, s36, 1\n"
"s_cmp_eq_u32 s36, 10\n"
"s_cselect_b32 s36, 0, s36\n"
"v_mfma_f32_16x16x32_bf16 v[244:247], a[44:47], a[4:7], v[244:247]\n"
"ds_read_b64_tr_b16 a[92:93], v51 offset:0\n"
"v_mfma_f32_16x16x32_bf16 v[136:139], a[16:19], a[8:11], v[136:139]\n"
"ds_read_b64_tr_b16 a[94:95], v51 offset:512\n"
"v_mfma_f32_16x16x32_bf16 v[152:155], a[20:23], a[8:11], v[152:155]\n"
"ds_read_b128 a[52:55], v53 offset:2048\n"
"v_mfma_f32_16x16x32_bf16 v[168:171], a[24:27], a[8:11], v[168:171]\n"
"ds_read_b128 a[56:59], v53 offset:4096\n"
"v_mfma_f32_16x16x32_bf16 v[184:187], a[28:31], a[8:11], v[184:187]\n"
"global_load_lds_dwordx4 v77, s[38:39] offset:1024\n"
"s_mul_i32 s37, s36, 245760\n"
"s_add_u32 s40, s82, s37\n"
"s_addc_u32 s41, s83, 0\n"
"v_mfma_f32_16x16x32_bf16 v[200:203], a[32:35], a[8:11], v[200:203]\n"
"ds_read_b128 a[60:63], v53 offset:6144\n"
"v_mfma_f32_16x16x32_bf16 v[216:219], a[36:39], a[8:11], v[216:219]\n"
"s_add_u32 s0, s66, s0\n"
"v_add_u32_e32 v22, s1, v19\n"
"s_addc_u32 s1, s67, 0\n"
"v_mfma_f32_16x16x32_bf16 v[232:235], a[40:43], a[8:11], v[232:235]\n"
"s_lshl_b32 s12, s17, 1\n"
"s_add_i32 s12, s12, s13\n"
"s_mul_i32 s12, s12, 0x14000\n"
"v_mfma_f32_16x16x32_bf16 v[248:251], a[44:47], a[8:11], v[248:251]\n"
"s_add_i32 s12, s12, 0x2d8a00\n"
"s_add_u32 s12, s66, s12\n"
"s_mul_i32 s14, s23, 54\n"
"s_addc_u32 s13, s67, 0\n"
"v_mfma_f32_16x16x32_bf16 v[140:143], a[16:19], a[12:15], v[140:143]\n"
"global_load_lds_dwordx4 v78, s[38:39] offset:2048\n"
"s_mul_i32 s37, s36, 221184\n"
"s_add_u32 s42, s84, s37\n"
"s_addc_u32 s43, s85, 0\n"
"v_mfma_f32_16x16x32_bf16 v[156:159], a[20:23], a[12:15], v[156:159]\n"
"s_add_i32 s14, s14, s29\n"
"s_lshl_b32 s14, s14, 12\n"
"s_add_i32 s14, s14, 0x7d8000\n"
"v_mfma_f32_16x16x32_bf16 v[172:175], a[24:27], a[12:15], v[172:175]\n"
"s_add_u32 s14, s66, s14\n"
"s_addc_u32 s15, s67, 0\n"
"s_add_i32 s16, s16, s29\n"
"v_mfma_f32_16x16x32_bf16 v[188:191], a[28:31], a[12:15], v[188:191]\n"
"v_lshl_or_b32 v3, v13, 3, v3\n"
"s_mulk_i32 s16, 0x1400\n"
"v_add_u32_e32 v3, 0x800, v3\n"
"v_mfma_f32_16x16x32_bf16 v[204:207], a[32:35], a[12:15], v[204:207]\n"
"s_add_i32 s16, s16, 0x1118800\n"
"v_lshl_add_u32 v25, s30, 7, v3\n"
"v_lshl_add_u32 v3, s19, 7, v3\n"
"v_mfma_f32_16x16x32_bf16 v[220:223], a[36:39], a[12:15], v[220:223]\n"
"global_load_lds_dwordx4 v79, s[38:39] offset:3072\n"
"s_add_u32 m0, s88, 20480\n"
"v_mfma_f32_16x16x32_bf16 v[236:239], a[40:43], a[12:15], v[236:239]\n"
"s_add_u32 s16, s66, s16\n"
"s_mul_i32 s19, s30, 0x5000\n"
"s_addc_u32 s17, s67, 0\n"
"v_mfma_f32_16x16x32_bf16 v[252:255], a[44:47], a[12:15], v[252:255]\n"
"s_add_i32 s18, s19, s18\n"
"s_mulk_i32 s20, 0x3c00\n"
"s_add_i32 s18, s18, 0x1334000\n"
"s_waitcnt vmcnt(16) lgkmcnt(0)\n"
"s_barrier\n"
"v_mfma_f32_16x16x32_bf16 v[128:131], a[64:67], a[48:51], v[128:131]\n"
"s_waitcnt vmcnt(14)\n"
"ds_read_b128 a[0:3], v52 offset:8192\n"
"v_add_u32_e32 v21, s20, v19\n"
"v_mfma_f32_16x16x32_bf16 v[144:147], a[68:71], a[48:51], v[144:147]\n"
"ds_read_b64_tr_b16 a[16:17], v36 offset:16384\n"
"v_mfma_f32_16x16x32_bf16 v[160:163], a[72:75], a[48:51], v[160:163]\n"
"global_load_lds_dwordx4 v80, s[38:39] offset:0\n"
"v_mfma_f32_16x16x32_bf16 v[176:179], a[76:79], a[48:51], v[176:179]\n"
"ds_read_b64_tr_b16 a[18:19], v36 offset:16896\n"
"v_mfma_f32_16x16x32_bf16 v[192:195], a[80:83], a[48:51], v[192:195]\n"
"ds_read_b64_tr_b16 a[20:21], v37 offset:16384\n"
"v_mfma_f32_16x16x32_bf16 v[208:211], a[84:87], a[48:51], v[208:211]\n"
"ds_read_b64_tr_b16 a[22:23], v37 offset:16896\n"
"v_mfma_f32_16x16x32_bf16 v[224:227], a[88:91], a[48:51], v[224:227]\n"
"ds_read_b64_tr_b16 a[24:25], v38 offset:16384\n"
"v_mfma_f32_16x16x32_bf16 v[240:243], a[92:95], a[48:51], v[240:243]\n"
"global_load_lds_dwordx4 v81, s[38:39] offset:1024\n"
"v_mfma_f32_16x16x32_bf16 v[132:135], a[64:67], a[52:55], v[132:135]\n"
"ds_read_b64_tr_b16 a[26:27], v38 offset:16896\n"
"v_mfma_f32_16x16x32_bf16 v[148:151], a[68:71], a[52:55], v[148:151]\n"
"ds_read_b64_tr_b16 a[28:29], v39 offset:16384\n"
"v_mfma_f32_16x16x32_bf16 v[164:167], a[72:75], a[52:55], v[164:167]\n"
"ds_read_b64_tr_b16 a[30:31], v39 offset:16896\n"
"v_mfma_f32_16x16x32_bf16 v[180:183], a[76:79], a[52:55], v[180:183]\n"
"ds_read_b64_tr_b16 a[32:33], v44 offset:16384\n"
"v_mfma_f32_16x16x32_bf16 v[196:199], a[80:83], a[52:55], v[196:199]\n"
"global_load_lds_dwordx4 v82, s[38:39] offset:2048\n"
"v_mfma_f32_16x16x32_bf16 v[212:215], a[84:87], a[52:55], v[212:215]\n"
"ds_read_b64_tr_b16 a[34:35], v44 offset:16896\n"
"v_mfma_f32_16x16x32_bf16 v[228:231], a[88:91], a[52:55], v[228:231]\n"
"ds_read_b64_tr_b16 a[36:37], v45 offset:16384\n"
"v_mfma_f32_16x16x32_bf16 v[244:247], a[92:95], a[52:55], v[244:247]\n"
"ds_read_b64_tr_b16 a[38:39], v45 offset:16896\n"
"v_mfma_f32_16x16x32_bf16 v[136:139], a[64:67], a[56:59], v[136:139]\n"
"ds_read_b64_tr_b16 a[40:41], v46 offset:16384\n"
"v_mfma_f32_16x16x32_bf16 v[152:155], a[68:71], a[56:59], v[152:155]\n"
"global_load_lds_dwordx4 v83, s[38:39] offset:3072\n"
"s_lshl_b32 s37, s36, 7\n"
"s_add_u32 s38, s80, s37\n"
"s_addc_u32 s39, s81, 0\n"
"s_add_u32 m0, s89, 0\n"
"v_mfma_f32_16x16x32_bf16 v[168:171], a[72:75], a[56:59], v[168:171]\n"
"ds_read_b64_tr_b16 a[42:43], v46 offset:16896\n"
"v_mfma_f32_16x16x32_bf16 v[184:187], a[76:79], a[56:59], v[184:187]\n"
"ds_read_b64_tr_b16 a[44:45], v47 offset:16384\n"
"v_mfma_f32_16x16x32_bf16 v[200:203], a[80:83], a[56:59], v[200:203]\n"
"ds_read_b64_tr_b16 a[46:47], v47 offset:16896\n"
"v_mfma_f32_16x16x32_bf16 v[216:219], a[84:87], a[56:59], v[216:219]\n"
"s_add_u32 s18, s66, s18\n"
"s_mul_i32 s20, s21, 0xa00\n"
"s_addc_u32 s19, s67, 0\n"
"v_mfma_f32_16x16x32_bf16 v[232:235], a[88:91], a[56:59], v[232:235]\n"
"global_load_lds_dwordx4 v84, s[40:41] offset:0\n"
"v_mfma_f32_16x16x32_bf16 v[248:251], a[92:95], a[56:59], v[248:251]\n"
"s_add_i32 s20, s20, 0xe98000\n"
"s_add_u32 s20, s66, s20\n"
"s_addc_u32 s21, s67, 0\n"
"v_mfma_f32_16x16x32_bf16 v[140:143], a[64:67], a[60:63], v[140:143]\n"
"s_lshl_b32 s23, s23, 1\n"
"v_or_b32_e32 v65, s51, v0\n"
"s_mul_i32 s51, s22, 0xa000\n"
"v_mfma_f32_16x16x32_bf16 v[156:159], a[68:71], a[60:63], v[156:159]\n"
"s_waitcnt vmcnt(17)\n"
"ds_read_b128 a[4:7], v52 offset:10240\n"
"s_or_b32 s22, s23, s22\n"
"s_mul_i32 s22, s22, 0xa000\n"
"v_mfma_f32_16x16x32_bf16 v[172:175], a[72:75], a[60:63], v[172:175]\n"
"s_waitcnt vmcnt(15)\n"
"ds_read_b128 a[8:11], v52 offset:12288\n"
"s_add_u32 s22, s66, s22\n"
"s_addc_u32 s23, s67, 0\n"
"v_mfma_f32_16x16x32_bf16 v[188:191], a[76:79], a[60:63], v[188:191]\n"
"global_load_lds_dwordx4 v85, s[40:41] offset:1024\n"
"s_add_u32 m0, s89, 2048\n"
"v_mfma_f32_16x16x32_bf16 v[204:207], a[80:83], a[60:63], v[204:207]\n"
"s_waitcnt vmcnt(14)\n"
"ds_read_b128 a[12:15], v52 offset:14336\n"
"v_mfma_f32_16x16x32_bf16 v[220:223], a[84:87], a[60:63], v[220:223]\n"
"s_add_u32 s22, s22, 0xe98a00\n"
"s_mulk_i32 s24, 0x3c00\n"
"s_addc_u32 s23, s23, 0\n"
"v_mfma_f32_16x16x32_bf16 v[236:239], a[88:91], a[60:63], v[236:239]\n"
"v_add_u32_e32 v19, s24, v19\n"
"v_mfma_f32_16x16x32_bf16 v[252:255], a[92:95], a[60:63], v[252:255]\n"
"s_add_u32 s24, s60, s26\n"
"v_lshl_or_b32 v71, s25, 5, v17\n"
"s_addc_u32 s25, s61, 0\n"
"s_waitcnt lgkmcnt(0)\n"
"v_mfma_f32_16x16x32_bf16 v[112:115], a[16:19], v[56:59], v[112:115]\n"
"global_load_lds_dwordx4 v86, s[42:43] offset:0\n"
"v_mfma_f32_16x16x32_bf16 v[116:119], a[20:23], v[56:59], v[116:119]\n"
"ds_read_b128 a[48:51], v53 offset:8192\n"
"s_add_u32 s26, s76, s26\n"
"s_addc_u32 s27, s77, 0\n"
"v_mfma_f32_16x16x32_bf16 v[120:123], a[32:35], v[56:59], v[120:123]\n"
"ds_read_b64_tr_b16 a[64:65], v40 offset:16384\n"
"v_or_b32_e32 v66, s52, v0\n"
"v_mfma_f32_16x16x32_bf16 v[124:127], a[36:39], v[56:59], v[124:127]\n"
"ds_read_b64_tr_b16 a[66:67], v40 offset:16896\n"
"v_or_b32_e32 v68, s53, v0\n"
"v_mfma_f32_16x16x32_bf16 v[128:131], a[16:19], a[0:3], v[128:131]\n"
"ds_read_b64_tr_b16 a[68:69], v41 offset:16384\n"
"v_mov_b32_e32 v18, 0x1000\n"
"v_mfma_f32_16x16x32_bf16 v[144:147], a[20:23], a[0:3], v[144:147]\n"
"global_load_lds_dwordx4 v87, s[42:43] offset:1024\n"
"s_add_u32 m0, s88, 0\n"
"v_mfma_f32_16x16x32_bf16 v[160:163], a[24:27], a[0:3], v[160:163]\n"
"ds_read_b64_tr_b16 a[70:71], v41 offset:16896\n"
"v_mul_u32_u24_e32 v13, 0x500, v13\n"
"v_mfma_f32_16x16x32_bf16 v[176:179], a[28:31], a[0:3], v[176:179]\n"
"ds_read_b64_tr_b16 a[72:73], v42 offset:16384\n"
"v_or_b32_e32 v17, 0x80, v17\n"
"v_mfma_f32_16x16x32_bf16 v[192:195], a[32:35], a[0:3], v[192:195]\n"
"ds_read_b64_tr_b16 a[74:75], v42 offset:16896\n"
"v_mfma_f32_16x16x32_bf16 v[208:211], a[36:39], a[0:3], v[208:211]\n"
"ds_read_b64_tr_b16 a[76:77], v43 offset:16384\n"
"v_mfma_f32_16x16x32_bf16 v[224:227], a[40:43], a[0:3], v[224:227]\n"
"global_load_lds_dwordx4 v76, s[38:39] offset:0\n"
"s_add_u32 s36, s36, 1\n"
"s_cmp_eq_u32 s36, 10\n"
"s_cselect_b32 s36, 0, s36\n"
"v_mfma_f32_16x16x32_bf16 v[240:243], a[44:47], a[0:3], v[240:243]\n"
"ds_read_b64_tr_b16 a[78:79], v43 offset:16896\n"
"v_mfma_f32_16x16x32_bf16 v[132:135], a[16:19], a[4:7], v[132:135]\n"
"ds_read_b64_tr_b16 a[80:81], v48 offset:16384\n"
"v_mfma_f32_16x16x32_bf16 v[148:151], a[20:23], a[4:7], v[148:151]\n"
"ds_read_b64_tr_b16 a[82:83], v48 offset:16896\n"
"v_mfma_f32_16x16x32_bf16 v[164:167], a[24:27], a[4:7], v[164:167]\n"
"ds_read_b64_tr_b16 a[84:85], v49 offset:16384\n"
"v_mfma_f32_16x16x32_bf16 v[180:183], a[28:31], a[4:7], v[180:183]\n"
"global_load_lds_dwordx4 v77, s[38:39] offset:1024\n"
"s_mul_i32 s37, s36, 245760\n"
"s_add_u32 s40, s82, s37\n"
"s_addc_u32 s41, s83, 0\n"
"v_mfma_f32_16x16x32_bf16 v[196:199], a[32:35], a[4:7], v[196:199]\n"
"ds_read_b64_tr_b16 a[86:87], v49 offset:16896\n"
"v_mfma_f32_16x16x32_bf16 v[212:215], a[36:39], a[4:7], v[212:215]\n"
"ds_read_b64_tr_b16 a[88:89], v50 offset:16384\n"
"v_mfma_f32_16x16x32_bf16 v[228:231], a[40:43], a[4:7], v[228:231]\n"
"ds_read_b64_tr_b16 a[90:91], v50 offset:16896\n"
"v_mfma_f32_16x16x32_bf16 v[244:247], a[44:47], a[4:7], v[244:247]\n"
"ds_read_b64_tr_b16 a[92:93], v51 offset:16384\n"
"v_mfma_f32_16x16x32_bf16 v[136:139], a[16:19], a[8:11], v[136:139]\n"
"global_load_lds_dwordx4 v78, s[38:39] offset:2048\n"
"s_mul_i32 s37, s36, 221184\n"
"s_add_u32 s42, s84, s37\n"
"s_addc_u32 s43, s85, 0\n"
"v_mfma_f32_16x16x32_bf16 v[152:155], a[20:23], a[8:11], v[152:155]\n"
"ds_read_b64_tr_b16 a[94:95], v51 offset:16896\n"
"v_mfma_f32_16x16x32_bf16 v[168:171], a[24:27], a[8:11], v[168:171]\n"
"ds_read_b128 a[52:55], v53 offset:10240\n"
"v_mfma_f32_16x16x32_bf16 v[184:187], a[28:31], a[8:11], v[184:187]\n"
"ds_read_b128 a[56:59], v53 offset:12288\n"
"v_mfma_f32_16x16x32_bf16 v[200:203], a[32:35], a[8:11], v[200:203]\n"
"ds_read_b128 a[60:63], v53 offset:14336\n"
"v_mfma_f32_16x16x32_bf16 v[216:219], a[36:39], a[8:11], v[216:219]\n"
"global_load_lds_dwordx4 v79, s[38:39] offset:3072\n"
"s_add_u32 m0, s88, 4096\n"
"v_mfma_f32_16x16x32_bf16 v[232:235], a[40:43], a[8:11], v[232:235]\n"
"s_add_u32 s28, s74, s28\n"
"v_bitop3_b32 v18, s50, v18, v9 bitop3:0x36\n"
"v_or_b32_e32 v23, 0x14000, v23\n"
"v_lshlrev_b32_e32 v6, 3, v6\n"
"v_or_b32_e32 v26, v13, v0\n"
"v_or_b32_e32 v13, v17, v13\n"
"s_addc_u32 s29, s75, 0\n"
"v_mfma_f32_16x16x32_bf16 v[248:251], a[44:47], a[8:11], v[248:251]\n"
"v_or_b32_e32 v27, s50, v9\n"
"v_or_b32_e32 v28, s33, v9\n"
"v_mfma_f32_16x16x32_bf16 v[140:143], a[16:19], a[12:15], v[140:143]\n"
"v_or_b32_e32 v29, s51, v9\n"
"v_xor_b32 v72, 64, v71\n"
"v_mfma_f32_16x16x32_bf16 v[156:159], a[20:23], a[12:15], v[156:159]\n"
"v_mfma_f32_16x16x32_bf16 v[172:175], a[24:27], a[12:15], v[172:175]\n"
"global_load_lds_dwordx4 v80, s[38:39] offset:0\n"
"v_mfma_f32_16x16x32_bf16 v[188:191], a[28:31], a[12:15], v[188:191]\n"
"v_mfma_f32_16x16x32_bf16 v[204:207], a[32:35], a[12:15], v[204:207]\n"
"v_mfma_f32_16x16x32_bf16 v[220:223], a[36:39], a[12:15], v[220:223]\n"
"v_mfma_f32_16x16x32_bf16 v[236:239], a[40:43], a[12:15], v[236:239]\n"
"v_mfma_f32_16x16x32_bf16 v[252:255], a[44:47], a[12:15], v[252:255]\n"
"global_load_lds_dwordx4 v81, s[38:39] offset:1024\n"
"s_waitcnt vmcnt(18) lgkmcnt(0)\n"
"s_barrier\n"
"v_mfma_f32_16x16x32_bf16 v[128:131], a[64:67], a[48:51], v[128:131]\n"
"s_waitcnt vmcnt(16)\n"
"ds_read_b128 a[0:3], v52 offset:16384\n"
"v_mfma_f32_16x16x32_bf16 v[144:147], a[68:71], a[48:51], v[144:147]\n"
"ds_read_b64_tr_b16 a[16:17], v36 offset:32768\n"
"v_mfma_f32_16x16x32_bf16 v[160:163], a[72:75], a[48:51], v[160:163]\n"
"ds_read_b64_tr_b16 a[18:19], v36 offset:33280\n"
"v_mfma_f32_16x16x32_bf16 v[176:179], a[76:79], a[48:51], v[176:179]\n"
"ds_read_b64_tr_b16 a[20:21], v37 offset:32768\n"
"v_mfma_f32_16x16x32_bf16 v[192:195], a[80:83], a[48:51], v[192:195]\n"
"global_load_lds_dwordx4 v82, s[38:39] offset:2048\n"
"v_mfma_f32_16x16x32_bf16 v[208:211], a[84:87], a[48:51], v[208:211]\n"
"ds_read_b64_tr_b16 a[22:23], v37 offset:33280\n"
"v_mfma_f32_16x16x32_bf16 v[224:227], a[88:91], a[48:51], v[224:227]\n"
"ds_read_b64_tr_b16 a[24:25], v38 offset:32768\n"
"v_mfma_f32_16x16x32_bf16 v[240:243], a[92:95], a[48:51], v[240:243]\n"
"ds_read_b64_tr_b16 a[26:27], v38 offset:33280\n"
"v_mfma_f32_16x16x32_bf16 v[132:135], a[64:67], a[52:55], v[132:135]\n"
"ds_read_b64_tr_b16 a[28:29], v39 offset:32768\n"
"v_mfma_f32_16x16x32_bf16 v[148:151], a[68:71], a[52:55], v[148:151]\n"
"global_load_lds_dwordx4 v83, s[38:39] offset:3072\n"
"s_lshl_b32 s37, s36, 7\n"
"s_add_u32 s38, s80, s37\n"
"s_addc_u32 s39, s81, 0\n"
"s_add_u32 m0, s89, 16384\n"
"v_mfma_f32_16x16x32_bf16 v[164:167], a[72:75], a[52:55], v[164:167]\n"
"ds_read_b64_tr_b16 a[30:31], v39 offset:33280\n"
"v_mfma_f32_16x16x32_bf16 v[180:183], a[76:79], a[52:55], v[180:183]\n"
"ds_read_b64_tr_b16 a[32:33], v44 offset:32768\n"
"v_mfma_f32_16x16x32_bf16 v[196:199], a[80:83], a[52:55], v[196:199]\n"
"ds_read_b64_tr_b16 a[34:35], v44 offset:33280\n"
"v_mfma_f32_16x16x32_bf16 v[212:215], a[84:87], a[52:55], v[212:215]\n"
"ds_read_b64_tr_b16 a[36:37], v45 offset:32768\n"
"v_mfma_f32_16x16x32_bf16 v[228:231], a[88:91], a[52:55], v[228:231]\n"
"global_load_lds_dwordx4 v84, s[40:41] offset:0\n"
"v_mfma_f32_16x16x32_bf16 v[244:247], a[92:95], a[52:55], v[244:247]\n"
"ds_read_b64_tr_b16 a[38:39], v45 offset:33280\n"
"v_mfma_f32_16x16x32_bf16 v[136:139], a[64:67], a[56:59], v[136:139]\n"
"ds_read_b64_tr_b16 a[40:41], v46 offset:32768\n"
"v_mfma_f32_16x16x32_bf16 v[152:155], a[68:71], a[56:59], v[152:155]\n"
"ds_read_b64_tr_b16 a[42:43], v46 offset:33280\n"
"v_mfma_f32_16x16x32_bf16 v[168:171], a[72:75], a[56:59], v[168:171]\n"
"ds_read_b64_tr_b16 a[44:45], v47 offset:32768\n"
"v_mfma_f32_16x16x32_bf16 v[184:187], a[76:79], a[56:59], v[184:187]\n"
"global_load_lds_dwordx4 v85, s[40:41] offset:1024\n"
"s_add_u32 m0, s89, 18432\n"
"v_mfma_f32_16x16x32_bf16 v[200:203], a[80:83], a[56:59], v[200:203]\n"
"ds_read_b64_tr_b16 a[46:47], v47 offset:33280\n"
"v_mfma_f32_16x16x32_bf16 v[216:219], a[84:87], a[56:59], v[216:219]\n"
"v_mfma_f32_16x16x32_bf16 v[232:235], a[88:91], a[56:59], v[232:235]\n"
"v_mfma_f32_16x16x32_bf16 v[248:251], a[92:95], a[56:59], v[248:251]\n"
"v_mfma_f32_16x16x32_bf16 v[140:143], a[64:67], a[60:63], v[140:143]\n"
"global_load_lds_dwordx4 v86, s[42:43] offset:0\n"
"v_mfma_f32_16x16x32_bf16 v[156:159], a[68:71], a[60:63], v[156:159]\n"
"s_waitcnt vmcnt(19)\n"
"ds_read_b128 a[4:7], v52 offset:18432\n"
"v_mfma_f32_16x16x32_bf16 v[172:175], a[72:75], a[60:63], v[172:175]\n"
"s_waitcnt vmcnt(17)\n"
"ds_read_b128 a[8:11], v52 offset:20480\n"
"v_mfma_f32_16x16x32_bf16 v[188:191], a[76:79], a[60:63], v[188:191]\n"
"s_waitcnt vmcnt(15)\n"
"ds_read_b128 a[12:15], v52 offset:22528\n"
"v_mfma_f32_16x16x32_bf16 v[204:207], a[80:83], a[60:63], v[204:207]\n"
"v_mfma_f32_16x16x32_bf16 v[220:223], a[84:87], a[60:63], v[220:223]\n"
"global_load_lds_dwordx4 v87, s[42:43] offset:1024\n"
"s_add_u32 m0, s88, 8192\n"
"v_mfma_f32_16x16x32_bf16 v[236:239], a[88:91], a[60:63], v[236:239]\n"
"v_mfma_f32_16x16x32_bf16 v[252:255], a[92:95], a[60:63], v[252:255]\n"
"s_waitcnt lgkmcnt(0)\n"
"v_mfma_f32_16x16x32_bf16 v[112:115], a[16:19], v[56:59], v[112:115]\n"
"ds_read_b128 a[48:51], v53 offset:16384\n"
"v_mfma_f32_16x16x32_bf16 v[116:119], a[20:23], v[56:59], v[116:119]\n"
"ds_read_b64_tr_b16 a[64:65], v40 offset:32768\n"
"v_mfma_f32_16x16x32_bf16 v[120:123], a[32:35], v[56:59], v[120:123]\n"
"global_load_lds_dwordx4 v76, s[38:39] offset:0\n"
"s_add_u32 s36, s36, 1\n"
"s_cmp_eq_u32 s36, 10\n"
"s_cselect_b32 s36, 0, s36\n"
"v_mfma_f32_16x16x32_bf16 v[124:127], a[36:39], v[56:59], v[124:127]\n"
"ds_read_b64_tr_b16 a[66:67], v40 offset:33280\n"
"v_mfma_f32_16x16x32_bf16 v[128:131], a[16:19], a[0:3], v[128:131]\n"
"ds_read_b64_tr_b16 a[68:69], v41 offset:32768\n"
"v_mfma_f32_16x16x32_bf16 v[144:147], a[20:23], a[0:3], v[144:147]\n"
"ds_read_b64_tr_b16 a[70:71], v41 offset:33280\n"
"v_mfma_f32_16x16x32_bf16 v[160:163], a[24:27], a[0:3], v[160:163]\n"
"ds_read_b64_tr_b16 a[72:73], v42 offset:32768\n"
"v_mfma_f32_16x16x32_bf16 v[176:179], a[28:31], a[0:3], v[176:179]\n"
"global_load_lds_dwordx4 v77, s[38:39] offset:1024\n"
"s_mul_i32 s37, s36, 245760\n"
"s_add_u32 s40, s82, s37\n"
"s_addc_u32 s41, s83, 0\n"
"v_mfma_f32_16x16x32_bf16 v[192:195], a[32:35], a[0:3], v[192:195]\n"
"ds_read_b64_tr_b16 a[74:75], v42 offset:33280\n"
"v_mfma_f32_16x16x32_bf16 v[208:211], a[36:39], a[0:3], v[208:211]\n"
"ds_read_b64_tr_b16 a[76:77], v43 offset:32768\n"
"v_mfma_f32_16x16x32_bf16 v[224:227], a[40:43], a[0:3], v[224:227]\n"
"ds_read_b64_tr_b16 a[78:79], v43 offset:33280\n"
"v_mfma_f32_16x16x32_bf16 v[240:243], a[44:47], a[0:3], v[240:243]\n"
"ds_read_b64_tr_b16 a[80:81], v48 offset:32768\n"
"v_mfma_f32_16x16x32_bf16 v[132:135], a[16:19], a[4:7], v[132:135]\n"
"global_load_lds_dwordx4 v78, s[38:39] offset:2048\n"
"s_mul_i32 s37, s36, 221184\n"
"s_add_u32 s44, s84, s37\n"
"s_addc_u32 s45, s85, 0\n"
"v_mfma_f32_16x16x32_bf16 v[148:151], a[20:23], a[4:7], v[148:151]\n"
"ds_read_b64_tr_b16 a[82:83], v48 offset:33280\n"
"v_mfma_f32_16x16x32_bf16 v[164:167], a[24:27], a[4:7], v[164:167]\n"
"ds_read_b64_tr_b16 a[84:85], v49 offset:32768\n"
"v_mfma_f32_16x16x32_bf16 v[180:183], a[28:31], a[4:7], v[180:183]\n"
"ds_read_b64_tr_b16 a[86:87], v49 offset:33280\n"
"v_mfma_f32_16x16x32_bf16 v[196:199], a[32:35], a[4:7], v[196:199]\n"
"ds_read_b64_tr_b16 a[88:89], v50 offset:32768\n"
"v_mfma_f32_16x16x32_bf16 v[212:215], a[36:39], a[4:7], v[212:215]\n"
"global_load_lds_dwordx4 v79, s[38:39] offset:3072\n"
"s_add_u32 m0, s88, 12288\n"
"v_mfma_f32_16x16x32_bf16 v[228:231], a[40:43], a[4:7], v[228:231]\n"
"ds_read_b64_tr_b16 a[90:91], v50 offset:33280\n"
"v_mfma_f32_16x16x32_bf16 v[244:247], a[44:47], a[4:7], v[244:247]\n"
"ds_read_b64_tr_b16 a[92:93], v51 offset:32768\n"
"v_mfma_f32_16x16x32_bf16 v[136:139], a[16:19], a[8:11], v[136:139]\n"
"ds_read_b64_tr_b16 a[94:95], v51 offset:33280\n"
"v_mfma_f32_16x16x32_bf16 v[152:155], a[20:23], a[8:11], v[152:155]\n"
"ds_read_b128 a[52:55], v53 offset:18432\n"
"v_mfma_f32_16x16x32_bf16 v[168:171], a[24:27], a[8:11], v[168:171]\n"
"global_load_lds_dwordx4 v80, s[38:39] offset:0\n"
"v_mfma_f32_16x16x32_bf16 v[184:187], a[28:31], a[8:11], v[184:187]\n"
"ds_read_b128 a[56:59], v53 offset:20480\n"
"v_mfma_f32_16x16x32_bf16 v[200:203], a[32:35], a[8:11], v[200:203]\n"
"ds_read_b128 a[60:63], v53 offset:22528\n"
"v_mfma_f32_16x16x32_bf16 v[216:219], a[36:39], a[8:11], v[216:219]\n"
"v_mfma_f32_16x16x32_bf16 v[232:235], a[40:43], a[8:11], v[232:235]\n"
"v_mfma_f32_16x16x32_bf16 v[248:251], a[44:47], a[8:11], v[248:251]\n"
"global_load_lds_dwordx4 v81, s[38:39] offset:1024\n"
"v_mfma_f32_16x16x32_bf16 v[140:143], a[16:19], a[12:15], v[140:143]\n"
"v_mfma_f32_16x16x32_bf16 v[156:159], a[20:23], a[12:15], v[156:159]\n"
"v_mfma_f32_16x16x32_bf16 v[172:175], a[24:27], a[12:15], v[172:175]\n"
"v_mfma_f32_16x16x32_bf16 v[188:191], a[28:31], a[12:15], v[188:191]\n"
"v_mfma_f32_16x16x32_bf16 v[204:207], a[32:35], a[12:15], v[204:207]\n"
"global_load_lds_dwordx4 v82, s[38:39] offset:2048\n"
"v_mfma_f32_16x16x32_bf16 v[220:223], a[36:39], a[12:15], v[220:223]\n"
"v_mfma_f32_16x16x32_bf16 v[236:239], a[40:43], a[12:15], v[236:239]\n"
"v_mfma_f32_16x16x32_bf16 v[252:255], a[44:47], a[12:15], v[252:255]\n"
"s_waitcnt vmcnt(19) lgkmcnt(0)\n"
"s_barrier\n"
"v_mfma_f32_16x16x32_bf16 v[128:131], a[64:67], a[48:51], v[128:131]\n"
"s_waitcnt vmcnt(17)\n"
"ds_read_b128 a[0:3], v52 offset:0\n"
"v_mfma_f32_16x16x32_bf16 v[144:147], a[68:71], a[48:51], v[144:147]\n"
"global_load_lds_dwordx4 v83, s[38:39] offset:3072\n"
"s_lshl_b32 s37, s36, 7\n"
"s_add_u32 s38, s80, s37\n"
"s_addc_u32 s39, s81, 0\n"
"s_add_u32 m0, s89, 32768\n"
"v_mfma_f32_16x16x32_bf16 v[160:163], a[72:75], a[48:51], v[160:163]\n"
"ds_read_b64_tr_b16 a[16:17], v36 offset:0\n"
"v_mfma_f32_16x16x32_bf16 v[176:179], a[76:79], a[48:51], v[176:179]\n"
"ds_read_b64_tr_b16 a[18:19], v36 offset:512\n"
"v_mfma_f32_16x16x32_bf16 v[192:195], a[80:83], a[48:51], v[192:195]\n"
"ds_read_b64_tr_b16 a[20:21], v37 offset:0\n"
"v_mfma_f32_16x16x32_bf16 v[208:211], a[84:87], a[48:51], v[208:211]\n"
"ds_read_b64_tr_b16 a[22:23], v37 offset:512\n"
"v_mfma_f32_16x16x32_bf16 v[224:227], a[88:91], a[48:51], v[224:227]\n"
"global_load_lds_dwordx4 v84, s[40:41] offset:0\n"
"v_mfma_f32_16x16x32_bf16 v[240:243], a[92:95], a[48:51], v[240:243]\n"
"ds_read_b64_tr_b16 a[24:25], v38 offset:0\n"
"v_mfma_f32_16x16x32_bf16 v[132:135], a[64:67], a[52:55], v[132:135]\n"
"ds_read_b64_tr_b16 a[26:27], v38 offset:512\n"
"v_mfma_f32_16x16x32_bf16 v[148:151], a[68:71], a[52:55], v[148:151]\n"
"ds_read_b64_tr_b16 a[28:29], v39 offset:0\n"
"v_mfma_f32_16x16x32_bf16 v[164:167], a[72:75], a[52:55], v[164:167]\n"
"ds_read_b64_tr_b16 a[30:31], v39 offset:512\n"
"v_mfma_f32_16x16x32_bf16 v[180:183], a[76:79], a[52:55], v[180:183]\n"
"global_load_lds_dwordx4 v85, s[40:41] offset:1024\n"
"s_add_u32 m0, s88, 16384\n"
"v_mfma_f32_16x16x32_bf16 v[196:199], a[80:83], a[52:55], v[196:199]\n"
"ds_read_b64_tr_b16 a[32:33], v44 offset:0\n"
"v_mfma_f32_16x16x32_bf16 v[212:215], a[84:87], a[52:55], v[212:215]\n"
"ds_read_b64_tr_b16 a[34:35], v44 offset:512\n"
"v_mfma_f32_16x16x32_bf16 v[228:231], a[88:91], a[52:55], v[228:231]\n"
"ds_read_b64_tr_b16 a[36:37], v45 offset:0\n"
"v_mfma_f32_16x16x32_bf16 v[244:247], a[92:95], a[52:55], v[244:247]\n"
"ds_read_b64_tr_b16 a[38:39], v45 offset:512\n"
"v_mfma_f32_16x16x32_bf16 v[136:139], a[64:67], a[56:59], v[136:139]\n"
"global_load_lds_dwordx4 v76, s[38:39] offset:0\n"
"s_add_u32 s36, s36, 1\n"
"s_cmp_eq_u32 s36, 10\n"
"s_cselect_b32 s36, 0, s36\n"
"v_mfma_f32_16x16x32_bf16 v[152:155], a[68:71], a[56:59], v[152:155]\n"
"ds_read_b64_tr_b16 a[40:41], v46 offset:0\n"
"v_mfma_f32_16x16x32_bf16 v[168:171], a[72:75], a[56:59], v[168:171]\n"
"ds_read_b64_tr_b16 a[42:43], v46 offset:512\n"
"v_mfma_f32_16x16x32_bf16 v[184:187], a[76:79], a[56:59], v[184:187]\n"
"ds_read_b64_tr_b16 a[44:45], v47 offset:0\n"
"v_mfma_f32_16x16x32_bf16 v[200:203], a[80:83], a[56:59], v[200:203]\n"
"ds_read_b64_tr_b16 a[46:47], v47 offset:512\n"
"v_mfma_f32_16x16x32_bf16 v[216:219], a[84:87], a[56:59], v[216:219]\n"
"global_load_lds_dwordx4 v77, s[38:39] offset:1024\n"
"s_mul_i32 s37, s36, 245760\n"
"s_add_u32 s40, s82, s37\n"
"s_addc_u32 s41, s83, 0\n"
"v_mfma_f32_16x16x32_bf16 v[232:235], a[88:91], a[56:59], v[232:235]\n"
"v_mfma_f32_16x16x32_bf16 v[248:251], a[92:95], a[56:59], v[248:251]\n"
"v_mfma_f32_16x16x32_bf16 v[140:143], a[64:67], a[60:63], v[140:143]\n"
"v_mfma_f32_16x16x32_bf16 v[156:159], a[68:71], a[60:63], v[156:159]\n"
"s_waitcnt vmcnt(20)\n"
"ds_read_b128 a[4:7], v52 offset:2048\n"
"v_mfma_f32_16x16x32_bf16 v[172:175], a[72:75], a[60:63], v[172:175]\n"
"global_load_lds_dwordx4 v78, s[38:39] offset:2048\n"
"s_mul_i32 s37, s36, 221184\n"
"s_add_u32 s48, s84, s37\n"
"s_addc_u32 s49, s85, 0\n"
"v_mfma_f32_16x16x32_bf16 v[188:191], a[76:79], a[60:63], v[188:191]\n"
"s_waitcnt vmcnt(19)\n"
"ds_read_b128 a[8:11], v52 offset:4096\n"
"v_mfma_f32_16x16x32_bf16 v[204:207], a[80:83], a[60:63], v[204:207]\n"
"s_waitcnt vmcnt(17)\n"
"ds_read_b128 a[12:15], v52 offset:6144\n"
"v_mfma_f32_16x16x32_bf16 v[220:223], a[84:87], a[60:63], v[220:223]\n"
"v_mfma_f32_16x16x32_bf16 v[236:239], a[88:91], a[60:63], v[236:239]\n"
"v_mfma_f32_16x16x32_bf16 v[252:255], a[92:95], a[60:63], v[252:255]\n"
"global_load_lds_dwordx4 v79, s[38:39] offset:3072\n"
"s_add_u32 m0, s88, 20480\n"
"s_waitcnt lgkmcnt(0)\n"
"v_mfma_f32_16x16x32_bf16 v[112:115], a[16:19], v[56:59], v[112:115]\n"
"ds_read_b128 a[48:51], v53 offset:0\n"
"v_mfma_f32_16x16x32_bf16 v[116:119], a[20:23], v[56:59], v[116:119]\n"
"s_mov_b32 exec_lo, 0\n"
"global_load_dword v109, v1, s[66:67] offset:0 sc1\n"
"s_mov_b32 exec_lo, -1\n"
"ds_read_b64_tr_b16 a[64:65], v40 offset:0\n"
"v_mfma_f32_16x16x32_bf16 v[120:123], a[32:35], v[56:59], v[120:123]\n"
"ds_read_b64_tr_b16 a[66:67], v40 offset:512\n"
"v_mfma_f32_16x16x32_bf16 v[124:127], a[36:39], v[56:59], v[124:127]\n"
"ds_read_b64_tr_b16 a[68:69], v41 offset:0\n"
"v_mfma_f32_16x16x32_bf16 v[128:131], a[16:19], a[0:3], v[128:131]\n"
"global_load_lds_dwordx4 v80, s[38:39] offset:0\n"
"v_mfma_f32_16x16x32_bf16 v[144:147], a[20:23], a[0:3], v[144:147]\n"
"ds_read_b64_tr_b16 a[70:71], v41 offset:512\n"
"v_mfma_f32_16x16x32_bf16 v[160:163], a[24:27], a[0:3], v[160:163]\n"
"ds_read_b64_tr_b16 a[72:73], v42 offset:0\n"
"v_mfma_f32_16x16x32_bf16 v[176:179], a[28:31], a[0:3], v[176:179]\n"
"ds_read_b64_tr_b16 a[74:75], v42 offset:512\n"
"v_mfma_f32_16x16x32_bf16 v[192:195], a[32:35], a[0:3], v[192:195]\n"
"ds_read_b64_tr_b16 a[76:77], v43 offset:0\n"
"v_mfma_f32_16x16x32_bf16 v[208:211], a[36:39], a[0:3], v[208:211]\n"
"global_load_lds_dwordx4 v81, s[38:39] offset:1024\n"
"v_mfma_f32_16x16x32_bf16 v[224:227], a[40:43], a[0:3], v[224:227]\n"
"ds_read_b64_tr_b16 a[78:79], v43 offset:512\n"
"v_mfma_f32_16x16x32_bf16 v[240:243], a[44:47], a[0:3], v[240:243]\n"
"ds_read_b64_tr_b16 a[80:81], v48 offset:0\n"
"v_mfma_f32_16x16x32_bf16 v[132:135], a[16:19], a[4:7], v[132:135]\n"
"ds_read_b64_tr_b16 a[82:83], v48 offset:512\n"
"v_mfma_f32_16x16x32_bf16 v[148:151], a[20:23], a[4:7], v[148:151]\n"
"ds_read_b64_tr_b16 a[84:85], v49 offset:0\n"
"v_mfma_f32_16x16x32_bf16 v[164:167], a[24:27], a[4:7], v[164:167]\n"
"global_load_lds_dwordx4 v82, s[38:39] offset:2048\n"
"v_mfma_f32_16x16x32_bf16 v[180:183], a[28:31], a[4:7], v[180:183]\n"
"ds_read_b64_tr_b16 a[86:87], v49 offset:512\n"
"v_mfma_f32_16x16x32_bf16 v[196:199], a[32:35], a[4:7], v[196:199]\n"
"ds_read_b64_tr_b16 a[88:89], v50 offset:0\n"
"v_mfma_f32_16x16x32_bf16 v[212:215], a[36:39], a[4:7], v[212:215]\n"
"ds_read_b64_tr_b16 a[90:91], v50 offset:512\n"
"v_mfma_f32_16x16x32_bf16 v[228:231], a[40:43], a[4:7], v[228:231]\n"
"ds_read_b64_tr_b16 a[92:93], v51 offset:0\n"
"v_mfma_f32_16x16x32_bf16 v[244:247], a[44:47], a[4:7], v[244:247]\n"
"global_load_lds_dwordx4 v83, s[38:39] offset:3072\n"
"s_lshl_b32 s37, s36, 7\n"
"s_add_u32 s38, s80, s37\n"
"s_addc_u32 s39, s81, 0\n"
"s_add_u32 m0, s89, 0\n"
"v_mfma_f32_16x16x32_bf16 v[136:139], a[16:19], a[8:11], v[136:139]\n"
"ds_read_b64_tr_b16 a[94:95], v51 offset:512\n"
"v_mfma_f32_16x16x32_bf16 v[152:155], a[20:23], a[8:11], v[152:155]\n"
"ds_read_b128 a[52:55], v53 offset:2048\n"
"v_mfma_f32_16x16x32_bf16 v[168:171], a[24:27], a[8:11], v[168:171]\n"
"ds_read_b128 a[56:59], v53 offset:4096\n"
"v_mfma_f32_16x16x32_bf16 v[184:187], a[28:31], a[8:11], v[184:187]\n"
"ds_read_b128 a[60:63], v53 offset:6144\n"
"v_mfma_f32_16x16x32_bf16 v[200:203], a[32:35], a[8:11], v[200:203]\n"
"v_mfma_f32_16x16x32_bf16 v[216:219], a[36:39], a[8:11], v[216:219]\n"
"v_mfma_f32_16x16x32_bf16 v[232:235], a[40:43], a[8:11], v[232:235]\n"
"v_mfma_f32_16x16x32_bf16 v[248:251], a[44:47], a[8:11], v[248:251]\n"
"v_mfma_f32_16x16x32_bf16 v[140:143], a[16:19], a[12:15], v[140:143]\n"
"v_mfma_f32_16x16x32_bf16 v[156:159], a[20:23], a[12:15], v[156:159]\n"
"v_mfma_f32_16x16x32_bf16 v[172:175], a[24:27], a[12:15], v[172:175]\n"
"v_mfma_f32_16x16x32_bf16 v[188:191], a[28:31], a[12:15], v[188:191]\n"
"v_mfma_f32_16x16x32_bf16 v[204:207], a[32:35], a[12:15], v[204:207]\n"
"v_mfma_f32_16x16x32_bf16 v[220:223], a[36:39], a[12:15], v[220:223]\n"
"v_mfma_f32_16x16x32_bf16 v[236:239], a[40:43], a[12:15], v[236:239]\n"
"v_mfma_f32_16x16x32_bf16 v[252:255], a[44:47], a[12:15], v[252:255]\n"
"s_waitcnt vmcnt(19) lgkmcnt(0)\n"
"s_barrier\n"
"v_mfma_f32_16x16x32_bf16 v[128:131], a[64:67], a[48:51], v[128:131]\n"
"global_load_lds_dwordx4 v84, s[40:41] offset:0\n"
"v_mfma_f32_16x16x32_bf16 v[144:147], a[68:71], a[48:51], v[144:147]\n"
"s_waitcnt vmcnt(18)\n"
"ds_read_b128 a[0:3], v52 offset:8192\n"
"v_mfma_f32_16x16x32_bf16 v[160:163], a[72:75], a[48:51], v[160:163]\n"
"ds_read_b64_tr_b16 a[16:17], v36 offset:16384\n"
"v_mfma_f32_16x16x32_bf16 v[176:179], a[76:79], a[48:51], v[176:179]\n"
"ds_read_b64_tr_b16 a[18:19], v36 offset:16896\n"
"v_mfma_f32_16x16x32_bf16 v[192:195], a[80:83], a[48:51], v[192:195]\n"
"ds_read_b64_tr_b16 a[20:21], v37 offset:16384\n"
"v_mfma_f32_16x16x32_bf16 v[208:211], a[84:87], a[48:51], v[208:211]\n"
"global_load_lds_dwordx4 v85, s[40:41] offset:1024\n"
"s_add_u32 m0, s88, 0\n"
"v_mfma_f32_16x16x32_bf16 v[224:227], a[88:91], a[48:51], v[224:227]\n"
"ds_read_b64_tr_b16 a[22:23], v37 offset:16896\n"
"v_mfma_f32_16x16x32_bf16 v[240:243], a[92:95], a[48:51], v[240:243]\n"
"ds_read_b64_tr_b16 a[24:25], v38 offset:16384\n"
"v_mfma_f32_16x16x32_bf16 v[132:135], a[64:67], a[52:55], v[132:135]\n"
"ds_read_b64_tr_b16 a[26:27], v38 offset:16896\n"
"v_mfma_f32_16x16x32_bf16 v[148:151], a[68:71], a[52:55], v[148:151]\n"
"ds_read_b64_tr_b16 a[28:29], v39 offset:16384\n"
"v_mfma_f32_16x16x32_bf16 v[164:167], a[72:75], a[52:55], v[164:167]\n"
"global_load_lds_dwordx4 v76, s[38:39] offset:0\n"
"v_mfma_f32_16x16x32_bf16 v[180:183], a[76:79], a[52:55], v[180:183]\n"
"ds_read_b64_tr_b16 a[30:31], v39 offset:16896\n"
"v_mfma_f32_16x16x32_bf16 v[196:199], a[80:83], a[52:55], v[196:199]\n"
"v_mfma_f32_16x16x32_bf16 v[212:215], a[84:87], a[52:55], v[212:215]\n"
"v_mfma_f32_16x16x32_bf16 v[228:231], a[88:91], a[52:55], v[228:231]\n"
"v_mfma_f32_16x16x32_bf16 v[244:247], a[92:95], a[52:55], v[244:247]\n"
"global_load_lds_dwordx4 v77, s[38:39] offset:1024\n"
"v_mfma_f32_16x16x32_bf16 v[136:139], a[64:67], a[56:59], v[136:139]\n"
"v_mfma_f32_16x16x32_bf16 v[152:155], a[68:71], a[56:59], v[152:155]\n"
"v_mfma_f32_16x16x32_bf16 v[168:171], a[72:75], a[56:59], v[168:171]\n"
"v_mfma_f32_16x16x32_bf16 v[184:187], a[76:79], a[56:59], v[184:187]\n"
"v_mfma_f32_16x16x32_bf16 v[200:203], a[80:83], a[56:59], v[200:203]\n"
"global_load_lds_dwordx4 v78, s[38:39] offset:2048\n"
"v_mfma_f32_16x16x32_bf16 v[216:219], a[84:87], a[56:59], v[216:219]\n"
"v_mfma_f32_16x16x32_bf16 v[232:235], a[88:91], a[56:59], v[232:235]\n"
"v_mfma_f32_16x16x32_bf16 v[248:251], a[92:95], a[56:59], v[248:251]\n"
"v_mfma_f32_16x16x32_bf16 v[140:143], a[64:67], a[60:63], v[140:143]\n"
"v_mfma_f32_16x16x32_bf16 v[156:159], a[68:71], a[60:63], v[156:159]\n"
"global_load_lds_dwordx4 v79, s[38:39] offset:3072\n"
"s_add_u32 m0, s88, 4096\n"
"v_mfma_f32_16x16x32_bf16 v[172:175], a[72:75], a[60:63], v[172:175]\n"
"s_waitcnt vmcnt(21)\n"
"ds_read_b128 a[4:7], v52 offset:10240\n"
"v_mfma_f32_16x16x32_bf16 v[188:191], a[76:79], a[60:63], v[188:191]\n"
"s_waitcnt vmcnt(19)\n"
"ds_read_b128 a[8:11], v52 offset:12288\n"
"v_mfma_f32_16x16x32_bf16 v[204:207], a[80:83], a[60:63], v[204:207]\n"
"s_waitcnt vmcnt(17)\n"
"ds_read_b128 a[12:15], v52 offset:14336\n"
"v_mfma_f32_16x16x32_bf16 v[220:223], a[84:87], a[60:63], v[220:223]\n"
"v_mfma_f32_16x16x32_bf16 v[236:239], a[88:91], a[60:63], v[236:239]\n"
"global_load_lds_dwordx4 v80, s[38:39] offset:0\n"
"v_mfma_f32_16x16x32_bf16 v[252:255], a[92:95], a[60:63], v[252:255]\n"
"s_waitcnt lgkmcnt(0)\n"
"v_mfma_f32_16x16x32_bf16 v[112:115], a[16:19], v[56:59], v[112:115]\n"
"s_waitcnt vmcnt(11)\n"
"v_cmp_eq_u32 vcc, v109, v35\n"
"ds_read_b128 a[48:51], v53 offset:8192\n"
"v_mfma_f32_16x16x32_bf16 v[116:119], a[20:23], v[56:59], v[116:119]\n"
"ds_read_b64_tr_b16 a[64:65], v40 offset:16384\n"
"9300:\n"
"s_cbranch_execz 9301f\n"
"ds_read_b64_tr_b16 a[66:67], v40 offset:16896\n"
"v_mfma_f32_16x16x32_bf16 v[128:131], a[16:19], a[0:3], v[128:131]\n"
"ds_read_b64_tr_b16 a[68:69], v41 offset:16384\n"
"v_mfma_f32_16x16x32_bf16 v[144:147], a[20:23], a[0:3], v[144:147]\n"
"s_bitcmp1_b32 vcc_hi, 31\n"
"s_cbranch_scc1 8f\n"
"9:\n"
"global_load_lds_dwordx4 v81, s[38:39] offset:1024\n"
"v_mfma_f32_16x16x32_bf16 v[160:163], a[24:27], a[0:3], v[160:163]\n"
"ds_read_b64_tr_b16 a[70:71], v41 offset:16896\n"
"v_mfma_f32_16x16x32_bf16 v[176:179], a[28:31], a[0:3], v[176:179]\n"
"ds_read_b64_tr_b16 a[72:73], v42 offset:16384\n"
"ds_read_b64_tr_b16 a[74:75], v42 offset:16896\n"
"v_mfma_f32_16x16x32_bf16 v[132:135], a[16:19], a[4:7], v[132:135]\n"
"ds_read_b64_tr_b16 a[76:77], v43 offset:16384\n"
"v_mfma_f32_16x16x32_bf16 v[148:151], a[20:23], a[4:7], v[148:151]\n"
"ds_read_b64_tr_b16 a[78:79], v43 offset:16896\n"
"v_mfma_f32_16x16x32_bf16 v[164:167], a[24:27], a[4:7], v[164:167]\n"
"global_load_lds_dwordx4 v82, s[38:39] offset:2048\n"
"v_mfma_f32_16x16x32_bf16 v[180:183], a[28:31], a[4:7], v[180:183]\n"
"ds_read_b128 a[52:55], v53 offset:10240\n"
"ds_read_b128 a[56:59], v53 offset:12288\n"
"v_mfma_f32_16x16x32_bf16 v[136:139], a[16:19], a[8:11], v[136:139]\n"
"ds_read_b128 a[60:63], v53 offset:14336\n"
"v_mfma_f32_16x16x32_bf16 v[152:155], a[20:23], a[8:11], v[152:155]\n"
"v_mfma_f32_16x16x32_bf16 v[168:171], a[24:27], a[8:11], v[168:171]\n"
"v_mfma_f32_16x16x32_bf16 v[184:187], a[28:31], a[8:11], v[184:187]\n"
"global_load_lds_dwordx4 v83, s[38:39] offset:3072\n"
"s_add_u32 m0, s89, 34816\n"
"v_mfma_f32_16x16x32_bf16 v[140:143], a[16:19], a[12:15], v[140:143]\n"
"v_mfma_f32_16x16x32_bf16 v[156:159], a[20:23], a[12:15], v[156:159]\n"
"v_mfma_f32_16x16x32_bf16 v[172:175], a[24:27], a[12:15], v[172:175]\n"
"v_mfma_f32_16x16x32_bf16 v[188:191], a[28:31], a[12:15], v[188:191]\n"
"s_waitcnt lgkmcnt(0)\n"
"s_barrier\n"
"v_mfma_f32_16x16x32_bf16 v[128:131], a[64:67], a[48:51], v[128:131]\n"
"global_load_lds_dwordx4 v86, s[44:45] offset:0\n"
"v_mfma_f32_16x16x32_bf16 v[144:147], a[68:71], a[48:51], v[144:147]\n"
"ds_read_b128 a[0:3], v52 offset:16384\n"
"ds_read_b64_tr_b16 a[16:17], v36 offset:32768\n"
"v_mfma_f32_16x16x32_bf16 v[160:163], a[72:75], a[48:51], v[160:163]\n"
"ds_read_b64_tr_b16 a[18:19], v36 offset:33280\n"
"v_mfma_f32_16x16x32_bf16 v[176:179], a[76:79], a[48:51], v[176:179]\n"
"ds_read_b64_tr_b16 a[20:21], v37 offset:32768\n"
"v_mfma_f32_16x16x32_bf16 v[132:135], a[64:67], a[52:55], v[132:135]\n"
"ds_read_b64_tr_b16 a[22:23], v37 offset:33280\n"
"v_mfma_f32_16x16x32_bf16 v[148:151], a[68:71], a[52:55], v[148:151]\n"
"global_load_lds_dwordx4 v87, s[44:45] offset:1024\n"
"s_lshl_b32 m0, s30, 10\n"
"s_add_u32 m0, m0, 147456\n"
"v_mfma_f32_16x16x32_bf16 v[164:167], a[72:75], a[52:55], v[164:167]\n"
"ds_read_b64_tr_b16 a[24:25], v38 offset:32768\n"
"v_mfma_f32_16x16x32_bf16 v[180:183], a[76:79], a[52:55], v[180:183]\n"
"ds_read_b64_tr_b16 a[26:27], v38 offset:33280\n"
"v_mfma_f32_16x16x32_bf16 v[136:139], a[64:67], a[56:59], v[136:139]\n"
"ds_read_b64_tr_b16 a[28:29], v39 offset:32768\n"
"v_mfma_f32_16x16x32_bf16 v[152:155], a[68:71], a[56:59], v[152:155]\n"
"ds_read_b64_tr_b16 a[30:31], v39 offset:33280\n"
"ds_read_b128 a[4:7], v52 offset:18432\n"
"v_mfma_f32_16x16x32_bf16 v[168:171], a[72:75], a[56:59], v[168:171]\n"
"global_load_lds_dwordx4 v9, s[2:3] sc1\n"
"s_add_u32 m0, s89, 2048\n"
"v_mfma_f32_16x16x32_bf16 v[184:187], a[76:79], a[56:59], v[184:187]\n"
"s_waitcnt vmcnt(15)\n"
"ds_read_b128 a[8:11], v52 offset:20480\n"
"v_mfma_f32_16x16x32_bf16 v[140:143], a[64:67], a[60:63], v[140:143]\n"
"s_waitcnt vmcnt(13)\n"
"ds_read_b128 a[12:15], v52 offset:22528\n"
"v_mfma_f32_16x16x32_bf16 v[156:159], a[68:71], a[60:63], v[156:159]\n"
"v_mfma_f32_16x16x32_bf16 v[172:175], a[72:75], a[60:63], v[172:175]\n"
"v_mfma_f32_16x16x32_bf16 v[188:191], a[76:79], a[60:63], v[188:191]\n"
"global_load_lds_dwordx4 v86, s[48:49] offset:0\n"
"s_waitcnt lgkmcnt(0)\n"
"v_mfma_f32_16x16x32_bf16 v[112:115], a[16:19], v[56:59], v[112:115]\n"
"ds_read_b128 a[48:51], v53 offset:16384\n"
"v_mfma_f32_16x16x32_bf16 v[116:119], a[20:23], v[56:59], v[116:119]\n"
"ds_read_b64_tr_b16 a[64:65], v40 offset:32768\n"
"ds_read_b64_tr_b16 a[66:67], v40 offset:33280\n"
"v_mfma_f32_16x16x32_bf16 v[128:131], a[16:19], a[0:3], v[128:131]\n"
"ds_read_b64_tr_b16 a[68:69], v41 offset:32768\n"
"v_mfma_f32_16x16x32_bf16 v[144:147], a[20:23], a[0:3], v[144:147]\n"
"ds_read_b64_tr_b16 a[70:71], v41 offset:33280\n"
"v_mfma_f32_16x16x32_bf16 v[160:163], a[24:27], a[0:3], v[160:163]\n"
"global_load_lds_dwordx4 v87, s[48:49] offset:1024\n"
"v_mfma_f32_16x16x32_bf16 v[176:179], a[28:31], a[0:3], v[176:179]\n"
"ds_read_b64_tr_b16 a[72:73], v42 offset:32768\n"
"ds_read_b64_tr_b16 a[74:75], v42 offset:33280\n"
"v_mfma_f32_16x16x32_bf16 v[132:135], a[16:19], a[4:7], v[132:135]\n"
"ds_read_b64_tr_b16 a[76:77], v43 offset:32768\n"
"v_mfma_f32_16x16x32_bf16 v[148:151], a[20:23], a[4:7], v[148:151]\n"
"ds_read_b64_tr_b16 a[78:79], v43 offset:33280\n"
"v_mfma_f32_16x16x32_bf16 v[164:167], a[24:27], a[4:7], v[164:167]\n"
"ds_read_b128 a[52:55], v53 offset:18432\n"
"v_mfma_f32_16x16x32_bf16 v[180:183], a[28:31], a[4:7], v[180:183]\n"
"global_load_dwordx2 a[128:129], v71, s[10:11] offset:0\n"
"v_mfma_f32_16x16x32_bf16 v[136:139], a[16:19], a[8:11], v[136:139]\n"
"global_load_dwordx2 a[130:131], v71, s[10:11] offset:32\n"
"v_mfma_f32_16x16x32_bf16 v[152:155], a[20:23], a[8:11], v[152:155]\n"
"ds_read_b128 a[56:59], v53 offset:20480\n"
"v_mfma_f32_16x16x32_bf16 v[168:171], a[24:27], a[8:11], v[168:171]\n"
"global_load_dwordx2 a[132:133], v72, s[10:11] offset:0\n"
"v_mfma_f32_16x16x32_bf16 v[184:187], a[28:31], a[8:11], v[184:187]\n"
"global_load_dwordx2 a[134:135], v72, s[10:11] offset:32\n"
"v_mfma_f32_16x16x32_bf16 v[140:143], a[16:19], a[12:15], v[140:143]\n"
"ds_read_b128 a[60:63], v53 offset:22528\n"
"v_mfma_f32_16x16x32_bf16 v[156:159], a[20:23], a[12:15], v[156:159]\n"
"global_load_dword v34, v14, s[66:67] offset:0 sc1\n"
"v_mfma_f32_16x16x32_bf16 v[172:175], a[24:27], a[12:15], v[172:175]\n"
"v_mfma_f32_16x16x32_bf16 v[188:191], a[28:31], a[12:15], v[188:191]\n"
"s_waitcnt vmcnt(18) lgkmcnt(0)\n"
"s_barrier\n"
"v_mfma_f32_16x16x32_bf16 v[128:131], a[64:67], a[48:51], v[128:131]\n"
"s_waitcnt vmcnt(16)\n"
"ds_read_b128 a[0:3], v52 offset:0\n"
"v_mfma_f32_16x16x32_bf16 v[144:147], a[68:71], a[48:51], v[144:147]\n"
"ds_read_b64_tr_b16 a[16:17], v36 offset:0\n"
"ds_read_b64_tr_b16 a[18:19], v36 offset:512\n"
"v_mfma_f32_16x16x32_bf16 v[160:163], a[72:75], a[48:51], v[160:163]\n"
"global_load_dword v33, v16, s[66:67] offset:0 sc1\n"
"v_mfma_f32_16x16x32_bf16 v[176:179], a[76:79], a[48:51], v[176:179]\n"
"ds_read_b64_tr_b16 a[20:21], v37 offset:0\n"
"v_mfma_f32_16x16x32_bf16 v[132:135], a[64:67], a[52:55], v[132:135]\n"
"ds_read_b64_tr_b16 a[22:23], v37 offset:512\n"
"v_mfma_f32_16x16x32_bf16 v[148:151], a[68:71], a[52:55], v[148:151]\n"
"ds_read_b64_tr_b16 a[24:25], v38 offset:0\n"
"ds_read_b64_tr_b16 a[26:27], v38 offset:512\n"
"v_mfma_f32_16x16x32_bf16 v[164:167], a[72:75], a[52:55], v[164:167]\n"
"ds_read_b64_tr_b16 a[28:29], v39 offset:0\n"
"v_mfma_f32_16x16x32_bf16 v[180:183], a[76:79], a[52:55], v[180:183]\n"
"ds_read_b64_tr_b16 a[30:31], v39 offset:512\n"
"v_mfma_f32_16x16x32_bf16 v[136:139], a[64:67], a[56:59], v[136:139]\n"
"v_mfma_f32_16x16x32_bf16 v[152:155], a[68:71], a[56:59], v[152:155]\n"
"s_waitcnt vmcnt(15)\n"
"ds_read_b128 a[4:7], v52 offset:2048\n"
"v_mfma_f32_16x16x32_bf16 v[168:171], a[72:75], a[56:59], v[168:171]\n"
"s_waitcnt vmcnt(13)\n"
"ds_read_b128 a[8:11], v52 offset:4096\n"
"9301:\n"
"s_cbranch_execz 9302f\n"
"v_mfma_f32_16x16x32_bf16 v[184:187], a[76:79], a[56:59], v[184:187]\n"
"s_waitcnt vmcnt(11)\n"
"ds_read_b128 a[12:15], v52 offset:6144\n"
"v_mfma_f32_16x16x32_bf16 v[140:143], a[64:67], a[60:63], v[140:143]\n"
"v_mfma_f32_16x16x32_bf16 v[156:159], a[68:71], a[60:63], v[156:159]\n"
"v_mfma_f32_16x16x32_bf16 v[172:175], a[72:75], a[60:63], v[172:175]\n"
"v_mfma_f32_16x16x32_bf16 v[188:191], a[76:79], a[60:63], v[188:191]\n"
"s_waitcnt lgkmcnt(0)\n"
"v_mfma_f32_16x16x32_bf16 v[112:115], a[16:19], v[56:59], v[112:115]\n"
"ds_read_b128 a[48:51], v53 offset:0\n"
"v_mfma_f32_16x16x32_bf16 v[116:119], a[20:23], v[56:59], v[116:119]\n"
"ds_read_b64_tr_b16 a[64:65], v40 offset:0\n"
"ds_read_b64_tr_b16 a[66:67], v40 offset:512\n"
"v_mfma_f32_16x16x32_bf16 v[128:131], a[16:19], a[0:3], v[128:131]\n"
"ds_read_b64_tr_b16 a[68:69], v41 offset:0\n"
"v_mfma_f32_16x16x32_bf16 v[144:147], a[20:23], a[0:3], v[144:147]\n"
"ds_read_b64_tr_b16 a[70:71], v41 offset:512\n"
"v_mfma_f32_16x16x32_bf16 v[160:163], a[24:27], a[0:3], v[160:163]\n"
"ds_read_b64_tr_b16 a[72:73], v42 offset:0\n"
"v_mfma_f32_16x16x32_bf16 v[176:179], a[28:31], a[0:3], v[176:179]\n"
"ds_read_b64_tr_b16 a[74:75], v42 offset:512\n"
"ds_read_b64_tr_b16 a[76:77], v43 offset:0\n"
"v_mfma_f32_16x16x32_bf16 v[132:135], a[16:19], a[4:7], v[132:135]\n"
"ds_read_b64_tr_b16 a[78:79], v43 offset:512\n"
"v_mfma_f32_16x16x32_bf16 v[148:151], a[20:23], a[4:7], v[148:151]\n"
"ds_read_b128 a[52:55], v53 offset:2048\n"
"v_mfma_f32_16x16x32_bf16 v[164:167], a[24:27], a[4:7], v[164:167]\n"
"ds_read_b128 a[56:59], v53 offset:4096\n"
"v_mfma_f32_16x16x32_bf16 v[180:183], a[28:31], a[4:7], v[180:183]\n"
"ds_read_b128 a[60:63], v53 offset:6144\n"
"v_mfma_f32_16x16x32_bf16 v[136:139], a[16:19], a[8:11], v[136:139]\n"
"s_mov_b32 exec_lo, 0x10001\n"
"s_mov_b32 exec_hi, 0x10001\n"
"ds_write_b128 v66, v[112:115] offset:0\n"
"ds_write_b128 v66, v[116:119] offset:64\n"
"s_mov_b64 exec, -1\n"
"v_mfma_f32_16x16x32_bf16 v[152:155], a[20:23], a[8:11], v[152:155]\n"
"v_mfma_f32_16x16x32_bf16 v[168:171], a[24:27], a[8:11], v[168:171]\n"
"v_mfma_f32_16x16x32_bf16 v[184:187], a[28:31], a[8:11], v[184:187]\n"
"v_mfma_f32_16x16x32_bf16 v[140:143], a[16:19], a[12:15], v[140:143]\n"
"v_mfma_f32_16x16x32_bf16 v[156:159], a[20:23], a[12:15], v[156:159]\n"
"v_mfma_f32_16x16x32_bf16 v[172:175], a[24:27], a[12:15], v[172:175]\n"
"v_mfma_f32_16x16x32_bf16 v[188:191], a[28:31], a[12:15], v[188:191]\n"
"s_waitcnt vmcnt(8) lgkmcnt(0)\n"
"s_barrier\n"
"v_mfma_f32_16x16x32_bf16 v[128:131], a[64:67], a[48:51], v[128:131]\n"
"ds_read_b128 a[0:3], v52 offset:8192\n"
"s_mov_b64 s[36:37], s[12:13]\n"
"s_mov_b64 s[38:39], s[14:15]\n"
"v_mfma_f32_16x16x32_bf16 v[144:147], a[68:71], a[48:51], v[144:147]\n"
"ds_read_b64_tr_b16 a[32:33], v44 offset:16384\n"
"ds_read_b64_tr_b16 a[34:35], v44 offset:16896\n"
"v_mfma_f32_16x16x32_bf16 v[160:163], a[72:75], a[48:51], v[160:163]\n"
"ds_read_b64_tr_b16 a[36:37], v45 offset:16384\n"
"s_mov_b64 s[40:41], s[16:17]\n"
"s_mov_b64 s[42:43], s[18:19]\n"
"v_mfma_f32_16x16x32_bf16 v[176:179], a[76:79], a[48:51], v[176:179]\n"
"ds_read_b64_tr_b16 a[38:39], v45 offset:16896\n"
"s_mov_b64 s[44:45], s[22:23]\n"
"v_mfma_f32_16x16x32_bf16 v[132:135], a[64:67], a[52:55], v[132:135]\n"
"ds_read_b64_tr_b16 a[40:41], v46 offset:16384\n"
"s_cmp_eq_u32 s30, 0\n"
"s_cselect_b64 s[50:51], 4, 0\n"
"v_mfma_f32_16x16x32_bf16 v[148:151], a[68:71], a[52:55], v[148:151]\n"
"ds_read_b64_tr_b16 a[42:43], v46 offset:16896\n"
"ds_read_b64_tr_b16 a[44:45], v47 offset:16384\n"
"v_mfma_f32_16x16x32_bf16 v[164:167], a[72:75], a[52:55], v[164:167]\n"
"ds_read_b64_tr_b16 a[46:47], v47 offset:16896\n"
"s_mov_b32 s48, 0xfffff\n"
"s_mov_b32 s49, 0xfffff\n"
"v_mfma_f32_16x16x32_bf16 v[180:183], a[76:79], a[52:55], v[180:183]\n"
"ds_read_b128 a[4:7], v52 offset:10240\n"
"s_mov_b64 s[52:53], -1\n"
"v_mfma_f32_16x16x32_bf16 v[136:139], a[64:67], a[56:59], v[136:139]\n"
"ds_read_b128 a[8:11], v52 offset:12288\n"
"v_mfma_f32_16x16x32_bf16 v[152:155], a[68:71], a[56:59], v[152:155]\n"
"ds_read_b128 a[12:15], v52 offset:14336\n"
"ds_read2_b32 v[36:37], v64 offset0:0 offset1:16\n"
"v_mfma_f32_16x16x32_bf16 v[168:171], a[72:75], a[56:59], v[168:171]\n"
"ds_read2_b32 v[38:39], v64 offset0:32 offset1:48\n"
"ds_read2_b32 v[40:41], v64 offset0:64 offset1:80\n"
"v_mfma_f32_16x16x32_bf16 v[184:187], a[76:79], a[56:59], v[184:187]\n"
"ds_read2_b32 v[42:43], v64 offset0:96 offset1:112\n"
"v_mfma_f32_16x16x32_bf16 v[140:143], a[64:67], a[60:63], v[140:143]\n"
"ds_read_b128 v[76:79], v67 offset:0\n"
"v_mfma_f32_16x16x32_bf16 v[156:159], a[68:71], a[60:63], v[156:159]\n"
"ds_read_b128 v[92:95], v67 offset:256\n"
"v_mfma_f32_16x16x32_bf16 v[172:175], a[72:75], a[60:63], v[172:175]\n"
"ds_read_b128 v[80:83], v67 offset:64\n"
"v_mfma_f32_16x16x32_bf16 v[188:191], a[76:79], a[60:63], v[188:191]\n"
"ds_read_b128 v[96:99], v67 offset:320\n"
"s_waitcnt lgkmcnt(0)\n"
"v_mfma_f32_16x16x32_bf16 v[120:123], a[32:35], v[56:59], v[120:123]\n"
"ds_read_b128 a[48:51], v53 offset:8192\n"
"v_mfma_f32_16x16x32_bf16 v[124:127], a[36:39], v[56:59], v[124:127]\n"
"ds_read_b64_tr_b16 a[80:81], v48 offset:16384\n"
"ds_read_b64_tr_b16 a[82:83], v48 offset:16896\n"
"v_mfma_f32_16x16x32_bf16 v[192:195], a[32:35], a[0:3], v[192:195]\n"
"ds_read_b64_tr_b16 a[84:85], v49 offset:16384\n"
"v_mfma_f32_16x16x32_bf16 v[208:211], a[36:39], a[0:3], v[208:211]\n"
"ds_read_b64_tr_b16 a[86:87], v49 offset:16896\n"
"v_mfma_f32_16x16x32_bf16 v[224:227], a[40:43], a[0:3], v[224:227]\n"
"ds_read_b64_tr_b16 a[88:89], v50 offset:16384\n"
"v_mfma_f32_16x16x32_bf16 v[240:243], a[44:47], a[0:3], v[240:243]\n"
"ds_read_b64_tr_b16 a[90:91], v50 offset:16896\n"
"ds_read_b64_tr_b16 a[92:93], v51 offset:16384\n"
"v_mfma_f32_16x16x32_bf16 v[196:199], a[32:35], a[4:7], v[196:199]\n"
"ds_read_b64_tr_b16 a[94:95], v51 offset:16896\n"
"v_mfma_f32_16x16x32_bf16 v[212:215], a[36:39], a[4:7], v[212:215]\n"
"ds_read_b128 a[52:55], v53 offset:10240\n"
"v_mfma_f32_16x16x32_bf16 v[228:231], a[40:43], a[4:7], v[228:231]\n"
"ds_read_b128 a[56:59], v53 offset:12288\n"
"v_mfma_f32_16x16x32_bf16 v[244:247], a[44:47], a[4:7], v[244:247]\n"
"ds_read_b128 a[60:63], v53 offset:14336\n"
"v_mfma_f32_16x16x32_bf16 v[200:203], a[32:35], a[8:11], v[200:203]\n"
"ds_read_b128 v[84:87], v68 offset:0\n"
"v_mfma_f32_16x16x32_bf16 v[216:219], a[36:39], a[8:11], v[216:219]\n"
"ds_read_b128 v[100:103], v68 offset:256\n"
"v_mfma_f32_16x16x32_bf16 v[232:235], a[40:43], a[8:11], v[232:235]\n"
"ds_read_b128 v[88:91], v68 offset:64\n"
"v_mfma_f32_16x16x32_bf16 v[248:251], a[44:47], a[8:11], v[248:251]\n"
"ds_read_b128 v[104:107], v68 offset:320\n"
"v_mul_f32 v40, v2, v40\n"
"v_mfma_f32_16x16x32_bf16 v[204:207], a[32:35], a[12:15], v[204:207]\n"
"v_mul_f32 v41, v2, v41\n"
"v_mul_f32 v42, v2, v42\n"
"v_mfma_f32_16x16x32_bf16 v[220:223], a[36:39], a[12:15], v[220:223]\n"
"v_mul_f32 v43, v2, v43\n"
"v_add_f32 v76, v76, v92\n"
"v_mfma_f32_16x16x32_bf16 v[236:239], a[40:43], a[12:15], v[236:239]\n"
"v_add_f32 v77, v77, v93\n"
"v_add_f32 v78, v78, v94\n"
"v_mfma_f32_16x16x32_bf16 v[252:255], a[44:47], a[12:15], v[252:255]\n"
"v_add_f32 v79, v79, v95\n"
"v_add_f32 v80, v80, v96\n"
"v_add_f32 v81, v81, v97\n"
"v_add_f32 v82, v82, v98\n"
"v_add_f32 v83, v83, v99\n"
"s_waitcnt lgkmcnt(2)\n"
"v_add_f32 v84, v84, v100\n"
"v_add_f32 v85, v85, v101\n"
"v_add_f32 v86, v86, v102\n"
"v_add_f32 v87, v87, v103\n"
"s_waitcnt lgkmcnt(0)\n"
"v_add_f32 v88, v88, v104\n"
"v_add_f32 v89, v89, v105\n"
"v_add_f32 v90, v90, v106\n"
"v_add_f32_e64 v91, v91, v107\n"
"9302:\n"
"s_cbranch_execz 97b\n"
"s_waitcnt vmcnt(6)\n"
"s_barrier\n"
"v_pk_fma_f32 v[128:129], v[36:37], v[76:77], v[128:129] op_sel:[0,0,0] op_sel_hi:[0,1,1] neg_lo:[1,0,0] neg_hi:[1,0,0]\n"
"v_pk_fma_f32 v[130:131], v[36:37], v[78:79], v[130:131] op_sel:[0,0,0] op_sel_hi:[0,1,1] neg_lo:[1,0,0] neg_hi:[1,0,0]\n"
"v_pk_fma_f32 v[144:145], v[36:37], v[80:81], v[144:145] op_sel:[0,0,0] op_sel_hi:[0,1,1] neg_lo:[1,0,0] neg_hi:[1,0,0]\n"
"v_pk_fma_f32 v[146:147], v[36:37], v[82:83], v[146:147] op_sel:[0,0,0] op_sel_hi:[0,1,1] neg_lo:[1,0,0] neg_hi:[1,0,0]\n"
"v_pk_fma_f32 v[132:133], v[36:37], v[76:77], v[132:133] op_sel:[1,0,0] op_sel_hi:[1,1,1] neg_lo:[1,0,0] neg_hi:[1,0,0]\n"
"v_pk_fma_f32 v[134:135], v[36:37], v[78:79], v[134:135] op_sel:[1,0,0] op_sel_hi:[1,1,1] neg_lo:[1,0,0] neg_hi:[1,0,0]\n"
"v_pk_fma_f32 v[148:149], v[36:37], v[80:81], v[148:149] op_sel:[1,0,0] op_sel_hi:[1,1,1] neg_lo:[1,0,0] neg_hi:[1,0,0]\n"
"v_pk_fma_f32 v[150:151], v[36:37], v[82:83], v[150:151] op_sel:[1,0,0] op_sel_hi:[1,1,1] neg_lo:[1,0,0] neg_hi:[1,0,0]\n"
"v_pk_mul_f32 v[128:129], v[128:129], v[40:41] op_sel:[0,0] op_sel_hi:[1,0]\n"
"v_pk_mul_f32 v[130:131], v[130:131], v[40:41] op_sel:[0,0] op_sel_hi:[1,0]\n"
"v_pk_mul_f32 v[144:145], v[144:145], v[40:41] op_sel:[0,0] op_sel_hi:[1,0]\n"
"v_pk_mul_f32 v[146:147], v[146:147], v[40:41] op_sel:[0,0] op_sel_hi:[1,0]\n"
"v_pk_mul_f32 v[132:133], v[132:133], v[40:41] op_sel:[0,1] op_sel_hi:[1,1]\n"
"v_pk_mul_f32 v[134:135], v[134:135], v[40:41] op_sel:[0,1] op_sel_hi:[1,1]\n"
"v_pk_mul_f32 v[148:149], v[148:149], v[40:41] op_sel:[0,1] op_sel_hi:[1,1]\n"
"v_pk_mul_f32 v[150:151], v[150:151], v[40:41] op_sel:[0,1] op_sel_hi:[1,1]\n"
"v_cvt_pk_bf16_f32 v92, v128, v129\n"
"v_cvt_pk_bf16_f32 v93, v130, v131\n"
"v_cvt_pk_bf16_f32 v94, v144, v145\n"
"v_cvt_pk_bf16_f32 v95, v146, v147\n"
"v_cvt_pk_bf16_f32 v96, v132, v133\n"
"v_cvt_pk_bf16_f32 v97, v134, v135\n"
"v_cvt_pk_bf16_f32 v98, v148, v149\n"
"v_cvt_pk_bf16_f32 v99, v150, v151\n"
"s_mov_b32 exec_lo, 0\n"
"s_mov_b32 exec_hi, s35\n"
"v_swap_b32 v94, v96\n"
"v_swap_b32 v95, v97\n"
"s_mov_b64 exec, -1\n"
"s_nop 0\n"
"global_store_dwordx4 v60, v[92:95], s[6:7] sc1\n"
"global_store_dwordx4 v61, v[96:99], s[6:7] sc1\n"
"v_pk_fma_f32 v[136:137], v[38:39], v[76:77], v[136:137] op_sel:[0,0,0] op_sel_hi:[0,1,1] neg_lo:[1,0,0] neg_hi:[1,0,0]\n"
"v_pk_fma_f32 v[138:139], v[38:39], v[78:79], v[138:139] op_sel:[0,0,0] op_sel_hi:[0,1,1] neg_lo:[1,0,0] neg_hi:[1,0,0]\n"
"v_pk_fma_f32 v[152:153], v[38:39], v[80:81], v[152:153] op_sel:[0,0,0] op_sel_hi:[0,1,1] neg_lo:[1,0,0] neg_hi:[1,0,0]\n"
"v_pk_fma_f32 v[154:155], v[38:39], v[82:83], v[154:155] op_sel:[0,0,0] op_sel_hi:[0,1,1] neg_lo:[1,0,0] neg_hi:[1,0,0]\n"
"v_pk_fma_f32 v[140:141], v[38:39], v[76:77], v[140:141] op_sel:[1,0,0] op_sel_hi:[1,1,1] neg_lo:[1,0,0] neg_hi:[1,0,0]\n"
"v_pk_fma_f32 v[142:143], v[38:39], v[78:79], v[142:143] op_sel:[1,0,0] op_sel_hi:[1,1,1] neg_lo:[1,0,0] neg_hi:[1,0,0]\n"
"v_pk_fma_f32 v[156:157], v[38:39], v[80:81], v[156:157] op_sel:[1,0,0] op_sel_hi:[1,1,1] neg_lo:[1,0,0] neg_hi:[1,0,0]\n"
"v_pk_fma_f32 v[158:159], v[38:39], v[82:83], v[158:159] op_sel:[1,0,0] op_sel_hi:[1,1,1] neg_lo:[1,0,0] neg_hi:[1,0,0]\n"
"v_pk_mul_f32 v[136:137], v[136:137], v[42:43] op_sel:[0,0] op_sel_hi:[1,0]\n"
"v_pk_mul_f32 v[138:139], v[138:139], v[42:43] op_sel:[0,0] op_sel_hi:[1,0]\n"
"v_pk_mul_f32 v[152:153], v[152:153], v[42:43] op_sel:[0,0] op_sel_hi:[1,0]\n"
"v_pk_mul_f32 v[154:155], v[154:155], v[42:43] op_sel:[0,0] op_sel_hi:[1,0]\n"
"v_pk_mul_f32 v[140:141], v[140:141], v[42:43] op_sel:[0,1] op_sel_hi:[1,1]\n"
"v_pk_mul_f32 v[142:143], v[142:143], v[42:43] op_sel:[0,1] op_sel_hi:[1,1]\n"
"v_pk_mul_f32 v[156:157], v[156:157], v[42:43] op_sel:[0,1] op_sel_hi:[1,1]\n"
"v_pk_mul_f32 v[158:159], v[158:159], v[42:43] op_sel:[0,1] op_sel_hi:[1,1]\n"
"v_cvt_pk_bf16_f32 v100, v136, v137\n"
"v_cvt_pk_bf16_f32 v101, v138, v139\n"
"v_cvt_pk_bf16_f32 v102, v152, v153\n"
"v_cvt_pk_bf16_f32 v103, v154, v155\n"
"v_cvt_pk_bf16_f32 v104, v140, v141\n"
"v_cvt_pk_bf16_f32 v105, v142, v143\n"
"v_cvt_pk_bf16_f32 v106, v156, v157\n"
"v_cvt_pk_bf16_f32 v107, v158, v159\n"
"s_mov_b32 exec_lo, 0\n"
"s_mov_b32 exec_hi, s35\n"
"v_swap_b32 v102, v104\n"
"v_swap_b32 v103, v105\n"
"s_mov_b64 exec, -1\n"
"s_nop 0\n"
"global_store_dwordx4 v60, v[100:103], s[8:9] sc1\n"
"global_store_dwordx4 v61, v[104:107], s[8:9] sc1\n"
"v_pk_fma_f32 v[160:161], v[36:37], v[84:85], v[160:161] op_sel:[0,0,0] op_sel_hi:[0,1,1] neg_lo:[1,0,0] neg_hi:[1,0,0]\n"
"v_pk_fma_f32 v[162:163], v[36:37], v[86:87], v[162:163] op_sel:[0,0,0] op_sel_hi:[0,1,1] neg_lo:[1,0,0] neg_hi:[1,0,0]\n"
"v_pk_fma_f32 v[176:177], v[36:37], v[88:89], v[176:177] op_sel:[0,0,0] op_sel_hi:[0,1,1] neg_lo:[1,0,0] neg_hi:[1,0,0]\n"
"v_pk_fma_f32 v[178:179], v[36:37], v[90:91], v[178:179] op_sel:[0,0,0] op_sel_hi:[0,1,1] neg_lo:[1,0,0] neg_hi:[1,0,0]\n"
"v_pk_fma_f32 v[164:165], v[36:37], v[84:85], v[164:165] op_sel:[1,0,0] op_sel_hi:[1,1,1] neg_lo:[1,0,0] neg_hi:[1,0,0]\n"
"v_pk_fma_f32 v[166:167], v[36:37], v[86:87], v[166:167] op_sel:[1,0,0] op_sel_hi:[1,1,1] neg_lo:[1,0,0] neg_hi:[1,0,0]\n"
"v_pk_fma_f32 v[180:181], v[36:37], v[88:89], v[180:181] op_sel:[1,0,0] op_sel_hi:[1,1,1] neg_lo:[1,0,0] neg_hi:[1,0,0]\n"
"v_pk_fma_f32 v[182:183], v[36:37], v[90:91], v[182:183] op_sel:[1,0,0] op_sel_hi:[1,1,1] neg_lo:[1,0,0] neg_hi:[1,0,0]\n"
"v_pk_mul_f32 v[160:161], v[160:161], v[40:41] op_sel:[0,0] op_sel_hi:[1,0]\n"
"v_pk_mul_f32 v[162:163], v[162:163], v[40:41] op_sel:[0,0] op_sel_hi:[1,0]\n"
"v_pk_mul_f32 v[176:177], v[176:177], v[40:41] op_sel:[0,0] op_sel_hi:[1,0]\n"
"v_pk_mul_f32 v[178:179], v[178:179], v[40:41] op_sel:[0,0] op_sel_hi:[1,0]\n"
"v_pk_mul_f32 v[164:165], v[164:165], v[40:41] op_sel:[0,1] op_sel_hi:[1,1]\n"
"v_pk_mul_f32 v[166:167], v[166:167], v[40:41] op_sel:[0,1] op_sel_hi:[1,1]\n"
"v_pk_mul_f32 v[180:181], v[180:181], v[40:41] op_sel:[0,1] op_sel_hi:[1,1]\n"
"v_pk_mul_f32 v[182:183], v[182:183], v[40:41] op_sel:[0,1] op_sel_hi:[1,1]\n"
"v_cvt_pk_bf16_f32 v128, v160, v161\n"
"v_cvt_pk_bf16_f32 v129, v162, v163\n"
"v_cvt_pk_bf16_f32 v130, v176, v177\n"
"v_cvt_pk_bf16_f32 v131, v178, v179\n"
"v_cvt_pk_bf16_f32 v132, v164, v165\n"
"v_cvt_pk_bf16_f32 v133, v166, v167\n"
"v_cvt_pk_bf16_f32 v134, v180, v181\n"
"v_cvt_pk_bf16_f32 v135, v182, v183\n"
"s_mov_b32 exec_lo, 0\n"
"s_mov_b32 exec_hi, s58\n"
"v_swap_b32 v130, v132\n"
"v_swap_b32 v131, v133\n"
"s_mov_b64 exec, -1\n"
"s_nop 0\n"
"global_store_dwordx4 v62, v[128:131], s[6:7] sc1\n"
"global_store_dwordx4 v63, v[132:135], s[6:7] sc1\n"
"v_pk_fma_f32 v[168:169], v[38:39], v[84:85], v[168:169] op_sel:[0,0,0] op_sel_hi:[0,1,1] neg_lo:[1,0,0] neg_hi:[1,0,0]\n"
"v_pk_fma_f32 v[170:171], v[38:39], v[86:87], v[170:171] op_sel:[0,0,0] op_sel_hi:[0,1,1] neg_lo:[1,0,0] neg_hi:[1,0,0]\n"
"v_pk_fma_f32 v[184:185], v[38:39], v[88:89], v[184:185] op_sel:[0,0,0] op_sel_hi:[0,1,1] neg_lo:[1,0,0] neg_hi:[1,0,0]\n"
"v_pk_fma_f32 v[186:187], v[38:39], v[90:91], v[186:187] op_sel:[0,0,0] op_sel_hi:[0,1,1] neg_lo:[1,0,0] neg_hi:[1,0,0]\n"
"v_pk_fma_f32 v[172:173], v[38:39], v[84:85], v[172:173] op_sel:[1,0,0] op_sel_hi:[1,1,1] neg_lo:[1,0,0] neg_hi:[1,0,0]\n"
"v_pk_fma_f32 v[174:175], v[38:39], v[86:87], v[174:175] op_sel:[1,0,0] op_sel_hi:[1,1,1] neg_lo:[1,0,0] neg_hi:[1,0,0]\n"
"v_pk_fma_f32 v[188:189], v[38:39], v[88:89], v[188:189] op_sel:[1,0,0] op_sel_hi:[1,1,1] neg_lo:[1,0,0] neg_hi:[1,0,0]\n"
"v_pk_fma_f32 v[190:191], v[38:39], v[90:91], v[190:191] op_sel:[1,0,0] op_sel_hi:[1,1,1] neg_lo:[1,0,0] neg_hi:[1,0,0]\n"
"v_pk_mul_f32 v[168:169], v[168:169], v[42:43] op_sel:[0,0] op_sel_hi:[1,0]\n"
"v_pk_mul_f32 v[170:171], v[170:171], v[42:43] op_sel:[0,0] op_sel_hi:[1,0]\n"
"v_pk_mul_f32 v[184:185], v[184:185], v[42:43] op_sel:[0,0] op_sel_hi:[1,0]\n"
"v_pk_mul_f32 v[186:187], v[186:187], v[42:43] op_sel:[0,0] op_sel_hi:[1,0]\n"
"v_pk_mul_f32 v[172:173], v[172:173], v[42:43] op_sel:[0,1] op_sel_hi:[1,1]\n"
"v_pk_mul_f32 v[174:175], v[174:175], v[42:43] op_sel:[0,1] op_sel_hi:[1,1]\n"
"v_pk_mul_f32 v[188:189], v[188:189], v[42:43] op_sel:[0,1] op_sel_hi:[1,1]\n"
"v_pk_mul_f32 v[190:191], v[190:191], v[42:43] op_sel:[0,1] op_sel_hi:[1,1]\n"
"v_cvt_pk_bf16_f32 v136, v168, v169\n"
"v_cvt_pk_bf16_f32 v137, v170, v171\n"
"v_cvt_pk_bf16_f32 v138, v184, v185\n"
"v_cvt_pk_bf16_f32 v139, v186, v187\n"
"v_cvt_pk_bf16_f32 v140, v172, v173\n"
"v_cvt_pk_bf16_f32 v141, v174, v175\n"
"v_cvt_pk_bf16_f32 v142, v188, v189\n"
"v_cvt_pk_bf16_f32 v143, v190, v191\n"
"s_mov_b32 exec_lo, 0\n"
"s_mov_b32 exec_hi, s58\n"
"v_swap_b32 v138, v140\n"
"v_swap_b32 v139, v141\n"
"s_mov_b64 exec, -1\n"
"global_store_dwordx4 v62, v[136:139], s[8:9] sc1\n"
"global_store_dwordx4 v63, v[140:143], s[8:9] sc1\n"
"s_branch 5f\n"
"8:\n"
"s_mov_b32 s46, 0\n"
"81:\n"
"s_sleep 4\n"
"s_mov_b32 exec_lo, 0\n"
"global_load_dword v109, v1, s[66:67] offset:0 sc1\n"
"s_mov_b32 exec_lo, -1\n"
"s_add_u32 s46, s46, 1\n"
"s_cmp_gt_u32 s46, 0x100000\n"
"s_cbranch_scc1 9b\n"
"s_waitcnt vmcnt(0)\n"
"v_cmp_eq_u32 vcc, v109, v35\n"
"s_nop 4\n"
"s_bitcmp1_b32 vcc_hi, 31\n"
"s_cbranch_scc1 81b\n"
"s_branch 9b\n"
"4:\n"
"9400:\n"
"s_cbranch_execz 9401f\n"
"s_mov_b32 s36, s86\n"
"s_add_u32 s36, s36, 1\n"
"s_cmp_eq_u32 s36, 10\n"
"s_cselect_b32 s36, 0, s36\n"
"s_mul_i32 s37, s36, 245760\n"
"s_add_u32 s40, s82, s37\n"
"s_addc_u32 s41, s83, 0\n"
"s_mul_i32 s37, s36, 221184\n"
"s_add_u32 s42, s84, s37\n"
"s_addc_u32 s43, s85, 0\n"
"s_lshl_b32 s37, s36, 7\n"
"s_add_u32 s38, s80, s37\n"
"s_addc_u32 s39, s81, 0\n"
"v_mov_b32 v56, 0x3f803f80\n"
"v_mov_b32 v57, 0x3f803f80\n"
"v_mov_b32 v58, 0x3f803f80\n"
"v_mov_b32 v59, 0x3f803f80\n"
"s_add_u32 m0, s89, 16384\n"
"s_nop 0\n"
"global_load_lds_dwordx4 v84, s[40:41] offset:0\n"
"s_and_b32 s3, s2, 3\n"
"s_lshl_b32 s3, s3, 3\n"
"s_bfe_u32 s4, s2, 0x10001\n"
"s_lshl_b32 s9, s2, 2\n"
"s_sub_i32 s10, s3, s4\n"
"s_lshr_b32 s14, s2, 3\n"
"s_and_b32 s3, s9, 4\n"
"s_lshr_b32 s18, s2, 5\n"
"s_add_i32 s3, s3, s14\n"
"s_add_i32 s10, s10, s18\n"
"s_and_b32 s3, s3, 32\n"
"s_or_b32 s4, s10, s3\n"
"s_lshl_b32 s3, s2, 1\n"
"s_and_b32 s3, s3, 12\n"
"s_bfe_u32 s7, s2, 0x20003\n"
"s_add_i32 s6, s3, s14\n"
"global_load_lds_dwordx4 v85, s[40:41] offset:1024\n"
"s_add_u32 m0, s89, 18432\n"
"s_and_b32 s3, s2, 4\n"
"s_lshr_b32 s13, s30, 1\n"
"s_sub_i32 s11, s6, 28\n"
"s_or_b32 s8, s7, s3\n"
"s_lshl_b32 s5, s8, 2\n"
"s_and_b32 s22, s30, 1\n"
"s_lshl_b32 s3, s11, 11\n"
"s_lshl_b32 s16, s13, 10\n"
"s_add_i32 s5, s5, s30\n"
"s_add_i32 s3, s3, s16\n"
"s_lshl_b32 s16, s22, 7\n"
"s_lshl_b32 s15, s10, 3\n"
"s_or_b32 s3, s3, s16\n"
"s_lshl_b32 s16, s5, 10\n"
"s_cmp_gt_u32 s4, 31\n"
"s_cselect_b32 s3, s3, s16\n"
"global_load_lds_dwordx4 v86, s[42:43] offset:0\n"
"s_addk_i32 s6, 0xd4\n"
"s_or_b32 s15, s15, s8\n"
"s_cmp_gt_u32 s4, 31\n"
"s_cselect_b32 s6, s6, s15\n"
"s_add_i32 s8, s15, 0x1fffff38\n"
"s_cmp_gt_u32 s4, 31\n"
"s_cselect_b32 s8, s11, s8\n"
"s_addk_i32 s15, 0xff58\n"
"s_cmp_gt_u32 s4, 31\n"
"s_mul_hi_u32 s16, s10, 0xcccccccd\n"
"s_cselect_b32 s31, s11, s15\n"
"s_lshr_b32 s11, s16, 2\n"
"s_lshr_b32 s15, s16, 3\n"
"s_sub_i32 s11, s10, s11\n"
"s_mul_i32 s15, s15, 10\n"
"s_and_b32 s11, s11, 1\n"
"global_load_lds_dwordx4 v87, s[42:43] offset:1024\n"
"s_add_u32 m0, s88, 8192\n"
"s_sub_i32 s16, s10, s15\n"
"s_or_b32 s17, s11, 10\n"
"s_lshl_b32 s15, s16, 4\n"
"s_cmp_lt_u32 s10, 10\n"
"s_cselect_b64 vcc, -1, 0\n"
"s_movk_i32 s19, 0xa00\n"
"s_and_b64 s[20:21], vcc, exec\n"
"s_cselect_b32 s54, 0x500, s19\n"
"s_mov_b32 s19, 0x58000\n"
"s_cselect_b32 s21, s19, 0x2d8000\n"
"s_lshl_b32 s55, s54, 6\n"
"s_and_b32 s25, s30, 2\n"
"v_mov_b32_e32 v2, 0x3ea52b5b\n"
"s_cmp_lt_u32 s10, 20\n"
"v_cndmask_b32_e32 v2, 1.0, v2, vcc\n"
"s_cselect_b64 vcc, -1, 0\n"
"global_load_lds_dwordx4 v76, s[38:39] offset:0\n"
"s_add_u32 s36, s36, 1\n"
"s_cmp_eq_u32 s36, 10\n"
"s_cselect_b32 s36, 0, s36\n"
"s_xor_b32 s20, s30, 2\n"
"s_lshr_b32 s58, s20, 1\n"
"s_lshl_b32 s19, s13, 3\n"
"s_lshl_b32 s23, s58, 3\n"
"s_sub_i32 s56, s17, s19\n"
"s_lshl_b32 s57, s25, 4\n"
"s_xor_b32 s19, s30, 1\n"
"s_sub_i32 s59, s17, s23\n"
"s_cmp_gt_u32 s20, 1\n"
"s_cselect_b32 s62, s1, s0\n"
"s_xor_b32 s24, s30, 3\n"
"s_lshr_b32 s63, s24, 1\n"
"s_lshl_b32 s23, s63, 3\n"
"s_sub_i32 s64, s17, s23\n"
"s_cmp_gt_u32 s24, 1\n"
"s_cselect_b32 s65, s1, s0\n"
"global_load_lds_dwordx4 v77, s[38:39] offset:1024\n"
"s_mul_i32 s37, s36, 245760\n"
"s_add_u32 s40, s82, s37\n"
"s_addc_u32 s41, s83, 0\n"
"s_and_b32 s17, s9, 28\n"
"s_mul_i32 s9, s5, s54\n"
"s_or_b32 s23, s17, s7\n"
"s_lshr_b32 s7, s2, 6\n"
"s_lshl_b32 s78, s25, 8\n"
"s_lshl_b32 s33, s30, 10\n"
"s_lshl_b32 s34, s30, 8\n"
"s_lshl_b32 s9, s9, 1\n"
"s_add_i32 s17, s17, s7\n"
"s_or_b32 s35, s78, 0x25000\n"
"s_add_i32 s51, s33, 0x24000\n"
"s_add_i32 s52, s34, 0x25000\n"
"s_xor_b32 s53, s78, 0x25200\n"
"s_add_i32 s9, s9, s21\n"
"s_cmp_eq_u32 s11, 0\n"
"s_cselect_b32 s50, -1, 0xffff0000\n"
"global_load_lds_dwordx4 v78, s[38:39] offset:2048\n"
"s_mul_i32 s37, s36, 221184\n"
"s_add_u32 s42, s84, s37\n"
"s_addc_u32 s43, s85, 0\n"
"s_lshr_b32 s11, s2, 1\n"
"s_lshl_b32 s21, s22, 1\n"
"s_and_b32 s11, s11, 28\n"
"s_add_i32 s27, s21, s13\n"
"s_add_i32 s27, s27, s11\n"
"s_lshl_b32 s11, s23, 6\n"
"s_lshl_b32 s21, s30, 4\n"
"s_add_i32 s11, s11, s21\n"
"s_lshl_b32 s21, s2, 15\n"
"s_lshl_b32 s26, s30, 13\n"
"s_add_i32 s21, s26, s21\n"
"s_mul_i32 s26, s5, 0x36000\n"
"s_lshl_b32 s28, s10, 13\n"
"s_add_i32 s29, s28, s26\n"
"s_mul_i32 s26, s30, 40\n"
"s_add_i32 s8, s26, s8\n"
"global_load_lds_dwordx4 v79, s[38:39] offset:3072\n"
"s_add_u32 m0, s88, 12288\n"
"s_mul_i32 s26, s11, 0x500\n"
"s_mul_i32 s28, s18, 0xa0\n"
"s_lshl_b32 s5, s5, 5\n"
"s_add_i32 s26, s26, s28\n"
"s_add_i32 s21, s21, 0x13fc000\n"
"s_add_i32 s29, s29, 0x7d8000\n"
"s_addk_i32 s8, 0x400\n"
"s_add_i32 s5, s5, s10\n"
"s_cmp_gt_u32 s4, 26\n"
"s_cselect_b32 s4, s21, s29\n"
"s_cselect_b32 s8, s8, s5\n"
"s_add_u32 s4, s66, s4\n"
"s_addc_u32 s5, s67, 0\n"
"s_lshl_b32 s21, s27, 5\n"
"s_mul_i32 s11, s7, 40\n"
"s_add_i32 s86, s7, 0xfffffee0\n"
"s_waitcnt vmcnt(15)\n"
"s_barrier\n"
"ds_read_b128 a[0:3], v52 offset:0\n"
"ds_read_b64_tr_b16 a[16:17], v36 offset:0\n"
"ds_read_b64_tr_b16 a[18:19], v36 offset:512\n"
"ds_read_b64_tr_b16 a[20:21], v37 offset:0\n"
"ds_read_b64_tr_b16 a[22:23], v37 offset:512\n"
"ds_read_b64_tr_b16 a[24:25], v38 offset:0\n"
"ds_read_b64_tr_b16 a[26:27], v38 offset:512\n"
"ds_read_b64_tr_b16 a[28:29], v39 offset:0\n"
"ds_read_b64_tr_b16 a[30:31], v39 offset:512\n"
"ds_read_b64_tr_b16 a[32:33], v44 offset:0\n"
"ds_read_b64_tr_b16 a[34:35], v44 offset:512\n"
"ds_read_b64_tr_b16 a[36:37], v45 offset:0\n"
"ds_read_b64_tr_b16 a[38:39], v45 offset:512\n"
"ds_read_b64_tr_b16 a[40:41], v46 offset:0\n"
"ds_read_b64_tr_b16 a[42:43], v46 offset:512\n"
"ds_read_b64_tr_b16 a[44:45], v47 offset:0\n"
"ds_read_b64_tr_b16 a[46:47], v47 offset:512\n"
"s_waitcnt lgkmcnt(14)\n"
"v_mfma_f32_16x16x32_bf16 v[112:115], a[16:19], v[56:59], 0\n"
"global_load_lds_dwordx4 v80, s[38:39] offset:0\n"
"s_waitcnt lgkmcnt(12)\n"
"v_mfma_f32_16x16x32_bf16 v[116:119], a[20:23], v[56:59], 0\n"
"ds_read_b128 a[48:51], v53 offset:0\n"
"s_lshl_b32 s87, s23, 5\n"
"s_lshl_b32 s7, s22, 4\n"
"s_waitcnt lgkmcnt(7)\n"
"v_mfma_f32_16x16x32_bf16 v[120:123], a[32:35], v[56:59], 0\n"
"ds_read_b64_tr_b16 a[64:65], v40 offset:0\n"
"s_add_i32 s21, s21, s17\n"
"s_or_b32 s90, s87, s7\n"
"s_waitcnt lgkmcnt(6)\n"
"v_mfma_f32_16x16x32_bf16 v[124:127], a[36:39], v[56:59], 0\n"
"ds_read_b64_tr_b16 a[66:67], v40 offset:512\n"
"s_lshl_b32 s7, s21, 4\n"
"s_add_i32 s91, s7, 0x2f000\n"
"v_mfma_f32_16x16x32_bf16 v[128:131], a[0:3], a[16:19], 0\n"
"ds_read_b64_tr_b16 a[68:69], v41 offset:0\n"
"s_lshl_b32 s7, s8, 3\n"
"s_lshl_b32 s2, s2, 3\n"
"v_mfma_f32_16x16x32_bf16 v[144:147], a[0:3], a[20:23], 0\n"
"s_waitcnt vmcnt(13)\n"
"ds_read_b128 a[4:7], v52 offset:2048\n"
"ds_read_b64_tr_b16 a[70:71], v41 offset:512\n"
"v_mfma_f32_16x16x32_bf16 v[160:163], a[0:3], a[24:27], 0\n"
"global_load_lds_dwordx4 v81, s[38:39] offset:1024\n"
"v_mfma_f32_16x16x32_bf16 v[176:179], a[0:3], a[28:31], 0\n"
"ds_read_b64_tr_b16 a[72:73], v42 offset:0\n"
"s_mul_i32 s29, s30, 13\n"
"s_add_i32 s79, s11, 0x400\n"
"v_mfma_f32_16x16x32_bf16 v[192:195], a[32:35], a[0:3], 0\n"
"ds_read_b64_tr_b16 a[74:75], v42 offset:512\n"
"s_add_i32 s92, s7, 0xa000\n"
"s_lshl_b32 s93, s6, 7\n"
"v_mfma_f32_16x16x32_bf16 v[208:211], a[36:39], a[0:3], 0\n"
"ds_read_b64_tr_b16 a[76:77], v43 offset:0\n"
"s_add_i32 s94, s2, 0x8000\n"
"s_add_i32 s3, s3, 0x50000\n"
"s_waitcnt lgkmcnt(11)\n"
"v_mfma_f32_16x16x32_bf16 v[224:227], a[40:43], a[0:3], 0\n"
"ds_read_b64_tr_b16 a[78:79], v43 offset:512\n"
"s_or_b32 s95, s22, 52\n"
"s_lshr_b32 s96, s29, 1\n"
"s_waitcnt lgkmcnt(10)\n"
"v_mfma_f32_16x16x32_bf16 v[240:243], a[44:47], a[0:3], 0\n"
"global_load_lds_dwordx4 v82, s[38:39] offset:2048\n"
"s_waitcnt lgkmcnt(5)\n"
"v_mfma_f32_16x16x32_bf16 v[132:135], a[4:7], a[16:19], 0\n"
"ds_read_b64_tr_b16 a[80:81], v48 offset:0\n"
"s_add_u32 s2, s66, s3\n"
"s_addc_u32 s3, s67, 0\n"
"v_mfma_f32_16x16x32_bf16 v[148:151], a[4:7], a[20:23], 0\n"
"s_waitcnt vmcnt(13)\n"
"ds_read_b128 a[8:11], v52 offset:4096\n"
"ds_read_b64_tr_b16 a[82:83], v48 offset:512\n"
"v_mfma_f32_16x16x32_bf16 v[164:167], a[4:7], a[24:27], 0\n"
"ds_read_b64_tr_b16 a[84:85], v49 offset:0\n"
"s_add_u32 s6, s66, s9\n"
"s_addc_u32 s7, s67, 0\n"
"v_mfma_f32_16x16x32_bf16 v[180:183], a[4:7], a[28:31], 0\n"
"ds_read_b64_tr_b16 a[86:87], v49 offset:512\n"
"s_add_i32 s9, s9, s54\n"
"v_mfma_f32_16x16x32_bf16 v[196:199], a[32:35], a[4:7], 0\n"
"global_load_lds_dwordx4 v83, s[38:39] offset:3072\n"
"s_lshl_b32 s37, s36, 7\n"
"s_add_u32 s38, s80, s37\n"
"s_addc_u32 s39, s81, 0\n"
"s_add_u32 m0, s89, 32768\n"
"v_mfma_f32_16x16x32_bf16 v[212:215], a[36:39], a[4:7], 0\n"
"ds_read_b64_tr_b16 a[88:89], v50 offset:0\n"
"s_add_u32 s8, s66, s9\n"
"s_addc_u32 s9, s67, 0\n"
"v_mfma_f32_16x16x32_bf16 v[228:231], a[40:43], a[4:7], 0\n"
"ds_read_b64_tr_b16 a[90:91], v50 offset:512\n"
"s_min_u32 s10, s10, 26\n"
"s_lshl_b32 s10, s10, 7\n"
"v_mfma_f32_16x16x32_bf16 v[244:247], a[44:47], a[4:7], 0\n"
"ds_read_b64_tr_b16 a[92:93], v51 offset:0\n"
"v_bfe_u32 v3, v0, 4, 2\n"
"s_waitcnt lgkmcnt(6)\n"
"v_mfma_f32_16x16x32_bf16 v[136:139], a[8:11], a[16:19], 0\n"
"ds_read_b64_tr_b16 a[94:95], v51 offset:512\n"
"v_mfma_f32_16x16x32_bf16 v[152:155], a[8:11], a[20:23], 0\n"
"s_waitcnt vmcnt(12)\n"
"ds_read_b128 a[12:15], v52 offset:6144\n"
"ds_read_b128 a[52:55], v53 offset:2048\n"
"v_mfma_f32_16x16x32_bf16 v[168:171], a[8:11], a[24:27], 0\n"
"global_load_lds_dwordx4 v84, s[40:41] offset:0\n"
"v_mfma_f32_16x16x32_bf16 v[184:187], a[8:11], a[28:31], 0\n"
"ds_read_b128 a[56:59], v53 offset:4096\n"
"v_mfma_f32_16x16x32_bf16 v[200:203], a[32:35], a[8:11], 0\n"
"ds_read_b128 a[60:63], v53 offset:6144\n"
"v_mfma_f32_16x16x32_bf16 v[216:219], a[36:39], a[8:11], 0\n"
"s_add_u32 s10, s70, s10\n"
"v_lshl_or_b32 v4, s58, 2, v3\n"
"s_addc_u32 s11, s71, 0\n"
"v_mfma_f32_16x16x32_bf16 v[232:235], a[40:43], a[8:11], 0\n"
"v_lshrrev_b32_e64 v4, v4, s12\n"
"v_mfma_f32_16x16x32_bf16 v[248:251], a[44:47], a[8:11], 0\n"
"global_load_lds_dwordx4 v85, s[40:41] offset:1024\n"
"s_add_u32 m0, s89, 34816\n"
"s_waitcnt lgkmcnt(3)\n"
"v_mfma_f32_16x16x32_bf16 v[140:143], a[12:15], a[16:19], 0\n"
"s_cmp_gt_u32 s30, 1\n"
"v_lshlrev_b32_e32 v4, 9, v4\n"
"s_cselect_b32 s54, s1, s0\n"
"v_mfma_f32_16x16x32_bf16 v[156:159], a[12:15], a[20:23], 0\n"
"v_and_b32_e32 v4, 0x200, v4\n"
"v_mov_b32_e32 v5, 0x400\n"
"v_mfma_f32_16x16x32_bf16 v[172:175], a[12:15], a[24:27], 0\n"
"v_cmp_gt_u32_e64 s[0:1], s59, v3\n"
"v_lshlrev_b32_e32 v10, 2, v3\n"
"v_mfma_f32_16x16x32_bf16 v[188:191], a[12:15], a[28:31], 0\n"
"v_lshl_or_b32 v6, s13, 2, v3\n"
"v_cndmask_b32_e64 v4, v5, v4, s[0:1]\n"
"v_mfma_f32_16x16x32_bf16 v[204:207], a[32:35], a[12:15], 0\n"
"global_load_lds_dwordx4 v86, s[42:43] offset:0\n"
"v_mfma_f32_16x16x32_bf16 v[220:223], a[36:39], a[12:15], 0\n"
"v_cmp_gt_u32_e64 s[0:1], s56, v3\n"
"v_lshrrev_b32_e64 v11, v10, s54\n"
"v_mfma_f32_16x16x32_bf16 v[236:239], a[40:43], a[12:15], 0\n"
"v_lshrrev_b32_e64 v6, v6, s12\n"
"v_cndmask_b32_e64 v8, 4, 0, s[0:1]\n"
"v_mfma_f32_16x16x32_bf16 v[252:255], a[44:47], a[12:15], 0\n"
"v_and_or_b32 v11, v11, 15, s15\n"
"v_lshlrev_b32_e32 v6, 9, v6\n"
"s_waitcnt vmcnt(11) lgkmcnt(0)\n"
"s_barrier\n"
"v_mfma_f32_16x16x32_bf16 v[128:131], a[48:51], a[64:67], v[128:131]\n"
"s_waitcnt vmcnt(9)\n"
"ds_read_b128 a[0:3], v52 offset:8192\n"
"v_or_b32_e32 v8, v8, v3\n"
"v_mfma_f32_16x16x32_bf16 v[144:147], a[48:51], a[68:71], v[144:147]\n"
"global_load_lds_dwordx4 v87, s[42:43] offset:1024\n"
"s_add_u32 m0, s88, 16384\n"
"v_mfma_f32_16x16x32_bf16 v[160:163], a[48:51], a[72:75], v[160:163]\n"
"ds_read_b64_tr_b16 a[16:17], v36 offset:16384\n"
"v_mul_lo_u16_e32 v11, 0xcd, v11\n"
"v_mfma_f32_16x16x32_bf16 v[176:179], a[48:51], a[76:79], v[176:179]\n"
"ds_read_b64_tr_b16 a[18:19], v36 offset:16896\n"
"v_and_b32_e32 v6, 0x200, v6\n"
"v_mfma_f32_16x16x32_bf16 v[192:195], a[80:83], a[48:51], v[192:195]\n"
"ds_read_b64_tr_b16 a[20:21], v37 offset:16384\n"
"v_lshlrev_b32_e32 v8, 2, v8\n"
"v_mfma_f32_16x16x32_bf16 v[208:211], a[84:87], a[48:51], v[208:211]\n"
"ds_read_b64_tr_b16 a[22:23], v37 offset:16896\n"
"v_lshrrev_b16_e32 v11, 10, v11\n"
"v_mfma_f32_16x16x32_bf16 v[224:227], a[88:91], a[48:51], v[224:227]\n"
"global_load_lds_dwordx4 v76, s[38:39] offset:0\n"
"s_add_u32 s36, s36, 1\n"
"s_cmp_eq_u32 s36, 10\n"
"s_cselect_b32 s36, 0, s36\n"
"v_mfma_f32_16x16x32_bf16 v[240:243], a[92:95], a[48:51], v[240:243]\n"
"ds_read_b64_tr_b16 a[24:25], v38 offset:16384\n"
"v_and_b32_e32 v13, 15, v0\n"
"v_mfma_f32_16x16x32_bf16 v[132:135], a[52:55], a[64:67], v[132:135]\n"
"ds_read_b64_tr_b16 a[26:27], v38 offset:16896\n"
"v_cndmask_b32_e64 v7, v5, v6, s[0:1]\n"
"v_mfma_f32_16x16x32_bf16 v[148:151], a[52:55], a[68:71], v[148:151]\n"
"ds_read_b64_tr_b16 a[28:29], v39 offset:16384\n"
"v_lshrrev_b32_e64 v8, v8, s54\n"
"v_mfma_f32_16x16x32_bf16 v[164:167], a[52:55], a[72:75], v[164:167]\n"
"ds_read_b64_tr_b16 a[30:31], v39 offset:16896\n"
"v_mul_u32_u24_e32 v11, s55, v11\n"
"v_mfma_f32_16x16x32_bf16 v[180:183], a[52:55], a[76:79], v[180:183]\n"
"global_load_lds_dwordx4 v77, s[38:39] offset:1024\n"
"s_mul_i32 s37, s36, 245760\n"
"s_add_u32 s40, s82, s37\n"
"s_addc_u32 s41, s83, 0\n"
"v_mfma_f32_16x16x32_bf16 v[196:199], a[80:83], a[52:55], v[196:199]\n"
"ds_read_b64_tr_b16 a[32:33], v44 offset:16384\n"
"v_lshlrev_b32_e32 v14, 4, v13\n"
"v_mfma_f32_16x16x32_bf16 v[212:215], a[84:87], a[52:55], v[212:215]\n"
"ds_read_b64_tr_b16 a[34:35], v44 offset:16896\n"
"v_or3_b32 v7, v11, v7, v14\n"
"v_mfma_f32_16x16x32_bf16 v[228:231], a[88:91], a[52:55], v[228:231]\n"
"ds_read_b64_tr_b16 a[36:37], v45 offset:16384\n"
"v_lshl_or_b32 v11, s16, 6, v13\n"
"v_mfma_f32_16x16x32_bf16 v[244:247], a[92:95], a[52:55], v[244:247]\n"
"ds_read_b64_tr_b16 a[38:39], v45 offset:16896\n"
"v_and_or_b32 v8, v8, 15, s15\n"
"v_mfma_f32_16x16x32_bf16 v[136:139], a[56:59], a[64:67], v[136:139]\n"
"global_load_lds_dwordx4 v78, s[38:39] offset:2048\n"
"s_mul_i32 s37, s36, 221184\n"
"s_add_u32 s42, s84, s37\n"
"s_addc_u32 s43, s85, 0\n"
"v_mfma_f32_16x16x32_bf16 v[152:155], a[56:59], a[68:71], v[152:155]\n"
"ds_read_b64_tr_b16 a[40:41], v46 offset:16384\n"
"v_lshl_or_b32 v12, s63, 2, v3\n"
"v_mfma_f32_16x16x32_bf16 v[168:171], a[56:59], a[72:75], v[168:171]\n"
"ds_read_b64_tr_b16 a[42:43], v46 offset:16896\n"
"v_or_b32_e32 v15, s57, v11\n"
"v_mfma_f32_16x16x32_bf16 v[184:187], a[56:59], a[76:79], v[184:187]\n"
"ds_read_b64_tr_b16 a[44:45], v47 offset:16384\n"
"v_mul_lo_u16_e32 v8, 0xcd, v8\n"
"v_mfma_f32_16x16x32_bf16 v[200:203], a[80:83], a[56:59], v[200:203]\n"
"ds_read_b64_tr_b16 a[46:47], v47 offset:16896\n"
"v_or_b32_e32 v6, 0x100, v6\n"
"v_mfma_f32_16x16x32_bf16 v[216:219], a[84:87], a[56:59], v[216:219]\n"
"global_load_lds_dwordx4 v79, s[38:39] offset:3072\n"
"s_add_u32 m0, s88, 20480\n"
"v_mfma_f32_16x16x32_bf16 v[232:235], a[88:91], a[56:59], v[232:235]\n"
"v_lshrrev_b32_e64 v12, v12, s12\n"
"v_mul_u32_u24_e32 v16, 0xccd, v15\n"
"s_mov_b32 s12, 0x28000\n"
"v_mfma_f32_16x16x32_bf16 v[248:251], a[92:95], a[56:59], v[248:251]\n"
"v_mov_b32_e32 v18, 20\n"
"v_lshrrev_b16_e32 v8, 10, v8\n"
"v_mfma_f32_16x16x32_bf16 v[140:143], a[60:63], a[64:67], v[140:143]\n"
"v_cndmask_b32_e64 v6, v5, v6, s[0:1]\n"
"v_mov_b32_e32 v9, 0x500\n"
"s_movk_i32 s0, 0x140\n"
"v_mfma_f32_16x16x32_bf16 v[156:159], a[60:63], a[68:71], v[156:159]\n"
"s_waitcnt vmcnt(12)\n"
"ds_read_b128 a[4:7], v52 offset:10240\n"
"v_mul_u32_u24_sdwa v17, v16, s12 dst_sel:DWORD dst_unused:UNUSED_PAD src0_sel:WORD_1 src1_sel:DWORD\n"
"v_mfma_f32_16x16x32_bf16 v[172:175], a[60:63], a[72:75], v[172:175]\n"
"global_load_lds_dwordx4 v80, s[38:39] offset:0\n"
"v_mfma_f32_16x16x32_bf16 v[188:191], a[60:63], a[76:79], v[188:191]\n"
"s_waitcnt vmcnt(11)\n"
"ds_read_b128 a[8:11], v52 offset:12288\n"
"v_mul_lo_u16_sdwa v16, v16, v18 dst_sel:DWORD dst_unused:UNUSED_PAD src0_sel:WORD_1 src1_sel:DWORD\n"
"v_mfma_f32_16x16x32_bf16 v[204:207], a[80:83], a[60:63], v[204:207]\n"
"s_waitcnt vmcnt(9)\n"
"ds_read_b128 a[12:15], v52 offset:14336\n"
"v_mul_u32_u24_e32 v8, s55, v8\n"
"v_mfma_f32_16x16x32_bf16 v[220:223], a[84:87], a[60:63], v[220:223]\n"
"v_mad_u32_u24 v9, v3, s0, v9\n"
"v_sub_u16_e32 v16, v15, v16\n"
"v_mfma_f32_16x16x32_bf16 v[236:239], a[88:91], a[60:63], v[236:239]\n"
"v_or3_b32 v6, v8, v6, v14\n"
"v_or_b32_e32 v8, 16, v15\n"
"v_mfma_f32_16x16x32_bf16 v[252:255], a[92:95], a[60:63], v[252:255]\n"
"global_load_lds_dwordx4 v81, s[38:39] offset:1024\n"
"s_waitcnt lgkmcnt(0)\n"
"v_mfma_f32_16x16x32_bf16 v[112:115], a[16:19], v[56:59], v[112:115]\n"
"ds_read_b128 a[48:51], v53 offset:8192\n"
"v_lshlrev_b16_e32 v16, 4, v16\n"
"v_mfma_f32_16x16x32_bf16 v[116:119], a[20:23], v[56:59], v[116:119]\n"
"ds_read_b64_tr_b16 a[64:65], v40 offset:16384\n"
"v_or_b32_e32 v17, v9, v17\n"
"v_mfma_f32_16x16x32_bf16 v[120:123], a[32:35], v[56:59], v[120:123]\n"
"ds_read_b64_tr_b16 a[66:67], v40 offset:16896\n"
"v_mul_u32_u24_e32 v15, 0xccd, v8\n"
"v_mfma_f32_16x16x32_bf16 v[124:127], a[36:39], v[56:59], v[124:127]\n"
"ds_read_b64_tr_b16 a[68:69], v41 offset:16384\n"
"v_add_u32_e32 v16, v17, v16\n"
"v_mfma_f32_16x16x32_bf16 v[128:131], a[0:3], a[16:19], v[128:131]\n"
"global_load_lds_dwordx4 v82, s[38:39] offset:2048\n"
"v_mfma_f32_16x16x32_bf16 v[144:147], a[0:3], a[20:23], v[144:147]\n"
"ds_read_b64_tr_b16 a[70:71], v41 offset:16896\n"
"v_mul_lo_u16_sdwa v17, v15, v18 dst_sel:DWORD dst_unused:UNUSED_PAD src0_sel:WORD_1 src1_sel:DWORD\n"
"v_mfma_f32_16x16x32_bf16 v[160:163], a[0:3], a[24:27], v[160:163]\n"
"ds_read_b64_tr_b16 a[72:73], v42 offset:16384\n"
"v_sub_u16_e32 v8, v8, v17\n"
"v_mfma_f32_16x16x32_bf16 v[176:179], a[0:3], a[28:31], v[176:179]\n"
"ds_read_b64_tr_b16 a[74:75], v42 offset:16896\n"
"v_mul_u32_u24_sdwa v15, v15, s12 dst_sel:DWORD dst_unused:UNUSED_PAD src0_sel:WORD_1 src1_sel:DWORD\n"
"v_mfma_f32_16x16x32_bf16 v[192:195], a[32:35], a[0:3], v[192:195]\n"
"ds_read_b64_tr_b16 a[76:77], v43 offset:16384\n"
"v_lshlrev_b16_e32 v8, 4, v8\n"
"v_mfma_f32_16x16x32_bf16 v[208:211], a[36:39], a[0:3], v[208:211]\n"
"global_load_lds_dwordx4 v83, s[38:39] offset:3072\n"
"s_lshl_b32 s37, s36, 7\n"
"s_add_u32 s38, s80, s37\n"
"s_addc_u32 s39, s81, 0\n"
"s_add_u32 m0, s89, 0\n"
"v_mfma_f32_16x16x32_bf16 v[224:227], a[40:43], a[0:3], v[224:227]\n"
"ds_read_b64_tr_b16 a[78:79], v43 offset:16896\n"
"v_or_b32_e32 v15, v9, v15\n"
"v_mfma_f32_16x16x32_bf16 v[240:243], a[44:47], a[0:3], v[240:243]\n"
"ds_read_b64_tr_b16 a[80:81], v48 offset:16384\n"
"v_lshrrev_b32_e64 v10, v10, s62\n"
"v_mfma_f32_16x16x32_bf16 v[132:135], a[4:7], a[16:19], v[132:135]\n"
"ds_read_b64_tr_b16 a[82:83], v48 offset:16896\n"
"v_add_u32_e32 v8, v15, v8\n"
"v_mfma_f32_16x16x32_bf16 v[148:151], a[4:7], a[20:23], v[148:151]\n"
"ds_read_b64_tr_b16 a[84:85], v49 offset:16384\n"
"v_cndmask_b32_e32 v61, v8, v6, vcc\n"
"v_mfma_f32_16x16x32_bf16 v[164:167], a[4:7], a[24:27], v[164:167]\n"
"global_load_lds_dwordx4 v84, s[40:41] offset:0\n"
"v_mfma_f32_16x16x32_bf16 v[180:183], a[4:7], a[28:31], v[180:183]\n"
"ds_read_b64_tr_b16 a[86:87], v49 offset:16896\n"
"v_and_or_b32 v6, v10, 15, s15\n"
"v_mfma_f32_16x16x32_bf16 v[196:199], a[32:35], a[4:7], v[196:199]\n"
"ds_read_b64_tr_b16 a[88:89], v50 offset:16384\n"
"v_mul_lo_u16_e32 v6, 0xcd, v6\n"
"v_mfma_f32_16x16x32_bf16 v[212:215], a[36:39], a[4:7], v[212:215]\n"
"ds_read_b64_tr_b16 a[90:91], v50 offset:16896\n"
"v_lshrrev_b16_e32 v6, 10, v6\n"
"v_mfma_f32_16x16x32_bf16 v[228:231], a[40:43], a[4:7], v[228:231]\n"
"ds_read_b64_tr_b16 a[92:93], v51 offset:16384\n"
"v_mul_u32_u24_e32 v6, s55, v6\n"
"v_mfma_f32_16x16x32_bf16 v[244:247], a[44:47], a[4:7], v[244:247]\n"
"global_load_lds_dwordx4 v85, s[40:41] offset:1024\n"
"s_add_u32 m0, s89, 2048\n"
"v_mfma_f32_16x16x32_bf16 v[136:139], a[8:11], a[16:19], v[136:139]\n"
"ds_read_b64_tr_b16 a[94:95], v51 offset:16896\n"
"v_or3_b32 v4, v6, v4, v14\n"
"v_mfma_f32_16x16x32_bf16 v[152:155], a[8:11], a[20:23], v[152:155]\n"
"ds_read_b128 a[52:55], v53 offset:10240\n"
"v_bitop3_b32 v6, s57, 32, v11 bitop3:0x36\n"
"v_mfma_f32_16x16x32_bf16 v[168:171], a[8:11], a[24:27], v[168:171]\n"
"ds_read_b128 a[56:59], v53 offset:12288\n"
"v_cndmask_b32_e32 v60, v16, v7, vcc\n"
"v_mfma_f32_16x16x32_bf16 v[184:187], a[8:11], a[28:31], v[184:187]\n"
"ds_read_b128 a[60:63], v53 offset:14336\n"
"v_mul_u32_u24_e32 v7, 0xccd, v6\n"
"v_mfma_f32_16x16x32_bf16 v[200:203], a[32:35], a[8:11], v[200:203]\n"
"global_load_lds_dwordx4 v86, s[42:43] offset:0\n"
"v_mfma_f32_16x16x32_bf16 v[216:219], a[36:39], a[8:11], v[216:219]\n"
"v_mul_lo_u16_sdwa v8, v7, v18 dst_sel:DWORD dst_unused:UNUSED_PAD src0_sel:WORD_1 src1_sel:DWORD\n"
"v_sub_u16_e32 v6, v6, v8\n"
"v_mfma_f32_16x16x32_bf16 v[232:235], a[40:43], a[8:11], v[232:235]\n"
"v_mul_u32_u24_sdwa v7, v7, s12 dst_sel:DWORD dst_unused:UNUSED_PAD src0_sel:WORD_1 src1_sel:DWORD\n"
"v_lshlrev_b16_e32 v6, 4, v6\n"
"v_mfma_f32_16x16x32_bf16 v[248:251], a[44:47], a[8:11], v[248:251]\n"
"v_or_b32_e32 v7, v9, v7\n"
"v_add_u32_e32 v6, v7, v6\n"
"v_mfma_f32_16x16x32_bf16 v[140:143], a[12:15], a[16:19], v[140:143]\n"
"v_cndmask_b32_e32 v62, v6, v4, vcc\n"
"v_lshlrev_b32_e32 v4, 9, v12\n"
"v_mfma_f32_16x16x32_bf16 v[156:159], a[12:15], a[20:23], v[156:159]\n"
"global_load_lds_dwordx4 v87, s[42:43] offset:1024\n"
"s_add_u32 m0, s88, 0\n"
"v_mfma_f32_16x16x32_bf16 v[172:175], a[12:15], a[24:27], v[172:175]\n"
"v_and_b32_e32 v4, 0x200, v4\n"
"v_or_b32_e32 v4, 0x100, v4\n"
"v_mfma_f32_16x16x32_bf16 v[188:191], a[12:15], a[28:31], v[188:191]\n"
"v_cmp_gt_u32_e64 s[0:1], s64, v3\n"
"v_mov_b32_e32 v15, s95\n"
"v_mfma_f32_16x16x32_bf16 v[204:207], a[32:35], a[12:15], v[204:207]\n"
"s_cmp_gt_u32 s30, 1\n"
"s_cselect_b32 s58, 0, s50\n"
"v_cndmask_b32_e64 v4, v5, v4, s[0:1]\n"
"v_mfma_f32_16x16x32_bf16 v[220:223], a[36:39], a[12:15], v[220:223]\n"
"v_cndmask_b32_e64 v5, 4, 0, s[0:1]\n"
"v_or_b32_e32 v5, v5, v3\n"
"v_mfma_f32_16x16x32_bf16 v[236:239], a[40:43], a[12:15], v[236:239]\n"
"global_load_lds_dwordx4 v76, s[38:39] offset:0\n"
"s_add_u32 s36, s36, 1\n"
"s_cmp_eq_u32 s36, 10\n"
"s_cselect_b32 s36, 0, s36\n"
"v_mfma_f32_16x16x32_bf16 v[252:255], a[44:47], a[12:15], v[252:255]\n"
"v_lshlrev_b32_e32 v5, 2, v5\n"
"v_lshrrev_b32_e64 v5, v5, s65\n"
"s_waitcnt vmcnt(13) lgkmcnt(0)\n"
"s_barrier\n"
"v_mfma_f32_16x16x32_bf16 v[128:131], a[48:51], a[64:67], v[128:131]\n"
"s_waitcnt vmcnt(11)\n"
"ds_read_b128 a[0:3], v52 offset:16384\n"
"v_and_or_b32 v5, v5, 15, s15\n"
"v_mfma_f32_16x16x32_bf16 v[144:147], a[48:51], a[68:71], v[144:147]\n"
"ds_read_b64_tr_b16 a[16:17], v36 offset:32768\n"
"v_mul_lo_u16_e32 v5, 0xcd, v5\n"
"v_mfma_f32_16x16x32_bf16 v[160:163], a[48:51], a[72:75], v[160:163]\n"
"ds_read_b64_tr_b16 a[18:19], v36 offset:33280\n"
"v_lshrrev_b16_e32 v5, 10, v5\n"
"v_mfma_f32_16x16x32_bf16 v[176:179], a[48:51], a[76:79], v[176:179]\n"
"global_load_lds_dwordx4 v77, s[38:39] offset:1024\n"
"s_mul_i32 s37, s36, 245760\n"
"s_add_u32 s40, s82, s37\n"
"s_addc_u32 s41, s83, 0\n"
"v_mfma_f32_16x16x32_bf16 v[192:195], a[80:83], a[48:51], v[192:195]\n"
"ds_read_b64_tr_b16 a[20:21], v37 offset:32768\n"
"v_mul_u32_u24_e32 v5, s55, v5\n"
"v_mfma_f32_16x16x32_bf16 v[208:211], a[84:87], a[48:51], v[208:211]\n"
"ds_read_b64_tr_b16 a[22:23], v37 offset:33280\n"
"v_or3_b32 v4, v5, v4, v14\n"
"v_mfma_f32_16x16x32_bf16 v[224:227], a[88:91], a[48:51], v[224:227]\n"
"ds_read_b64_tr_b16 a[24:25], v38 offset:32768\n"
"v_bitop3_b32 v5, s57, 48, v11 bitop3:0x36\n"
"v_mfma_f32_16x16x32_bf16 v[240:243], a[92:95], a[48:51], v[240:243]\n"
"ds_read_b64_tr_b16 a[26:27], v38 offset:33280\n"
"v_mul_u32_u24_e32 v6, 0xccd, v5\n"
"v_mfma_f32_16x16x32_bf16 v[132:135], a[52:55], a[64:67], v[132:135]\n"
"global_load_lds_dwordx4 v78, s[38:39] offset:2048\n"
"s_mul_i32 s37, s36, 221184\n"
"s_add_u32 s42, s84, s37\n"
"s_addc_u32 s43, s85, 0\n"
"v_mfma_f32_16x16x32_bf16 v[148:151], a[52:55], a[68:71], v[148:151]\n"
"ds_read_b64_tr_b16 a[28:29], v39 offset:32768\n"
"v_mul_lo_u16_sdwa v7, v6, v18 dst_sel:DWORD dst_unused:UNUSED_PAD src0_sel:WORD_1 src1_sel:DWORD\n"
"v_mfma_f32_16x16x32_bf16 v[164:167], a[52:55], a[72:75], v[164:167]\n"
"ds_read_b64_tr_b16 a[30:31], v39 offset:33280\n"
"v_sub_u16_e32 v5, v5, v7\n"
"v_mfma_f32_16x16x32_bf16 v[180:183], a[52:55], a[76:79], v[180:183]\n"
"ds_read_b64_tr_b16 a[32:33], v44 offset:32768\n"
"v_mul_u32_u24_sdwa v6, v6, s12 dst_sel:DWORD dst_unused:UNUSED_PAD src0_sel:WORD_1 src1_sel:DWORD\n"
"v_mfma_f32_16x16x32_bf16 v[196:199], a[80:83], a[52:55], v[196:199]\n"
"ds_read_b64_tr_b16 a[34:35], v44 offset:33280\n"
"v_or_b32_e32 v6, v9, v6\n"
"v_mfma_f32_16x16x32_bf16 v[212:215], a[84:87], a[52:55], v[212:215]\n"
"global_load_lds_dwordx4 v79, s[38:39] offset:3072\n"
"s_add_u32 m0, s88, 4096\n"
"v_mfma_f32_16x16x32_bf16 v[228:231], a[88:91], a[52:55], v[228:231]\n"
"ds_read_b64_tr_b16 a[36:37], v45 offset:32768\n"
"v_lshlrev_b16_e32 v5, 4, v5\n"
"v_mfma_f32_16x16x32_bf16 v[244:247], a[92:95], a[52:55], v[244:247]\n"
"ds_read_b64_tr_b16 a[38:39], v45 offset:33280\n"
"v_add_u32_e32 v5, v6, v5\n"
"v_mfma_f32_16x16x32_bf16 v[136:139], a[56:59], a[64:67], v[136:139]\n"
"ds_read_b64_tr_b16 a[40:41], v46 offset:32768\n"
"v_cndmask_b32_e32 v63, v5, v4, vcc\n"
"v_mfma_f32_16x16x32_bf16 v[152:155], a[56:59], a[68:71], v[152:155]\n"
"ds_read_b64_tr_b16 a[42:43], v46 offset:33280\n"
"v_lshrrev_b32_e32 v4, 1, v0\n"
"v_mfma_f32_16x16x32_bf16 v[168:171], a[56:59], a[72:75], v[168:171]\n"
"global_load_lds_dwordx4 v80, s[38:39] offset:0\n"
"v_mfma_f32_16x16x32_bf16 v[184:187], a[56:59], a[76:79], v[184:187]\n"
"ds_read_b64_tr_b16 a[44:45], v47 offset:32768\n"
"v_and_b32_e32 v5, 3, v0\n"
"v_mfma_f32_16x16x32_bf16 v[200:203], a[80:83], a[56:59], v[200:203]\n"
"ds_read_b64_tr_b16 a[46:47], v47 offset:33280\n"
"v_and_or_b32 v4, v4, 12, v5\n"
"v_mfma_f32_16x16x32_bf16 v[216:219], a[84:87], a[56:59], v[216:219]\n"
"v_bfe_u32 v5, v0, 5, 1\n"
"v_bfe_u32 v6, v0, 2, 1\n"
"v_mfma_f32_16x16x32_bf16 v[232:235], a[88:91], a[56:59], v[232:235]\n"
"v_lshlrev_b32_e32 v4, 4, v4\n"
"v_lshlrev_b32_e32 v7, 9, v5\n"
"v_mfma_f32_16x16x32_bf16 v[248:251], a[92:95], a[56:59], v[248:251]\n"
"global_load_lds_dwordx4 v81, s[38:39] offset:1024\n"
"v_mfma_f32_16x16x32_bf16 v[140:143], a[60:63], a[64:67], v[140:143]\n"
"v_lshlrev_b32_e32 v8, 8, v6\n"
"v_or3_b32 v7, v7, v8, v4\n"
"v_mfma_f32_16x16x32_bf16 v[156:159], a[60:63], a[68:71], v[156:159]\n"
"s_waitcnt vmcnt(14)\n"
"ds_read_b128 a[4:7], v52 offset:18432\n"
"v_lshlrev_b32_e32 v6, 3, v6\n"
"v_mfma_f32_16x16x32_bf16 v[172:175], a[60:63], a[72:75], v[172:175]\n"
"s_waitcnt vmcnt(12)\n"
"ds_read_b128 a[8:11], v52 offset:20480\n"
"v_mul_u32_u24_e32 v8, 0xa00, v5\n"
"v_mfma_f32_16x16x32_bf16 v[188:191], a[60:63], a[76:79], v[188:191]\n"
"s_waitcnt vmcnt(10)\n"
"ds_read_b128 a[12:15], v52 offset:22528\n"
"v_or3_b32 v4, v4, v6, v8\n"
"v_mfma_f32_16x16x32_bf16 v[204:207], a[80:83], a[60:63], v[204:207]\n"
"global_load_lds_dwordx4 v82, s[38:39] offset:2048\n"
"v_mfma_f32_16x16x32_bf16 v[220:223], a[84:87], a[60:63], v[220:223]\n"
"v_and_b32_e32 v6, 63, v0\n"
"v_lshlrev_b32_e32 v9, 4, v6\n"
"v_mfma_f32_16x16x32_bf16 v[236:239], a[88:91], a[60:63], v[236:239]\n"
"v_and_b32_e32 v10, 0x100, v9\n"
"v_bfe_u32 v11, v0, 4, 1\n"
"v_mfma_f32_16x16x32_bf16 v[252:255], a[92:95], a[60:63], v[252:255]\n"
"v_or3_b32 v8, v14, v10, v8\n"
"v_mul_u32_u24_e32 v10, 0x500, v5\n"
"s_waitcnt lgkmcnt(0)\n"
"v_mfma_f32_16x16x32_bf16 v[112:115], a[16:19], v[56:59], v[112:115]\n"
"ds_read_b128 a[48:51], v53 offset:16384\n"
"v_lshlrev_b32_e32 v12, 3, v11\n"
"v_mfma_f32_16x16x32_bf16 v[116:119], a[20:23], v[56:59], v[116:119]\n"
"global_load_lds_dwordx4 v83, s[38:39] offset:3072\n"
"s_lshl_b32 s37, s36, 7\n"
"s_add_u32 s38, s80, s37\n"
"s_addc_u32 s39, s81, 0\n"
"s_add_u32 m0, s89, 16384\n"
"v_mfma_f32_16x16x32_bf16 v[120:123], a[32:35], v[56:59], v[120:123]\n"
"ds_read_b64_tr_b16 a[64:65], v40 offset:32768\n"
"v_or3_b32 v10, v12, v10, v14\n"
"v_mfma_f32_16x16x32_bf16 v[124:127], a[36:39], v[56:59], v[124:127]\n"
"ds_read_b64_tr_b16 a[66:67], v40 offset:33280\n"
"v_lshlrev_b32_e32 v11, 9, v11\n"
"v_mfma_f32_16x16x32_bf16 v[128:131], a[0:3], a[16:19], v[128:131]\n"
"ds_read_b64_tr_b16 a[68:69], v41 offset:32768\n"
"v_lshlrev_b32_e32 v12, 8, v5\n"
"v_mfma_f32_16x16x32_bf16 v[144:147], a[0:3], a[20:23], v[144:147]\n"
"ds_read_b64_tr_b16 a[70:71], v41 offset:33280\n"
"v_or3_b32 v11, v11, v12, v14\n"
"v_mfma_f32_16x16x32_bf16 v[160:163], a[0:3], a[24:27], v[160:163]\n"
"global_load_lds_dwordx4 v84, s[40:41] offset:0\n"
"v_mfma_f32_16x16x32_bf16 v[176:179], a[0:3], a[28:31], v[176:179]\n"
"ds_read_b64_tr_b16 a[72:73], v42 offset:32768\n"
"v_lshlrev_b32_e32 v12, 2, v13\n"
"v_mfma_f32_16x16x32_bf16 v[192:195], a[32:35], a[0:3], v[192:195]\n"
"ds_read_b64_tr_b16 a[74:75], v42 offset:33280\n"
"v_mov_b32_e32 v14, 0x25200\n"
"v_mfma_f32_16x16x32_bf16 v[208:211], a[36:39], a[0:3], v[208:211]\n"
"ds_read_b64_tr_b16 a[76:77], v43 offset:32768\n"
"v_bitop3_b32 v70, s78, v12, v14 bitop3:0xde\n"
"v_mfma_f32_16x16x32_bf16 v[224:227], a[40:43], a[0:3], v[224:227]\n"
"ds_read_b64_tr_b16 a[78:79], v43 offset:33280\n"
"v_or_b32_e32 v64, s51, v12\n"
"v_mfma_f32_16x16x32_bf16 v[240:243], a[44:47], a[0:3], v[240:243]\n"
"global_load_lds_dwordx4 v85, s[40:41] offset:1024\n"
"s_add_u32 m0, s89, 18432\n"
"v_mfma_f32_16x16x32_bf16 v[132:135], a[4:7], a[16:19], v[132:135]\n"
"ds_read_b64_tr_b16 a[80:81], v48 offset:32768\n"
"v_or_b32_e32 v69, s35, v12\n"
"v_mfma_f32_16x16x32_bf16 v[148:151], a[4:7], a[20:23], v[148:151]\n"
"ds_read_b64_tr_b16 a[82:83], v48 offset:33280\n"
"v_and_b32_e32 v12, 31, v0\n"
"s_mul_i32 s0, s30, 5\n"
"v_mfma_f32_16x16x32_bf16 v[164:167], a[4:7], a[24:27], v[164:167]\n"
"ds_read_b64_tr_b16 a[84:85], v49 offset:32768\n"
"v_min_u32_e32 v14, 26, v12\n"
"v_mfma_f32_16x16x32_bf16 v[180:183], a[4:7], a[28:31], v[180:183]\n"
"ds_read_b64_tr_b16 a[86:87], v49 offset:33280\n"
"v_add3_u32 v14, s0, 32, v14\n"
"v_mfma_f32_16x16x32_bf16 v[196:199], a[32:35], a[4:7], v[196:199]\n"
"global_load_lds_dwordx4 v86, s[42:43] offset:0\n"
"v_mfma_f32_16x16x32_bf16 v[212:215], a[36:39], a[4:7], v[212:215]\n"
"ds_read_b64_tr_b16 a[88:89], v50 offset:32768\n"
"v_cmp_eq_u32_e32 vcc, 21, v12\n"
"v_mfma_f32_16x16x32_bf16 v[228:231], a[40:43], a[4:7], v[228:231]\n"
"ds_read_b64_tr_b16 a[90:91], v50 offset:33280\n"
"v_cmp_gt_u32_e64 s[0:1], 21, v12\n"
"v_mfma_f32_16x16x32_bf16 v[244:247], a[44:47], a[4:7], v[244:247]\n"
"ds_read_b64_tr_b16 a[92:93], v51 offset:32768\n"
"v_mul_u32_u24_e32 v5, 20, v5\n"
"v_mfma_f32_16x16x32_bf16 v[136:139], a[8:11], a[16:19], v[136:139]\n"
"ds_read_b64_tr_b16 a[94:95], v51 offset:33280\n"
"v_cndmask_b32_e32 v14, v14, v15, vcc\n"
"v_mfma_f32_16x16x32_bf16 v[152:155], a[8:11], a[20:23], v[152:155]\n"
"global_load_lds_dwordx4 v87, s[42:43] offset:1024\n"
"s_add_u32 m0, s88, 8192\n"
"v_mfma_f32_16x16x32_bf16 v[168:171], a[8:11], a[24:27], v[168:171]\n"
"ds_read_b128 a[52:55], v53 offset:18432\n"
"v_add_u32_e32 v15, s96, v12\n"
"v_mfma_f32_16x16x32_bf16 v[184:187], a[8:11], a[28:31], v[184:187]\n"
"ds_read_b128 a[56:59], v53 offset:20480\n"
"v_cmp_gt_u32_e32 vcc, 7, v12\n"
"v_mfma_f32_16x16x32_bf16 v[200:203], a[32:35], a[8:11], v[200:203]\n"
"ds_read_b128 a[60:63], v53 offset:22528\n"
"v_mov_b32_e32 v19, 0x3f803f80\n"
"v_mfma_f32_16x16x32_bf16 v[216:219], a[36:39], a[8:11], v[216:219]\n"
"v_add_lshl_u32 v5, v5, v12, 4\n"
"v_cndmask_b32_e32 v15, 26, v15, vcc\n"
"v_mfma_f32_16x16x32_bf16 v[232:235], a[40:43], a[8:11], v[232:235]\n"
"global_load_lds_dwordx4 v76, s[38:39] offset:0\n"
"s_add_u32 s36, s36, 1\n"
"s_cmp_eq_u32 s36, 10\n"
"s_cselect_b32 s36, 0, s36\n"
"v_mfma_f32_16x16x32_bf16 v[248:251], a[44:47], a[8:11], v[248:251]\n"
"v_subrev_co_u32_e32 v16, vcc, 8, v12\n"
"v_add_u32_e32 v16, s29, v16\n"
"v_mfma_f32_16x16x32_bf16 v[140:143], a[12:15], a[16:19], v[140:143]\n"
"v_cndmask_b32_e64 v14, v14, v16, s[0:1]\n"
"s_movk_i32 s0, 0x48\n"
"v_add_u32_e32 v16, 0x1fffffe0, v14\n"
"v_mfma_f32_16x16x32_bf16 v[156:159], a[12:15], a[20:23], v[156:159]\n"
"v_lshl_add_u32 v17, v14, 2, s86\n"
"v_cmp_gt_u32_e64 s[0:1], s0, v14\n"
"v_mfma_f32_16x16x32_bf16 v[172:175], a[12:15], a[24:27], v[172:175]\n"
"v_add_u32_e32 v15, s87, v15\n"
"s_mul_i32 s16, s18, 54\n"
"v_cndmask_b32_e64 v16, v17, v16, s[0:1]\n"
"v_mfma_f32_16x16x32_bf16 v[188:191], a[12:15], a[28:31], v[188:191]\n"
"global_load_lds_dwordx4 v77, s[38:39] offset:1024\n"
"s_mul_i32 s37, s36, 245760\n"
"s_add_u32 s40, s82, s37\n"
"s_addc_u32 s41, s83, 0\n"
"v_mfma_f32_16x16x32_bf16 v[204:207], a[32:35], a[12:15], v[204:207]\n"
"v_and_b32_e32 v17, 15, v14\n"
"v_cmp_gt_u32_e64 s[0:1], 48, v14\n"
"v_mfma_f32_16x16x32_bf16 v[220:223], a[36:39], a[12:15], v[220:223]\n"
"v_lshl_add_u32 v24, s30, 9, v8\n"
"v_lshl_add_u32 v8, s19, 9, v8\n"
"v_mfma_f32_16x16x32_bf16 v[236:239], a[40:43], a[12:15], v[236:239]\n"
"v_cndmask_b32_e64 v14, v16, v17, s[0:1]\n"
"v_add_u32_e32 v14, s79, v14\n"
"v_mfma_f32_16x16x32_bf16 v[252:255], a[44:47], a[12:15], v[252:255]\n"
"v_cndmask_b32_e32 v14, v14, v15, vcc\n"
"v_or_b32_e32 v15, s90, v13\n"
"s_waitcnt vmcnt(14) lgkmcnt(0)\n"
"s_barrier\n"
"v_mfma_f32_16x16x32_bf16 v[128:131], a[48:51], a[64:67], v[128:131]\n"
"global_load_lds_dwordx4 v78, s[38:39] offset:2048\n"
"s_mul_i32 s37, s36, 221184\n"
"s_add_u32 s42, s84, s37\n"
"s_addc_u32 s43, s85, 0\n"
"v_mfma_f32_16x16x32_bf16 v[144:147], a[48:51], a[68:71], v[144:147]\n"
"s_waitcnt vmcnt(13)\n"
"ds_read_b128 a[0:3], v52 offset:0\n"
"v_mov_b32_e32 v16, 0x2f000\n"
"v_mfma_f32_16x16x32_bf16 v[160:163], a[48:51], a[72:75], v[160:163]\n"
"ds_read_b64_tr_b16 a[16:17], v36 offset:0\n"
"v_lshl_add_u32 v15, v15, 4, v16\n"
"v_mfma_f32_16x16x32_bf16 v[176:179], a[48:51], a[76:79], v[176:179]\n"
"ds_read_b64_tr_b16 a[18:19], v36 offset:512\n"
"v_mov_b32_e32 v16, 0xa000\n"
"v_mfma_f32_16x16x32_bf16 v[192:195], a[80:83], a[48:51], v[192:195]\n"
"ds_read_b64_tr_b16 a[20:21], v37 offset:0\n"
"v_lshl_add_u32 v14, v14, 3, v16\n"
"v_mfma_f32_16x16x32_bf16 v[208:211], a[84:87], a[48:51], v[208:211]\n"
"global_load_lds_dwordx4 v79, s[38:39] offset:3072\n"
"s_add_u32 m0, s88, 12288\n"
"v_mfma_f32_16x16x32_bf16 v[224:227], a[88:91], a[48:51], v[224:227]\n"
"ds_read_b64_tr_b16 a[22:23], v37 offset:512\n"
"v_cmp_gt_u32_e32 vcc, 32, v6\n"
"v_mfma_f32_16x16x32_bf16 v[240:243], a[92:95], a[48:51], v[240:243]\n"
"ds_read_b64_tr_b16 a[24:25], v38 offset:0\n"
"v_mov_b32_e32 v16, s94\n"
"v_mfma_f32_16x16x32_bf16 v[132:135], a[52:55], a[64:67], v[132:135]\n"
"ds_read_b64_tr_b16 a[26:27], v38 offset:512\n"
"v_mov_b32_e32 v17, s93\n"
"v_mfma_f32_16x16x32_bf16 v[148:151], a[52:55], a[68:71], v[148:151]\n"
"ds_read_b64_tr_b16 a[28:29], v39 offset:0\n"
"v_cndmask_b32_e32 v14, v14, v15, vcc\n"
"v_mfma_f32_16x16x32_bf16 v[164:167], a[52:55], a[72:75], v[164:167]\n"
"global_load_lds_dwordx4 v80, s[38:39] offset:0\n"
"v_mfma_f32_16x16x32_bf16 v[180:183], a[52:55], a[76:79], v[180:183]\n"
"ds_read_b64_tr_b16 a[30:31], v39 offset:512\n"
"v_and_b32_e32 v15, 1, v0\n"
"v_mfma_f32_16x16x32_bf16 v[196:199], a[80:83], a[52:55], v[196:199]\n"
"ds_read_b64_tr_b16 a[32:33], v44 offset:0\n"
"v_cmp_eq_u32_e32 vcc, 0, v15\n"
"s_mul_i32 s1, s19, 0x3c00\n"
"v_mfma_f32_16x16x32_bf16 v[212:215], a[84:87], a[52:55], v[212:215]\n"
"ds_read_b64_tr_b16 a[34:35], v44 offset:512\n"
"s_mul_i32 s18, s18, 0x19000\n"
"v_cndmask_b32_e32 v15, v16, v17, vcc\n"
"v_mfma_f32_16x16x32_bf16 v[228:231], a[88:91], a[52:55], v[228:231]\n"
"ds_read_b64_tr_b16 a[36:37], v45 offset:0\n"
"v_mov_b32_e32 v16, s92\n"
"v_mfma_f32_16x16x32_bf16 v[244:247], a[92:95], a[52:55], v[244:247]\n"
"global_load_lds_dwordx4 v81, s[38:39] offset:1024\n"
"v_mfma_f32_16x16x32_bf16 v[136:139], a[56:59], a[64:67], v[136:139]\n"
"ds_read_b64_tr_b16 a[38:39], v45 offset:512\n"
"v_mov_b32_e32 v17, s91\n"
"v_mfma_f32_16x16x32_bf16 v[152:155], a[56:59], a[68:71], v[152:155]\n"
"ds_read_b64_tr_b16 a[40:41], v46 offset:0\n"
"v_cndmask_b32_e32 v16, v16, v17, vcc\n"
"v_mfma_f32_16x16x32_bf16 v[168:171], a[56:59], a[72:75], v[168:171]\n"
"ds_read_b64_tr_b16 a[42:43], v46 offset:512\n"
"v_and_b32_e32 v17, 2, v0\n"
"v_mfma_f32_16x16x32_bf16 v[184:187], a[56:59], a[76:79], v[184:187]\n"
"ds_read_b64_tr_b16 a[44:45], v47 offset:0\n"
"v_cmp_eq_u32_e32 vcc, 0, v17\n"
"v_mfma_f32_16x16x32_bf16 v[200:203], a[80:83], a[56:59], v[200:203]\n"
"global_load_lds_dwordx4 v82, s[38:39] offset:2048\n"
"v_mfma_f32_16x16x32_bf16 v[216:219], a[84:87], a[56:59], v[216:219]\n"
"ds_read_b64_tr_b16 a[46:47], v47 offset:512\n"
"v_lshlrev_b32_e32 v17, 3, v3\n"
"v_mfma_f32_16x16x32_bf16 v[232:235], a[88:91], a[56:59], v[232:235]\n"
"v_mul_u32_u24_e32 v3, 0x1400, v3\n"
"v_cndmask_b32_e32 v15, v15, v16, vcc\n"
"v_mfma_f32_16x16x32_bf16 v[248:251], a[92:95], a[56:59], v[248:251]\n"
"v_mov_b32_e32 v16, 0x8000\n"
"v_lshl_add_u32 v16, v0, 3, v16\n"
"v_mfma_f32_16x16x32_bf16 v[140:143], a[60:63], a[64:67], v[140:143]\n"
"v_and_b32_e32 v0, 48, v0\n"
"v_or_b32_e32 v67, s35, v0\n"
"v_mfma_f32_16x16x32_bf16 v[156:159], a[60:63], a[68:71], v[156:159]\n"
"global_load_lds_dwordx4 v83, s[38:39] offset:3072\n"
"s_lshl_b32 s37, s36, 7\n"
"s_add_u32 s38, s80, s37\n"
"s_addc_u32 s39, s81, 0\n"
"s_add_u32 m0, s89, 32768\n"
"v_mfma_f32_16x16x32_bf16 v[172:175], a[60:63], a[72:75], v[172:175]\n"
"s_waitcnt vmcnt(16)\n"
"ds_read_b128 a[4:7], v52 offset:2048\n"
"s_cmp_gt_u32 s30, 1\n"
"s_cselect_b32 s35, s50, 0\n"
"v_mfma_f32_16x16x32_bf16 v[188:191], a[60:63], a[76:79], v[188:191]\n"
"s_waitcnt vmcnt(14)\n"
"ds_read_b128 a[8:11], v52 offset:4096\n"
"s_lshl_b32 s0, s14, 10\n"
"v_cmp_eq_u32_e32 vcc, 20, v12\n"
"v_mfma_f32_16x16x32_bf16 v[204:207], a[80:83], a[60:63], v[204:207]\n"
"s_waitcnt vmcnt(12)\n"
"ds_read_b128 a[12:15], v52 offset:6144\n"
"s_add_i32 s34, s0, s34\n"
"s_mul_i32 s0, s30, 0x3c00\n"
"v_mfma_f32_16x16x32_bf16 v[220:223], a[84:87], a[60:63], v[220:223]\n"
"v_cndmask_b32_e32 v12, 0, v19, vcc\n"
"v_or_b32_e32 v19, 0x19000, v9\n"
"v_mfma_f32_16x16x32_bf16 v[236:239], a[88:91], a[60:63], v[236:239]\n"
"global_load_lds_dwordx4 v84, s[40:41] offset:0\n"
"v_mfma_f32_16x16x32_bf16 v[252:255], a[92:95], a[60:63], v[252:255]\n"
"v_add_u32_e32 v20, s0, v19\n"
"s_mul_i32 s0, s22, 0x2800\n"
"v_or_b32_e32 v23, s0, v9\n"
"s_waitcnt lgkmcnt(0)\n"
"v_mfma_f32_16x16x32_bf16 v[112:115], a[16:19], v[56:59], v[112:115]\n"
"ds_read_b128 a[48:51], v53 offset:0\n"
"s_lshl_b32 s0, s17, 5\n"
"s_add_i32 s0, s27, s0\n"
"v_mfma_f32_16x16x32_bf16 v[116:119], a[20:23], v[56:59], v[116:119]\n"
"ds_read_b64_tr_b16 a[64:65], v40 offset:0\n"
"s_mulk_i32 s0, 0xa00\n"
"s_lshl_b32 s50, s13, 12\n"
"v_mfma_f32_16x16x32_bf16 v[120:123], a[32:35], v[56:59], v[120:123]\n"
"ds_read_b64_tr_b16 a[66:67], v40 offset:512\n"
"s_add_i32 s0, s0, 0x58000\n"
"v_mfma_f32_16x16x32_bf16 v[124:127], a[36:39], v[56:59], v[124:127]\n"
"global_load_lds_dwordx4 v85, s[40:41] offset:1024\n"
"s_add_u32 m0, s89, 34816\n"
"v_mfma_f32_16x16x32_bf16 v[128:131], a[0:3], a[16:19], v[128:131]\n"
"ds_read_b64_tr_b16 a[68:69], v41 offset:0\n"
"v_mfma_f32_16x16x32_bf16 v[144:147], a[0:3], a[20:23], v[144:147]\n"
"ds_read_b64_tr_b16 a[70:71], v41 offset:512\n"
"v_mfma_f32_16x16x32_bf16 v[160:163], a[0:3], a[24:27], v[160:163]\n"
"ds_read_b64_tr_b16 a[72:73], v42 offset:0\n"
"v_mfma_f32_16x16x32_bf16 v[176:179], a[0:3], a[28:31], v[176:179]\n"
"ds_read_b64_tr_b16 a[74:75], v42 offset:512\n"
"v_mfma_f32_16x16x32_bf16 v[192:195], a[32:35], a[0:3], v[192:195]\n"
"global_load_lds_dwordx4 v86, s[42:43] offset:0\n"
"v_mfma_f32_16x16x32_bf16 v[208:211], a[36:39], a[0:3], v[208:211]\n"
"ds_read_b64_tr_b16 a[76:77], v43 offset:0\n"
"v_mfma_f32_16x16x32_bf16 v[224:227], a[40:43], a[0:3], v[224:227]\n"
"ds_read_b64_tr_b16 a[78:79], v43 offset:512\n"
"v_mfma_f32_16x16x32_bf16 v[240:243], a[44:47], a[0:3], v[240:243]\n"
"ds_read_b64_tr_b16 a[80:81], v48 offset:0\n"
"v_mfma_f32_16x16x32_bf16 v[132:135], a[4:7], a[16:19], v[132:135]\n"
"ds_read_b64_tr_b16 a[82:83], v48 offset:512\n"
"v_mfma_f32_16x16x32_bf16 v[148:151], a[4:7], a[20:23], v[148:151]\n"
"global_load_lds_dwordx4 v87, s[42:43] offset:1024\n"
"s_add_u32 m0, s88, 16384\n"
"v_mfma_f32_16x16x32_bf16 v[164:167], a[4:7], a[24:27], v[164:167]\n"
"ds_read_b64_tr_b16 a[84:85], v49 offset:0\n"
"v_mfma_f32_16x16x32_bf16 v[180:183], a[4:7], a[28:31], v[180:183]\n"
"ds_read_b64_tr_b16 a[86:87], v49 offset:512\n"
"v_mfma_f32_16x16x32_bf16 v[196:199], a[32:35], a[4:7], v[196:199]\n"
"ds_read_b64_tr_b16 a[88:89], v50 offset:0\n"
"v_mfma_f32_16x16x32_bf16 v[212:215], a[36:39], a[4:7], v[212:215]\n"
"ds_read_b64_tr_b16 a[90:91], v50 offset:512\n"
"v_mfma_f32_16x16x32_bf16 v[228:231], a[40:43], a[4:7], v[228:231]\n"
"global_load_lds_dwordx4 v76, s[38:39] offset:0\n"
"s_add_u32 s36, s36, 1\n"
"s_cmp_eq_u32 s36, 10\n"
"s_cselect_b32 s36, 0, s36\n"
"v_mfma_f32_16x16x32_bf16 v[244:247], a[44:47], a[4:7], v[244:247]\n"
"ds_read_b64_tr_b16 a[92:93], v51 offset:0\n"
"v_mfma_f32_16x16x32_bf16 v[136:139], a[8:11], a[16:19], v[136:139]\n"
"ds_read_b64_tr_b16 a[94:95], v51 offset:512\n"
"v_mfma_f32_16x16x32_bf16 v[152:155], a[8:11], a[20:23], v[152:155]\n"
"ds_read_b128 a[52:55], v53 offset:2048\n"
"v_mfma_f32_16x16x32_bf16 v[168:171], a[8:11], a[24:27], v[168:171]\n"
"ds_read_b128 a[56:59], v53 offset:4096\n"
"v_mfma_f32_16x16x32_bf16 v[184:187], a[8:11], a[28:31], v[184:187]\n"
"global_load_lds_dwordx4 v77, s[38:39] offset:1024\n"
"s_mul_i32 s37, s36, 245760\n"
"s_add_u32 s40, s82, s37\n"
"s_addc_u32 s41, s83, 0\n"
"v_mfma_f32_16x16x32_bf16 v[200:203], a[32:35], a[8:11], v[200:203]\n"
"ds_read_b128 a[60:63], v53 offset:6144\n"
"v_mfma_f32_16x16x32_bf16 v[216:219], a[36:39], a[8:11], v[216:219]\n"
"s_add_u32 s0, s66, s0\n"
"v_add_u32_e32 v22, s1, v19\n"
"s_addc_u32 s1, s67, 0\n"
"v_mfma_f32_16x16x32_bf16 v[232:235], a[40:43], a[8:11], v[232:235]\n"
"s_lshl_b32 s12, s17, 1\n"
"s_add_i32 s12, s12, s13\n"
"s_mul_i32 s12, s12, 0x14000\n"
"v_mfma_f32_16x16x32_bf16 v[248:251], a[44:47], a[8:11], v[248:251]\n"
"s_add_i32 s12, s12, 0x2d8a00\n"
"s_add_u32 s12, s66, s12\n"
"s_mul_i32 s14, s23, 54\n"
"s_addc_u32 s13, s67, 0\n"
"v_mfma_f32_16x16x32_bf16 v[140:143], a[12:15], a[16:19], v[140:143]\n"
"global_load_lds_dwordx4 v78, s[38:39] offset:2048\n"
"s_mul_i32 s37, s36, 221184\n"
"s_add_u32 s42, s84, s37\n"
"s_addc_u32 s43, s85, 0\n"
"v_mfma_f32_16x16x32_bf16 v[156:159], a[12:15], a[20:23], v[156:159]\n"
"s_add_i32 s14, s14, s29\n"
"s_lshl_b32 s14, s14, 12\n"
"s_add_i32 s14, s14, 0x7d8000\n"
"v_mfma_f32_16x16x32_bf16 v[172:175], a[12:15], a[24:27], v[172:175]\n"
"s_add_u32 s14, s66, s14\n"
"s_addc_u32 s15, s67, 0\n"
"s_add_i32 s16, s16, s29\n"
"v_mfma_f32_16x16x32_bf16 v[188:191], a[12:15], a[28:31], v[188:191]\n"
"v_lshl_or_b32 v3, v13, 3, v3\n"
"s_mulk_i32 s16, 0x1400\n"
"v_add_u32_e32 v3, 0x800, v3\n"
"v_mfma_f32_16x16x32_bf16 v[204:207], a[32:35], a[12:15], v[204:207]\n"
"s_add_i32 s16, s16, 0x1118800\n"
"v_lshl_add_u32 v25, s30, 7, v3\n"
"v_lshl_add_u32 v3, s19, 7, v3\n"
"v_mfma_f32_16x16x32_bf16 v[220:223], a[36:39], a[12:15], v[220:223]\n"
"global_load_lds_dwordx4 v79, s[38:39] offset:3072\n"
"s_add_u32 m0, s88, 20480\n"
"v_mfma_f32_16x16x32_bf16 v[236:239], a[40:43], a[12:15], v[236:239]\n"
"s_add_u32 s16, s66, s16\n"
"s_mul_i32 s19, s30, 0x5000\n"
"s_addc_u32 s17, s67, 0\n"
"v_mfma_f32_16x16x32_bf16 v[252:255], a[44:47], a[12:15], v[252:255]\n"
"s_add_i32 s18, s19, s18\n"
"s_mulk_i32 s20, 0x3c00\n"
"s_add_i32 s18, s18, 0x1334000\n"
"s_waitcnt vmcnt(16) lgkmcnt(0)\n"
"s_barrier\n"
"v_mfma_f32_16x16x32_bf16 v[128:131], a[48:51], a[64:67], v[128:131]\n"
"s_waitcnt vmcnt(14)\n"
"ds_read_b128 a[0:3], v52 offset:8192\n"
"v_add_u32_e32 v21, s20, v19\n"
"v_mfma_f32_16x16x32_bf16 v[144:147], a[48:51], a[68:71], v[144:147]\n"
"ds_read_b64_tr_b16 a[16:17], v36 offset:16384\n"
"v_mfma_f32_16x16x32_bf16 v[160:163], a[48:51], a[72:75], v[160:163]\n"
"global_load_lds_dwordx4 v80, s[38:39] offset:0\n"
"v_mfma_f32_16x16x32_bf16 v[176:179], a[48:51], a[76:79], v[176:179]\n"
"ds_read_b64_tr_b16 a[18:19], v36 offset:16896\n"
"v_mfma_f32_16x16x32_bf16 v[192:195], a[80:83], a[48:51], v[192:195]\n"
"ds_read_b64_tr_b16 a[20:21], v37 offset:16384\n"
"v_mfma_f32_16x16x32_bf16 v[208:211], a[84:87], a[48:51], v[208:211]\n"
"ds_read_b64_tr_b16 a[22:23], v37 offset:16896\n"
"v_mfma_f32_16x16x32_bf16 v[224:227], a[88:91], a[48:51], v[224:227]\n"
"ds_read_b64_tr_b16 a[24:25], v38 offset:16384\n"
"v_mfma_f32_16x16x32_bf16 v[240:243], a[92:95], a[48:51], v[240:243]\n"
"global_load_lds_dwordx4 v81, s[38:39] offset:1024\n"
"v_mfma_f32_16x16x32_bf16 v[132:135], a[52:55], a[64:67], v[132:135]\n"
"ds_read_b64_tr_b16 a[26:27], v38 offset:16896\n"
"v_mfma_f32_16x16x32_bf16 v[148:151], a[52:55], a[68:71], v[148:151]\n"
"ds_read_b64_tr_b16 a[28:29], v39 offset:16384\n"
"v_mfma_f32_16x16x32_bf16 v[164:167], a[52:55], a[72:75], v[164:167]\n"
"ds_read_b64_tr_b16 a[30:31], v39 offset:16896\n"
"v_mfma_f32_16x16x32_bf16 v[180:183], a[52:55], a[76:79], v[180:183]\n"
"ds_read_b64_tr_b16 a[32:33], v44 offset:16384\n"
"v_mfma_f32_16x16x32_bf16 v[196:199], a[80:83], a[52:55], v[196:199]\n"
"global_load_lds_dwordx4 v82, s[38:39] offset:2048\n"
"v_mfma_f32_16x16x32_bf16 v[212:215], a[84:87], a[52:55], v[212:215]\n"
"ds_read_b64_tr_b16 a[34:35], v44 offset:16896\n"
"v_mfma_f32_16x16x32_bf16 v[228:231], a[88:91], a[52:55], v[228:231]\n"
"ds_read_b64_tr_b16 a[36:37], v45 offset:16384\n"
"v_mfma_f32_16x16x32_bf16 v[244:247], a[92:95], a[52:55], v[244:247]\n"
"ds_read_b64_tr_b16 a[38:39], v45 offset:16896\n"
"v_mfma_f32_16x16x32_bf16 v[136:139], a[56:59], a[64:67], v[136:139]\n"
"ds_read_b64_tr_b16 a[40:41], v46 offset:16384\n"
"v_mfma_f32_16x16x32_bf16 v[152:155], a[56:59], a[68:71], v[152:155]\n"
"global_load_lds_dwordx4 v83, s[38:39] offset:3072\n"
"s_lshl_b32 s37, s36, 7\n"
"s_add_u32 s38, s80, s37\n"
"s_addc_u32 s39, s81, 0\n"
"s_add_u32 m0, s89, 0\n"
"v_mfma_f32_16x16x32_bf16 v[168:171], a[56:59], a[72:75], v[168:171]\n"
"ds_read_b64_tr_b16 a[42:43], v46 offset:16896\n"
"v_mfma_f32_16x16x32_bf16 v[184:187], a[56:59], a[76:79], v[184:187]\n"
"ds_read_b64_tr_b16 a[44:45], v47 offset:16384\n"
"v_mfma_f32_16x16x32_bf16 v[200:203], a[80:83], a[56:59], v[200:203]\n"
"ds_read_b64_tr_b16 a[46:47], v47 offset:16896\n"
"v_mfma_f32_16x16x32_bf16 v[216:219], a[84:87], a[56:59], v[216:219]\n"
"s_add_u32 s18, s66, s18\n"
"s_mul_i32 s20, s21, 0xa00\n"
"s_addc_u32 s19, s67, 0\n"
"v_mfma_f32_16x16x32_bf16 v[232:235], a[88:91], a[56:59], v[232:235]\n"
"global_load_lds_dwordx4 v84, s[40:41] offset:0\n"
"v_mfma_f32_16x16x32_bf16 v[248:251], a[92:95], a[56:59], v[248:251]\n"
"s_add_i32 s20, s20, 0xe98000\n"
"s_add_u32 s20, s66, s20\n"
"s_addc_u32 s21, s67, 0\n"
"v_mfma_f32_16x16x32_bf16 v[140:143], a[60:63], a[64:67], v[140:143]\n"
"s_lshl_b32 s23, s23, 1\n"
"v_or_b32_e32 v65, s51, v0\n"
"s_mul_i32 s51, s22, 0xa000\n"
"v_mfma_f32_16x16x32_bf16 v[156:159], a[60:63], a[68:71], v[156:159]\n"
"s_waitcnt vmcnt(17)\n"
"ds_read_b128 a[4:7], v52 offset:10240\n"
"s_or_b32 s22, s23, s22\n"
"s_mul_i32 s22, s22, 0xa000\n"
"v_mfma_f32_16x16x32_bf16 v[172:175], a[60:63], a[72:75], v[172:175]\n"
"s_waitcnt vmcnt(15)\n"
"ds_read_b128 a[8:11], v52 offset:12288\n"
"s_add_u32 s22, s66, s22\n"
"s_addc_u32 s23, s67, 0\n"
"v_mfma_f32_16x16x32_bf16 v[188:191], a[60:63], a[76:79], v[188:191]\n"
"global_load_lds_dwordx4 v85, s[40:41] offset:1024\n"
"s_add_u32 m0, s89, 2048\n"
"v_mfma_f32_16x16x32_bf16 v[204:207], a[80:83], a[60:63], v[204:207]\n"
"s_waitcnt vmcnt(14)\n"
"ds_read_b128 a[12:15], v52 offset:14336\n"
"v_mfma_f32_16x16x32_bf16 v[220:223], a[84:87], a[60:63], v[220:223]\n"
"s_add_u32 s22, s22, 0xe98a00\n"
"s_mulk_i32 s24, 0x3c00\n"
"s_addc_u32 s23, s23, 0\n"
"v_mfma_f32_16x16x32_bf16 v[236:239], a[88:91], a[60:63], v[236:239]\n"
"v_add_u32_e32 v19, s24, v19\n"
"v_mfma_f32_16x16x32_bf16 v[252:255], a[92:95], a[60:63], v[252:255]\n"
"s_add_u32 s24, s60, s26\n"
"v_lshl_or_b32 v71, s25, 5, v17\n"
"s_addc_u32 s25, s61, 0\n"
"s_waitcnt lgkmcnt(0)\n"
"v_mfma_f32_16x16x32_bf16 v[112:115], a[16:19], v[56:59], v[112:115]\n"
"global_load_lds_dwordx4 v86, s[42:43] offset:0\n"
"v_mfma_f32_16x16x32_bf16 v[116:119], a[20:23], v[56:59], v[116:119]\n"
"ds_read_b128 a[48:51], v53 offset:8192\n"
"s_add_u32 s26, s76, s26\n"
"s_addc_u32 s27, s77, 0\n"
"v_mfma_f32_16x16x32_bf16 v[120:123], a[32:35], v[56:59], v[120:123]\n"
"ds_read_b64_tr_b16 a[64:65], v40 offset:16384\n"
"v_or_b32_e32 v66, s52, v0\n"
"v_mfma_f32_16x16x32_bf16 v[124:127], a[36:39], v[56:59], v[124:127]\n"
"ds_read_b64_tr_b16 a[66:67], v40 offset:16896\n"
"v_or_b32_e32 v68, s53, v0\n"
"v_mfma_f32_16x16x32_bf16 v[128:131], a[0:3], a[16:19], v[128:131]\n"
"ds_read_b64_tr_b16 a[68:69], v41 offset:16384\n"
"v_mov_b32_e32 v18, 0x1000\n"
"v_mfma_f32_16x16x32_bf16 v[144:147], a[0:3], a[20:23], v[144:147]\n"
"global_load_lds_dwordx4 v87, s[42:43] offset:1024\n"
"s_add_u32 m0, s88, 0\n"
"v_mfma_f32_16x16x32_bf16 v[160:163], a[0:3], a[24:27], v[160:163]\n"
"ds_read_b64_tr_b16 a[70:71], v41 offset:16896\n"
"v_mul_u32_u24_e32 v13, 0x500, v13\n"
"v_mfma_f32_16x16x32_bf16 v[176:179], a[0:3], a[28:31], v[176:179]\n"
"ds_read_b64_tr_b16 a[72:73], v42 offset:16384\n"
"v_or_b32_e32 v17, 0x80, v17\n"
"v_mfma_f32_16x16x32_bf16 v[192:195], a[32:35], a[0:3], v[192:195]\n"
"ds_read_b64_tr_b16 a[74:75], v42 offset:16896\n"
"v_mfma_f32_16x16x32_bf16 v[208:211], a[36:39], a[0:3], v[208:211]\n"
"ds_read_b64_tr_b16 a[76:77], v43 offset:16384\n"
"v_mfma_f32_16x16x32_bf16 v[224:227], a[40:43], a[0:3], v[224:227]\n"
"global_load_lds_dwordx4 v76, s[38:39] offset:0\n"
"s_add_u32 s36, s36, 1\n"
"s_cmp_eq_u32 s36, 10\n"
"s_cselect_b32 s36, 0, s36\n"
"v_mfma_f32_16x16x32_bf16 v[240:243], a[44:47], a[0:3], v[240:243]\n"
"ds_read_b64_tr_b16 a[78:79], v43 offset:16896\n"
"v_mfma_f32_16x16x32_bf16 v[132:135], a[4:7], a[16:19], v[132:135]\n"
"ds_read_b64_tr_b16 a[80:81], v48 offset:16384\n"
"v_mfma_f32_16x16x32_bf16 v[148:151], a[4:7], a[20:23], v[148:151]\n"
"ds_read_b64_tr_b16 a[82:83], v48 offset:16896\n"
"v_mfma_f32_16x16x32_bf16 v[164:167], a[4:7], a[24:27], v[164:167]\n"
"ds_read_b64_tr_b16 a[84:85], v49 offset:16384\n"
"v_mfma_f32_16x16x32_bf16 v[180:183], a[4:7], a[28:31], v[180:183]\n"
"global_load_lds_dwordx4 v77, s[38:39] offset:1024\n"
"s_mul_i32 s37, s36, 245760\n"
"s_add_u32 s40, s82, s37\n"
"s_addc_u32 s41, s83, 0\n"
"v_mfma_f32_16x16x32_bf16 v[196:199], a[32:35], a[4:7], v[196:199]\n"
"ds_read_b64_tr_b16 a[86:87], v49 offset:16896\n"
"v_mfma_f32_16x16x32_bf16 v[212:215], a[36:39], a[4:7], v[212:215]\n"
"ds_read_b64_tr_b16 a[88:89], v50 offset:16384\n"
"v_mfma_f32_16x16x32_bf16 v[228:231], a[40:43], a[4:7], v[228:231]\n"
"ds_read_b64_tr_b16 a[90:91], v50 offset:16896\n"
"v_mfma_f32_16x16x32_bf16 v[244:247], a[44:47], a[4:7], v[244:247]\n"
"ds_read_b64_tr_b16 a[92:93], v51 offset:16384\n"
"v_mfma_f32_16x16x32_bf16 v[136:139], a[8:11], a[16:19], v[136:139]\n"
"global_load_lds_dwordx4 v78, s[38:39] offset:2048\n"
"s_mul_i32 s37, s36, 221184\n"
"s_add_u32 s42, s84, s37\n"
"s_addc_u32 s43, s85, 0\n"
"v_mfma_f32_16x16x32_bf16 v[152:155], a[8:11], a[20:23], v[152:155]\n"
"ds_read_b64_tr_b16 a[94:95], v51 offset:16896\n"
"v_mfma_f32_16x16x32_bf16 v[168:171], a[8:11], a[24:27], v[168:171]\n"
"ds_read_b128 a[52:55], v53 offset:10240\n"
"v_mfma_f32_16x16x32_bf16 v[184:187], a[8:11], a[28:31], v[184:187]\n"
"ds_read_b128 a[56:59], v53 offset:12288\n"
"v_mfma_f32_16x16x32_bf16 v[200:203], a[32:35], a[8:11], v[200:203]\n"
"ds_read_b128 a[60:63], v53 offset:14336\n"
"v_mfma_f32_16x16x32_bf16 v[216:219], a[36:39], a[8:11], v[216:219]\n"
"global_load_lds_dwordx4 v79, s[38:39] offset:3072\n"
"s_add_u32 m0, s88, 4096\n"
"v_mfma_f32_16x16x32_bf16 v[232:235], a[40:43], a[8:11], v[232:235]\n"
"s_add_u32 s28, s74, s28\n"
"v_bitop3_b32 v18, s50, v18, v9 bitop3:0x36\n"
"v_or_b32_e32 v23, 0x14000, v23\n"
"v_lshlrev_b32_e32 v6, 3, v6\n"
"v_or_b32_e32 v26, v13, v0\n"
"v_or_b32_e32 v13, v17, v13\n"
"s_addc_u32 s29, s75, 0\n"
"v_mfma_f32_16x16x32_bf16 v[248:251], a[44:47], a[8:11], v[248:251]\n"
"v_or_b32_e32 v27, s50, v9\n"
"v_or_b32_e32 v28, s33, v9\n"
"v_mfma_f32_16x16x32_bf16 v[140:143], a[12:15], a[16:19], v[140:143]\n"
"v_or_b32_e32 v29, s51, v9\n"
"v_xor_b32 v72, 64, v71\n"
"v_mfma_f32_16x16x32_bf16 v[156:159], a[12:15], a[20:23], v[156:159]\n"
"v_mfma_f32_16x16x32_bf16 v[172:175], a[12:15], a[24:27], v[172:175]\n"
"global_load_lds_dwordx4 v80, s[38:39] offset:0\n"
"v_mfma_f32_16x16x32_bf16 v[188:191], a[12:15], a[28:31], v[188:191]\n"
"v_mfma_f32_16x16x32_bf16 v[204:207], a[32:35], a[12:15], v[204:207]\n"
"v_mfma_f32_16x16x32_bf16 v[220:223], a[36:39], a[12:15], v[220:223]\n"
"v_mfma_f32_16x16x32_bf16 v[236:239], a[40:43], a[12:15], v[236:239]\n"
"v_mfma_f32_16x16x32_bf16 v[252:255], a[44:47], a[12:15], v[252:255]\n"
"global_load_lds_dwordx4 v81, s[38:39] offset:1024\n"
"s_waitcnt vmcnt(18) lgkmcnt(0)\n"
"s_barrier\n"
"v_mfma_f32_16x16x32_bf16 v[128:131], a[48:51], a[64:67], v[128:131]\n"
"s_waitcnt vmcnt(16)\n"
"ds_read_b128 a[0:3], v52 offset:16384\n"
"v_mfma_f32_16x16x32_bf16 v[144:147], a[48:51], a[68:71], v[144:147]\n"
"ds_read_b64_tr_b16 a[16:17], v36 offset:32768\n"
"v_mfma_f32_16x16x32_bf16 v[160:163], a[48:51], a[72:75], v[160:163]\n"
"ds_read_b64_tr_b16 a[18:19], v36 offset:33280\n"
"v_mfma_f32_16x16x32_bf16 v[176:179], a[48:51], a[76:79], v[176:179]\n"
"ds_read_b64_tr_b16 a[20:21], v37 offset:32768\n"
"v_mfma_f32_16x16x32_bf16 v[192:195], a[80:83], a[48:51], v[192:195]\n"
"global_load_lds_dwordx4 v82, s[38:39] offset:2048\n"
"v_mfma_f32_16x16x32_bf16 v[208:211], a[84:87], a[48:51], v[208:211]\n"
"ds_read_b64_tr_b16 a[22:23], v37 offset:33280\n"
"v_mfma_f32_16x16x32_bf16 v[224:227], a[88:91], a[48:51], v[224:227]\n"
"ds_read_b64_tr_b16 a[24:25], v38 offset:32768\n"
"v_mfma_f32_16x16x32_bf16 v[240:243], a[92:95], a[48:51], v[240:243]\n"
"ds_read_b64_tr_b16 a[26:27], v38 offset:33280\n"
"v_mfma_f32_16x16x32_bf16 v[132:135], a[52:55], a[64:67], v[132:135]\n"
"ds_read_b64_tr_b16 a[28:29], v39 offset:32768\n"
"v_mfma_f32_16x16x32_bf16 v[148:151], a[52:55], a[68:71], v[148:151]\n"
"global_load_lds_dwordx4 v83, s[38:39] offset:3072\n"
"s_lshl_b32 s37, s36, 7\n"
"s_add_u32 s38, s80, s37\n"
"s_addc_u32 s39, s81, 0\n"
"s_add_u32 m0, s89, 16384\n"
"v_mfma_f32_16x16x32_bf16 v[164:167], a[52:55], a[72:75], v[164:167]\n"
"ds_read_b64_tr_b16 a[30:31], v39 offset:33280\n"
"v_mfma_f32_16x16x32_bf16 v[180:183], a[52:55], a[76:79], v[180:183]\n"
"ds_read_b64_tr_b16 a[32:33], v44 offset:32768\n"
"v_mfma_f32_16x16x32_bf16 v[196:199], a[80:83], a[52:55], v[196:199]\n"
"ds_read_b64_tr_b16 a[34:35], v44 offset:33280\n"
"v_mfma_f32_16x16x32_bf16 v[212:215], a[84:87], a[52:55], v[212:215]\n"
"ds_read_b64_tr_b16 a[36:37], v45 offset:32768\n"
"v_mfma_f32_16x16x32_bf16 v[228:231], a[88:91], a[52:55], v[228:231]\n"
"global_load_lds_dwordx4 v84, s[40:41] offset:0\n"
"v_mfma_f32_16x16x32_bf16 v[244:247], a[92:95], a[52:55], v[244:247]\n"
"ds_read_b64_tr_b16 a[38:39], v45 offset:33280\n"
"v_mfma_f32_16x16x32_bf16 v[136:139], a[56:59], a[64:67], v[136:139]\n"
"ds_read_b64_tr_b16 a[40:41], v46 offset:32768\n"
"v_mfma_f32_16x16x32_bf16 v[152:155], a[56:59], a[68:71], v[152:155]\n"
"ds_read_b64_tr_b16 a[42:43], v46 offset:33280\n"
"v_mfma_f32_16x16x32_bf16 v[168:171], a[56:59], a[72:75], v[168:171]\n"
"ds_read_b64_tr_b16 a[44:45], v47 offset:32768\n"
"v_mfma_f32_16x16x32_bf16 v[184:187], a[56:59], a[76:79], v[184:187]\n"
"global_load_lds_dwordx4 v85, s[40:41] offset:1024\n"
"s_add_u32 m0, s89, 18432\n"
"v_mfma_f32_16x16x32_bf16 v[200:203], a[80:83], a[56:59], v[200:203]\n"
"ds_read_b64_tr_b16 a[46:47], v47 offset:33280\n"
"v_mfma_f32_16x16x32_bf16 v[216:219], a[84:87], a[56:59], v[216:219]\n"
"v_mfma_f32_16x16x32_bf16 v[232:235], a[88:91], a[56:59], v[232:235]\n"
"v_mfma_f32_16x16x32_bf16 v[248:251], a[92:95], a[56:59], v[248:251]\n"
"v_mfma_f32_16x16x32_bf16 v[140:143], a[60:63], a[64:67], v[140:143]\n"
"global_load_lds_dwordx4 v86, s[42:43] offset:0\n"
"v_mfma_f32_16x16x32_bf16 v[156:159], a[60:63], a[68:71], v[156:159]\n"
"s_waitcnt vmcnt(19)\n"
"ds_read_b128 a[4:7], v52 offset:18432\n"
"v_mfma_f32_16x16x32_bf16 v[172:175], a[60:63], a[72:75], v[172:175]\n"
"s_waitcnt vmcnt(17)\n"
"ds_read_b128 a[8:11], v52 offset:20480\n"
"v_mfma_f32_16x16x32_bf16 v[188:191], a[60:63], a[76:79], v[188:191]\n"
"s_waitcnt vmcnt(15)\n"
"ds_read_b128 a[12:15], v52 offset:22528\n"
"v_mfma_f32_16x16x32_bf16 v[204:207], a[80:83], a[60:63], v[204:207]\n"
"v_mfma_f32_16x16x32_bf16 v[220:223], a[84:87], a[60:63], v[220:223]\n"
"global_load_lds_dwordx4 v87, s[42:43] offset:1024\n"
"s_add_u32 m0, s88, 8192\n"
"v_mfma_f32_16x16x32_bf16 v[236:239], a[88:91], a[60:63], v[236:239]\n"
"v_mfma_f32_16x16x32_bf16 v[252:255], a[92:95], a[60:63], v[252:255]\n"
"s_waitcnt lgkmcnt(0)\n"
"v_mfma_f32_16x16x32_bf16 v[112:115], a[16:19], v[56:59], v[112:115]\n"
"ds_read_b128 a[48:51], v53 offset:16384\n"
"v_mfma_f32_16x16x32_bf16 v[116:119], a[20:23], v[56:59], v[116:119]\n"
"ds_read_b64_tr_b16 a[64:65], v40 offset:32768\n"
"v_mfma_f32_16x16x32_bf16 v[120:123], a[32:35], v[56:59], v[120:123]\n"
"global_load_lds_dwordx4 v76, s[38:39] offset:0\n"
"s_add_u32 s36, s36, 1\n"
"s_cmp_eq_u32 s36, 10\n"
"s_cselect_b32 s36, 0, s36\n"
"v_mfma_f32_16x16x32_bf16 v[124:127], a[36:39], v[56:59], v[124:127]\n"
"ds_read_b64_tr_b16 a[66:67], v40 offset:33280\n"
"v_mfma_f32_16x16x32_bf16 v[128:131], a[0:3], a[16:19], v[128:131]\n"
"ds_read_b64_tr_b16 a[68:69], v41 offset:32768\n"
"v_mfma_f32_16x16x32_bf16 v[144:147], a[0:3], a[20:23], v[144:147]\n"
"ds_read_b64_tr_b16 a[70:71], v41 offset:33280\n"
"v_mfma_f32_16x16x32_bf16 v[160:163], a[0:3], a[24:27], v[160:163]\n"
"ds_read_b64_tr_b16 a[72:73], v42 offset:32768\n"
"v_mfma_f32_16x16x32_bf16 v[176:179], a[0:3], a[28:31], v[176:179]\n"
"global_load_lds_dwordx4 v77, s[38:39] offset:1024\n"
"s_mul_i32 s37, s36, 245760\n"
"s_add_u32 s40, s82, s37\n"
"s_addc_u32 s41, s83, 0\n"
"v_mfma_f32_16x16x32_bf16 v[192:195], a[32:35], a[0:3], v[192:195]\n"
"ds_read_b64_tr_b16 a[74:75], v42 offset:33280\n"
"v_mfma_f32_16x16x32_bf16 v[208:211], a[36:39], a[0:3], v[208:211]\n"
"ds_read_b64_tr_b16 a[76:77], v43 offset:32768\n"
"v_mfma_f32_16x16x32_bf16 v[224:227], a[40:43], a[0:3], v[224:227]\n"
"ds_read_b64_tr_b16 a[78:79], v43 offset:33280\n"
"v_mfma_f32_16x16x32_bf16 v[240:243], a[44:47], a[0:3], v[240:243]\n"
"ds_read_b64_tr_b16 a[80:81], v48 offset:32768\n"
"v_mfma_f32_16x16x32_bf16 v[132:135], a[4:7], a[16:19], v[132:135]\n"
"global_load_lds_dwordx4 v78, s[38:39] offset:2048\n"
"s_mul_i32 s37, s36, 221184\n"
"s_add_u32 s44, s84, s37\n"
"s_addc_u32 s45, s85, 0\n"
"v_mfma_f32_16x16x32_bf16 v[148:151], a[4:7], a[20:23], v[148:151]\n"
"ds_read_b64_tr_b16 a[82:83], v48 offset:33280\n"
"v_mfma_f32_16x16x32_bf16 v[164:167], a[4:7], a[24:27], v[164:167]\n"
"ds_read_b64_tr_b16 a[84:85], v49 offset:32768\n"
"v_mfma_f32_16x16x32_bf16 v[180:183], a[4:7], a[28:31], v[180:183]\n"
"ds_read_b64_tr_b16 a[86:87], v49 offset:33280\n"
"v_mfma_f32_16x16x32_bf16 v[196:199], a[32:35], a[4:7], v[196:199]\n"
"ds_read_b64_tr_b16 a[88:89], v50 offset:32768\n"
"v_mfma_f32_16x16x32_bf16 v[212:215], a[36:39], a[4:7], v[212:215]\n"
"global_load_lds_dwordx4 v79, s[38:39] offset:3072\n"
"s_add_u32 m0, s88, 12288\n"
"v_mfma_f32_16x16x32_bf16 v[228:231], a[40:43], a[4:7], v[228:231]\n"
"ds_read_b64_tr_b16 a[90:91], v50 offset:33280\n"
"v_mfma_f32_16x16x32_bf16 v[244:247], a[44:47], a[4:7], v[244:247]\n"
"ds_read_b64_tr_b16 a[92:93], v51 offset:32768\n"
"v_mfma_f32_16x16x32_bf16 v[136:139], a[8:11], a[16:19], v[136:139]\n"
"ds_read_b64_tr_b16 a[94:95], v51 offset:33280\n"
"v_mfma_f32_16x16x32_bf16 v[152:155], a[8:11], a[20:23], v[152:155]\n"
"ds_read_b128 a[52:55], v53 offset:18432\n"
"v_mfma_f32_16x16x32_bf16 v[168:171], a[8:11], a[24:27], v[168:171]\n"
"global_load_lds_dwordx4 v80, s[38:39] offset:0\n"
"v_mfma_f32_16x16x32_bf16 v[184:187], a[8:11], a[28:31], v[184:187]\n"
"ds_read_b128 a[56:59], v53 offset:20480\n"
"v_mfma_f32_16x16x32_bf16 v[200:203], a[32:35], a[8:11], v[200:203]\n"
"ds_read_b128 a[60:63], v53 offset:22528\n"
"v_mfma_f32_16x16x32_bf16 v[216:219], a[36:39], a[8:11], v[216:219]\n"
"v_mfma_f32_16x16x32_bf16 v[232:235], a[40:43], a[8:11], v[232:235]\n"
"v_mfma_f32_16x16x32_bf16 v[248:251], a[44:47], a[8:11], v[248:251]\n"
"global_load_lds_dwordx4 v81, s[38:39] offset:1024\n"
"v_mfma_f32_16x16x32_bf16 v[140:143], a[12:15], a[16:19], v[140:143]\n"
"v_mfma_f32_16x16x32_bf16 v[156:159], a[12:15], a[20:23], v[156:159]\n"
"v_mfma_f32_16x16x32_bf16 v[172:175], a[12:15], a[24:27], v[172:175]\n"
"v_mfma_f32_16x16x32_bf16 v[188:191], a[12:15], a[28:31], v[188:191]\n"
"v_mfma_f32_16x16x32_bf16 v[204:207], a[32:35], a[12:15], v[204:207]\n"
"global_load_lds_dwordx4 v82, s[38:39] offset:2048\n"
"v_mfma_f32_16x16x32_bf16 v[220:223], a[36:39], a[12:15], v[220:223]\n"
"v_mfma_f32_16x16x32_bf16 v[236:239], a[40:43], a[12:15], v[236:239]\n"
"v_mfma_f32_16x16x32_bf16 v[252:255], a[44:47], a[12:15], v[252:255]\n"
"s_waitcnt vmcnt(19) lgkmcnt(0)\n"
"s_barrier\n"
"v_mfma_f32_16x16x32_bf16 v[128:131], a[48:51], a[64:67], v[128:131]\n"
"s_waitcnt vmcnt(17)\n"
"ds_read_b128 a[0:3], v52 offset:0\n"
"v_mfma_f32_16x16x32_bf16 v[144:147], a[48:51], a[68:71], v[144:147]\n"
"global_load_lds_dwordx4 v83, s[38:39] offset:3072\n"
"s_lshl_b32 s37, s36, 7\n"
"s_add_u32 s38, s80, s37\n"
"s_addc_u32 s39, s81, 0\n"
"s_add_u32 m0, s89, 32768\n"
"v_mfma_f32_16x16x32_bf16 v[160:163], a[48:51], a[72:75], v[160:163]\n"
"ds_read_b64_tr_b16 a[16:17], v36 offset:0\n"
"v_mfma_f32_16x16x32_bf16 v[176:179], a[48:51], a[76:79], v[176:179]\n"
"ds_read_b64_tr_b16 a[18:19], v36 offset:512\n"
"v_mfma_f32_16x16x32_bf16 v[192:195], a[80:83], a[48:51], v[192:195]\n"
"ds_read_b64_tr_b16 a[20:21], v37 offset:0\n"
"v_mfma_f32_16x16x32_bf16 v[208:211], a[84:87], a[48:51], v[208:211]\n"
"ds_read_b64_tr_b16 a[22:23], v37 offset:512\n"
"v_mfma_f32_16x16x32_bf16 v[224:227], a[88:91], a[48:51], v[224:227]\n"
"global_load_lds_dwordx4 v84, s[40:41] offset:0\n"
"v_mfma_f32_16x16x32_bf16 v[240:243], a[92:95], a[48:51], v[240:243]\n"
"ds_read_b64_tr_b16 a[24:25], v38 offset:0\n"
"v_mfma_f32_16x16x32_bf16 v[132:135], a[52:55], a[64:67], v[132:135]\n"
"ds_read_b64_tr_b16 a[26:27], v38 offset:512\n"
"v_mfma_f32_16x16x32_bf16 v[148:151], a[52:55], a[68:71], v[148:151]\n"
"ds_read_b64_tr_b16 a[28:29], v39 offset:0\n"
"v_mfma_f32_16x16x32_bf16 v[164:167], a[52:55], a[72:75], v[164:167]\n"
"ds_read_b64_tr_b16 a[30:31], v39 offset:512\n"
"v_mfma_f32_16x16x32_bf16 v[180:183], a[52:55], a[76:79], v[180:183]\n"
"global_load_lds_dwordx4 v85, s[40:41] offset:1024\n"
"s_add_u32 m0, s88, 16384\n"
"v_mfma_f32_16x16x32_bf16 v[196:199], a[80:83], a[52:55], v[196:199]\n"
"ds_read_b64_tr_b16 a[32:33], v44 offset:0\n"
"v_mfma_f32_16x16x32_bf16 v[212:215], a[84:87], a[52:55], v[212:215]\n"
"ds_read_b64_tr_b16 a[34:35], v44 offset:512\n"
"v_mfma_f32_16x16x32_bf16 v[228:231], a[88:91], a[52:55], v[228:231]\n"
"ds_read_b64_tr_b16 a[36:37], v45 offset:0\n"
"v_mfma_f32_16x16x32_bf16 v[244:247], a[92:95], a[52:55], v[244:247]\n"
"ds_read_b64_tr_b16 a[38:39], v45 offset:512\n"
"v_mfma_f32_16x16x32_bf16 v[136:139], a[56:59], a[64:67], v[136:139]\n"
"global_load_lds_dwordx4 v76, s[38:39] offset:0\n"
"s_add_u32 s36, s36, 1\n"
"s_cmp_eq_u32 s36, 10\n"
"s_cselect_b32 s36, 0, s36\n"
"v_mfma_f32_16x16x32_bf16 v[152:155], a[56:59], a[68:71], v[152:155]\n"
"ds_read_b64_tr_b16 a[40:41], v46 offset:0\n"
"v_mfma_f32_16x16x32_bf16 v[168:171], a[56:59], a[72:75], v[168:171]\n"
"ds_read_b64_tr_b16 a[42:43], v46 offset:512\n"
"v_mfma_f32_16x16x32_bf16 v[184:187], a[56:59], a[76:79], v[184:187]\n"
"ds_read_b64_tr_b16 a[44:45], v47 offset:0\n"
"v_mfma_f32_16x16x32_bf16 v[200:203], a[80:83], a[56:59], v[200:203]\n"
"ds_read_b64_tr_b16 a[46:47], v47 offset:512\n"
"v_mfma_f32_16x16x32_bf16 v[216:219], a[84:87], a[56:59], v[216:219]\n"
"global_load_lds_dwordx4 v77, s[38:39] offset:1024\n"
"s_mul_i32 s37, s36, 245760\n"
"s_add_u32 s40, s82, s37\n"
"s_addc_u32 s41, s83, 0\n"
"v_mfma_f32_16x16x32_bf16 v[232:235], a[88:91], a[56:59], v[232:235]\n"
"v_mfma_f32_16x16x32_bf16 v[248:251], a[92:95], a[56:59], v[248:251]\n"
"v_mfma_f32_16x16x32_bf16 v[140:143], a[60:63], a[64:67], v[140:143]\n"
"v_mfma_f32_16x16x32_bf16 v[156:159], a[60:63], a[68:71], v[156:159]\n"
"s_waitcnt vmcnt(20)\n"
"ds_read_b128 a[4:7], v52 offset:2048\n"
"v_mfma_f32_16x16x32_bf16 v[172:175], a[60:63], a[72:75], v[172:175]\n"
"global_load_lds_dwordx4 v78, s[38:39] offset:2048\n"
"s_mul_i32 s37, s36, 221184\n"
"s_add_u32 s48, s84, s37\n"
"s_addc_u32 s49, s85, 0\n"
"v_mfma_f32_16x16x32_bf16 v[188:191], a[60:63], a[76:79], v[188:191]\n"
"s_waitcnt vmcnt(19)\n"
"ds_read_b128 a[8:11], v52 offset:4096\n"
"v_mfma_f32_16x16x32_bf16 v[204:207], a[80:83], a[60:63], v[204:207]\n"
"s_waitcnt vmcnt(17)\n"
"ds_read_b128 a[12:15], v52 offset:6144\n"
"v_mfma_f32_16x16x32_bf16 v[220:223], a[84:87], a[60:63], v[220:223]\n"
"v_mfma_f32_16x16x32_bf16 v[236:239], a[88:91], a[60:63], v[236:239]\n"
"v_mfma_f32_16x16x32_bf16 v[252:255], a[92:95], a[60:63], v[252:255]\n"
"global_load_lds_dwordx4 v79, s[38:39] offset:3072\n"
"s_add_u32 m0, s88, 20480\n"
"s_waitcnt lgkmcnt(0)\n"
"v_mfma_f32_16x16x32_bf16 v[112:115], a[16:19], v[56:59], v[112:115]\n"
"ds_read_b128 a[48:51], v53 offset:0\n"
"v_mfma_f32_16x16x32_bf16 v[116:119], a[20:23], v[56:59], v[116:119]\n"
"s_mov_b32 exec_lo, 0\n"
"global_load_dword v109, v1, s[66:67] offset:0 sc1\n"
"s_mov_b32 exec_lo, -1\n"
"ds_read_b64_tr_b16 a[64:65], v40 offset:0\n"
"v_mfma_f32_16x16x32_bf16 v[120:123], a[32:35], v[56:59], v[120:123]\n"
"ds_read_b64_tr_b16 a[66:67], v40 offset:512\n"
"v_mfma_f32_16x16x32_bf16 v[124:127], a[36:39], v[56:59], v[124:127]\n"
"ds_read_b64_tr_b16 a[68:69], v41 offset:0\n"
"v_mfma_f32_16x16x32_bf16 v[128:131], a[0:3], a[16:19], v[128:131]\n"
"global_load_lds_dwordx4 v80, s[38:39] offset:0\n"
"v_mfma_f32_16x16x32_bf16 v[144:147], a[0:3], a[20:23], v[144:147]\n"
"ds_read_b64_tr_b16 a[70:71], v41 offset:512\n"
"v_mfma_f32_16x16x32_bf16 v[160:163], a[0:3], a[24:27], v[160:163]\n"
"ds_read_b64_tr_b16 a[72:73], v42 offset:0\n"
"v_mfma_f32_16x16x32_bf16 v[176:179], a[0:3], a[28:31], v[176:179]\n"
"ds_read_b64_tr_b16 a[74:75], v42 offset:512\n"
"v_mfma_f32_16x16x32_bf16 v[192:195], a[32:35], a[0:3], v[192:195]\n"
"ds_read_b64_tr_b16 a[76:77], v43 offset:0\n"
"v_mfma_f32_16x16x32_bf16 v[208:211], a[36:39], a[0:3], v[208:211]\n"
"global_load_lds_dwordx4 v81, s[38:39] offset:1024\n"
"v_mfma_f32_16x16x32_bf16 v[224:227], a[40:43], a[0:3], v[224:227]\n"
"ds_read_b64_tr_b16 a[78:79], v43 offset:512\n"
"v_mfma_f32_16x16x32_bf16 v[240:243], a[44:47], a[0:3], v[240:243]\n"
"ds_read_b64_tr_b16 a[80:81], v48 offset:0\n"
"v_mfma_f32_16x16x32_bf16 v[132:135], a[4:7], a[16:19], v[132:135]\n"
"ds_read_b64_tr_b16 a[82:83], v48 offset:512\n"
"v_mfma_f32_16x16x32_bf16 v[148:151], a[4:7], a[20:23], v[148:151]\n"
"ds_read_b64_tr_b16 a[84:85], v49 offset:0\n"
"v_mfma_f32_16x16x32_bf16 v[164:167], a[4:7], a[24:27], v[164:167]\n"
"global_load_lds_dwordx4 v82, s[38:39] offset:2048\n"
"v_mfma_f32_16x16x32_bf16 v[180:183], a[4:7], a[28:31], v[180:183]\n"
"ds_read_b64_tr_b16 a[86:87], v49 offset:512\n"
"v_mfma_f32_16x16x32_bf16 v[196:199], a[32:35], a[4:7], v[196:199]\n"
"ds_read_b64_tr_b16 a[88:89], v50 offset:0\n"
"v_mfma_f32_16x16x32_bf16 v[212:215], a[36:39], a[4:7], v[212:215]\n"
"ds_read_b64_tr_b16 a[90:91], v50 offset:512\n"
"v_mfma_f32_16x16x32_bf16 v[228:231], a[40:43], a[4:7], v[228:231]\n"
"ds_read_b64_tr_b16 a[92:93], v51 offset:0\n"
"v_mfma_f32_16x16x32_bf16 v[244:247], a[44:47], a[4:7], v[244:247]\n"
"global_load_lds_dwordx4 v83, s[38:39] offset:3072\n"
"s_lshl_b32 s37, s36, 7\n"
"s_add_u32 s38, s80, s37\n"
"s_addc_u32 s39, s81, 0\n"
"s_add_u32 m0, s89, 0\n"
"v_mfma_f32_16x16x32_bf16 v[136:139], a[8:11], a[16:19], v[136:139]\n"
"ds_read_b64_tr_b16 a[94:95], v51 offset:512\n"
"v_mfma_f32_16x16x32_bf16 v[152:155], a[8:11], a[20:23], v[152:155]\n"
"ds_read_b128 a[52:55], v53 offset:2048\n"
"v_mfma_f32_16x16x32_bf16 v[168:171], a[8:11], a[24:27], v[168:171]\n"
"ds_read_b128 a[56:59], v53 offset:4096\n"
"v_mfma_f32_16x16x32_bf16 v[184:187], a[8:11], a[28:31], v[184:187]\n"
"ds_read_b128 a[60:63], v53 offset:6144\n"
"v_mfma_f32_16x16x32_bf16 v[200:203], a[32:35], a[8:11], v[200:203]\n"
"v_mfma_f32_16x16x32_bf16 v[216:219], a[36:39], a[8:11], v[216:219]\n"
"v_mfma_f32_16x16x32_bf16 v[232:235], a[40:43], a[8:11], v[232:235]\n"
"v_mfma_f32_16x16x32_bf16 v[248:251], a[44:47], a[8:11], v[248:251]\n"
"v_mfma_f32_16x16x32_bf16 v[140:143], a[12:15], a[16:19], v[140:143]\n"
"v_mfma_f32_16x16x32_bf16 v[156:159], a[12:15], a[20:23], v[156:159]\n"
"v_mfma_f32_16x16x32_bf16 v[172:175], a[12:15], a[24:27], v[172:175]\n"
"v_mfma_f32_16x16x32_bf16 v[188:191], a[12:15], a[28:31], v[188:191]\n"
"v_mfma_f32_16x16x32_bf16 v[204:207], a[32:35], a[12:15], v[204:207]\n"
"v_mfma_f32_16x16x32_bf16 v[220:223], a[36:39], a[12:15], v[220:223]\n"
"v_mfma_f32_16x16x32_bf16 v[236:239], a[40:43], a[12:15], v[236:239]\n"
"v_mfma_f32_16x16x32_bf16 v[252:255], a[44:47], a[12:15], v[252:255]\n"
"s_waitcnt vmcnt(19) lgkmcnt(0)\n"
"s_barrier\n"
"v_mfma_f32_16x16x32_bf16 v[128:131], a[48:51], a[64:67], v[128:131]\n"
"global_load_lds_dwordx4 v84, s[40:41] offset:0\n"
"v_mfma_f32_16x16x32_bf16 v[144:147], a[48:51], a[68:71], v[144:147]\n"
"s_waitcnt vmcnt(18)\n"
"ds_read_b128 a[0:3], v52 offset:8192\n"
"v_mfma_f32_16x16x32_bf16 v[160:163], a[48:51], a[72:75], v[160:163]\n"
"ds_read_b64_tr_b16 a[16:17], v36 offset:16384\n"
"v_mfma_f32_16x16x32_bf16 v[176:179], a[48:51], a[76:79], v[176:179]\n"
"ds_read_b64_tr_b16 a[18:19], v36 offset:16896\n"
"v_mfma_f32_16x16x32_bf16 v[192:195], a[80:83], a[48:51], v[192:195]\n"
"ds_read_b64_tr_b16 a[20:21], v37 offset:16384\n"
"v_mfma_f32_16x16x32_bf16 v[208:211], a[84:87], a[48:51], v[208:211]\n"
"global_load_lds_dwordx4 v85, s[40:41] offset:1024\n"
"s_add_u32 m0, s88, 0\n"
"v_mfma_f32_16x16x32_bf16 v[224:227], a[88:91], a[48:51], v[224:227]\n"
"ds_read_b64_tr_b16 a[22:23], v37 offset:16896\n"
"v_mfma_f32_16x16x32_bf16 v[240:243], a[92:95], a[48:51], v[240:243]\n"
"ds_read_b64_tr_b16 a[24:25], v38 offset:16384\n"
"v_mfma_f32_16x16x32_bf16 v[132:135], a[52:55], a[64:67], v[132:135]\n"
"ds_read_b64_tr_b16 a[26:27], v38 offset:16896\n"
"v_mfma_f32_16x16x32_bf16 v[148:151], a[52:55], a[68:71], v[148:151]\n"
"ds_read_b64_tr_b16 a[28:29], v39 offset:16384\n"
"v_mfma_f32_16x16x32_bf16 v[164:167], a[52:55], a[72:75], v[164:167]\n"
"global_load_lds_dwordx4 v76, s[38:39] offset:0\n"
"v_mfma_f32_16x16x32_bf16 v[180:183], a[52:55], a[76:79], v[180:183]\n"
"ds_read_b64_tr_b16 a[30:31], v39 offset:16896\n"
"v_mfma_f32_16x16x32_bf16 v[196:199], a[80:83], a[52:55], v[196:199]\n"
"v_mfma_f32_16x16x32_bf16 v[212:215], a[84:87], a[52:55], v[212:215]\n"
"v_mfma_f32_16x16x32_bf16 v[228:231], a[88:91], a[52:55], v[228:231]\n"
"v_mfma_f32_16x16x32_bf16 v[244:247], a[92:95], a[52:55], v[244:247]\n"
"global_load_lds_dwordx4 v77, s[38:39] offset:1024\n"
"v_mfma_f32_16x16x32_bf16 v[136:139], a[56:59], a[64:67], v[136:139]\n"
"v_mfma_f32_16x16x32_bf16 v[152:155], a[56:59], a[68:71], v[152:155]\n"
"v_mfma_f32_16x16x32_bf16 v[168:171], a[56:59], a[72:75], v[168:171]\n"
"v_mfma_f32_16x16x32_bf16 v[184:187], a[56:59], a[76:79], v[184:187]\n"
"v_mfma_f32_16x16x32_bf16 v[200:203], a[80:83], a[56:59], v[200:203]\n"
"global_load_lds_dwordx4 v78, s[38:39] offset:2048\n"
"v_mfma_f32_16x16x32_bf16 v[216:219], a[84:87], a[56:59], v[216:219]\n"
"9401:\n"
"s_cbranch_execz 9402f\n"
"v_mfma_f32_16x16x32_bf16 v[232:235], a[88:91], a[56:59], v[232:235]\n"
"v_mfma_f32_16x16x32_bf16 v[248:251], a[92:95], a[56:59], v[248:251]\n"
"v_mfma_f32_16x16x32_bf16 v[140:143], a[60:63], a[64:67], v[140:143]\n"
"v_mfma_f32_16x16x32_bf16 v[156:159], a[60:63], a[68:71], v[156:159]\n"
"global_load_lds_dwordx4 v79, s[38:39] offset:3072\n"
"s_add_u32 m0, s88, 4096\n"
"v_mfma_f32_16x16x32_bf16 v[172:175], a[60:63], a[72:75], v[172:175]\n"
"s_waitcnt vmcnt(21)\n"
"ds_read_b128 a[4:7], v52 offset:10240\n"
"v_mfma_f32_16x16x32_bf16 v[188:191], a[60:63], a[76:79], v[188:191]\n"
"s_waitcnt vmcnt(19)\n"
"ds_read_b128 a[8:11], v52 offset:12288\n"
"v_mfma_f32_16x16x32_bf16 v[204:207], a[80:83], a[60:63], v[204:207]\n"
"s_waitcnt vmcnt(17)\n"
"ds_read_b128 a[12:15], v52 offset:14336\n"
"v_mfma_f32_16x16x32_bf16 v[220:223], a[84:87], a[60:63], v[220:223]\n"
"v_mfma_f32_16x16x32_bf16 v[236:239], a[88:91], a[60:63], v[236:239]\n"
"global_load_lds_dwordx4 v80, s[38:39] offset:0\n"
"v_mfma_f32_16x16x32_bf16 v[252:255], a[92:95], a[60:63], v[252:255]\n"
"s_waitcnt lgkmcnt(0)\n"
"v_mfma_f32_16x16x32_bf16 v[112:115], a[16:19], v[56:59], v[112:115]\n"
"s_waitcnt vmcnt(11)\n"
"v_cmp_eq_u32 vcc, v109, v35\n"
"ds_read_b128 a[48:51], v53 offset:8192\n"
"v_mfma_f32_16x16x32_bf16 v[116:119], a[20:23], v[56:59], v[116:119]\n"
"ds_read_b64_tr_b16 a[64:65], v40 offset:16384\n"
"ds_read_b64_tr_b16 a[66:67], v40 offset:16896\n"
"v_mfma_f32_16x16x32_bf16 v[128:131], a[0:3], a[16:19], v[128:131]\n"
"ds_read_b64_tr_b16 a[68:69], v41 offset:16384\n"
"v_mfma_f32_16x16x32_bf16 v[144:147], a[0:3], a[20:23], v[144:147]\n"
"s_bitcmp1_b32 vcc_hi, 31\n"
"s_cbranch_scc1 8f\n"
"9:\n"
"global_load_lds_dwordx4 v81, s[38:39] offset:1024\n"
"v_mfma_f32_16x16x32_bf16 v[160:163], a[0:3], a[24:27], v[160:163]\n"
"ds_read_b64_tr_b16 a[70:71], v41 offset:16896\n"
"v_mfma_f32_16x16x32_bf16 v[176:179], a[0:3], a[28:31], v[176:179]\n"
"ds_read_b64_tr_b16 a[72:73], v42 offset:16384\n"
"ds_read_b64_tr_b16 a[74:75], v42 offset:16896\n"
"v_mfma_f32_16x16x32_bf16 v[132:135], a[4:7], a[16:19], v[132:135]\n"
"ds_read_b64_tr_b16 a[76:77], v43 offset:16384\n"
"v_mfma_f32_16x16x32_bf16 v[148:151], a[4:7], a[20:23], v[148:151]\n"
"ds_read_b64_tr_b16 a[78:79], v43 offset:16896\n"
"v_mfma_f32_16x16x32_bf16 v[164:167], a[4:7], a[24:27], v[164:167]\n"
"global_load_lds_dwordx4 v82, s[38:39] offset:2048\n"
"v_mfma_f32_16x16x32_bf16 v[180:183], a[4:7], a[28:31], v[180:183]\n"
"ds_read_b128 a[52:55], v53 offset:10240\n"
"ds_read_b128 a[56:59], v53 offset:12288\n"
"v_mfma_f32_16x16x32_bf16 v[136:139], a[8:11], a[16:19], v[136:139]\n"
"ds_read_b128 a[60:63], v53 offset:14336\n"
"v_mfma_f32_16x16x32_bf16 v[152:155], a[8:11], a[20:23], v[152:155]\n"
"v_mfma_f32_16x16x32_bf16 v[168:171], a[8:11], a[24:27], v[168:171]\n"
"v_mfma_f32_16x16x32_bf16 v[184:187], a[8:11], a[28:31], v[184:187]\n"
"global_load_lds_dwordx4 v83, s[38:39] offset:3072\n"
"s_add_u32 m0, s89, 34816\n"
"v_mfma_f32_16x16x32_bf16 v[140:143], a[12:15], a[16:19], v[140:143]\n"
"v_mfma_f32_16x16x32_bf16 v[156:159], a[12:15], a[20:23], v[156:159]\n"
"v_mfma_f32_16x16x32_bf16 v[172:175], a[12:15], a[24:27], v[172:175]\n"
"v_mfma_f32_16x16x32_bf16 v[188:191], a[12:15], a[28:31], v[188:191]\n"
"s_waitcnt lgkmcnt(0)\n"
"s_barrier\n"
"v_mfma_f32_16x16x32_bf16 v[128:131], a[48:51], a[64:67], v[128:131]\n"
"global_load_lds_dwordx4 v86, s[44:45] offset:0\n"
"v_mfma_f32_16x16x32_bf16 v[144:147], a[48:51], a[68:71], v[144:147]\n"
"ds_read_b128 a[0:3], v52 offset:16384\n"
"ds_read_b64_tr_b16 a[16:17], v36 offset:32768\n"
"v_mfma_f32_16x16x32_bf16 v[160:163], a[48:51], a[72:75], v[160:163]\n"
"ds_read_b64_tr_b16 a[18:19], v36 offset:33280\n"
"v_mfma_f32_16x16x32_bf16 v[176:179], a[48:51], a[76:79], v[176:179]\n"
"ds_read_b64_tr_b16 a[20:21], v37 offset:32768\n"
"v_mfma_f32_16x16x32_bf16 v[132:135], a[52:55], a[64:67], v[132:135]\n"
"ds_read_b64_tr_b16 a[22:23], v37 offset:33280\n"
"v_mfma_f32_16x16x32_bf16 v[148:151], a[52:55], a[68:71], v[148:151]\n"
"global_load_lds_dwordx4 v87, s[44:45] offset:1024\n"
"s_lshl_b32 m0, s30, 10\n"
"s_add_u32 m0, m0, 147456\n"
"v_mfma_f32_16x16x32_bf16 v[164:167], a[52:55], a[72:75], v[164:167]\n"
"ds_read_b64_tr_b16 a[24:25], v38 offset:32768\n"
"v_mfma_f32_16x16x32_bf16 v[180:183], a[52:55], a[76:79], v[180:183]\n"
"ds_read_b64_tr_b16 a[26:27], v38 offset:33280\n"
"v_mfma_f32_16x16x32_bf16 v[136:139], a[56:59], a[64:67], v[136:139]\n"
"ds_read_b64_tr_b16 a[28:29], v39 offset:32768\n"
"v_mfma_f32_16x16x32_bf16 v[152:155], a[56:59], a[68:71], v[152:155]\n"
"ds_read_b64_tr_b16 a[30:31], v39 offset:33280\n"
"ds_read_b128 a[4:7], v52 offset:18432\n"
"v_mfma_f32_16x16x32_bf16 v[168:171], a[56:59], a[72:75], v[168:171]\n"
"global_load_lds_dwordx4 v9, s[2:3] sc1\n"
"s_add_u32 m0, s89, 2048\n"
"v_mfma_f32_16x16x32_bf16 v[184:187], a[56:59], a[76:79], v[184:187]\n"
"s_waitcnt vmcnt(15)\n"
"ds_read_b128 a[8:11], v52 offset:20480\n"
"v_mfma_f32_16x16x32_bf16 v[140:143], a[60:63], a[64:67], v[140:143]\n"
"s_waitcnt vmcnt(13)\n"
"ds_read_b128 a[12:15], v52 offset:22528\n"
"v_mfma_f32_16x16x32_bf16 v[156:159], a[60:63], a[68:71], v[156:159]\n"
"v_mfma_f32_16x16x32_bf16 v[172:175], a[60:63], a[72:75], v[172:175]\n"
"v_mfma_f32_16x16x32_bf16 v[188:191], a[60:63], a[76:79], v[188:191]\n"
"global_load_lds_dwordx4 v86, s[48:49] offset:0\n"
"s_waitcnt lgkmcnt(0)\n"
"v_mfma_f32_16x16x32_bf16 v[112:115], a[16:19], v[56:59], v[112:115]\n"
"ds_read_b128 a[48:51], v53 offset:16384\n"
"v_mfma_f32_16x16x32_bf16 v[116:119], a[20:23], v[56:59], v[116:119]\n"
"ds_read_b64_tr_b16 a[64:65], v40 offset:32768\n"
"ds_read_b64_tr_b16 a[66:67], v40 offset:33280\n"
"v_mfma_f32_16x16x32_bf16 v[128:131], a[0:3], a[16:19], v[128:131]\n"
"ds_read_b64_tr_b16 a[68:69], v41 offset:32768\n"
"v_mfma_f32_16x16x32_bf16 v[144:147], a[0:3], a[20:23], v[144:147]\n"
"ds_read_b64_tr_b16 a[70:71], v41 offset:33280\n"
"v_mfma_f32_16x16x32_bf16 v[160:163], a[0:3], a[24:27], v[160:163]\n"
"global_load_lds_dwordx4 v87, s[48:49] offset:1024\n"
"v_mfma_f32_16x16x32_bf16 v[176:179], a[0:3], a[28:31], v[176:179]\n"
"ds_read_b64_tr_b16 a[72:73], v42 offset:32768\n"
"ds_read_b64_tr_b16 a[74:75], v42 offset:33280\n"
"v_mfma_f32_16x16x32_bf16 v[132:135], a[4:7], a[16:19], v[132:135]\n"
"ds_read_b64_tr_b16 a[76:77], v43 offset:32768\n"
"v_mfma_f32_16x16x32_bf16 v[148:151], a[4:7], a[20:23], v[148:151]\n"
"ds_read_b64_tr_b16 a[78:79], v43 offset:33280\n"
"v_mfma_f32_16x16x32_bf16 v[164:167], a[4:7], a[24:27], v[164:167]\n"
"ds_read_b128 a[52:55], v53 offset:18432\n"
"v_mfma_f32_16x16x32_bf16 v[180:183], a[4:7], a[28:31], v[180:183]\n"
"global_load_dwordx2 a[128:129], v71, s[10:11] offset:0\n"
"v_mfma_f32_16x16x32_bf16 v[136:139], a[8:11], a[16:19], v[136:139]\n"
"global_load_dwordx2 a[130:131], v71, s[10:11] offset:32\n"
"v_mfma_f32_16x16x32_bf16 v[152:155], a[8:11], a[20:23], v[152:155]\n"
"ds_read_b128 a[56:59], v53 offset:20480\n"
"v_mfma_f32_16x16x32_bf16 v[168:171], a[8:11], a[24:27], v[168:171]\n"
"global_load_dwordx2 a[132:133], v72, s[10:11] offset:0\n"
"v_mfma_f32_16x16x32_bf16 v[184:187], a[8:11], a[28:31], v[184:187]\n"
"global_load_dwordx2 a[134:135], v72, s[10:11] offset:32\n"
"v_mfma_f32_16x16x32_bf16 v[140:143], a[12:15], a[16:19], v[140:143]\n"
"ds_read_b128 a[60:63], v53 offset:22528\n"
"v_mfma_f32_16x16x32_bf16 v[156:159], a[12:15], a[20:23], v[156:159]\n"
"global_load_dword v34, v14, s[66:67] offset:0 sc1\n"
"v_mfma_f32_16x16x32_bf16 v[172:175], a[12:15], a[24:27], v[172:175]\n"
"v_mfma_f32_16x16x32_bf16 v[188:191], a[12:15], a[28:31], v[188:191]\n"
"s_waitcnt vmcnt(18) lgkmcnt(0)\n"
"s_barrier\n"
"v_mfma_f32_16x16x32_bf16 v[128:131], a[48:51], a[64:67], v[128:131]\n"
"s_waitcnt vmcnt(16)\n"
"9402:\n"
"s_cbranch_execz 9403f\n"
"ds_read_b128 a[0:3], v52 offset:0\n"
"v_mfma_f32_16x16x32_bf16 v[144:147], a[48:51], a[68:71], v[144:147]\n"
"ds_read_b64_tr_b16 a[16:17], v36 offset:0\n"
"ds_read_b64_tr_b16 a[18:19], v36 offset:512\n"
"v_mfma_f32_16x16x32_bf16 v[160:163], a[48:51], a[72:75], v[160:163]\n"
"global_load_dword v33, v16, s[66:67] offset:0 sc1\n"
"v_mfma_f32_16x16x32_bf16 v[176:179], a[48:51], a[76:79], v[176:179]\n"
"ds_read_b64_tr_b16 a[20:21], v37 offset:0\n"
"v_mfma_f32_16x16x32_bf16 v[132:135], a[52:55], a[64:67], v[132:135]\n"
"ds_read_b64_tr_b16 a[22:23], v37 offset:512\n"
"v_mfma_f32_16x16x32_bf16 v[148:151], a[52:55], a[68:71], v[148:151]\n"
"ds_read_b64_tr_b16 a[24:25], v38 offset:0\n"
"ds_read_b64_tr_b16 a[26:27], v38 offset:512\n"
"v_mfma_f32_16x16x32_bf16 v[164:167], a[52:55], a[72:75], v[164:167]\n"
"ds_read_b64_tr_b16 a[28:29], v39 offset:0\n"
"v_mfma_f32_16x16x32_bf16 v[180:183], a[52:55], a[76:79], v[180:183]\n"
"ds_read_b64_tr_b16 a[30:31], v39 offset:512\n"
"v_mfma_f32_16x16x32_bf16 v[136:139], a[56:59], a[64:67], v[136:139]\n"
"v_mfma_f32_16x16x32_bf16 v[152:155], a[56:59], a[68:71], v[152:155]\n"
"s_waitcnt vmcnt(15)\n"
"ds_read_b128 a[4:7], v52 offset:2048\n"
"v_mfma_f32_16x16x32_bf16 v[168:171], a[56:59], a[72:75], v[168:171]\n"
"s_waitcnt vmcnt(13)\n"
"ds_read_b128 a[8:11], v52 offset:4096\n"
"v_mfma_f32_16x16x32_bf16 v[184:187], a[56:59], a[76:79], v[184:187]\n"
"s_waitcnt vmcnt(11)\n"
"ds_read_b128 a[12:15], v52 offset:6144\n"
"v_mfma_f32_16x16x32_bf16 v[140:143], a[60:63], a[64:67], v[140:143]\n"
"v_mfma_f32_16x16x32_bf16 v[156:159], a[60:63], a[68:71], v[156:159]\n"
"v_mfma_f32_16x16x32_bf16 v[172:175], a[60:63], a[72:75], v[172:175]\n"
"v_mfma_f32_16x16x32_bf16 v[188:191], a[60:63], a[76:79], v[188:191]\n"
"s_waitcnt lgkmcnt(0)\n"
"v_mfma_f32_16x16x32_bf16 v[112:115], a[16:19], v[56:59], v[112:115]\n"
"ds_read_b128 a[48:51], v53 offset:0\n"
"v_mfma_f32_16x16x32_bf16 v[116:119], a[20:23], v[56:59], v[116:119]\n"
"ds_read_b64_tr_b16 a[64:65], v40 offset:0\n"
"ds_read_b64_tr_b16 a[66:67], v40 offset:512\n"
"v_mfma_f32_16x16x32_bf16 v[128:131], a[0:3], a[16:19], v[128:131]\n"
"ds_read_b64_tr_b16 a[68:69], v41 offset:0\n"
"v_mfma_f32_16x16x32_bf16 v[144:147], a[0:3], a[20:23], v[144:147]\n"
"ds_read_b64_tr_b16 a[70:71], v41 offset:512\n"
"v_mfma_f32_16x16x32_bf16 v[160:163], a[0:3], a[24:27], v[160:163]\n"
"ds_read_b64_tr_b16 a[72:73], v42 offset:0\n"
"v_mfma_f32_16x16x32_bf16 v[176:179], a[0:3], a[28:31], v[176:179]\n"
"ds_read_b64_tr_b16 a[74:75], v42 offset:512\n"
"ds_read_b64_tr_b16 a[76:77], v43 offset:0\n"
"v_mfma_f32_16x16x32_bf16 v[132:135], a[4:7], a[16:19], v[132:135]\n"
"ds_read_b64_tr_b16 a[78:79], v43 offset:512\n"
"v_mfma_f32_16x16x32_bf16 v[148:151], a[4:7], a[20:23], v[148:151]\n"
"ds_read_b128 a[52:55], v53 offset:2048\n"
"v_mfma_f32_16x16x32_bf16 v[164:167], a[4:7], a[24:27], v[164:167]\n"
"ds_read_b128 a[56:59], v53 offset:4096\n"
"v_mfma_f32_16x16x32_bf16 v[180:183], a[4:7], a[28:31], v[180:183]\n"
"ds_read_b128 a[60:63], v53 offset:6144\n"
"v_mfma_f32_16x16x32_bf16 v[136:139], a[8:11], a[16:19], v[136:139]\n"
"s_mov_b32 exec_lo, 0x10001\n"
"s_mov_b32 exec_hi, 0x10001\n"
"ds_write_b128 v66, v[112:115] offset:0\n"
"ds_write_b128 v66, v[116:119] offset:64\n"
"s_mov_b64 exec, -1\n"
"v_mfma_f32_16x16x32_bf16 v[152:155], a[8:11], a[20:23], v[152:155]\n"
"v_mfma_f32_16x16x32_bf16 v[168:171], a[8:11], a[24:27], v[168:171]\n"
"v_mfma_f32_16x16x32_bf16 v[184:187], a[8:11], a[28:31], v[184:187]\n"
"v_mfma_f32_16x16x32_bf16 v[140:143], a[12:15], a[16:19], v[140:143]\n"
"v_mfma_f32_16x16x32_bf16 v[156:159], a[12:15], a[20:23], v[156:159]\n"
"v_mfma_f32_16x16x32_bf16 v[172:175], a[12:15], a[24:27], v[172:175]\n"
"v_mfma_f32_16x16x32_bf16 v[188:191], a[12:15], a[28:31], v[188:191]\n"
"s_waitcnt vmcnt(8) lgkmcnt(0)\n"
"s_barrier\n"
"v_mfma_f32_16x16x32_bf16 v[128:131], a[48:51], a[64:67], v[128:131]\n"
"ds_read_b128 a[0:3], v52 offset:8192\n"
"s_mov_b64 s[36:37], s[12:13]\n"
"s_mov_b64 s[38:39], s[14:15]\n"
"v_mfma_f32_16x16x32_bf16 v[144:147], a[48:51], a[68:71], v[144:147]\n"
"ds_read_b64_tr_b16 a[32:33], v44 offset:16384\n"
"ds_read_b64_tr_b16 a[34:35], v44 offset:16896\n"
"v_mfma_f32_16x16x32_bf16 v[160:163], a[48:51], a[72:75], v[160:163]\n"
"ds_read_b64_tr_b16 a[36:37], v45 offset:16384\n"
"s_mov_b64 s[40:41], s[16:17]\n"
"s_mov_b64 s[42:43], s[18:19]\n"
"v_mfma_f32_16x16x32_bf16 v[176:179], a[48:51], a[76:79], v[176:179]\n"
"ds_read_b64_tr_b16 a[38:39], v45 offset:16896\n"
"s_mov_b64 s[44:45], s[22:23]\n"
"v_mfma_f32_16x16x32_bf16 v[132:135], a[52:55], a[64:67], v[132:135]\n"
"ds_read_b64_tr_b16 a[40:41], v46 offset:16384\n"
"s_cmp_eq_u32 s30, 0\n"
"s_cselect_b64 s[50:51], 4, 0\n"
"v_mfma_f32_16x16x32_bf16 v[148:151], a[52:55], a[68:71], v[148:151]\n"
"ds_read_b64_tr_b16 a[42:43], v46 offset:16896\n"
"ds_read_b64_tr_b16 a[44:45], v47 offset:16384\n"
"v_mfma_f32_16x16x32_bf16 v[164:167], a[52:55], a[72:75], v[164:167]\n"
"ds_read_b64_tr_b16 a[46:47], v47 offset:16896\n"
"s_mov_b32 s48, 0xfffff\n"
"s_mov_b32 s49, 0xfffff\n"
"v_mfma_f32_16x16x32_bf16 v[180:183], a[52:55], a[76:79], v[180:183]\n"
"ds_read_b128 a[4:7], v52 offset:10240\n"
"s_mov_b64 s[52:53], -1\n"
"v_mfma_f32_16x16x32_bf16 v[136:139], a[56:59], a[64:67], v[136:139]\n"
"ds_read_b128 a[8:11], v52 offset:12288\n"
"v_mfma_f32_16x16x32_bf16 v[152:155], a[56:59], a[68:71], v[152:155]\n"
"ds_read_b128 a[12:15], v52 offset:14336\n"
"ds_read2_b32 v[36:37], v69 offset0:0 offset1:64\n"
"v_mfma_f32_16x16x32_bf16 v[168:171], a[56:59], a[72:75], v[168:171]\n"
"ds_read2_b32 v[38:39], v69 offset0:16 offset1:80\n"
"ds_read2_b32 v[40:41], v70 offset0:0 offset1:64\n"
"v_mfma_f32_16x16x32_bf16 v[184:187], a[56:59], a[76:79], v[184:187]\n"
"ds_read2_b32 v[42:43], v70 offset0:16 offset1:80\n"
"v_mfma_f32_16x16x32_bf16 v[140:143], a[60:63], a[64:67], v[140:143]\n"
"ds_read_b128 v[76:79], v65 offset:0\n"
"v_mfma_f32_16x16x32_bf16 v[156:159], a[60:63], a[68:71], v[156:159]\n"
"ds_read_b128 v[92:95], v65 offset:256\n"
"v_mfma_f32_16x16x32_bf16 v[172:175], a[60:63], a[72:75], v[172:175]\n"
"ds_read_b128 v[80:83], v65 offset:64\n"
"v_mfma_f32_16x16x32_bf16 v[188:191], a[60:63], a[76:79], v[188:191]\n"
"ds_read_b128 v[96:99], v65 offset:320\n"
"s_waitcnt lgkmcnt(0)\n"
"v_mfma_f32_16x16x32_bf16 v[120:123], a[32:35], v[56:59], v[120:123]\n"
"ds_read_b128 a[48:51], v53 offset:8192\n"
"v_mfma_f32_16x16x32_bf16 v[124:127], a[36:39], v[56:59], v[124:127]\n"
"ds_read_b64_tr_b16 a[80:81], v48 offset:16384\n"
"ds_read_b64_tr_b16 a[82:83], v48 offset:16896\n"
"v_mfma_f32_16x16x32_bf16 v[192:195], a[32:35], a[0:3], v[192:195]\n"
"ds_read_b64_tr_b16 a[84:85], v49 offset:16384\n"
"v_mfma_f32_16x16x32_bf16 v[208:211], a[36:39], a[0:3], v[208:211]\n"
"ds_read_b64_tr_b16 a[86:87], v49 offset:16896\n"
"v_mfma_f32_16x16x32_bf16 v[224:227], a[40:43], a[0:3], v[224:227]\n"
"ds_read_b64_tr_b16 a[88:89], v50 offset:16384\n"
"v_mfma_f32_16x16x32_bf16 v[240:243], a[44:47], a[0:3], v[240:243]\n"
"ds_read_b64_tr_b16 a[90:91], v50 offset:16896\n"
"ds_read_b64_tr_b16 a[92:93], v51 offset:16384\n"
"v_mfma_f32_16x16x32_bf16 v[196:199], a[32:35], a[4:7], v[196:199]\n"
"ds_read_b64_tr_b16 a[94:95], v51 offset:16896\n"
"v_mfma_f32_16x16x32_bf16 v[212:215], a[36:39], a[4:7], v[212:215]\n"
"ds_read_b128 a[52:55], v53 offset:10240\n"
"v_mfma_f32_16x16x32_bf16 v[228:231], a[40:43], a[4:7], v[228:231]\n"
"ds_read_b128 a[56:59], v53 offset:12288\n"
"v_mfma_f32_16x16x32_bf16 v[244:247], a[44:47], a[4:7], v[244:247]\n"
"ds_read_b128 a[60:63], v53 offset:14336\n"
"9403:\n"
"s_cbranch_execz 97b\n"
"v_mfma_f32_16x16x32_bf16 v[200:203], a[32:35], a[8:11], v[200:203]\n"
"ds_read_b128 v[84:87], v65 offset:128\n"
"v_mfma_f32_16x16x32_bf16 v[216:219], a[36:39], a[8:11], v[216:219]\n"
"ds_read_b128 v[100:103], v65 offset:384\n"
"v_mfma_f32_16x16x32_bf16 v[232:235], a[40:43], a[8:11], v[232:235]\n"
"ds_read_b128 v[88:91], v65 offset:192\n"
"v_mfma_f32_16x16x32_bf16 v[248:251], a[44:47], a[8:11], v[248:251]\n"
"ds_read_b128 v[104:107], v65 offset:448\n"
"v_add_f32 v36, v36, v37\n"
"v_mfma_f32_16x16x32_bf16 v[204:207], a[32:35], a[12:15], v[204:207]\n"
"v_add_f32 v38, v38, v39\n"
"v_add_f32 v40, v40, v41\n"
"v_mfma_f32_16x16x32_bf16 v[220:223], a[36:39], a[12:15], v[220:223]\n"
"v_add_f32 v42, v42, v43\n"
"v_mfma_f32_16x16x32_bf16 v[236:239], a[40:43], a[12:15], v[236:239]\n"
"v_mfma_f32_16x16x32_bf16 v[252:255], a[44:47], a[12:15], v[252:255]\n"
"s_waitcnt vmcnt(6) lgkmcnt(0)\n"
"s_barrier\n"
"v_pk_fma_f32 v[128:129], v[76:77], v[36:37], v[128:129] op_sel_hi:[1,0,1] neg_lo:[0,1,0] neg_hi:[0,1,0]\n"
"v_pk_fma_f32 v[130:131], v[78:79], v[36:37], v[130:131] op_sel_hi:[1,0,1] neg_lo:[0,1,0] neg_hi:[0,1,0]\n"
"v_pk_fma_f32 v[132:133], v[80:81], v[36:37], v[132:133] op_sel_hi:[1,0,1] neg_lo:[0,1,0] neg_hi:[0,1,0]\n"
"v_pk_fma_f32 v[134:135], v[82:83], v[36:37], v[134:135] op_sel_hi:[1,0,1] neg_lo:[0,1,0] neg_hi:[0,1,0]\n"
"v_pk_fma_f32 v[144:145], v[76:77], v[38:39], v[144:145] op_sel_hi:[1,0,1] neg_lo:[0,1,0] neg_hi:[0,1,0]\n"
"v_pk_fma_f32 v[146:147], v[78:79], v[38:39], v[146:147] op_sel_hi:[1,0,1] neg_lo:[0,1,0] neg_hi:[0,1,0]\n"
"v_pk_fma_f32 v[148:149], v[80:81], v[38:39], v[148:149] op_sel_hi:[1,0,1] neg_lo:[0,1,0] neg_hi:[0,1,0]\n"
"v_pk_fma_f32 v[150:151], v[82:83], v[38:39], v[150:151] op_sel_hi:[1,0,1] neg_lo:[0,1,0] neg_hi:[0,1,0]\n"
"v_pk_mul_f32 v[128:129], v[128:129], v[92:93]\n"
"v_pk_mul_f32 v[130:131], v[130:131], v[94:95]\n"
"v_pk_mul_f32 v[132:133], v[132:133], v[96:97]\n"
"v_pk_mul_f32 v[134:135], v[134:135], v[98:99]\n"
"v_pk_mul_f32 v[144:145], v[144:145], v[92:93]\n"
"v_pk_mul_f32 v[146:147], v[146:147], v[94:95]\n"
"v_pk_mul_f32 v[148:149], v[148:149], v[96:97]\n"
"v_pk_mul_f32 v[150:151], v[150:151], v[98:99]\n"
"v_cvt_pk_bf16_f32 v128, v128, v129\n"
"v_cvt_pk_bf16_f32 v129, v130, v131\n"
"v_cvt_pk_bf16_f32 v130, v132, v133\n"
"v_cvt_pk_bf16_f32 v131, v134, v135\n"
"v_cvt_pk_bf16_f32 v144, v144, v145\n"
"v_cvt_pk_bf16_f32 v145, v146, v147\n"
"v_cvt_pk_bf16_f32 v146, v148, v149\n"
"v_cvt_pk_bf16_f32 v147, v150, v151\n"
"global_store_dwordx4 v60, v[128:131], s[6:7] sc1\n"
"global_store_dwordx4 v61, v[144:147], s[6:7] sc1\n"
"v_pk_fma_f32 v[136:137], v[84:85], v[36:37], v[136:137] op_sel_hi:[1,0,1] neg_lo:[0,1,0] neg_hi:[0,1,0]\n"
"v_pk_fma_f32 v[138:139], v[86:87], v[36:37], v[138:139] op_sel_hi:[1,0,1] neg_lo:[0,1,0] neg_hi:[0,1,0]\n"
"v_pk_fma_f32 v[140:141], v[88:89], v[36:37], v[140:141] op_sel_hi:[1,0,1] neg_lo:[0,1,0] neg_hi:[0,1,0]\n"
"v_pk_fma_f32 v[142:143], v[90:91], v[36:37], v[142:143] op_sel_hi:[1,0,1] neg_lo:[0,1,0] neg_hi:[0,1,0]\n"
"v_pk_fma_f32 v[152:153], v[84:85], v[38:39], v[152:153] op_sel_hi:[1,0,1] neg_lo:[0,1,0] neg_hi:[0,1,0]\n"
"v_pk_fma_f32 v[154:155], v[86:87], v[38:39], v[154:155] op_sel_hi:[1,0,1] neg_lo:[0,1,0] neg_hi:[0,1,0]\n"
"v_pk_fma_f32 v[156:157], v[88:89], v[38:39], v[156:157] op_sel_hi:[1,0,1] neg_lo:[0,1,0] neg_hi:[0,1,0]\n"
"v_pk_fma_f32 v[158:159], v[90:91], v[38:39], v[158:159] op_sel_hi:[1,0,1] neg_lo:[0,1,0] neg_hi:[0,1,0]\n"
"v_pk_mul_f32 v[136:137], v[136:137], v[100:101]\n"
"v_pk_mul_f32 v[138:139], v[138:139], v[102:103]\n"
"v_pk_mul_f32 v[140:141], v[140:141], v[104:105]\n"
"v_pk_mul_f32 v[142:143], v[142:143], v[106:107]\n"
"v_pk_mul_f32 v[152:153], v[152:153], v[100:101]\n"
"v_pk_mul_f32 v[154:155], v[154:155], v[102:103]\n"
"v_pk_mul_f32 v[156:157], v[156:157], v[104:105]\n"
"v_pk_mul_f32 v[158:159], v[158:159], v[106:107]\n"
"v_cvt_pk_bf16_f32 v136, v136, v137\n"
"v_cvt_pk_bf16_f32 v137, v138, v139\n"
"v_cvt_pk_bf16_f32 v138, v140, v141\n"
"v_cvt_pk_bf16_f32 v139, v142, v143\n"
"v_cvt_pk_bf16_f32 v152, v152, v153\n"
"v_cvt_pk_bf16_f32 v153, v154, v155\n"
"v_cvt_pk_bf16_f32 v154, v156, v157\n"
"v_cvt_pk_bf16_f32 v155, v158, v159\n"
"global_store_dwordx4 v60, v[136:139], s[8:9] sc1\n"
"global_store_dwordx4 v61, v[152:155], s[8:9] sc1\n"
"v_pk_fma_f32 v[160:161], v[76:77], v[40:41], v[160:161] op_sel_hi:[1,0,1] neg_lo:[0,1,0] neg_hi:[0,1,0]\n"
"v_pk_fma_f32 v[162:163], v[78:79], v[40:41], v[162:163] op_sel_hi:[1,0,1] neg_lo:[0,1,0] neg_hi:[0,1,0]\n"
"v_pk_fma_f32 v[164:165], v[80:81], v[40:41], v[164:165] op_sel_hi:[1,0,1] neg_lo:[0,1,0] neg_hi:[0,1,0]\n"
"v_pk_fma_f32 v[166:167], v[82:83], v[40:41], v[166:167] op_sel_hi:[1,0,1] neg_lo:[0,1,0] neg_hi:[0,1,0]\n"
"v_pk_fma_f32 v[176:177], v[76:77], v[42:43], v[176:177] op_sel_hi:[1,0,1] neg_lo:[0,1,0] neg_hi:[0,1,0]\n"
"v_pk_fma_f32 v[178:179], v[78:79], v[42:43], v[178:179] op_sel_hi:[1,0,1] neg_lo:[0,1,0] neg_hi:[0,1,0]\n"
"v_pk_fma_f32 v[180:181], v[80:81], v[42:43], v[180:181] op_sel_hi:[1,0,1] neg_lo:[0,1,0] neg_hi:[0,1,0]\n"
"v_pk_fma_f32 v[182:183], v[82:83], v[42:43], v[182:183] op_sel_hi:[1,0,1] neg_lo:[0,1,0] neg_hi:[0,1,0]\n"
"v_pk_mul_f32 v[160:161], v[160:161], v[92:93]\n"
"v_pk_mul_f32 v[162:163], v[162:163], v[94:95]\n"
"v_pk_mul_f32 v[164:165], v[164:165], v[96:97]\n"
"v_pk_mul_f32 v[166:167], v[166:167], v[98:99]\n"
"v_pk_mul_f32 v[176:177], v[176:177], v[92:93]\n"
"v_pk_mul_f32 v[178:179], v[178:179], v[94:95]\n"
"v_pk_mul_f32 v[180:181], v[180:181], v[96:97]\n"
"v_pk_mul_f32 v[182:183], v[182:183], v[98:99]\n"
"v_cvt_pk_bf16_f32 v128, v160, v161\n"
"v_cvt_pk_bf16_f32 v129, v162, v163\n"
"v_cvt_pk_bf16_f32 v130, v164, v165\n"
"v_cvt_pk_bf16_f32 v131, v166, v167\n"
"v_cvt_pk_bf16_f32 v132, v176, v177\n"
"v_cvt_pk_bf16_f32 v133, v178, v179\n"
"v_cvt_pk_bf16_f32 v134, v180, v181\n"
"v_cvt_pk_bf16_f32 v135, v182, v183\n"
"global_store_dwordx4 v62, v[128:131], s[6:7] sc1\n"
"global_store_dwordx4 v63, v[132:135], s[6:7] sc1\n"
"v_pk_fma_f32 v[168:169], v[84:85], v[40:41], v[168:169] op_sel_hi:[1,0,1] neg_lo:[0,1,0] neg_hi:[0,1,0]\n"
"v_pk_fma_f32 v[170:171], v[86:87], v[40:41], v[170:171] op_sel_hi:[1,0,1] neg_lo:[0,1,0] neg_hi:[0,1,0]\n"
"v_pk_fma_f32 v[172:173], v[88:89], v[40:41], v[172:173] op_sel_hi:[1,0,1] neg_lo:[0,1,0] neg_hi:[0,1,0]\n"
"v_pk_fma_f32 v[174:175], v[90:91], v[40:41], v[174:175] op_sel_hi:[1,0,1] neg_lo:[0,1,0] neg_hi:[0,1,0]\n"
"v_pk_fma_f32 v[184:185], v[84:85], v[42:43], v[184:185] op_sel_hi:[1,0,1] neg_lo:[0,1,0] neg_hi:[0,1,0]\n"
"v_pk_fma_f32 v[186:187], v[86:87], v[42:43], v[186:187] op_sel_hi:[1,0,1] neg_lo:[0,1,0] neg_hi:[0,1,0]\n"
"v_pk_fma_f32 v[188:189], v[88:89], v[42:43], v[188:189] op_sel_hi:[1,0,1] neg_lo:[0,1,0] neg_hi:[0,1,0]\n"
"v_pk_fma_f32 v[190:191], v[90:91], v[42:43], v[190:191] op_sel_hi:[1,0,1] neg_lo:[0,1,0] neg_hi:[0,1,0]\n"
"v_pk_mul_f32 v[168:169], v[168:169], v[100:101]\n"
"v_pk_mul_f32 v[170:171], v[170:171], v[102:103]\n"
"v_pk_mul_f32 v[172:173], v[172:173], v[104:105]\n"
"v_pk_mul_f32 v[174:175], v[174:175], v[106:107]\n"
"v_pk_mul_f32 v[184:185], v[184:185], v[100:101]\n"
"v_pk_mul_f32 v[186:187], v[186:187], v[102:103]\n"
"v_pk_mul_f32 v[188:189], v[188:189], v[104:105]\n"
"v_pk_mul_f32 v[190:191], v[190:191], v[106:107]\n"
"v_cvt_pk_bf16_f32 v136, v168, v169\n"
"v_cvt_pk_bf16_f32 v137, v170, v171\n"
"v_cvt_pk_bf16_f32 v138, v172, v173\n"
"v_cvt_pk_bf16_f32 v139, v174, v175\n"
"v_cvt_pk_bf16_f32 v140, v184, v185\n"
"v_cvt_pk_bf16_f32 v141, v186, v187\n"
"v_cvt_pk_bf16_f32 v142, v188, v189\n"
"v_cvt_pk_bf16_f32 v143, v190, v191\n"
"global_store_dwordx4 v62, v[136:139], s[8:9] sc1\n"
"global_store_dwordx4 v63, v[140:143], s[8:9] sc1\n"
"5:\n"
"9000:\n"
"s_cbranch_execz 9001f\n"
"v_mfma_f32_16x16x32_bf16 v[192:195], a[80:83], a[48:51], v[192:195]\n"
"ds_read_b128 a[0:3], v52 offset:16384\n"
"v_mov_b32 v110, 1\n"
"v_mfma_f32_16x16x32_bf16 v[208:211], a[84:87], a[48:51], v[208:211]\n"
"ds_read_b64_tr_b16 a[32:33], v44 offset:32768\n"
"ds_read_b64_tr_b16 a[34:35], v44 offset:33280\n"
"v_mfma_f32_16x16x32_bf16 v[224:227], a[88:91], a[48:51], v[224:227]\n"
"ds_read_b64_tr_b16 a[36:37], v45 offset:32768\n"
"v_mov_b32 v184, 0x3eeff558\n"
"v_mfma_f32_16x16x32_bf16 v[240:243], a[92:95], a[48:51], v[240:243]\n"
"ds_read_b64_tr_b16 a[38:39], v45 offset:33280\n"
"v_mov_b32 v185, 0xc09d30fc\n"
"v_mfma_f32_16x16x32_bf16 v[196:199], a[80:83], a[52:55], v[196:199]\n"
"ds_read_b64_tr_b16 a[40:41], v46 offset:32768\n"
"v_mov_b32 v190, 0x3ea65394\n"
"v_mfma_f32_16x16x32_bf16 v[212:215], a[84:87], a[52:55], v[212:215]\n"
"ds_read_b64_tr_b16 a[42:43], v46 offset:33280\n"
"ds_read_b64_tr_b16 a[44:45], v47 offset:32768\n"
"v_mfma_f32_16x16x32_bf16 v[228:231], a[88:91], a[52:55], v[228:231]\n"
"ds_read_b64_tr_b16 a[46:47], v47 offset:33280\n"
"v_mov_b32 v108, 0xdddddddd\n"
"v_mfma_f32_16x16x32_bf16 v[244:247], a[92:95], a[52:55], v[244:247]\n"
"ds_read_b128 a[4:7], v52 offset:18432\n"
"v_mov_b32 v186, 0\n"
"v_mfma_f32_16x16x32_bf16 v[200:203], a[80:83], a[56:59], v[200:203]\n"
"ds_read_b128 a[8:11], v52 offset:20480\n"
"v_mov_b32 v187, 0\n"
"v_mfma_f32_16x16x32_bf16 v[216:219], a[84:87], a[56:59], v[216:219]\n"
"ds_read_b128 a[12:15], v52 offset:22528\n"
"v_mov_b32 v188, 0\n"
"v_mfma_f32_16x16x32_bf16 v[232:235], a[88:91], a[56:59], v[232:235]\n"
"v_mov_b32 v189, 0\n"
"v_mov_b32 v191, v12\n"
"v_mfma_f32_16x16x32_bf16 v[248:251], a[92:95], a[56:59], v[248:251]\n"
"v_mov_b32 v74, 0\n"
"v_mov_b32 v75, 0x3afe3f80\n"
"v_mfma_f32_16x16x32_bf16 v[204:207], a[80:83], a[60:63], v[204:207]\n"
"ds_read2_b32 v[176:177], v64 offset0:128 offset1:144\n"
"ds_read2_b32 v[178:179], v64 offset0:160 offset1:176\n"
"v_mfma_f32_16x16x32_bf16 v[220:223], a[84:87], a[60:63], v[220:223]\n"
"ds_read2_b32 v[180:181], v64 offset0:0 offset1:16\n"
"ds_read2_b32 v[182:183], v64 offset0:32 offset1:48\n"
"v_mfma_f32_16x16x32_bf16 v[236:239], a[88:91], a[60:63], v[236:239]\n"
"s_waitcnt vmcnt(13)\n"
"v_accvgpr_read_b32 v129, a128\n"
"v_accvgpr_read_b32 v131, a129\n"
"v_mfma_f32_16x16x32_bf16 v[252:255], a[92:95], a[60:63], v[252:255]\n"
"s_waitcnt vmcnt(12)\n"
"v_accvgpr_read_b32 v133, a130\n"
"v_accvgpr_read_b32 v135, a131\n"
"s_waitcnt lgkmcnt(0)\n"
"v_mfma_f32_16x16x32_bf16 v[120:123], a[32:35], v[56:59], v[120:123]\n"
"ds_read_b128 a[48:51], v53 offset:16384\n"
"s_waitcnt vmcnt(11)\n"
"v_accvgpr_read_b32 v137, a132\n"
"v_mfma_f32_16x16x32_bf16 v[124:127], a[36:39], v[56:59], v[124:127]\n"
"ds_read_b64_tr_b16 a[80:81], v48 offset:32768\n"
"ds_read_b64_tr_b16 a[82:83], v48 offset:33280\n"
"v_mfma_f32_16x16x32_bf16 v[192:195], a[32:35], a[0:3], v[192:195]\n"
"ds_read_b64_tr_b16 a[84:85], v49 offset:32768\n"
"v_accvgpr_read_b32 v139, a133\n"
"v_mfma_f32_16x16x32_bf16 v[208:211], a[36:39], a[0:3], v[208:211]\n"
"ds_read_b64_tr_b16 a[86:87], v49 offset:33280\n"
"s_waitcnt vmcnt(10)\n"
"v_accvgpr_read_b32 v141, a134\n"
"v_mfma_f32_16x16x32_bf16 v[224:227], a[40:43], a[0:3], v[224:227]\n"
"ds_read_b64_tr_b16 a[88:89], v50 offset:32768\n"
"v_accvgpr_read_b32 v143, a135\n"
"v_mfma_f32_16x16x32_bf16 v[240:243], a[44:47], a[0:3], v[240:243]\n"
"ds_read_b64_tr_b16 a[90:91], v50 offset:33280\n"
"ds_read_b64_tr_b16 a[92:93], v51 offset:32768\n"
"v_mfma_f32_16x16x32_bf16 v[196:199], a[32:35], a[4:7], v[196:199]\n"
"ds_read_b64_tr_b16 a[94:95], v51 offset:33280\n"
"v_lshlrev_b32 v128, 16, v129\n"
"v_mfma_f32_16x16x32_bf16 v[212:215], a[36:39], a[4:7], v[212:215]\n"
"ds_read_b128 a[52:55], v53 offset:18432\n"
"v_and_b32 v129, 0xffff0000, v129\n"
"v_mfma_f32_16x16x32_bf16 v[228:231], a[40:43], a[4:7], v[228:231]\n"
"ds_read_b128 a[56:59], v53 offset:20480\n"
"v_lshlrev_b32 v130, 16, v131\n"
"v_mfma_f32_16x16x32_bf16 v[244:247], a[44:47], a[4:7], v[244:247]\n"
"ds_read_b128 a[60:63], v53 offset:22528\n"
"v_and_b32 v131, 0xffff0000, v131\n"
"v_mfma_f32_16x16x32_bf16 v[200:203], a[32:35], a[8:11], v[200:203]\n"
"v_lshlrev_b32 v132, 16, v133\n"
"v_and_b32 v133, 0xffff0000, v133\n"
"v_mfma_f32_16x16x32_bf16 v[216:219], a[36:39], a[8:11], v[216:219]\n"
"v_lshlrev_b32 v134, 16, v135\n"
"v_and_b32 v135, 0xffff0000, v135\n"
"v_mfma_f32_16x16x32_bf16 v[232:235], a[40:43], a[8:11], v[232:235]\n"
"v_lshlrev_b32 v136, 16, v137\n"
"v_and_b32 v137, 0xffff0000, v137\n"
"v_mfma_f32_16x16x32_bf16 v[248:251], a[44:47], a[8:11], v[248:251]\n"
"v_lshlrev_b32 v138, 16, v139\n"
"v_and_b32 v139, 0xffff0000, v139\n"
"v_mfma_f32_16x16x32_bf16 v[204:207], a[32:35], a[12:15], v[204:207]\n"
"v_lshlrev_b32 v140, 16, v141\n"
"v_and_b32 v141, 0xffff0000, v141\n"
"v_mfma_f32_16x16x32_bf16 v[220:223], a[36:39], a[12:15], v[220:223]\n"
"v_lshlrev_b32 v142, 16, v143\n"
"v_and_b32 v143, 0xffff0000, v143\n"
"v_mfma_f32_16x16x32_bf16 v[236:239], a[40:43], a[12:15], v[236:239]\n"
"v_mul_f32 v128, v184, v128\n"
"v_mul_f32 v129, v184, v129\n"
"v_mfma_f32_16x16x32_bf16 v[252:255], a[44:47], a[12:15], v[252:255]\n"
"v_mul_f32 v130, v184, v130\n"
"v_mul_f32 v131, v184, v131\n"
"s_waitcnt lgkmcnt(0)\n"
"v_mfma_f32_16x16x32_bf16 v[192:195], a[80:83], a[48:51], v[192:195]\n"
"ds_read_b128 a[0:3], v52 offset:0\n"
"v_mul_f32 v132, v184, v132\n"
"v_mfma_f32_16x16x32_bf16 v[208:211], a[84:87], a[48:51], v[208:211]\n"
"ds_read_b64_tr_b16 a[32:33], v44 offset:0\n"
"ds_read_b64_tr_b16 a[34:35], v44 offset:512\n"
"v_mfma_f32_16x16x32_bf16 v[224:227], a[88:91], a[48:51], v[224:227]\n"
"ds_read_b64_tr_b16 a[36:37], v45 offset:0\n"
"v_mul_f32 v133, v184, v133\n"
"v_mfma_f32_16x16x32_bf16 v[240:243], a[92:95], a[48:51], v[240:243]\n"
"ds_read_b64_tr_b16 a[38:39], v45 offset:512\n"
"v_mul_f32 v134, v184, v134\n"
"v_mfma_f32_16x16x32_bf16 v[196:199], a[80:83], a[52:55], v[196:199]\n"
"ds_read_b64_tr_b16 a[40:41], v46 offset:0\n"
"v_mul_f32 v135, v184, v135\n"
"v_mfma_f32_16x16x32_bf16 v[212:215], a[84:87], a[52:55], v[212:215]\n"
"ds_read_b64_tr_b16 a[42:43], v46 offset:512\n"
"ds_read_b64_tr_b16 a[44:45], v47 offset:0\n"
"v_mfma_f32_16x16x32_bf16 v[228:231], a[88:91], a[52:55], v[228:231]\n"
"ds_read_b64_tr_b16 a[46:47], v47 offset:512\n"
"v_mul_f32 v136, v184, v136\n"
"v_mfma_f32_16x16x32_bf16 v[244:247], a[92:95], a[52:55], v[244:247]\n"
"ds_read_b128 a[4:7], v52 offset:2048\n"
"v_mul_f32 v137, v184, v137\n"
"v_mfma_f32_16x16x32_bf16 v[200:203], a[80:83], a[56:59], v[200:203]\n"
"ds_read_b128 a[8:11], v52 offset:4096\n"
"v_mul_f32 v138, v184, v138\n"
"v_mfma_f32_16x16x32_bf16 v[216:219], a[84:87], a[56:59], v[216:219]\n"
"ds_read_b128 a[12:15], v52 offset:6144\n"
"v_mul_f32 v139, v184, v139\n"
"v_mfma_f32_16x16x32_bf16 v[232:235], a[88:91], a[56:59], v[232:235]\n"
"v_mul_f32 v140, v184, v140\n"
"v_mul_f32 v141, v184, v141\n"
"v_mfma_f32_16x16x32_bf16 v[248:251], a[92:95], a[56:59], v[248:251]\n"
"v_mul_f32 v142, v184, v142\n"
"v_mul_f32 v143, v184, v143\n"
"v_mfma_f32_16x16x32_bf16 v[204:207], a[80:83], a[60:63], v[204:207]\n"
"9001:\n"
"s_cbranch_execz 9002f\n"
"v_mul_f32 v176, v184, v176\n"
"v_mul_f32 v177, v184, v177\n"
"v_mfma_f32_16x16x32_bf16 v[220:223], a[84:87], a[60:63], v[220:223]\n"
"v_mul_f32 v178, v184, v178\n"
"v_mul_f32 v179, v184, v179\n"
"v_mfma_f32_16x16x32_bf16 v[236:239], a[88:91], a[60:63], v[236:239]\n"
"v_mul_f32 v180, -v180, v176\n"
"v_mul_f32 v181, -v181, v177\n"
"v_mfma_f32_16x16x32_bf16 v[252:255], a[92:95], a[60:63], v[252:255]\n"
"v_mul_f32 v182, -v182, v178\n"
"v_mul_f32 v183, -v183, v179\n"
"s_waitcnt lgkmcnt(0)\n"
"v_mfma_f32_16x16x32_bf16 v[120:123], a[32:35], v[56:59], v[120:123]\n"
"ds_read_b128 a[48:51], v53 offset:0\n"
"s_lshl_b32 s35, s30, 3\n"
"s_lshr_b32 s35, 0x020e1c28, s35\n"
"v_mfma_f32_16x16x32_bf16 v[124:127], a[36:39], v[56:59], v[124:127]\n"
"ds_read_b64_tr_b16 a[80:81], v48 offset:0\n"
"ds_read_b64_tr_b16 a[82:83], v48 offset:512\n"
"v_mfma_f32_16x16x32_bf16 v[192:195], a[32:35], a[0:3], v[192:195]\n"
"ds_read_b64_tr_b16 a[84:85], v49 offset:0\n"
"s_and_b32 s35, s35, 63\n"
"s_movk_i32 s6, 2560\n"
"v_mfma_f32_16x16x32_bf16 v[208:211], a[36:39], a[0:3], v[208:211]\n"
"ds_read_b64_tr_b16 a[86:87], v49 offset:512\n"
"v_mfma_f32_16x16x32_bf16 v[224:227], a[40:43], a[0:3], v[224:227]\n"
"ds_read_b64_tr_b16 a[88:89], v50 offset:0\n"
"v_mfma_f32_16x16x32_bf16 v[240:243], a[44:47], a[0:3], v[240:243]\n"
"ds_read_b64_tr_b16 a[90:91], v50 offset:512\n"
"ds_read_b64_tr_b16 a[92:93], v51 offset:0\n"
"v_mfma_f32_16x16x32_bf16 v[196:199], a[32:35], a[4:7], v[196:199]\n"
"ds_read_b64_tr_b16 a[94:95], v51 offset:512\n"
"v_mfma_f32_16x16x32_bf16 v[212:215], a[36:39], a[4:7], v[212:215]\n"
"ds_read_b128 a[52:55], v53 offset:2048\n"
"v_mfma_f32_16x16x32_bf16 v[228:231], a[40:43], a[4:7], v[228:231]\n"
"s_mov_b32 exec_lo, 0x10001\n"
"s_mov_b32 exec_hi, 0x10001\n"
"ds_write_b128 v66, v[120:123] offset:128\n"
"ds_write_b128 v66, v[124:127] offset:192\n"
"s_mov_b64 exec, -1\n"
"v_mfma_f32_16x16x32_bf16 v[244:247], a[44:47], a[4:7], v[244:247]\n"
"ds_read_b128 a[56:59], v53 offset:4096\n"
"ds_read_b128 a[60:63], v53 offset:6144\n"
"v_mfma_f32_16x16x32_bf16 v[200:203], a[32:35], a[8:11], v[200:203]\n"
"s_cmp_lt_u32 s30, 2\n"
"s_cselect_b32 s6, s6, -2560\n"
"s_cselect_b32 s7, 0, -1\n"
"v_mfma_f32_16x16x32_bf16 v[216:219], a[36:39], a[8:11], v[216:219]\n"
"s_add_u32 s6, s6, s0\n"
"s_addc_u32 s7, s7, s1\n"
"v_mfma_f32_16x16x32_bf16 v[232:235], a[40:43], a[8:11], v[232:235]\n"
"v_mfma_f32_16x16x32_bf16 v[248:251], a[44:47], a[8:11], v[248:251]\n"
"v_mfma_f32_16x16x32_bf16 v[204:207], a[32:35], a[12:15], v[204:207]\n"
"v_mfma_f32_16x16x32_bf16 v[220:223], a[36:39], a[12:15], v[220:223]\n"
"v_mfma_f32_16x16x32_bf16 v[236:239], a[40:43], a[12:15], v[236:239]\n"
"v_mfma_f32_16x16x32_bf16 v[252:255], a[44:47], a[12:15], v[252:255]\n"
"s_waitcnt lgkmcnt(0)\n"
"s_barrier\n"
"v_mfma_f32_16x16x32_bf16 v[192:195], a[80:83], a[48:51], v[192:195]\n"
"ds_read_b128 v[144:147], v67 offset:128\n"
"v_xor_b32 v122, 0, v28\n"
"v_mfma_f32_16x16x32_bf16 v[208:211], a[84:87], a[48:51], v[208:211]\n"
"ds_read_b128 v[160:163], v67 offset:384\n"
"v_xor_b32 v123, 1024, v28\n"
"v_mfma_f32_16x16x32_bf16 v[224:227], a[88:91], a[48:51], v[224:227]\n"
"ds_read_b128 v[148:151], v67 offset:192\n"
"v_xor_b32 v124, 2048, v28\n"
"v_mfma_f32_16x16x32_bf16 v[240:243], a[92:95], a[48:51], v[240:243]\n"
"ds_read_b128 v[164:167], v67 offset:448\n"
"v_xor_b32 v125, 3072, v28\n"
"v_mfma_f32_16x16x32_bf16 v[196:199], a[80:83], a[52:55], v[196:199]\n"
"ds_read_b128 v[152:155], v68 offset:128\n"
"s_waitcnt lgkmcnt(3)\n"
"v_add_f32 v144, v144, v160\n"
"v_mfma_f32_16x16x32_bf16 v[212:215], a[84:87], a[52:55], v[212:215]\n"
"ds_read_b128 v[168:171], v68 offset:384\n"
"v_add_f32 v145, v145, v161\n"
"v_mfma_f32_16x16x32_bf16 v[228:231], a[88:91], a[52:55], v[228:231]\n"
"ds_read_b128 v[156:159], v68 offset:192\n"
"v_add_f32 v146, v146, v162\n"
"v_mfma_f32_16x16x32_bf16 v[244:247], a[92:95], a[52:55], v[244:247]\n"
"ds_read_b128 v[172:175], v68 offset:448\n"
"v_add_f32 v147, v147, v163\n"
"v_mfma_f32_16x16x32_bf16 v[200:203], a[80:83], a[56:59], v[200:203]\n"
"s_waitcnt lgkmcnt(4)\n"
"v_add_f32 v148, v148, v164\n"
"v_add_f32 v149, v149, v165\n"
"v_mfma_f32_16x16x32_bf16 v[216:219], a[84:87], a[56:59], v[216:219]\n"
"v_add_f32 v150, v150, v166\n"
"v_add_f32 v151, v151, v167\n"
"v_mfma_f32_16x16x32_bf16 v[232:235], a[88:91], a[56:59], v[232:235]\n"
"s_waitcnt lgkmcnt(2)\n"
"v_add_f32 v152, v152, v168\n"
"v_add_f32 v153, v153, v169\n"
"v_mfma_f32_16x16x32_bf16 v[248:251], a[92:95], a[56:59], v[248:251]\n"
"v_add_f32 v154, v154, v170\n"
"v_add_f32 v155, v155, v171\n"
"v_mfma_f32_16x16x32_bf16 v[204:207], a[80:83], a[60:63], v[204:207]\n"
"s_waitcnt lgkmcnt(0)\n"
"v_add_f32 v156, v156, v172\n"
"v_add_f32 v157, v157, v173\n"
"v_mfma_f32_16x16x32_bf16 v[220:223], a[84:87], a[60:63], v[220:223]\n"
"v_add_f32 v158, v158, v174\n"
"v_add_f32_e64 v159, v159, v175\n"
"v_mfma_f32_16x16x32_bf16 v[236:239], a[88:91], a[60:63], v[236:239]\n"
"v_mfma_f32_16x16x32_bf16 v[252:255], a[92:95], a[60:63], v[252:255]\n"
"s_waitcnt vmcnt(0) lgkmcnt(0)\n"
"s_barrier\n"
"s_mov_b64 exec, s[50:51]\n"
"global_atomic_add v15, v110, s[66:67]\n"
"s_mov_b64 exec, -1\n"
"v_pk_fma_f32 v[160:161], v[180:181], v[152:153], v[136:137] op_sel:[0,0,0] op_sel_hi:[0,1,1]\n"
"v_pk_fma_f32 v[162:163], v[180:181], v[154:155], v[138:139] op_sel:[0,0,0] op_sel_hi:[0,1,1]\n"
"v_pk_fma_f32 v[164:165], v[180:181], v[152:153], v[136:137] op_sel:[1,0,0] op_sel_hi:[1,1,1]\n"
"v_pk_fma_f32 v[166:167], v[180:181], v[154:155], v[138:139] op_sel:[1,0,0] op_sel_hi:[1,1,1]\n"
"v_pk_fma_f32 v[168:169], v[182:183], v[152:153], v[136:137] op_sel:[0,0,0] op_sel_hi:[0,1,1]\n"
"v_pk_fma_f32 v[170:171], v[182:183], v[154:155], v[138:139] op_sel:[0,0,0] op_sel_hi:[0,1,1]\n"
"v_pk_fma_f32 v[172:173], v[182:183], v[152:153], v[136:137] op_sel:[1,0,0] op_sel_hi:[1,1,1]\n"
"v_pk_fma_f32 v[174:175], v[182:183], v[154:155], v[138:139] op_sel:[1,0,0] op_sel_hi:[1,1,1]\n"
"v_pk_fma_f32 v[224:225], v[224:225], v[176:177], v[160:161] op_sel:[0,0,0] op_sel_hi:[1,0,1]\n"
"v_pk_fma_f32 v[226:227], v[226:227], v[176:177], v[162:163] op_sel:[0,0,0] op_sel_hi:[1,0,1]\n"
"v_pk_fma_f32 v[228:229], v[228:229], v[176:177], v[164:165] op_sel:[0,1,0] op_sel_hi:[1,1,1]\n"
"v_pk_fma_f32 v[230:231], v[230:231], v[176:177], v[166:167] op_sel:[0,1,0] op_sel_hi:[1,1,1]\n"
"v_pk_fma_f32 v[232:233], v[232:233], v[178:179], v[168:169] op_sel:[0,0,0] op_sel_hi:[1,0,1]\n"
"v_pk_fma_f32 v[234:235], v[234:235], v[178:179], v[170:171] op_sel:[0,0,0] op_sel_hi:[1,0,1]\n"
"v_pk_fma_f32 v[236:237], v[236:237], v[178:179], v[172:173] op_sel:[0,1,0] op_sel_hi:[1,1,1]\n"
"v_pk_fma_f32 v[238:239], v[238:239], v[178:179], v[174:175] op_sel:[0,1,0] op_sel_hi:[1,1,1]\n"
"v_pk_fma_f32 v[160:161], v[224:225], v[224:225], v[184:185] op_sel:[0,0,1] op_sel_hi:[1,1,1] neg_lo:[1,0,0] neg_hi:[1,0,0]\n"
"v_pk_fma_f32 v[162:163], v[226:227], v[226:227], v[184:185] op_sel:[0,0,1] op_sel_hi:[1,1,1] neg_lo:[1,0,0] neg_hi:[1,0,0]\n"
"v_pk_fma_f32 v[164:165], v[228:229], v[228:229], v[184:185] op_sel:[0,0,1] op_sel_hi:[1,1,1] neg_lo:[1,0,0] neg_hi:[1,0,0]\n"
"v_pk_fma_f32 v[166:167], v[230:231], v[230:231], v[184:185] op_sel:[0,0,1] op_sel_hi:[1,1,1] neg_lo:[1,0,0] neg_hi:[1,0,0]\n"
"v_pk_fma_f32 v[168:169], v[232:233], v[232:233], v[184:185] op_sel:[0,0,1] op_sel_hi:[1,1,1] neg_lo:[1,0,0] neg_hi:[1,0,0]\n"
"v_pk_fma_f32 v[170:171], v[234:235], v[234:235], v[184:185] op_sel:[0,0,1] op_sel_hi:[1,1,1] neg_lo:[1,0,0] neg_hi:[1,0,0]\n"
"v_pk_fma_f32 v[172:173], v[236:237], v[236:237], v[184:185] op_sel:[0,0,1] op_sel_hi:[1,1,1] neg_lo:[1,0,0] neg_hi:[1,0,0]\n"
"v_pk_fma_f32 v[174:175], v[238:239], v[238:239], v[184:185] op_sel:[0,0,1] op_sel_hi:[1,1,1] neg_lo:[1,0,0] neg_hi:[1,0,0]\n"
"v_pk_mul_f32 v[160:161], v[160:161], v[224:225]\n"
"v_pk_mul_f32 v[162:163], v[162:163], v[226:227]\n"
"v_pk_mul_f32 v[164:165], v[164:165], v[228:229]\n"
"v_pk_mul_f32 v[166:167], v[166:167], v[230:231]\n"
"v_pk_mul_f32 v[168:169], v[168:169], v[232:233]\n"
"v_pk_mul_f32 v[170:171], v[170:171], v[234:235]\n"
"v_pk_mul_f32 v[172:173], v[172:173], v[236:237]\n"
"v_pk_mul_f32 v[174:175], v[174:175], v[238:239]\n"
"v_mfma_f32_32x32x4_2b_bf16 v[76:107], v[74:75], v[190:191], 0 cbsz:1 abid:0\n"
"v_exp_f32 v160, v160\n"
"v_exp_f32 v161, v161\n"
"v_exp_f32 v162, v162\n"
"v_exp_f32 v163, v163\n"
"v_exp_f32 v164, v164\n"
"v_exp_f32 v165, v165\n"
"v_exp_f32 v166, v166\n"
"v_exp_f32 v167, v167\n"
"v_exp_f32 v168, v168\n"
"v_exp_f32 v169, v169\n"
"v_exp_f32 v170, v170\n"
"v_exp_f32 v171, v171\n"
"v_exp_f32 v172, v172\n"
"v_exp_f32 v173, v173\n"
"v_exp_f32 v174, v174\n"
"9002:\n"
"s_cbranch_execz 97b\n"
"v_exp_f32_e64 v175, v175\n"
"v_pk_fma_f32 v[160:161], v[160:161], v[190:191], v[190:191] op_sel_hi:[1,0,0]\n"
"v_pk_fma_f32 v[162:163], v[162:163], v[190:191], v[190:191] op_sel_hi:[1,0,0]\n"
"v_pk_fma_f32 v[164:165], v[164:165], v[190:191], v[190:191] op_sel_hi:[1,0,0]\n"
"v_pk_fma_f32 v[166:167], v[166:167], v[190:191], v[190:191] op_sel_hi:[1,0,0]\n"
"v_pk_fma_f32 v[168:169], v[168:169], v[190:191], v[190:191] op_sel_hi:[1,0,0]\n"
"v_pk_fma_f32 v[170:171], v[170:171], v[190:191], v[190:191] op_sel_hi:[1,0,0]\n"
"v_pk_fma_f32 v[172:173], v[172:173], v[190:191], v[190:191] op_sel_hi:[1,0,0]\n"
"v_pk_fma_f32 v[174:175], v[174:175], v[190:191], v[190:191] op_sel_hi:[1,0,0]\n"
"v_rcp_f32 v160, v160\n"
"v_rcp_f32 v161, v161\n"
"v_rcp_f32 v162, v162\n"
"v_rcp_f32 v163, v163\n"
"v_rcp_f32 v164, v164\n"
"v_rcp_f32 v165, v165\n"
"v_rcp_f32 v166, v166\n"
"v_rcp_f32 v167, v167\n"
"v_rcp_f32 v168, v168\n"
"v_rcp_f32 v169, v169\n"
"v_rcp_f32 v170, v170\n"
"v_rcp_f32 v171, v171\n"
"v_rcp_f32 v172, v172\n"
"v_rcp_f32 v173, v173\n"
"v_rcp_f32 v174, v174\n"
"v_rcp_f32 v175, v175\n"
"v_pk_mul_f32 v[160:161], v[160:161], v[224:225]\n"
"v_pk_mul_f32 v[162:163], v[162:163], v[226:227]\n"
"v_pk_mul_f32 v[164:165], v[164:165], v[228:229]\n"
"v_pk_mul_f32 v[166:167], v[166:167], v[230:231]\n"
"v_pk_mul_f32 v[168:169], v[168:169], v[232:233]\n"
"v_pk_mul_f32 v[170:171], v[170:171], v[234:235]\n"
"v_pk_mul_f32 v[172:173], v[172:173], v[236:237]\n"
"v_pk_mul_f32 v[174:175], v[174:175], v[238:239]\n"
"v_cvt_pk_bf16_f32 v160, v160, v161\n"
"v_cvt_pk_bf16_f32 v162, v162, v163\n"
"v_cvt_pk_bf16_f32 v164, v164, v165\n"
"v_cvt_pk_bf16_f32 v166, v166, v167\n"
"v_cvt_pk_bf16_f32 v168, v168, v169\n"
"v_cvt_pk_bf16_f32 v170, v170, v171\n"
"v_cvt_pk_bf16_f32 v172, v172, v173\n"
"v_cvt_pk_bf16_f32 v174, v174, v175\n"
"v_accvgpr_write_b32 a112, v160\n"
"v_accvgpr_write_b32 a113, v162\n"
"v_accvgpr_write_b32 a116, v164\n"
"s_mov_b64 exec, 200977\n"
"global_load_dword v109, v1, s[66:67] offset:0 sc1\n"
"s_mov_b64 exec, -1\n"
"s_nop 0\n"
"v_accvgpr_write_b32 a117, v166\n"
"v_accvgpr_write_b32 a120, v168\n"
"v_accvgpr_write_b32 a121, v170\n"
"v_accvgpr_write_b32 a124, v172\n"
"v_accvgpr_write_b32 a125, v174\n"
"v_pk_fma_f32 v[160:161], v[180:181], v[156:157], v[140:141] op_sel:[0,0,0] op_sel_hi:[0,1,1]\n"
"v_pk_fma_f32 v[162:163], v[180:181], v[158:159], v[142:143] op_sel:[0,0,0] op_sel_hi:[0,1,1]\n"
"v_pk_fma_f32 v[164:165], v[180:181], v[156:157], v[140:141] op_sel:[1,0,0] op_sel_hi:[1,1,1]\n"
"v_pk_fma_f32 v[166:167], v[180:181], v[158:159], v[142:143] op_sel:[1,0,0] op_sel_hi:[1,1,1]\n"
"v_pk_fma_f32 v[168:169], v[182:183], v[156:157], v[140:141] op_sel:[0,0,0] op_sel_hi:[0,1,1]\n"
"v_pk_fma_f32 v[170:171], v[182:183], v[158:159], v[142:143] op_sel:[0,0,0] op_sel_hi:[0,1,1]\n"
"v_pk_fma_f32 v[172:173], v[182:183], v[156:157], v[140:141] op_sel:[1,0,0] op_sel_hi:[1,1,1]\n"
"v_pk_fma_f32 v[174:175], v[182:183], v[158:159], v[142:143] op_sel:[1,0,0] op_sel_hi:[1,1,1]\n"
"v_pk_fma_f32 v[240:241], v[240:241], v[176:177], v[160:161] op_sel:[0,0,0] op_sel_hi:[1,0,1]\n"
"v_pk_fma_f32 v[242:243], v[242:243], v[176:177], v[162:163] op_sel:[0,0,0] op_sel_hi:[1,0,1]\n"
"v_pk_fma_f32 v[244:245], v[244:245], v[176:177], v[164:165] op_sel:[0,1,0] op_sel_hi:[1,1,1]\n"
"v_pk_fma_f32 v[246:247], v[246:247], v[176:177], v[166:167] op_sel:[0,1,0] op_sel_hi:[1,1,1]\n"
"v_pk_fma_f32 v[248:249], v[248:249], v[178:179], v[168:169] op_sel:[0,0,0] op_sel_hi:[1,0,1]\n"
"v_pk_fma_f32 v[250:251], v[250:251], v[178:179], v[170:171] op_sel:[0,0,0] op_sel_hi:[1,0,1]\n"
"v_pk_fma_f32 v[252:253], v[252:253], v[178:179], v[172:173] op_sel:[0,1,0] op_sel_hi:[1,1,1]\n"
"v_pk_fma_f32 v[254:255], v[254:255], v[178:179], v[174:175] op_sel:[0,1,0] op_sel_hi:[1,1,1]\n"
"v_pk_fma_f32 v[160:161], v[240:241], v[240:241], v[184:185] op_sel:[0,0,1] op_sel_hi:[1,1,1] neg_lo:[1,0,0] neg_hi:[1,0,0]\n"
"9100:\n"
"s_cbranch_execz 9101f\n"
"s_nop 0\n"
"v_pk_fma_f32 v[162:163], v[242:243], v[242:243], v[184:185] op_sel:[0,0,1] op_sel_hi:[1,1,1] neg_lo:[1,0,0] neg_hi:[1,0,0]\n"
"v_pk_fma_f32 v[164:165], v[244:245], v[244:245], v[184:185] op_sel:[0,0,1] op_sel_hi:[1,1,1] neg_lo:[1,0,0] neg_hi:[1,0,0]\n"
"v_pk_fma_f32 v[166:167], v[246:247], v[246:247], v[184:185] op_sel:[0,0,1] op_sel_hi:[1,1,1] neg_lo:[1,0,0] neg_hi:[1,0,0]\n"
"v_pk_fma_f32 v[168:169], v[248:249], v[248:249], v[184:185] op_sel:[0,0,1] op_sel_hi:[1,1,1] neg_lo:[1,0,0] neg_hi:[1,0,0]\n"
"v_pk_fma_f32 v[170:171], v[250:251], v[250:251], v[184:185] op_sel:[0,0,1] op_sel_hi:[1,1,1] neg_lo:[1,0,0] neg_hi:[1,0,0]\n"
"v_pk_fma_f32 v[172:173], v[252:253], v[252:253], v[184:185] op_sel:[0,0,1] op_sel_hi:[1,1,1] neg_lo:[1,0,0] neg_hi:[1,0,0]\n"
"v_pk_fma_f32 v[174:175], v[254:255], v[254:255], v[184:185] op_sel:[0,0,1] op_sel_hi:[1,1,1] neg_lo:[1,0,0] neg_hi:[1,0,0]\n"
"v_pk_mul_f32 v[160:161], v[160:161], v[240:241]\n"
"v_pk_mul_f32 v[162:163], v[162:163], v[242:243]\n"
"v_pk_mul_f32 v[164:165], v[164:165], v[244:245]\n"
"v_pk_mul_f32 v[166:167], v[166:167], v[246:247]\n"
"v_pk_mul_f32 v[168:169], v[168:169], v[248:249]\n"
"v_pk_mul_f32 v[170:171], v[170:171], v[250:251]\n"
"v_pk_mul_f32 v[172:173], v[172:173], v[252:253]\n"
"v_pk_mul_f32 v[174:175], v[174:175], v[254:255]\n"
"v_exp_f32 v160, v160\n"
"v_exp_f32 v161, v161\n"
"v_exp_f32 v162, v162\n"
"v_exp_f32 v163, v163\n"
"v_exp_f32 v164, v164\n"
"v_exp_f32 v165, v165\n"
"v_exp_f32 v166, v166\n"
"v_exp_f32 v167, v167\n"
"v_exp_f32 v168, v168\n"
"v_exp_f32 v169, v169\n"
"v_exp_f32 v170, v170\n"
"v_exp_f32 v171, v171\n"
"v_exp_f32 v172, v172\n"
"v_exp_f32 v173, v173\n"
"v_exp_f32 v174, v174\n"
"v_exp_f32 v175, v175\n"
"v_pk_fma_f32 v[160:161], v[160:161], v[190:191], v[190:191] op_sel_hi:[1,0,0]\n"
"v_pk_fma_f32 v[162:163], v[162:163], v[190:191], v[190:191] op_sel_hi:[1,0,0]\n"
"v_pk_fma_f32 v[164:165], v[164:165], v[190:191], v[190:191] op_sel_hi:[1,0,0]\n"
"v_pk_fma_f32 v[166:167], v[166:167], v[190:191], v[190:191] op_sel_hi:[1,0,0]\n"
"v_pk_fma_f32 v[168:169], v[168:169], v[190:191], v[190:191] op_sel_hi:[1,0,0]\n"
"v_pk_fma_f32 v[170:171], v[170:171], v[190:191], v[190:191] op_sel_hi:[1,0,0]\n"
"v_pk_fma_f32 v[172:173], v[172:173], v[190:191], v[190:191] op_sel_hi:[1,0,0]\n"
"v_pk_fma_f32 v[174:175], v[174:175], v[190:191], v[190:191] op_sel_hi:[1,0,0]\n"
"v_rcp_f32 v160, v160\n"
"v_rcp_f32 v161, v161\n"
"v_rcp_f32 v162, v162\n"
"v_rcp_f32 v163, v163\n"
"v_rcp_f32 v164, v164\n"
"v_rcp_f32 v165, v165\n"
"v_rcp_f32 v166, v166\n"
"v_rcp_f32 v167, v167\n"
"v_rcp_f32 v168, v168\n"
"v_rcp_f32 v169, v169\n"
"v_rcp_f32 v170, v170\n"
"v_rcp_f32 v171, v171\n"
"v_rcp_f32 v172, v172\n"
"v_rcp_f32 v173, v173\n"
"v_rcp_f32 v174, v174\n"
"v_rcp_f32 v175, v175\n"
"v_pk_mul_f32 v[160:161], v[160:161], v[240:241]\n"
"v_pk_mul_f32 v[162:163], v[162:163], v[242:243]\n"
"v_pk_mul_f32 v[164:165], v[164:165], v[244:245]\n"
"v_pk_mul_f32 v[166:167], v[166:167], v[246:247]\n"
"v_pk_mul_f32 v[168:169], v[168:169], v[248:249]\n"
"v_pk_mul_f32 v[170:171], v[170:171], v[250:251]\n"
"v_pk_mul_f32 v[172:173], v[172:173], v[252:253]\n"
"v_pk_mul_f32 v[174:175], v[174:175], v[254:255]\n"
"v_cvt_pk_bf16_f32 v160, v160, v161\n"
"v_cvt_pk_bf16_f32 v162, v162, v163\n"
"v_cvt_pk_bf16_f32 v164, v164, v165\n"
"v_cvt_pk_bf16_f32 v166, v166, v167\n"
"v_cvt_pk_bf16_f32 v168, v168, v169\n"
"v_cvt_pk_bf16_f32 v170, v170, v171\n"
"v_cvt_pk_bf16_f32 v172, v172, v173\n"
"v_cvt_pk_bf16_f32 v174, v174, v175\n"
"v_accvgpr_write_b32 a114, v160\n"
"v_accvgpr_write_b32 a115, v162\n"
"v_accvgpr_write_b32 a118, v164\n"
"v_accvgpr_write_b32 a119, v166\n"
"v_accvgpr_write_b32 a122, v168\n"
"v_accvgpr_write_b32 a123, v170\n"
"v_accvgpr_write_b32 a126, v172\n"
"v_accvgpr_write_b32 a127, v174\n"
"v_pk_fma_f32 v[160:161], v[180:181], v[144:145], v[128:129] op_sel:[0,0,0] op_sel_hi:[0,1,1]\n"
"v_pk_fma_f32 v[162:163], v[180:181], v[146:147], v[130:131] op_sel:[0,0,0] op_sel_hi:[0,1,1]\n"
"v_pk_fma_f32 v[164:165], v[180:181], v[144:145], v[128:129] op_sel:[1,0,0] op_sel_hi:[1,1,1]\n"
"v_pk_fma_f32 v[166:167], v[180:181], v[146:147], v[130:131] op_sel:[1,0,0] op_sel_hi:[1,1,1]\n"
"v_pk_fma_f32 v[168:169], v[182:183], v[144:145], v[128:129] op_sel:[0,0,0] op_sel_hi:[0,1,1]\n"
"v_pk_fma_f32 v[170:171], v[182:183], v[146:147], v[130:131] op_sel:[0,0,0] op_sel_hi:[0,1,1]\n"
"v_pk_fma_f32 v[172:173], v[182:183], v[144:145], v[128:129] op_sel:[1,0,0] op_sel_hi:[1,1,1]\n"
"v_pk_fma_f32 v[174:175], v[182:183], v[146:147], v[130:131] op_sel:[1,0,0] op_sel_hi:[1,1,1]\n"
"v_pk_fma_f32 v[192:193], v[192:193], v[176:177], v[160:161] op_sel:[0,0,0] op_sel_hi:[1,0,1]\n"
"v_pk_fma_f32 v[194:195], v[194:195], v[176:177], v[162:163] op_sel:[0,0,0] op_sel_hi:[1,0,1]\n"
"v_pk_fma_f32 v[196:197], v[196:197], v[176:177], v[164:165] op_sel:[0,1,0] op_sel_hi:[1,1,1]\n"
"v_pk_fma_f32 v[198:199], v[198:199], v[176:177], v[166:167] op_sel:[0,1,0] op_sel_hi:[1,1,1]\n"
"v_pk_fma_f32 v[200:201], v[200:201], v[178:179], v[168:169] op_sel:[0,0,0] op_sel_hi:[1,0,1]\n"
"v_pk_fma_f32 v[202:203], v[202:203], v[178:179], v[170:171] op_sel:[0,0,0] op_sel_hi:[1,0,1]\n"
"v_pk_fma_f32 v[204:205], v[204:205], v[178:179], v[172:173] op_sel:[0,1,0] op_sel_hi:[1,1,1]\n"
"v_pk_fma_f32 v[206:207], v[206:207], v[178:179], v[174:175] op_sel:[0,1,0] op_sel_hi:[1,1,1]\n"
"v_pk_fma_f32 v[160:161], v[192:193], v[192:193], v[184:185] op_sel:[0,0,1] op_sel_hi:[1,1,1] neg_lo:[1,0,0] neg_hi:[1,0,0]\n"
"s_waitcnt vmcnt(0)\n"
"v_cmp_eq_u32 vcc, v109, v35\n"
"v_pk_fma_f32 v[162:163], v[194:195], v[194:195], v[184:185] op_sel:[0,0,1] op_sel_hi:[1,1,1] neg_lo:[1,0,0] neg_hi:[1,0,0]\n"
"v_pk_fma_f32 v[164:165], v[196:197], v[196:197], v[184:185] op_sel:[0,0,1] op_sel_hi:[1,1,1] neg_lo:[1,0,0] neg_hi:[1,0,0]\n"
"s_and_b32 s47, vcc_lo, 200977\n"
"s_cbranch_scc1 12f\n"
"global_load_dwordx2 v[66:67], v4, s[36:37] offset:-1536 sc1\n"
"s_nop 0\n"
"v_pk_fma_f32 v[166:167], v[198:199], v[198:199], v[184:185] op_sel:[0,0,1] op_sel_hi:[1,1,1] neg_lo:[1,0,0] neg_hi:[1,0,0]\n"
"v_pk_fma_f32 v[168:169], v[200:201], v[200:201], v[184:185] op_sel:[0,0,1] op_sel_hi:[1,1,1] neg_lo:[1,0,0] neg_hi:[1,0,0]\n"
"v_pk_fma_f32 v[170:171], v[202:203], v[202:203], v[184:185] op_sel:[0,0,1] op_sel_hi:[1,1,1] neg_lo:[1,0,0] neg_hi:[1,0,0]\n"
"v_pk_fma_f32 v[172:173], v[204:205], v[204:205], v[184:185] op_sel:[0,0,1] op_sel_hi:[1,1,1] neg_lo:[1,0,0] neg_hi:[1,0,0]\n"
"v_pk_fma_f32 v[174:175], v[206:207], v[206:207], v[184:185] op_sel:[0,0,1] op_sel_hi:[1,1,1] neg_lo:[1,0,0] neg_hi:[1,0,0]\n"
"v_pk_mul_f32 v[160:161], v[160:161], v[192:193]\n"
"v_pk_mul_f32 v[162:163], v[162:163], v[194:195]\n"
"v_pk_mul_f32 v[164:165], v[164:165], v[196:197]\n"
"v_pk_mul_f32 v[166:167], v[166:167], v[198:199]\n"
"v_pk_mul_f32 v[168:169], v[168:169], v[200:201]\n"
"v_pk_mul_f32 v[170:171], v[170:171], v[202:203]\n"
"v_pk_mul_f32 v[172:173], v[172:173], v[204:205]\n"
"global_load_dwordx2 v[64:65], v10, s[6:7] offset:1024 sc1\n"
"v_pk_mul_f32 v[174:175], v[174:175], v[206:207]\n"
"v_mfma_f32_32x32x4_2b_bf16 v[224:255], v[186:187], v[186:187], 0 cbsz:1 abid:0\n"
"v_exp_f32 v160, v160\n"
"v_exp_f32 v161, v161\n"
"v_exp_f32 v162, v162\n"
"v_exp_f32 v163, v163\n"
"v_exp_f32 v164, v164\n"
"global_load_dwordx4 v[56:59], v7, s[36:37] offset:-2560 sc1\n"
"v_exp_f32 v165, v165\n"
"v_exp_f32 v166, v166\n"
"v_exp_f32 v167, v167\n"
"v_exp_f32 v168, v168\n"
"v_exp_f32 v169, v169\n"
"v_exp_f32 v170, v170\n"
"global_load_dwordx4 v[44:47], v9, s[6:7] offset:0 sc1\n"
"v_exp_f32 v171, v171\n"
"v_exp_f32 v172, v172\n"
"v_exp_f32 v173, v173\n"
"v_exp_f32 v174, v174\n"
"v_exp_f32 v175, v175\n"
"v_pk_fma_f32 v[160:161], v[160:161], v[190:191], v[190:191] op_sel_hi:[1,0,0]\n"
"global_load_dwordx4 v[48:51], v9, s[6:7] offset:1280 sc1\n"
"v_pk_fma_f32 v[162:163], v[162:163], v[190:191], v[190:191] op_sel_hi:[1,0,0]\n"
"v_pk_fma_f32 v[164:165], v[164:165], v[190:191], v[190:191] op_sel_hi:[1,0,0]\n"
"v_pk_fma_f32 v[166:167], v[166:167], v[190:191], v[190:191] op_sel_hi:[1,0,0]\n"
"v_pk_fma_f32 v[168:169], v[168:169], v[190:191], v[190:191] op_sel_hi:[1,0,0]\n"
"v_pk_fma_f32 v[170:171], v[170:171], v[190:191], v[190:191] op_sel_hi:[1,0,0]\n"
"v_pk_fma_f32 v[172:173], v[172:173], v[190:191], v[190:191] op_sel_hi:[1,0,0]\n"
"v_pk_fma_f32 v[174:175], v[174:175], v[190:191], v[190:191] op_sel_hi:[1,0,0]\n"
"v_rcp_f32 v160, v160\n"
"v_rcp_f32 v161, v161\n"
"v_rcp_f32 v162, v162\n"
"global_load_dwordx2 v[52:53], v10, s[0:1] offset:1024 sc1\n"
"v_rcp_f32 v163, v163\n"
"v_rcp_f32 v164, v164\n"
"v_rcp_f32 v165, v165\n"
"v_rcp_f32 v166, v166\n"
"v_rcp_f32 v167, v167\n"
"v_rcp_f32 v168, v168\n"
"s_mov_b64 exec, s[48:49]\n"
"global_load_dwordx4 v[76:79], v5, s[36:37] offset:-1280 sc1\n"
"s_mov_b64 exec, -1\n"
"v_rcp_f32 v169, v169\n"
"9101:\n"
"s_cbranch_execz 9102f\n"
"v_rcp_f32 v170, v170\n"
"v_rcp_f32 v171, v171\n"
"v_rcp_f32 v172, v172\n"
"v_rcp_f32 v173, v173\n"
"v_rcp_f32 v174, v174\n"
"s_mov_b64 exec, s[48:49]\n"
"global_load_dwordx4 v[80:83], v5, s[36:37] offset:-640 sc1\n"
"s_mov_b64 exec, -1\n"
"v_rcp_f32_e64 v175, v175\n"
"v_pk_mul_f32 v[160:161], v[160:161], v[192:193]\n"
"v_pk_mul_f32 v[162:163], v[162:163], v[194:195]\n"
"v_pk_mul_f32 v[164:165], v[164:165], v[196:197]\n"
"v_pk_mul_f32 v[166:167], v[166:167], v[198:199]\n"
"v_pk_mul_f32 v[168:169], v[168:169], v[200:201]\n"
"v_pk_mul_f32 v[170:171], v[170:171], v[202:203]\n"
"v_pk_mul_f32 v[172:173], v[172:173], v[204:205]\n"
"v_pk_mul_f32 v[174:175], v[174:175], v[206:207]\n"
"v_cvt_pk_bf16_f32 v160, v160, v161\n"
"global_load_dwordx4 v[36:39], v9, s[0:1] offset:0 sc1\n"
"v_cvt_pk_bf16_f32 v162, v162, v163\n"
"v_cvt_pk_bf16_f32 v164, v164, v165\n"
"v_cvt_pk_bf16_f32 v166, v166, v167\n"
"v_cvt_pk_bf16_f32 v168, v168, v169\n"
"v_cvt_pk_bf16_f32 v170, v170, v171\n"
"v_cvt_pk_bf16_f32 v172, v172, v173\n"
"v_cvt_pk_bf16_f32 v174, v174, v175\n"
"v_accvgpr_write_b32 a96, v160\n"
"v_accvgpr_write_b32 a97, v162\n"
"v_accvgpr_write_b32 a100, v164\n"
"v_accvgpr_write_b32 a101, v166\n"
"v_accvgpr_write_b32 a104, v168\n"
"global_load_dwordx4 v[40:43], v9, s[0:1] offset:1280 sc1\n"
"v_accvgpr_write_b32 a105, v170\n"
"v_accvgpr_write_b32 a108, v172\n"
"v_accvgpr_write_b32 a109, v174\n"
"v_pk_fma_f32 v[160:161], v[180:181], v[148:149], v[132:133] op_sel:[0,0,0] op_sel_hi:[0,1,1]\n"
"v_pk_fma_f32 v[162:163], v[180:181], v[150:151], v[134:135] op_sel:[0,0,0] op_sel_hi:[0,1,1]\n"
"v_pk_fma_f32 v[164:165], v[180:181], v[148:149], v[132:133] op_sel:[1,0,0] op_sel_hi:[1,1,1]\n"
"v_pk_fma_f32 v[166:167], v[180:181], v[150:151], v[134:135] op_sel:[1,0,0] op_sel_hi:[1,1,1]\n"
"v_pk_fma_f32 v[168:169], v[182:183], v[148:149], v[132:133] op_sel:[0,0,0] op_sel_hi:[0,1,1]\n"
"v_pk_fma_f32 v[170:171], v[182:183], v[150:151], v[134:135] op_sel:[0,0,0] op_sel_hi:[0,1,1]\n"
"v_pk_fma_f32 v[172:173], v[182:183], v[148:149], v[132:133] op_sel:[1,0,0] op_sel_hi:[1,1,1]\n"
"v_pk_fma_f32 v[174:175], v[182:183], v[150:151], v[134:135] op_sel:[1,0,0] op_sel_hi:[1,1,1]\n"
"v_pk_fma_f32 v[208:209], v[208:209], v[176:177], v[160:161] op_sel:[0,0,0] op_sel_hi:[1,0,1]\n"
"global_load_dwordx4 v[60:63], v7, s[36:37] offset:0 sc1\n"
"v_pk_fma_f32 v[210:211], v[210:211], v[176:177], v[162:163] op_sel:[0,0,0] op_sel_hi:[1,0,1]\n"
"v_pk_fma_f32 v[212:213], v[212:213], v[176:177], v[164:165] op_sel:[0,1,0] op_sel_hi:[1,1,1]\n"
"v_pk_fma_f32 v[214:215], v[214:215], v[176:177], v[166:167] op_sel:[0,1,0] op_sel_hi:[1,1,1]\n"
"v_pk_fma_f32 v[216:217], v[216:217], v[178:179], v[168:169] op_sel:[0,0,0] op_sel_hi:[1,0,1]\n"
"v_pk_fma_f32 v[218:219], v[218:219], v[178:179], v[170:171] op_sel:[0,0,0] op_sel_hi:[1,0,1]\n"
"v_pk_fma_f32 v[220:221], v[220:221], v[178:179], v[172:173] op_sel:[0,1,0] op_sel_hi:[1,1,1]\n"
"v_pk_fma_f32 v[222:223], v[222:223], v[178:179], v[174:175] op_sel:[0,1,0] op_sel_hi:[1,1,1]\n"
"v_pk_fma_f32 v[160:161], v[208:209], v[208:209], v[184:185] op_sel:[0,0,1] op_sel_hi:[1,1,1] neg_lo:[1,0,0] neg_hi:[1,0,0]\n"
"v_pk_fma_f32 v[162:163], v[210:211], v[210:211], v[184:185] op_sel:[0,0,1] op_sel_hi:[1,1,1] neg_lo:[1,0,0] neg_hi:[1,0,0]\n"
"v_pk_fma_f32 v[164:165], v[212:213], v[212:213], v[184:185] op_sel:[0,0,1] op_sel_hi:[1,1,1] neg_lo:[1,0,0] neg_hi:[1,0,0]\n"
"v_pk_fma_f32 v[166:167], v[214:215], v[214:215], v[184:185] op_sel:[0,0,1] op_sel_hi:[1,1,1] neg_lo:[1,0,0] neg_hi:[1,0,0]\n"
"v_pk_fma_f32 v[168:169], v[216:217], v[216:217], v[184:185] op_sel:[0,0,1] op_sel_hi:[1,1,1] neg_lo:[1,0,0] neg_hi:[1,0,0]\n"
"s_mov_b64 exec, s[48:49]\n"
"global_load_dwordx4 v[84:87], v5, s[36:37] offset:1280 sc1\n"
"s_mov_b64 exec, -1\n"
"v_pk_fma_f32 v[170:171], v[218:219], v[218:219], v[184:185] op_sel:[0,0,1] op_sel_hi:[1,1,1] neg_lo:[1,0,0] neg_hi:[1,0,0]\n"
"v_pk_fma_f32 v[172:173], v[220:221], v[220:221], v[184:185] op_sel:[0,0,1] op_sel_hi:[1,1,1] neg_lo:[1,0,0] neg_hi:[1,0,0]\n"
"v_pk_fma_f32 v[174:175], v[222:223], v[222:223], v[184:185] op_sel:[0,0,1] op_sel_hi:[1,1,1] neg_lo:[1,0,0] neg_hi:[1,0,0]\n"
"v_pk_mul_f32 v[160:161], v[160:161], v[208:209]\n"
"v_pk_mul_f32 v[162:163], v[162:163], v[210:211]\n"
"v_pk_mul_f32 v[164:165], v[164:165], v[212:213]\n"
"v_pk_mul_f32 v[166:167], v[166:167], v[214:215]\n"
"v_pk_mul_f32 v[168:169], v[168:169], v[216:217]\n"
"v_pk_mul_f32 v[170:171], v[170:171], v[218:219]\n"
"v_pk_mul_f32 v[172:173], v[172:173], v[220:221]\n"
"v_pk_mul_f32 v[174:175], v[174:175], v[222:223]\n"
"s_waitcnt vmcnt(10)\n"
"v_mfma_f32_32x32x4_2b_bf16 v[128:159], v[66:67], v[64:65], 0 cbsz:1 abid:0\n"
"v_exp_f32 v160, v160\n"
"s_mov_b64 exec, s[48:49]\n"
"global_load_dwordx4 v[88:91], v5, s[36:37] offset:1920 sc1\n"
"s_mov_b64 exec, -1\n"
"s_add_u32 s36, s36, 5120\n"
"s_addc_u32 s37, s37, 0\n"
"v_exp_f32 v161, v161\n"
"v_exp_f32 v162, v162\n"
"v_exp_f32 v163, v163\n"
"v_exp_f32 v164, v164\n"
"v_exp_f32 v165, v165\n"
"v_exp_f32 v166, v166\n"
"v_exp_f32 v167, v167\n"
"v_exp_f32 v168, v168\n"
"v_exp_f32 v169, v169\n"
"v_exp_f32 v170, v170\n"
"v_exp_f32 v171, v171\n"
"v_exp_f32 v172, v172\n"
"v_exp_f32 v173, v173\n"
"v_exp_f32 v174, v174\n"
"v_exp_f32 v175, v175\n"
"v_pk_fma_f32 v[160:161], v[160:161], v[190:191], v[190:191] op_sel_hi:[1,0,0]\n"
"v_pk_fma_f32 v[162:163], v[162:163], v[190:191], v[190:191] op_sel_hi:[1,0,0]\n"
"v_pk_fma_f32 v[164:165], v[164:165], v[190:191], v[190:191] op_sel_hi:[1,0,0]\n"
"v_pk_fma_f32 v[166:167], v[166:167], v[190:191], v[190:191] op_sel_hi:[1,0,0]\n"
"v_pk_fma_f32 v[168:169], v[168:169], v[190:191], v[190:191] op_sel_hi:[1,0,0]\n"
"v_pk_fma_f32 v[170:171], v[170:171], v[190:191], v[190:191] op_sel_hi:[1,0,0]\n"
"v_pk_fma_f32 v[172:173], v[172:173], v[190:191], v[190:191] op_sel_hi:[1,0,0]\n"
"v_pk_fma_f32 v[174:175], v[174:175], v[190:191], v[190:191] op_sel_hi:[1,0,0]\n"
"s_waitcnt vmcnt(9)\n"
"v_mfma_f32_32x32x16_bf16 v[128:143], v[56:59], v[44:47], v[128:143]\n"
"s_waitcnt vmcnt(8)\n"
"v_mfma_f32_32x32x16_bf16 v[144:159], v[56:59], v[48:51], v[144:159]\n"
"v_mfma_f32_32x32x16_bf16 v[192:207], v[186:189], v[186:189], 0\n"
"v_rcp_f32 v160, v160\n"
"v_rcp_f32 v161, v161\n"
"v_rcp_f32 v162, v162\n"
"v_rcp_f32 v163, v163\n"
"v_rcp_f32 v164, v164\n"
"v_rcp_f32 v165, v165\n"
"v_rcp_f32 v166, v166\n"
"v_rcp_f32 v167, v167\n"
"v_rcp_f32 v168, v168\n"
"v_rcp_f32 v169, v169\n"
"v_rcp_f32 v170, v170\n"
"v_rcp_f32 v171, v171\n"
"v_rcp_f32 v172, v172\n"
"v_rcp_f32 v173, v173\n"
"v_rcp_f32 v174, v174\n"
"v_rcp_f32 v175, v175\n"
"v_pk_mul_f32 v[160:161], v[160:161], v[208:209]\n"
"v_pk_mul_f32 v[162:163], v[162:163], v[210:211]\n"
"v_pk_mul_f32 v[164:165], v[164:165], v[212:213]\n"
"v_pk_mul_f32 v[166:167], v[166:167], v[214:215]\n"
"v_pk_mul_f32 v[168:169], v[168:169], v[216:217]\n"
"v_pk_mul_f32 v[170:171], v[170:171], v[218:219]\n"
"v_pk_mul_f32 v[172:173], v[172:173], v[220:221]\n"
"v_pk_mul_f32 v[174:175], v[174:175], v[222:223]\n"
"v_cvt_pk_bf16_f32 v160, v160, v161\n"
"v_cvt_pk_bf16_f32 v162, v162, v163\n"
"v_cvt_pk_bf16_f32 v164, v164, v165\n"
"v_cvt_pk_bf16_f32 v166, v166, v167\n"
"v_cvt_pk_bf16_f32 v168, v168, v169\n"
"v_cvt_pk_bf16_f32 v170, v170, v171\n"
"v_cvt_pk_bf16_f32 v172, v172, v173\n"
"v_cvt_pk_bf16_f32 v174, v174, v175\n"
"v_accvgpr_write_b32 a98, v160\n"
"v_accvgpr_write_b32 a99, v162\n"
"v_accvgpr_write_b32 a102, v164\n"
"v_accvgpr_write_b32 a103, v166\n"
"v_accvgpr_write_b32 a106, v168\n"
"v_accvgpr_write_b32 a107, v170\n"
"v_accvgpr_write_b32 a110, v172\n"
"v_accvgpr_write_b32 a111, v174\n"
"13:\n"
"v_exp_f32 v128, v128\n"
"v_exp_f32 v129, v129\n"
"v_mfma_f32_32x32x16_bf16 v[208:223], v[186:189], v[186:189], 0\n"
"global_load_dwordx4 v[68:71], v7, s[36:37] offset:-2560 sc1\n"
"v_exp_f32 v130, v130\n"
"v_exp_f32 v131, v131\n"
"v_exp_f32 v132, v132\n"
"s_waitcnt vmcnt(8)\n"
"v_mfma_f32_32x32x4_2b_bf16 v[160:191], v[66:67], v[52:53], 0 cbsz:1 abid:0\n"
"9102:\n"
"s_cbranch_execz 97b\n"
"v_exp_f32 v133, v133\n"
"v_exp_f32 v134, v134\n"
"v_exp_f32 v135, v135\n"
"v_exp_f32 v136, v136\n"
"v_exp_f32 v137, v137\n"
"v_exp_f32 v138, v138\n"
"v_exp_f32 v139, v139\n"
"v_exp_f32 v140, v140\n"
"v_exp_f32 v141, v141\n"
"s_waitcnt vmcnt(7)\n"
"v_smfmac_f32_32x32x32_bf16 v[224:239], v[76:79], v[128:135], v108\n"
"global_load_dwordx2 v[54:55], v4, s[36:37] offset:-1536 sc1\n"
"v_exp_f32 v142, v142\n"
"v_exp_f32 v143, v143\n"
"v_exp_f32 v144, v144\n"
"v_exp_f32 v145, v145\n"
"s_waitcnt vmcnt(7)\n"
"v_smfmac_f32_32x32x32_bf16 v[224:239], v[80:83], v[136:143], v108\n"
"global_load_dwordx4 v[72:75], v7, s[36:37] offset:0 sc1\n"
"v_exp_f32 v146, v146\n"
"v_exp_f32 v147, v147\n"
"v_exp_f32 v148, v148\n"
"v_exp_f32 v149, v149\n"
"s_waitcnt vmcnt(7)\n"
"v_mfma_f32_32x32x16_bf16 v[160:175], v[56:59], v[36:39], v[160:175]\n"
"v_exp_f32 v150, v150\n"
"v_exp_f32 v151, v151\n"
"v_exp_f32 v152, v152\n"
"v_exp_f32 v153, v153\n"
"v_smfmac_f32_32x32x32_bf16 v[240:255], v[76:79], v[144:151], v108\n"
"s_mov_b64 exec, s[48:49]\n"
"global_load_dwordx4 v[92:95], v5, s[36:37] offset:-1280 sc1\n"
"global_load_dwordx4 v[96:99], v5, s[36:37] offset:-640 sc1\n"
"global_load_dwordx4 v[100:103], v5, s[36:37] offset:1280 sc1\n"
"global_load_dwordx4 v[104:107], v5, s[36:37] offset:1920 sc1\n"
"s_mov_b64 exec, -1\n"
"s_add_u32 s36, s36, 5120\n"
"s_addc_u32 s37, s37, 0\n"
"v_exp_f32 v154, v154\n"
"v_exp_f32 v155, v155\n"
"v_exp_f32 v156, v156\n"
"v_exp_f32 v157, v157\n"
"s_waitcnt vmcnt(10)\n"
"v_mfma_f32_32x32x16_bf16 v[176:191], v[56:59], v[40:43], v[176:191]\n"
"v_exp_f32 v158, v158\n"
"v_exp_f32 v159, v159\n"
"v_exp_f32 v160, v160\n"
"v_exp_f32 v161, v161\n"
"v_smfmac_f32_32x32x32_bf16 v[240:255], v[80:83], v[152:159], v108\n"
"s_mov_b64 exec, s[52:53]\n"
"global_store_dwordx4 v27, a[96:99], s[4:5] offset:0 sc1\n"
"s_mov_b64 exec, -1\n"
"v_exp_f32 v162, v162\n"
"v_exp_f32 v163, v163\n"
"v_exp_f32 v164, v164\n"
"v_mfma_f32_32x32x4_2b_bf16 v[128:159], v[66:67], v[64:65], 0 cbsz:1 abid:1\n"
"v_exp_f32 v165, v165\n"
"v_exp_f32 v166, v166\n"
"v_exp_f32 v167, v167\n"
"v_exp_f32 v168, v168\n"
"v_exp_f32 v169, v169\n"
"v_exp_f32 v170, v170\n"
"v_exp_f32 v171, v171\n"
"v_exp_f32 v172, v172\n"
"v_exp_f32 v173, v173\n"
"v_smfmac_f32_32x32x32_bf16 v[192:207], v[76:79], v[160:167], v108\n"
"s_mov_b64 exec, s[52:53]\n"
"global_store_dwordx4 v27, a[100:103], s[4:5] offset:1024 sc1\n"
"s_mov_b64 exec, -1\n"
"v_exp_f32 v174, v174\n"
"v_exp_f32 v175, v175\n"
"v_exp_f32 v176, v176\n"
"v_exp_f32 v177, v177\n"
"v_smfmac_f32_32x32x32_bf16 v[192:207], v[80:83], v[168:175], v108\n"
"v_exp_f32 v178, v178\n"
"v_exp_f32 v179, v179\n"
"v_exp_f32 v180, v180\n"
"v_exp_f32 v181, v181\n"
"s_waitcnt vmcnt(11)\n"
"v_mfma_f32_32x32x16_bf16 v[128:143], v[60:63], v[44:47], v[128:143]\n"
"v_exp_f32 v182, v182\n"
"v_exp_f32 v183, v183\n"
"v_exp_f32 v184, v184\n"
"v_exp_f32 v185, v185\n"
"v_smfmac_f32_32x32x32_bf16 v[208:223], v[76:79], v[176:183], v108\n"
"v_exp_f32 v186, v186\n"
"v_exp_f32 v187, v187\n"
"v_exp_f32 v188, v188\n"
"v_exp_f32 v189, v189\n"
"v_mfma_f32_32x32x16_bf16 v[144:159], v[60:63], v[48:51], v[144:159]\n"
"v_exp_f32 v190, v190\n"
"v_exp_f32 v191, v191\n"
"v_exp_f32 v128, v128\n"
"v_exp_f32 v129, v129\n"
"v_smfmac_f32_32x32x32_bf16 v[208:223], v[80:83], v[184:191], v108\n"
"v_exp_f32 v130, v130\n"
"v_exp_f32 v131, v131\n"
"v_exp_f32 v132, v132\n"
"v_mfma_f32_32x32x4_2b_bf16 v[160:191], v[66:67], v[52:53], 0 cbsz:1 abid:1\n"
"v_exp_f32 v133, v133\n"
"v_exp_f32 v134, v134\n"
"v_exp_f32 v135, v135\n"
"v_exp_f32 v136, v136\n"
"v_exp_f32 v137, v137\n"
"v_exp_f32 v138, v138\n"
"v_exp_f32 v139, v139\n"
"v_exp_f32 v140, v140\n"
"v_exp_f32 v141, v141\n"
"s_waitcnt vmcnt(10)\n"
"v_smfmac_f32_32x32x32_bf16 v[224:239], v[84:87], v[128:135], v108\n"
"v_exp_f32 v142, v142\n"
"v_exp_f32 v143, v143\n"
"v_exp_f32 v144, v144\n"
"v_exp_f32 v145, v145\n"
"s_waitcnt vmcnt(9)\n"
"v_smfmac_f32_32x32x32_bf16 v[224:239], v[88:91], v[136:143], v108\n"
"v_exp_f32 v146, v146\n"
"v_exp_f32 v147, v147\n"
"v_exp_f32 v148, v148\n"
"v_exp_f32 v149, v149\n"
"v_mfma_f32_32x32x16_bf16 v[160:175], v[60:63], v[36:39], v[160:175]\n"
"v_exp_f32 v150, v150\n"
"v_exp_f32 v151, v151\n"
"v_exp_f32 v152, v152\n"
"v_exp_f32 v153, v153\n"
"v_smfmac_f32_32x32x32_bf16 v[240:255], v[84:87], v[144:151], v108\n"
"v_exp_f32 v154, v154\n"
"v_exp_f32 v155, v155\n"
"v_exp_f32 v156, v156\n"
"v_exp_f32 v157, v157\n"
"v_mfma_f32_32x32x16_bf16 v[176:191], v[60:63], v[40:43], v[176:191]\n"
"v_exp_f32 v158, v158\n"
"v_exp_f32 v159, v159\n"
"v_exp_f32 v160, v160\n"
"v_exp_f32 v161, v161\n"
"v_smfmac_f32_32x32x32_bf16 v[240:255], v[88:91], v[152:159], v108\n"
"s_waitcnt vmcnt(6)\n"
"v_exp_f32 v162, v162\n"
"v_exp_f32 v163, v163\n"
"v_exp_f32 v164, v164\n"
"v_mfma_f32_32x32x4_2b_bf16 v[128:159], v[54:55], v[64:65], 0 cbsz:1 abid:0\n"
"v_exp_f32 v165, v165\n"
"v_exp_f32 v166, v166\n"
"v_exp_f32 v167, v167\n"
"v_exp_f32 v168, v168\n"
"v_exp_f32 v169, v169\n"
"v_exp_f32 v170, v170\n"
"v_exp_f32 v171, v171\n"
"v_exp_f32 v172, v172\n"
"v_exp_f32 v173, v173\n"
"v_smfmac_f32_32x32x32_bf16 v[192:207], v[84:87], v[160:167], v108\n"
"v_exp_f32 v174, v174\n"
"v_exp_f32 v175, v175\n"
"v_exp_f32 v176, v176\n"
"v_exp_f32 v177, v177\n"
"v_smfmac_f32_32x32x32_bf16 v[192:207], v[88:91], v[168:175], v108\n"
"v_exp_f32 v178, v178\n"
"v_exp_f32 v179, v179\n"
"v_exp_f32 v180, v180\n"
"v_exp_f32 v181, v181\n"
"v_mfma_f32_32x32x16_bf16 v[128:143], v[68:71], v[44:47], v[128:143]\n"
"v_exp_f32 v182, v182\n"
"v_exp_f32 v183, v183\n"
"v_exp_f32 v184, v184\n"
"v_exp_f32 v185, v185\n"
"v_smfmac_f32_32x32x32_bf16 v[208:223], v[84:87], v[176:183], v108\n"
"v_exp_f32 v186, v186\n"
"v_exp_f32 v187, v187\n"
"v_exp_f32 v188, v188\n"
"v_exp_f32 v189, v189\n"
"v_mfma_f32_32x32x16_bf16 v[144:159], v[68:71], v[48:51], v[144:159]\n"
"v_exp_f32 v190, v190\n"
"v_exp_f32 v191, v191\n"
"v_exp_f32 v128, v128\n"
"v_exp_f32 v129, v129\n"
"v_smfmac_f32_32x32x32_bf16 v[208:223], v[88:91], v[184:191], v108\n"
"s_waitcnt vmcnt(2)\n"
"global_load_dwordx4 v[56:59], v7, s[36:37] offset:-2560 sc1\n"
"v_exp_f32 v130, v130\n"
"v_exp_f32 v131, v131\n"
"v_exp_f32 v132, v132\n"
"v_mfma_f32_32x32x4_2b_bf16 v[160:191], v[54:55], v[52:53], 0 cbsz:1 abid:0\n"
"v_exp_f32 v133, v133\n"
"v_exp_f32 v134, v134\n"
"v_exp_f32 v135, v135\n"
"v_exp_f32 v136, v136\n"
"v_exp_f32 v137, v137\n"
"v_exp_f32 v138, v138\n"
"v_exp_f32 v139, v139\n"
"v_exp_f32 v140, v140\n"
"v_exp_f32 v141, v141\n"
"v_smfmac_f32_32x32x32_bf16 v[224:239], v[92:95], v[128:135], v108\n"
"global_load_dwordx2 v[66:67], v4, s[36:37] offset:-1536 sc1\n"
"v_exp_f32 v142, v142\n"
"v_exp_f32 v143, v143\n"
"v_exp_f32 v144, v144\n"
"v_exp_f32 v145, v145\n"
"v_smfmac_f32_32x32x32_bf16 v[224:239], v[96:99], v[136:143], v108\n"
"global_load_dwordx4 v[60:63], v7, s[36:37] offset:0 sc1\n"
"v_exp_f32 v146, v146\n"
"v_exp_f32 v147, v147\n"
"v_exp_f32 v148, v148\n"
"v_exp_f32 v149, v149\n"
"v_mfma_f32_32x32x16_bf16 v[160:175], v[68:71], v[36:39], v[160:175]\n"
"v_exp_f32 v150, v150\n"
"v_exp_f32 v151, v151\n"
"v_exp_f32 v152, v152\n"
"v_exp_f32 v153, v153\n"
"v_smfmac_f32_32x32x32_bf16 v[240:255], v[92:95], v[144:151], v108\n"
"s_mov_b64 exec, s[48:49]\n"
"global_load_dwordx4 v[76:79], v5, s[36:37] offset:-1280 sc1\n"
"global_load_dwordx4 v[80:83], v5, s[36:37] offset:-640 sc1\n"
"global_load_dwordx4 v[84:87], v5, s[36:37] offset:1280 sc1\n"
"global_load_dwordx4 v[88:91], v5, s[36:37] offset:1920 sc1\n"
"s_mov_b64 exec, -1\n"
"s_add_u32 s36, s36, 5120\n"
"s_addc_u32 s37, s37, 0\n"
"v_exp_f32 v154, v154\n"
"v_exp_f32 v155, v155\n"
"v_exp_f32 v156, v156\n"
"v_exp_f32 v157, v157\n"
"v_mfma_f32_32x32x16_bf16 v[176:191], v[68:71], v[40:43], v[176:191]\n"
"v_exp_f32 v158, v158\n"
"v_exp_f32 v159, v159\n"
"v_exp_f32 v160, v160\n"
"v_exp_f32 v161, v161\n"
"v_smfmac_f32_32x32x32_bf16 v[240:255], v[96:99], v[152:159], v108\n"
"s_mov_b64 exec, s[52:53]\n"
"global_store_dwordx4 v27, a[104:107], s[4:5] offset:2048 sc1\n"
"s_mov_b64 exec, -1\n"
"v_exp_f32 v162, v162\n"
"v_exp_f32 v163, v163\n"
"v_exp_f32 v164, v164\n"
"v_mfma_f32_32x32x4_2b_bf16 v[128:159], v[54:55], v[64:65], 0 cbsz:1 abid:1\n"
"v_exp_f32 v165, v165\n"
"v_exp_f32 v166, v166\n"
"v_exp_f32 v167, v167\n"
"v_exp_f32 v168, v168\n"
"v_exp_f32 v169, v169\n"
"v_exp_f32 v170, v170\n"
"v_exp_f32 v171, v171\n"
"v_exp_f32 v172, v172\n"
"v_exp_f32 v173, v173\n"
"v_smfmac_f32_32x32x32_bf16 v[192:207], v[92:95], v[160:167], v108\n"
"s_mov_b64 exec, s[52:53]\n"
"global_store_dwordx4 v27, a[108:111], s[4:5] offset:3072 sc1\n"
"s_mov_b64 exec, -1\n"
"v_exp_f32 v174, v174\n"
"v_exp_f32 v175, v175\n"
"v_exp_f32 v176, v176\n"
"v_exp_f32 v177, v177\n"
"v_smfmac_f32_32x32x32_bf16 v[192:207], v[96:99], v[168:175], v108\n"
"v_exp_f32 v178, v178\n"
"v_exp_f32 v179, v179\n"
"v_exp_f32 v180, v180\n"
"v_exp_f32 v181, v181\n"
"v_mfma_f32_32x32x16_bf16 v[128:143], v[72:75], v[44:47], v[128:143]\n"
"v_exp_f32 v182, v182\n"
"v_exp_f32 v183, v183\n"
"v_exp_f32 v184, v184\n"
"v_exp_f32 v185, v185\n"
"v_smfmac_f32_32x32x32_bf16 v[208:223], v[92:95], v[176:183], v108\n"
"v_exp_f32 v186, v186\n"
"v_exp_f32 v187, v187\n"
"v_exp_f32 v188, v188\n"
"v_exp_f32 v189, v189\n"
"v_mfma_f32_32x32x16_bf16 v[144:159], v[72:75], v[48:51], v[144:159]\n"
"v_exp_f32 v190, v190\n"
"v_exp_f32 v191, v191\n"
"v_exp_f32 v128, v128\n"
"v_exp_f32 v129, v129\n"
"v_smfmac_f32_32x32x32_bf16 v[208:223], v[96:99], v[184:191], v108\n"
"v_exp_f32 v130, v130\n"
"v_exp_f32 v131, v131\n"
"v_exp_f32 v132, v132\n"
"v_mfma_f32_32x32x4_2b_bf16 v[160:191], v[54:55], v[52:53], 0 cbsz:1 abid:1\n"
"v_exp_f32 v133, v133\n"
"v_exp_f32 v134, v134\n"
"v_exp_f32 v135, v135\n"
"v_exp_f32 v136, v136\n"
"v_exp_f32 v137, v137\n"
"v_exp_f32 v138, v138\n"
"v_exp_f32 v139, v139\n"
"v_exp_f32 v140, v140\n"
"v_exp_f32 v141, v141\n"
"v_smfmac_f32_32x32x32_bf16 v[224:239], v[100:103], v[128:135], v108\n"
"v_exp_f32 v142, v142\n"
"v_exp_f32 v143, v143\n"
"v_exp_f32 v144, v144\n"
"v_exp_f32 v145, v145\n"
"v_smfmac_f32_32x32x32_bf16 v[224:239], v[104:107], v[136:143], v108\n"
"v_exp_f32 v146, v146\n"
"v_exp_f32 v147, v147\n"
"v_exp_f32 v148, v148\n"
"v_exp_f32 v149, v149\n"
"v_mfma_f32_32x32x16_bf16 v[160:175], v[72:75], v[36:39], v[160:175]\n"
"v_exp_f32 v150, v150\n"
"v_exp_f32 v151, v151\n"
"v_exp_f32 v152, v152\n"
"v_exp_f32 v153, v153\n"
"v_smfmac_f32_32x32x32_bf16 v[240:255], v[100:103], v[144:151], v108\n"
"v_exp_f32 v154, v154\n"
"v_exp_f32 v155, v155\n"
"v_exp_f32 v156, v156\n"
"v_exp_f32 v157, v157\n"
"v_mfma_f32_32x32x16_bf16 v[176:191], v[72:75], v[40:43], v[176:191]\n"
"v_exp_f32 v158, v158\n"
"v_exp_f32 v159, v159\n"
"v_exp_f32 v160, v160\n"
"v_exp_f32 v161, v161\n"
"v_smfmac_f32_32x32x32_bf16 v[240:255], v[104:107], v[152:159], v108\n"
"s_waitcnt vmcnt(6)\n"
"v_exp_f32 v162, v162\n"
"v_exp_f32 v163, v163\n"
"v_exp_f32 v164, v164\n"
"v_mfma_f32_32x32x4_2b_bf16 v[128:159], v[66:67], v[64:65], 0 cbsz:1 abid:0\n"
"v_exp_f32 v165, v165\n"
"v_exp_f32 v166, v166\n"
"v_exp_f32 v167, v167\n"
"v_exp_f32 v168, v168\n"
"v_exp_f32 v169, v169\n"
"v_exp_f32 v170, v170\n"
"v_exp_f32 v171, v171\n"
"v_exp_f32 v172, v172\n"
"v_exp_f32 v173, v173\n"
"v_smfmac_f32_32x32x32_bf16 v[192:207], v[100:103], v[160:167], v108\n"
"v_exp_f32 v174, v174\n"
"v_exp_f32 v175, v175\n"
"v_exp_f32 v176, v176\n"
"v_exp_f32 v177, v177\n"
"v_smfmac_f32_32x32x32_bf16 v[192:207], v[104:107], v[168:175], v108\n"
"v_exp_f32 v178, v178\n"
"v_exp_f32 v179, v179\n"
"v_exp_f32 v180, v180\n"
"v_exp_f32 v181, v181\n"
"v_mfma_f32_32x32x16_bf16 v[128:143], v[56:59], v[44:47], v[128:143]\n"
"global_load_dword v109, v1, s[66:67] offset:0 sc1\n"
"v_exp_f32 v182, v182\n"
"v_exp_f32 v183, v183\n"
"v_exp_f32 v184, v184\n"
"v_exp_f32 v185, v185\n"
"v_smfmac_f32_32x32x32_bf16 v[208:223], v[100:103], v[176:183], v108\n"
"v_exp_f32 v186, v186\n"
"v_exp_f32 v187, v187\n"
"v_exp_f32 v188, v188\n"
"v_exp_f32 v189, v189\n"
"v_mfma_f32_32x32x16_bf16 v[144:159], v[56:59], v[48:51], v[144:159]\n"
"v_exp_f32 v190, v190\n"
"v_exp_f32 v191, v191\n"
"v_exp_f32 v128, v128\n"
"v_exp_f32 v129, v129\n"
"v_smfmac_f32_32x32x32_bf16 v[208:223], v[104:107], v[184:191], v108\n"
"s_waitcnt vmcnt(3)\n"
"global_load_dwordx4 v[68:71], v7, s[36:37] offset:-2560 sc1\n"
"v_exp_f32 v130, v130\n"
"v_exp_f32 v131, v131\n"
"v_exp_f32 v132, v132\n"
"v_mfma_f32_32x32x4_2b_bf16 v[160:191], v[66:67], v[52:53], 0 cbsz:1 abid:0\n"
"v_exp_f32 v133, v133\n"
"v_exp_f32 v134, v134\n"
"v_exp_f32 v135, v135\n"
"v_exp_f32 v136, v136\n"
"v_exp_f32 v137, v137\n"
"v_exp_f32 v138, v138\n"
"v_exp_f32 v139, v139\n"
"v_exp_f32 v140, v140\n"
"v_exp_f32 v141, v141\n"
"v_smfmac_f32_32x32x32_bf16 v[224:239], v[76:79], v[128:135], v108\n"
"global_load_dwordx2 v[54:55], v4, s[36:37] offset:-1536 sc1\n"
"v_exp_f32 v142, v142\n"
"v_exp_f32 v143, v143\n"
"v_exp_f32 v144, v144\n"
"v_exp_f32 v145, v145\n"
"v_smfmac_f32_32x32x32_bf16 v[224:239], v[80:83], v[136:143], v108\n"
"global_load_dwordx4 v[72:75], v7, s[36:37] offset:0 sc1\n"
"v_exp_f32 v146, v146\n"
"v_exp_f32 v147, v147\n"
"v_exp_f32 v148, v148\n"
"v_exp_f32 v149, v149\n"
"v_mfma_f32_32x32x16_bf16 v[160:175], v[56:59], v[36:39], v[160:175]\n"
"v_exp_f32 v150, v150\n"
"v_exp_f32 v151, v151\n"
"v_exp_f32 v152, v152\n"
"v_exp_f32 v153, v153\n"
"v_smfmac_f32_32x32x32_bf16 v[240:255], v[76:79], v[144:151], v108\n"
"s_mov_b64 exec, s[48:49]\n"
"global_load_dwordx4 v[92:95], v5, s[36:37] offset:-1280 sc1\n"
"global_load_dwordx4 v[96:99], v5, s[36:37] offset:-640 sc1\n"
"global_load_dwordx4 v[100:103], v5, s[36:37] offset:1280 sc1\n"
"global_load_dwordx4 v[104:107], v5, s[36:37] offset:1920 sc1\n"
"s_mov_b64 exec, -1\n"
"s_add_u32 s36, s36, 5120\n"
"s_addc_u32 s37, s37, 0\n"
"v_exp_f32 v154, v154\n"
"v_exp_f32 v155, v155\n"
"v_exp_f32 v156, v156\n"
"v_exp_f32 v157, v157\n"
"v_mfma_f32_32x32x16_bf16 v[176:191], v[56:59], v[40:43], v[176:191]\n"
"v_exp_f32 v158, v158\n"
"v_exp_f32 v159, v159\n"
"v_exp_f32 v160, v160\n"
"v_exp_f32 v161, v161\n"
"v_smfmac_f32_32x32x32_bf16 v[240:255], v[80:83], v[152:159], v108\n"
"s_mov_b64 exec, s[52:53]\n"
"global_store_dwordx4 v18, a[112:115], s[4:5] offset:0 sc1\n"
"s_mov_b64 exec, -1\n"
"v_exp_f32 v162, v162\n"
"v_exp_f32 v163, v163\n"
"v_exp_f32 v164, v164\n"
"v_mfma_f32_32x32x4_2b_bf16 v[128:159], v[66:67], v[64:65], 0 cbsz:1 abid:1\n"
"v_exp_f32 v165, v165\n"
"v_exp_f32 v166, v166\n"
"v_exp_f32 v167, v167\n"
"v_exp_f32 v168, v168\n"
"v_exp_f32 v169, v169\n"
"v_exp_f32 v170, v170\n"
"v_exp_f32 v171, v171\n"
"v_exp_f32 v172, v172\n"
"v_exp_f32 v173, v173\n"
"v_smfmac_f32_32x32x32_bf16 v[192:207], v[76:79], v[160:167], v108\n"
"s_mov_b64 exec, s[52:53]\n"
"global_store_dwordx4 v18, a[116:119], s[4:5] offset:1024 sc1\n"
"s_mov_b64 exec, -1\n"
"v_exp_f32 v174, v174\n"
"v_exp_f32 v175, v175\n"
"v_exp_f32 v176, v176\n"
"v_exp_f32 v177, v177\n"
"v_smfmac_f32_32x32x32_bf16 v[192:207], v[80:83], v[168:175], v108\n"
"v_exp_f32 v178, v178\n"
"v_exp_f32 v179, v179\n"
"v_exp_f32 v180, v180\n"
"v_exp_f32 v181, v181\n"
"v_mfma_f32_32x32x16_bf16 v[128:143], v[60:63], v[44:47], v[128:143]\n"
"v_exp_f32 v182, v182\n"
"v_exp_f32 v183, v183\n"
"v_exp_f32 v184, v184\n"
"v_exp_f32 v185, v185\n"
"v_smfmac_f32_32x32x32_bf16 v[208:223], v[76:79], v[176:183], v108\n"
"v_exp_f32 v186, v186\n"
"v_exp_f32 v187, v187\n"
"v_exp_f32 v188, v188\n"
"v_exp_f32 v189, v189\n"
"v_mfma_f32_32x32x16_bf16 v[144:159], v[60:63], v[48:51], v[144:159]\n"
"v_exp_f32 v190, v190\n"
"v_exp_f32 v191, v191\n"
"v_exp_f32 v128, v128\n"
"v_exp_f32 v129, v129\n"
"v_smfmac_f32_32x32x32_bf16 v[208:223], v[80:83], v[184:191], v108\n"
"v_exp_f32 v130, v130\n"
"v_exp_f32 v131, v131\n"
"v_exp_f32 v132, v132\n"
"v_mfma_f32_32x32x4_2b_bf16 v[160:191], v[66:67], v[52:53], 0 cbsz:1 abid:1\n"
"v_exp_f32 v133, v133\n"
"v_exp_f32 v134, v134\n"
"v_exp_f32 v135, v135\n"
"v_exp_f32 v136, v136\n"
"v_exp_f32 v137, v137\n"
"v_exp_f32 v138, v138\n"
"v_exp_f32 v139, v139\n"
"v_exp_f32 v140, v140\n"
"v_exp_f32 v141, v141\n"
"v_smfmac_f32_32x32x32_bf16 v[224:239], v[84:87], v[128:135], v108\n"
"s_waitcnt vmcnt(9)\n"
"v_cmp_eq_u32 vcc, v109, v35\n"
"v_exp_f32 v142, v142\n"
"v_exp_f32 v143, v143\n"
"v_exp_f32 v144, v144\n"
"v_exp_f32 v145, v145\n"
"v_smfmac_f32_32x32x32_bf16 v[224:239], v[88:91], v[136:143], v108\n"
"s_cbranch_vccnz 24f\n"
"25:\n"
"v_exp_f32 v146, v146\n"
"v_exp_f32 v147, v147\n"
"v_exp_f32 v148, v148\n"
"v_exp_f32 v149, v149\n"
"v_mfma_f32_32x32x16_bf16 v[160:175], v[60:63], v[36:39], v[160:175]\n"
"v_exp_f32 v150, v150\n"
"v_exp_f32 v151, v151\n"
"v_exp_f32 v152, v152\n"
"v_exp_f32 v153, v153\n"
"v_smfmac_f32_32x32x32_bf16 v[240:255], v[84:87], v[144:151], v108\n"
"v_exp_f32 v154, v154\n"
"v_exp_f32 v155, v155\n"
"v_exp_f32 v156, v156\n"
"v_exp_f32 v157, v157\n"
"v_mfma_f32_32x32x16_bf16 v[176:191], v[60:63], v[40:43], v[176:191]\n"
"v_exp_f32 v158, v158\n"
"v_exp_f32 v159, v159\n"
"v_exp_f32 v160, v160\n"
"v_exp_f32 v161, v161\n"
"v_smfmac_f32_32x32x32_bf16 v[240:255], v[88:91], v[152:159], v108\n"
"s_waitcnt vmcnt(6)\n"
"v_exp_f32 v162, v162\n"
"v_exp_f32 v163, v163\n"
"v_exp_f32 v164, v164\n"
"v_mfma_f32_32x32x4_2b_bf16 v[128:159], v[54:55], v[64:65], 0 cbsz:1 abid:0\n"
"v_exp_f32 v165, v165\n"
"v_exp_f32 v166, v166\n"
"v_exp_f32 v167, v167\n"
"v_exp_f32 v168, v168\n"
"v_exp_f32 v169, v169\n"
"v_exp_f32 v170, v170\n"
"v_exp_f32 v171, v171\n"
"v_exp_f32 v172, v172\n"
"v_exp_f32 v173, v173\n"
"v_smfmac_f32_32x32x32_bf16 v[192:207], v[84:87], v[160:167], v108\n"
"v_exp_f32 v174, v174\n"
"v_exp_f32 v175, v175\n"
"v_exp_f32 v176, v176\n"
"v_exp_f32 v177, v177\n"
"v_smfmac_f32_32x32x32_bf16 v[192:207], v[88:91], v[168:175], v108\n"
"v_exp_f32 v178, v178\n"
"v_exp_f32 v179, v179\n"
"v_exp_f32 v180, v180\n"
"v_exp_f32 v181, v181\n"
"v_mfma_f32_32x32x16_bf16 v[128:143], v[68:71], v[44:47], v[128:143]\n"
"v_exp_f32 v182, v182\n"
"v_exp_f32 v183, v183\n"
"v_exp_f32 v184, v184\n"
"v_exp_f32 v185, v185\n"
"v_smfmac_f32_32x32x32_bf16 v[208:223], v[84:87], v[176:183], v108\n"
"v_exp_f32 v186, v186\n"
"v_exp_f32 v187, v187\n"
"v_exp_f32 v188, v188\n"
"v_exp_f32 v189, v189\n"
"v_mfma_f32_32x32x16_bf16 v[144:159], v[68:71], v[48:51], v[144:159]\n"
"v_exp_f32 v190, v190\n"
"v_exp_f32 v191, v191\n"
"v_exp_f32 v128, v128\n"
"v_exp_f32 v129, v129\n"
"v_smfmac_f32_32x32x32_bf16 v[208:223], v[88:91], v[184:191], v108\n"
"s_waitcnt vmcnt(2)\n"
"global_load_dwordx4 v[56:59], v7, s[36:37] offset:-2560 sc1\n"
"v_exp_f32 v130, v130\n"
"v_exp_f32 v131, v131\n"
"v_exp_f32 v132, v132\n"
"v_mfma_f32_32x32x4_2b_bf16 v[160:191], v[54:55], v[52:53], 0 cbsz:1 abid:0\n"
"v_exp_f32 v133, v133\n"
"v_exp_f32 v134, v134\n"
"v_exp_f32 v135, v135\n"
"v_exp_f32 v136, v136\n"
"v_exp_f32 v137, v137\n"
"v_exp_f32 v138, v138\n"
"v_exp_f32 v139, v139\n"
"v_exp_f32 v140, v140\n"
"v_exp_f32 v141, v141\n"
"v_smfmac_f32_32x32x32_bf16 v[224:239], v[92:95], v[128:135], v108\n"
"global_load_dwordx2 v[66:67], v4, s[36:37] offset:-1536 sc1\n"
"v_exp_f32 v142, v142\n"
"v_exp_f32 v143, v143\n"
"v_exp_f32 v144, v144\n"
"v_exp_f32 v145, v145\n"
"v_smfmac_f32_32x32x32_bf16 v[224:239], v[96:99], v[136:143], v108\n"
"global_load_dwordx4 v[60:63], v7, s[36:37] offset:0 sc1\n"
"v_exp_f32 v146, v146\n"
"v_exp_f32 v147, v147\n"
"v_exp_f32 v148, v148\n"
"v_exp_f32 v149, v149\n"
"v_mfma_f32_32x32x16_bf16 v[160:175], v[68:71], v[36:39], v[160:175]\n"
"v_exp_f32 v150, v150\n"
"v_exp_f32 v151, v151\n"
"v_exp_f32 v152, v152\n"
"v_exp_f32 v153, v153\n"
"v_smfmac_f32_32x32x32_bf16 v[240:255], v[92:95], v[144:151], v108\n"
"s_mov_b64 exec, s[48:49]\n"
"global_load_dwordx4 v[76:79], v5, s[36:37] offset:-1280 sc1\n"
"global_load_dwordx4 v[80:83], v5, s[36:37] offset:-640 sc1\n"
"global_load_dwordx4 v[84:87], v5, s[36:37] offset:1280 sc1\n"
"global_load_dwordx4 v[88:91], v5, s[36:37] offset:1920 sc1\n"
"s_mov_b64 exec, -1\n"
"s_add_u32 s36, s36, 5120\n"
"s_addc_u32 s37, s37, 0\n"
"v_exp_f32 v154, v154\n"
"v_exp_f32 v155, v155\n"
"v_exp_f32 v156, v156\n"
"v_exp_f32 v157, v157\n"
"v_mfma_f32_32x32x16_bf16 v[176:191], v[68:71], v[40:43], v[176:191]\n"
"v_exp_f32 v158, v158\n"
"v_exp_f32 v159, v159\n"
"v_exp_f32 v160, v160\n"
"v_exp_f32 v161, v161\n"
"v_smfmac_f32_32x32x32_bf16 v[240:255], v[96:99], v[152:159], v108\n"
"s_mov_b64 exec, s[52:53]\n"
"global_store_dwordx4 v18, a[120:123], s[4:5] offset:2048 sc1\n"
"s_mov_b64 exec, -1\n"
"v_exp_f32 v162, v162\n"
"v_exp_f32 v163, v163\n"
"v_exp_f32 v164, v164\n"
"v_mfma_f32_32x32x4_2b_bf16 v[128:159], v[54:55], v[64:65], 0 cbsz:1 abid:1\n"
"v_exp_f32 v165, v165\n"
"v_exp_f32 v166, v166\n"
"v_exp_f32 v167, v167\n"
"v_exp_f32 v168, v168\n"
"v_exp_f32 v169, v169\n"
"v_exp_f32 v170, v170\n"
"v_exp_f32 v171, v171\n"
"v_exp_f32 v172, v172\n"
"v_exp_f32 v173, v173\n"
"v_smfmac_f32_32x32x32_bf16 v[192:207], v[92:95], v[160:167], v108\n"
"s_mov_b64 exec, s[52:53]\n"
"global_store_dwordx4 v18, a[124:127], s[4:5] offset:3072 sc1\n"
"s_mov_b64 exec, -1\n"
"v_exp_f32 v174, v174\n"
"v_exp_f32 v175, v175\n"
"v_exp_f32 v176, v176\n"
"v_exp_f32 v177, v177\n"
"v_smfmac_f32_32x32x32_bf16 v[192:207], v[96:99], v[168:175], v108\n"
"v_exp_f32 v178, v178\n"
"v_exp_f32 v179, v179\n"
"v_exp_f32 v180, v180\n"
"v_exp_f32 v181, v181\n"
"v_mfma_f32_32x32x16_bf16 v[128:143], v[72:75], v[44:47], v[128:143]\n"
"v_exp_f32 v182, v182\n"
"v_exp_f32 v183, v183\n"
"v_exp_f32 v184, v184\n"
"v_exp_f32 v185, v185\n"
"v_smfmac_f32_32x32x32_bf16 v[208:223], v[92:95], v[176:183], v108\n"
"v_exp_f32 v186, v186\n"
"v_exp_f32 v187, v187\n"
"v_exp_f32 v188, v188\n"
"v_exp_f32 v189, v189\n"
"v_mfma_f32_32x32x16_bf16 v[144:159], v[72:75], v[48:51], v[144:159]\n"
"v_exp_f32 v190, v190\n"
"v_exp_f32 v191, v191\n"
"v_exp_f32 v128, v128\n"
"v_exp_f32 v129, v129\n"
"v_smfmac_f32_32x32x32_bf16 v[208:223], v[96:99], v[184:191], v108\n"
"v_exp_f32 v130, v130\n"
"v_exp_f32 v131, v131\n"
"v_exp_f32 v132, v132\n"
"v_mfma_f32_32x32x4_2b_bf16 v[160:191], v[54:55], v[52:53], 0 cbsz:1 abid:1\n"
"v_exp_f32 v133, v133\n"
"v_exp_f32 v134, v134\n"
"v_exp_f32 v135, v135\n"
"v_exp_f32 v136, v136\n"
"v_exp_f32 v137, v137\n"
"v_exp_f32 v138, v138\n"
"v_exp_f32 v139, v139\n"
"v_exp_f32 v140, v140\n"
"v_exp_f32 v141, v141\n"
"v_smfmac_f32_32x32x32_bf16 v[224:239], v[100:103], v[128:135], v108\n"
"v_exp_f32 v142, v142\n"
"v_exp_f32 v143, v143\n"
"v_exp_f32 v144, v144\n"
"v_exp_f32 v145, v145\n"
"v_smfmac_f32_32x32x32_bf16 v[224:239], v[104:107], v[136:143], v108\n"
"v_exp_f32 v146, v146\n"
"v_exp_f32 v147, v147\n"
"v_exp_f32 v148, v148\n"
"v_exp_f32 v149, v149\n"
"v_mfma_f32_32x32x16_bf16 v[160:175], v[72:75], v[36:39], v[160:175]\n"
"v_exp_f32 v150, v150\n"
"v_exp_f32 v151, v151\n"
"v_exp_f32 v152, v152\n"
"v_exp_f32 v153, v153\n"
"v_smfmac_f32_32x32x32_bf16 v[240:255], v[100:103], v[144:151], v108\n"
"v_exp_f32 v154, v154\n"
"v_exp_f32 v155, v155\n"
"v_exp_f32 v156, v156\n"
"v_exp_f32 v157, v157\n"
"v_mfma_f32_32x32x16_bf16 v[176:191], v[72:75], v[40:43], v[176:191]\n"
"v_exp_f32 v158, v158\n"
"v_exp_f32 v159, v159\n"
"v_exp_f32 v160, v160\n"
"v_exp_f32 v161, v161\n"
"v_smfmac_f32_32x32x32_bf16 v[240:255], v[104:107], v[152:159], v108\n"
"s_waitcnt vmcnt(6)\n"
"v_exp_f32 v162, v162\n"
"v_exp_f32 v163, v163\n"
"v_exp_f32 v164, v164\n"
"v_mfma_f32_32x32x4_2b_bf16 v[128:159], v[66:67], v[64:65], 0 cbsz:1 abid:0\n"
"v_exp_f32 v165, v165\n"
"v_exp_f32 v166, v166\n"
"v_exp_f32 v167, v167\n"
"v_exp_f32 v168, v168\n"
"v_exp_f32 v169, v169\n"
"v_exp_f32 v170, v170\n"
"v_exp_f32 v171, v171\n"
"v_exp_f32 v172, v172\n"
"v_exp_f32 v173, v173\n"
"v_smfmac_f32_32x32x32_bf16 v[192:207], v[100:103], v[160:167], v108\n"
"v_exp_f32 v174, v174\n"
"v_exp_f32 v175, v175\n"
"v_exp_f32 v176, v176\n"
"v_exp_f32 v177, v177\n"
"v_smfmac_f32_32x32x32_bf16 v[192:207], v[104:107], v[168:175], v108\n"
"v_exp_f32 v178, v178\n"
"v_exp_f32 v179, v179\n"
"v_exp_f32 v180, v180\n"
"v_exp_f32 v181, v181\n"
"v_mfma_f32_32x32x16_bf16 v[128:143], v[56:59], v[44:47], v[128:143]\n"
"v_exp_f32 v182, v182\n"
"v_exp_f32 v183, v183\n"
"v_exp_f32 v184, v184\n"
"v_exp_f32 v185, v185\n"
"v_smfmac_f32_32x32x32_bf16 v[208:223], v[100:103], v[176:183], v108\n"
"v_exp_f32 v186, v186\n"
"v_exp_f32 v187, v187\n"
"v_exp_f32 v188, v188\n"
"v_exp_f32 v189, v189\n"
"v_mfma_f32_32x32x16_bf16 v[144:159], v[56:59], v[48:51], v[144:159]\n"
"v_exp_f32 v190, v190\n"
"v_exp_f32 v191, v191\n"
"v_exp_f32 v128, v128\n"
"v_exp_f32 v129, v129\n"
"v_smfmac_f32_32x32x32_bf16 v[208:223], v[104:107], v[184:191], v108\n"
"s_waitcnt vmcnt(2)\n"
"global_load_dwordx4 v[68:71], v7, s[36:37] offset:-2560 sc1\n"
"v_exp_f32 v130, v130\n"
"v_exp_f32 v131, v131\n"
"v_exp_f32 v132, v132\n"
"v_mfma_f32_32x32x4_2b_bf16 v[160:191], v[66:67], v[52:53], 0 cbsz:1 abid:0\n"
"v_exp_f32 v133, v133\n"
"v_exp_f32 v134, v134\n"
"v_exp_f32 v135, v135\n"
"v_exp_f32 v136, v136\n"
"v_exp_f32 v137, v137\n"
"v_exp_f32 v138, v138\n"
"v_exp_f32 v139, v139\n"
"v_exp_f32 v140, v140\n"
"v_exp_f32 v141, v141\n"
"v_smfmac_f32_32x32x32_bf16 v[224:239], v[76:79], v[128:135], v108\n"
"global_load_dwordx2 v[54:55], v4, s[36:37] offset:-1536 sc1\n"
"v_exp_f32 v142, v142\n"
"v_exp_f32 v143, v143\n"
"v_exp_f32 v144, v144\n"
"v_exp_f32 v145, v145\n"
"v_smfmac_f32_32x32x32_bf16 v[224:239], v[80:83], v[136:143], v108\n"
"global_load_dwordx4 v[72:75], v7, s[36:37] offset:0 sc1\n"
"v_exp_f32 v146, v146\n"
"v_exp_f32 v147, v147\n"
"v_exp_f32 v148, v148\n"
"v_exp_f32 v149, v149\n"
"v_mfma_f32_32x32x16_bf16 v[160:175], v[56:59], v[36:39], v[160:175]\n"
"v_exp_f32 v150, v150\n"
"v_exp_f32 v151, v151\n"
"v_exp_f32 v152, v152\n"
"v_exp_f32 v153, v153\n"
"v_smfmac_f32_32x32x32_bf16 v[240:255], v[76:79], v[144:151], v108\n"
"s_mov_b64 exec, s[48:49]\n"
"global_load_dwordx4 v[92:95], v5, s[36:37] offset:-1280 sc1\n"
"global_load_dwordx4 v[96:99], v5, s[36:37] offset:-640 sc1\n"
"global_load_dwordx4 v[100:103], v5, s[36:37] offset:1280 sc1\n"
"global_load_dwordx4 v[104:107], v5, s[36:37] offset:1920 sc1\n"
"s_mov_b64 exec, -1\n"
"s_add_u32 s36, s36, 5120\n"
"s_addc_u32 s37, s37, 0\n"
"v_exp_f32 v154, v154\n"
"v_exp_f32 v155, v155\n"
"v_exp_f32 v156, v156\n"
"v_exp_f32 v157, v157\n"
"v_mfma_f32_32x32x16_bf16 v[176:191], v[56:59], v[40:43], v[176:191]\n"
"v_exp_f32 v158, v158\n"
"v_exp_f32 v159, v159\n"
"v_exp_f32 v160, v160\n"
"v_exp_f32 v161, v161\n"
"v_smfmac_f32_32x32x32_bf16 v[240:255], v[80:83], v[152:159], v108\n"
"v_exp_f32 v162, v162\n"
"v_exp_f32 v163, v163\n"
"v_exp_f32 v164, v164\n"
"v_mfma_f32_32x32x4_2b_bf16 v[128:159], v[66:67], v[64:65], 0 cbsz:1 abid:1\n"
"v_exp_f32 v165, v165\n"
"v_exp_f32 v166, v166\n"
"v_exp_f32 v167, v167\n"
"v_exp_f32 v168, v168\n"
"v_exp_f32 v169, v169\n"
"v_exp_f32 v170, v170\n"
"v_exp_f32 v171, v171\n"
"v_exp_f32 v172, v172\n"
"v_exp_f32 v173, v173\n"
"v_smfmac_f32_32x32x32_bf16 v[192:207], v[76:79], v[160:167], v108\n"
"s_waitcnt vmcnt(7)\n"
"s_cmp_eq_u32 s30, 0\n"
"s_cselect_b64 exec, 10, 2\n"
"global_atomic_add v15, v110, s[66:67]\n"
"s_mov_b64 exec, -1\n"
"v_exp_f32 v174, v174\n"
"v_exp_f32 v175, v175\n"
"v_exp_f32 v176, v176\n"
"v_exp_f32 v177, v177\n"
"v_smfmac_f32_32x32x32_bf16 v[192:207], v[80:83], v[168:175], v108\n"
"v_exp_f32 v178, v178\n"
"v_exp_f32 v179, v179\n"
"v_exp_f32 v180, v180\n"
"v_exp_f32 v181, v181\n"
"v_mfma_f32_32x32x16_bf16 v[128:143], v[60:63], v[44:47], v[128:143]\n"
"v_exp_f32 v182, v182\n"
"v_exp_f32 v183, v183\n"
"v_exp_f32 v184, v184\n"
"v_exp_f32 v185, v185\n"
"v_smfmac_f32_32x32x32_bf16 v[208:223], v[76:79], v[176:183], v108\n"
"v_exp_f32 v186, v186\n"
"v_exp_f32 v187, v187\n"
"v_exp_f32 v188, v188\n"
"v_exp_f32 v189, v189\n"
"v_mfma_f32_32x32x16_bf16 v[144:159], v[60:63], v[48:51], v[144:159]\n"
"v_exp_f32 v190, v190\n"
"v_exp_f32 v191, v191\n"
"v_exp_f32 v128, v128\n"
"v_exp_f32 v129, v129\n"
"v_smfmac_f32_32x32x32_bf16 v[208:223], v[80:83], v[184:191], v108\n"
"v_exp_f32 v130, v130\n"
"v_exp_f32 v131, v131\n"
"v_exp_f32 v132, v132\n"
"v_mfma_f32_32x32x4_2b_bf16 v[160:191], v[66:67], v[52:53], 0 cbsz:1 abid:1\n"
"v_exp_f32 v133, v133\n"
"v_exp_f32 v134, v134\n"
"v_exp_f32 v135, v135\n"
"v_exp_f32 v136, v136\n"
"v_exp_f32 v137, v137\n"
"v_exp_f32 v138, v138\n"
"v_exp_f32 v139, v139\n"
"v_exp_f32 v140, v140\n"
"v_exp_f32 v141, v141\n"
"v_smfmac_f32_32x32x32_bf16 v[224:239], v[84:87], v[128:135], v108\n"
"v_exp_f32 v142, v142\n"
"v_exp_f32 v143, v143\n"
"v_exp_f32 v144, v144\n"
"v_exp_f32 v145, v145\n"
"v_smfmac_f32_32x32x32_bf16 v[224:239], v[88:91], v[136:143], v108\n"
"v_exp_f32 v146, v146\n"
"v_exp_f32 v147, v147\n"
"v_exp_f32 v148, v148\n"
"v_exp_f32 v149, v149\n"
"v_mfma_f32_32x32x16_bf16 v[160:175], v[60:63], v[36:39], v[160:175]\n"
"v_exp_f32 v150, v150\n"
"v_exp_f32 v151, v151\n"
"v_exp_f32 v152, v152\n"
"v_exp_f32 v153, v153\n"
"v_smfmac_f32_32x32x32_bf16 v[240:255], v[84:87], v[144:151], v108\n"
"v_exp_f32 v154, v154\n"
"v_exp_f32 v155, v155\n"
"v_exp_f32 v156, v156\n"
"v_exp_f32 v157, v157\n"
"v_mfma_f32_32x32x16_bf16 v[176:191], v[60:63], v[40:43], v[176:191]\n"
"v_exp_f32 v158, v158\n"
"v_exp_f32 v159, v159\n"
"v_exp_f32 v160, v160\n"
"v_exp_f32 v161, v161\n"
"v_smfmac_f32_32x32x32_bf16 v[240:255], v[88:91], v[152:159], v108\n"
"s_waitcnt vmcnt(5)\n"
"v_exp_f32 v162, v162\n"
"v_exp_f32 v163, v163\n"
"v_exp_f32 v164, v164\n"
"v_mfma_f32_32x32x4_2b_bf16 v[128:159], v[54:55], v[64:65], 0 cbsz:1 abid:0\n"
"v_exp_f32 v165, v165\n"
"v_exp_f32 v166, v166\n"
"v_exp_f32 v167, v167\n"
"v_exp_f32 v168, v168\n"
"v_exp_f32 v169, v169\n"
"v_exp_f32 v170, v170\n"
"v_exp_f32 v171, v171\n"
"v_exp_f32 v172, v172\n"
"v_exp_f32 v173, v173\n"
"v_smfmac_f32_32x32x32_bf16 v[192:207], v[84:87], v[160:167], v108\n"
"v_exp_f32 v174, v174\n"
"v_exp_f32 v175, v175\n"
"v_exp_f32 v176, v176\n"
"v_exp_f32 v177, v177\n"
"v_smfmac_f32_32x32x32_bf16 v[192:207], v[88:91], v[168:175], v108\n"
"v_exp_f32 v178, v178\n"
"v_exp_f32 v179, v179\n"
"v_exp_f32 v180, v180\n"
"v_exp_f32 v181, v181\n"
"v_mfma_f32_32x32x16_bf16 v[128:143], v[68:71], v[44:47], v[128:143]\n"
"v_exp_f32 v182, v182\n"
"v_exp_f32 v183, v183\n"
"v_exp_f32 v184, v184\n"
"v_exp_f32 v185, v185\n"
"v_smfmac_f32_32x32x32_bf16 v[208:223], v[84:87], v[176:183], v108\n"
"v_exp_f32 v186, v186\n"
"v_exp_f32 v187, v187\n"
"v_exp_f32 v188, v188\n"
"v_exp_f32 v189, v189\n"
"v_mfma_f32_32x32x16_bf16 v[144:159], v[68:71], v[48:51], v[144:159]\n"
"v_exp_f32 v190, v190\n"
"v_exp_f32 v191, v191\n"
"v_exp_f32 v128, v128\n"
"v_exp_f32 v129, v129\n"
"v_smfmac_f32_32x32x32_bf16 v[208:223], v[88:91], v[184:191], v108\n"
"s_waitcnt vmcnt(1)\n"
"global_load_dwordx4 v[56:59], v7, s[36:37] offset:-2560 sc1\n"
"v_exp_f32 v130, v130\n"
"v_exp_f32 v131, v131\n"
"v_exp_f32 v132, v132\n"
"v_mfma_f32_32x32x4_2b_bf16 v[160:191], v[54:55], v[52:53], 0 cbsz:1 abid:0\n"
"v_exp_f32 v133, v133\n"
"v_exp_f32 v134, v134\n"
"v_exp_f32 v135, v135\n"
"v_exp_f32 v136, v136\n"
"v_exp_f32 v137, v137\n"
"v_exp_f32 v138, v138\n"
"v_exp_f32 v139, v139\n"
"v_exp_f32 v140, v140\n"
"v_exp_f32 v141, v141\n"
"v_smfmac_f32_32x32x32_bf16 v[224:239], v[92:95], v[128:135], v108\n"
"global_load_dwordx2 v[66:67], v4, s[36:37] offset:-1536 sc1\n"
"v_exp_f32 v142, v142\n"
"v_exp_f32 v143, v143\n"
"v_exp_f32 v144, v144\n"
"v_exp_f32 v145, v145\n"
"v_smfmac_f32_32x32x32_bf16 v[224:239], v[96:99], v[136:143], v108\n"
"global_load_dwordx4 v[60:63], v7, s[36:37] offset:0 sc1\n"
"v_exp_f32 v146, v146\n"
"v_exp_f32 v147, v147\n"
"v_exp_f32 v148, v148\n"
"v_exp_f32 v149, v149\n"
"v_mfma_f32_32x32x16_bf16 v[160:175], v[68:71], v[36:39], v[160:175]\n"
"v_exp_f32 v150, v150\n"
"v_exp_f32 v151, v151\n"
"v_exp_f32 v152, v152\n"
"v_exp_f32 v153, v153\n"
"v_smfmac_f32_32x32x32_bf16 v[240:255], v[92:95], v[144:151], v108\n"
"s_mov_b64 exec, s[48:49]\n"
"global_load_dwordx4 v[76:79], v5, s[36:37] offset:-1280 sc1\n"
"global_load_dwordx4 v[80:83], v5, s[36:37] offset:-640 sc1\n"
"global_load_dwordx4 v[84:87], v5, s[36:37] offset:1280 sc1\n"
"global_load_dwordx4 v[88:91], v5, s[36:37] offset:1920 sc1\n"
"s_mov_b64 exec, -1\n"
"s_add_u32 s36, s36, 5120\n"
"s_addc_u32 s37, s37, 0\n"
"v_exp_f32 v154, v154\n"
"v_exp_f32 v155, v155\n"
"v_exp_f32 v156, v156\n"
"v_exp_f32 v157, v157\n"
"v_mfma_f32_32x32x16_bf16 v[176:191], v[68:71], v[40:43], v[176:191]\n"
"v_exp_f32 v158, v158\n"
"v_exp_f32 v159, v159\n"
"v_exp_f32 v160, v160\n"
"v_exp_f32 v161, v161\n"
"v_smfmac_f32_32x32x32_bf16 v[240:255], v[96:99], v[152:159], v108\n"
"v_exp_f32 v162, v162\n"
"v_exp_f32 v163, v163\n"
"v_exp_f32 v164, v164\n"
"v_mfma_f32_32x32x4_2b_bf16 v[128:159], v[54:55], v[64:65], 0 cbsz:1 abid:1\n"
"v_exp_f32 v165, v165\n"
"v_exp_f32 v166, v166\n"
"v_exp_f32 v167, v167\n"
"v_exp_f32 v168, v168\n"
"v_exp_f32 v169, v169\n"
"v_exp_f32 v170, v170\n"
"v_exp_f32 v171, v171\n"
"v_exp_f32 v172, v172\n"
"v_exp_f32 v173, v173\n"
"v_smfmac_f32_32x32x32_bf16 v[192:207], v[92:95], v[160:167], v108\n"
"v_exp_f32 v174, v174\n"
"v_exp_f32 v175, v175\n"
"v_exp_f32 v176, v176\n"
"v_exp_f32 v177, v177\n"
"v_smfmac_f32_32x32x32_bf16 v[192:207], v[96:99], v[168:175], v108\n"
"v_exp_f32 v178, v178\n"
"v_exp_f32 v179, v179\n"
"v_exp_f32 v180, v180\n"
"v_exp_f32 v181, v181\n"
"v_mfma_f32_32x32x16_bf16 v[128:143], v[72:75], v[44:47], v[128:143]\n"
"s_getpc_b64 s[46:47]\n"
"v_bfe_u32 v109, v9, 9, 1\n"
"v_lshl_add_u32 v109, v109, 7, s34\n"
"global_load_dword v109, v109, s[46:47] offset:0\n"
"v_exp_f32 v182, v182\n"
"v_exp_f32 v183, v183\n"
"v_exp_f32 v184, v184\n"
"v_exp_f32 v185, v185\n"
"v_smfmac_f32_32x32x32_bf16 v[208:223], v[92:95], v[176:183], v108\n"
"v_exp_f32 v186, v186\n"
"v_exp_f32 v187, v187\n"
"v_exp_f32 v188, v188\n"
"v_exp_f32 v189, v189\n"
"v_mfma_f32_32x32x16_bf16 v[144:159], v[72:75], v[48:51], v[144:159]\n"
"v_exp_f32 v190, v190\n"
"v_exp_f32 v191, v191\n"
"v_exp_f32 v128, v128\n"
"v_exp_f32 v129, v129\n"
"v_smfmac_f32_32x32x32_bf16 v[208:223], v[96:99], v[184:191], v108\n"
"v_exp_f32 v130, v130\n"
"v_exp_f32 v131, v131\n"
"v_exp_f32 v132, v132\n"
"v_mfma_f32_32x32x4_2b_bf16 v[160:191], v[54:55], v[52:53], 0 cbsz:1 abid:1\n"
"v_exp_f32 v133, v133\n"
"v_exp_f32 v134, v134\n"
"v_exp_f32 v135, v135\n"
"v_exp_f32 v136, v136\n"
"v_exp_f32 v137, v137\n"
"v_exp_f32 v138, v138\n"
"v_exp_f32 v139, v139\n"
"v_exp_f32 v140, v140\n"
"v_exp_f32 v141, v141\n"
"v_smfmac_f32_32x32x32_bf16 v[224:239], v[100:103], v[128:135], v108\n"
"s_mov_b32 exec_lo, 0\n"
"s_waitcnt vmcnt(0)\n"
"global_load_dword v109, v14, s[66:67] offset:0 sc1\n"
"s_mov_b32 exec_lo, -1\n"
"v_exp_f32 v142, v142\n"
"v_exp_f32 v143, v143\n"
"v_exp_f32 v144, v144\n"
"v_exp_f32 v145, v145\n"
"v_smfmac_f32_32x32x32_bf16 v[224:239], v[104:107], v[136:143], v108\n"
"v_exp_f32 v146, v146\n"
"v_exp_f32 v147, v147\n"
"v_exp_f32 v148, v148\n"
"v_exp_f32 v149, v149\n"
"v_mfma_f32_32x32x16_bf16 v[160:175], v[72:75], v[36:39], v[160:175]\n"
"v_exp_f32 v150, v150\n"
"v_exp_f32 v151, v151\n"
"v_exp_f32 v152, v152\n"
"v_exp_f32 v153, v153\n"
"v_smfmac_f32_32x32x32_bf16 v[240:255], v[100:103], v[144:151], v108\n"
"v_exp_f32 v154, v154\n"
"v_exp_f32 v155, v155\n"
"v_exp_f32 v156, v156\n"
"v_exp_f32 v157, v157\n"
"v_mfma_f32_32x32x16_bf16 v[176:191], v[72:75], v[40:43], v[176:191]\n"
"v_exp_f32 v158, v158\n"
"v_exp_f32 v159, v159\n"
"v_exp_f32 v160, v160\n"
"v_exp_f32 v161, v161\n"
"v_smfmac_f32_32x32x32_bf16 v[240:255], v[104:107], v[152:159], v108\n"
"v_exp_f32 v162, v162\n"
"v_exp_f32 v163, v163\n"
"v_exp_f32 v164, v164\n"
"v_mfma_f32_32x32x4_2b_bf16 v[128:159], v[66:67], v[64:65], 0 cbsz:1 abid:0\n"
"v_exp_f32 v165, v165\n"
"v_exp_f32 v166, v166\n"
"v_exp_f32 v167, v167\n"
"v_exp_f32 v168, v168\n"
"v_exp_f32 v169, v169\n"
"v_exp_f32 v170, v170\n"
"v_exp_f32 v171, v171\n"
"v_exp_f32 v172, v172\n"
"v_exp_f32 v173, v173\n"
"v_smfmac_f32_32x32x32_bf16 v[192:207], v[100:103], v[160:167], v108\n"
"v_exp_f32 v174, v174\n"
"v_exp_f32 v175, v175\n"
"v_exp_f32 v176, v176\n"
"v_exp_f32 v177, v177\n"
"v_smfmac_f32_32x32x32_bf16 v[192:207], v[104:107], v[168:175], v108\n"
"v_exp_f32 v178, v178\n"
"v_exp_f32 v179, v179\n"
"v_exp_f32 v180, v180\n"
"v_exp_f32 v181, v181\n"
"v_mfma_f32_32x32x16_bf16 v[128:143], v[56:59], v[44:47], v[128:143]\n"
"v_exp_f32 v182, v182\n"
"v_exp_f32 v183, v183\n"
"v_exp_f32 v184, v184\n"
"v_exp_f32 v185, v185\n"
"v_smfmac_f32_32x32x32_bf16 v[208:223], v[100:103], v[176:183], v108\n"
"v_exp_f32 v186, v186\n"
"v_exp_f32 v187, v187\n"
"v_exp_f32 v188, v188\n"
"v_exp_f32 v189, v189\n"
"v_mfma_f32_32x32x16_bf16 v[144:159], v[56:59], v[48:51], v[144:159]\n"
"v_exp_f32 v190, v190\n"
"v_exp_f32 v191, v191\n"
"v_exp_f32 v128, v128\n"
"v_exp_f32 v129, v129\n"
"v_smfmac_f32_32x32x32_bf16 v[208:223], v[104:107], v[184:191], v108\n"
"global_load_dwordx4 v[68:71], v7, s[36:37] offset:-2560 sc1\n"
"v_exp_f32 v130, v130\n"
"v_exp_f32 v131, v131\n"
"v_exp_f32 v132, v132\n"
"v_mfma_f32_32x32x4_2b_bf16 v[160:191], v[66:67], v[52:53], 0 cbsz:1 abid:0\n"
"v_exp_f32 v133, v133\n"
"v_exp_f32 v134, v134\n"
"v_exp_f32 v135, v135\n"
"v_exp_f32 v136, v136\n"
"v_exp_f32 v137, v137\n"
"v_exp_f32 v138, v138\n"
"v_exp_f32 v139, v139\n"
"v_exp_f32 v140, v140\n"
"v_exp_f32 v141, v141\n"
"v_smfmac_f32_32x32x32_bf16 v[224:239], v[76:79], v[128:135], v108\n"
"global_load_dwordx2 v[54:55], v4, s[36:37] offset:-1536 sc1\n"
"v_exp_f32 v142, v142\n"
"v_exp_f32 v143, v143\n"
"v_exp_f32 v144, v144\n"
"v_exp_f32 v145, v145\n"
"v_smfmac_f32_32x32x32_bf16 v[224:239], v[80:83], v[136:143], v108\n"
"global_load_dwordx4 v[72:75], v7, s[36:37] offset:0 sc1\n"
"v_exp_f32 v146, v146\n"
"v_exp_f32 v147, v147\n"
"v_exp_f32 v148, v148\n"
"v_exp_f32 v149, v149\n"
"v_mfma_f32_32x32x16_bf16 v[160:175], v[56:59], v[36:39], v[160:175]\n"
"v_exp_f32 v150, v150\n"
"v_exp_f32 v151, v151\n"
"v_exp_f32 v152, v152\n"
"v_exp_f32 v153, v153\n"
"v_smfmac_f32_32x32x32_bf16 v[240:255], v[76:79], v[144:151], v108\n"
"s_mov_b64 exec, s[48:49]\n"
"global_load_dwordx4 v[92:95], v5, s[36:37] offset:-1280 sc1\n"
"global_load_dwordx4 v[96:99], v5, s[36:37] offset:-640 sc1\n"
"global_load_dwordx4 v[100:103], v5, s[36:37] offset:1280 sc1\n"
"global_load_dwordx4 v[104:107], v5, s[36:37] offset:1920 sc1\n"
"s_mov_b64 exec, -1\n"
"s_add_u32 s36, s36, 5120\n"
"s_addc_u32 s37, s37, 0\n"
"v_exp_f32 v154, v154\n"
"v_exp_f32 v155, v155\n"
"v_exp_f32 v156, v156\n"
"v_exp_f32 v157, v157\n"
"v_mfma_f32_32x32x16_bf16 v[176:191], v[56:59], v[40:43], v[176:191]\n"
"v_exp_f32 v158, v158\n"
"v_exp_f32 v159, v159\n"
"v_exp_f32 v160, v160\n"
"v_exp_f32 v161, v161\n"
"v_smfmac_f32_32x32x32_bf16 v[240:255], v[80:83], v[152:159], v108\n"
"v_exp_f32 v162, v162\n"
"v_exp_f32 v163, v163\n"
"v_exp_f32 v164, v164\n"
"v_mfma_f32_32x32x4_2b_bf16 v[128:159], v[66:67], v[64:65], 0 cbsz:1 abid:1\n"
"v_exp_f32 v165, v165\n"
"v_exp_f32 v166, v166\n"
"v_exp_f32 v167, v167\n"
"v_exp_f32 v168, v168\n"
"v_exp_f32 v169, v169\n"
"v_exp_f32 v170, v170\n"
"v_exp_f32 v171, v171\n"
"v_exp_f32 v172, v172\n"
"v_exp_f32 v173, v173\n"
"v_smfmac_f32_32x32x32_bf16 v[192:207], v[76:79], v[160:167], v108\n"
"s_waitcnt vmcnt(7)\n"
"v_cmp_eq_u32 vcc, v109, v34\n"
"v_exp_f32 v174, v174\n"
"v_exp_f32 v175, v175\n"
"v_exp_f32 v176, v176\n"
"v_exp_f32 v177, v177\n"
"v_smfmac_f32_32x32x32_bf16 v[192:207], v[80:83], v[168:175], v108\n"
"s_cmp_lg_u32 vcc_hi, 0\n"
"s_cbranch_scc1 20f\n"
"21:\n"
"v_exp_f32 v178, v178\n"
"v_exp_f32 v179, v179\n"
"v_exp_f32 v180, v180\n"
"v_exp_f32 v181, v181\n"
"v_mfma_f32_32x32x16_bf16 v[128:143], v[60:63], v[44:47], v[128:143]\n"
"v_exp_f32 v182, v182\n"
"v_exp_f32 v183, v183\n"
"v_exp_f32 v184, v184\n"
"v_exp_f32 v185, v185\n"
"v_smfmac_f32_32x32x32_bf16 v[208:223], v[76:79], v[176:183], v108\n"
"v_exp_f32 v186, v186\n"
"v_exp_f32 v187, v187\n"
"v_exp_f32 v188, v188\n"
"v_exp_f32 v189, v189\n"
"v_mfma_f32_32x32x16_bf16 v[144:159], v[60:63], v[48:51], v[144:159]\n"
"v_exp_f32 v190, v190\n"
"v_exp_f32 v191, v191\n"
"v_exp_f32 v128, v128\n"
"v_exp_f32 v129, v129\n"
"v_smfmac_f32_32x32x32_bf16 v[208:223], v[80:83], v[184:191], v108\n"
"global_load_dwordx4 a[0:3], v122, s[38:39] offset:0 sc1\n"
"v_exp_f32 v130, v130\n"
"v_exp_f32 v131, v131\n"
"v_exp_f32 v132, v132\n"
"v_mfma_f32_32x32x4_2b_bf16 v[160:191], v[66:67], v[52:53], 0 cbsz:1 abid:1\n"
"v_exp_f32 v133, v133\n"
"v_exp_f32 v134, v134\n"
"v_exp_f32 v135, v135\n"
"v_exp_f32 v136, v136\n"
"v_exp_f32 v137, v137\n"
"v_exp_f32 v138, v138\n"
"v_exp_f32 v139, v139\n"
"v_exp_f32 v140, v140\n"
"v_exp_f32 v141, v141\n"
"v_smfmac_f32_32x32x32_bf16 v[224:239], v[84:87], v[128:135], v108\n"
"global_load_dwordx4 a[4:7], v123, s[38:39] offset:0 sc1\n"
"v_exp_f32 v142, v142\n"
"v_exp_f32 v143, v143\n"
"v_exp_f32 v144, v144\n"
"v_exp_f32 v145, v145\n"
"v_smfmac_f32_32x32x32_bf16 v[224:239], v[88:91], v[136:143], v108\n"
"global_load_dwordx4 a[8:11], v124, s[38:39] offset:0 sc1\n"
"v_exp_f32 v146, v146\n"
"v_exp_f32 v147, v147\n"
"v_exp_f32 v148, v148\n"
"v_exp_f32 v149, v149\n"
"v_mfma_f32_32x32x16_bf16 v[160:175], v[60:63], v[36:39], v[160:175]\n"
"v_exp_f32 v150, v150\n"
"v_exp_f32 v151, v151\n"
"v_exp_f32 v152, v152\n"
"v_exp_f32 v153, v153\n"
"v_smfmac_f32_32x32x32_bf16 v[240:255], v[84:87], v[144:151], v108\n"
"global_load_dwordx4 a[12:15], v125, s[38:39] offset:0 sc1\n"
"s_add_u32 s38, s38, 4096\n"
"s_addc_u32 s39, s39, 0\n"
"v_exp_f32 v154, v154\n"
"v_exp_f32 v155, v155\n"
"v_exp_f32 v156, v156\n"
"v_exp_f32 v157, v157\n"
"v_mfma_f32_32x32x16_bf16 v[176:191], v[60:63], v[40:43], v[176:191]\n"
"global_load_dwordx4 a[16:19], v9, s[40:41] offset:-2048 sc1\n"
"v_exp_f32 v158, v158\n"
"v_exp_f32 v159, v159\n"
"v_exp_f32 v160, v160\n"
"v_exp_f32 v161, v161\n"
"v_smfmac_f32_32x32x32_bf16 v[240:255], v[88:91], v[152:159], v108\n"
"s_waitcnt vmcnt(9)\n"
"global_load_dwordx4 a[20:23], v9, s[40:41] offset:-1024 sc1\n"
"v_exp_f32 v162, v162\n"
"v_exp_f32 v163, v163\n"
"v_exp_f32 v164, v164\n"
"v_mfma_f32_32x32x4_2b_bf16 v[128:159], v[54:55], v[64:65], 0 cbsz:1 abid:0\n"
"v_exp_f32 v165, v165\n"
"v_exp_f32 v166, v166\n"
"v_exp_f32 v167, v167\n"
"v_exp_f32 v168, v168\n"
"v_exp_f32 v169, v169\n"
"v_exp_f32 v170, v170\n"
"v_exp_f32 v171, v171\n"
"v_exp_f32 v172, v172\n"
"v_exp_f32 v173, v173\n"
"v_smfmac_f32_32x32x32_bf16 v[192:207], v[84:87], v[160:167], v108\n"
"global_load_dwordx4 a[24:27], v9, s[40:41] offset:0 sc1\n"
"v_exp_f32 v174, v174\n"
"v_exp_f32 v175, v175\n"
"v_exp_f32 v176, v176\n"
"v_exp_f32 v177, v177\n"
"v_smfmac_f32_32x32x32_bf16 v[192:207], v[88:91], v[168:175], v108\n"
"global_load_dwordx4 a[28:31], v9, s[40:41] offset:1024 sc1\n"
"v_exp_f32 v178, v178\n"
"v_exp_f32 v179, v179\n"
"v_exp_f32 v180, v180\n"
"v_exp_f32 v181, v181\n"
"v_mfma_f32_32x32x16_bf16 v[128:143], v[68:71], v[44:47], v[128:143]\n"
"v_exp_f32 v182, v182\n"
"v_exp_f32 v183, v183\n"
"v_exp_f32 v184, v184\n"
"v_exp_f32 v185, v185\n"
"v_smfmac_f32_32x32x32_bf16 v[208:223], v[84:87], v[176:183], v108\n"
"global_load_dwordx4 a[32:35], v9, s[40:41] offset:2048 sc1\n"
"s_add_u32 s40, s40, 5120\n"
"s_addc_u32 s41, s41, 0\n"
"v_exp_f32 v186, v186\n"
"v_exp_f32 v187, v187\n"
"v_exp_f32 v188, v188\n"
"v_exp_f32 v189, v189\n"
"v_mfma_f32_32x32x16_bf16 v[144:159], v[68:71], v[48:51], v[144:159]\n"
"global_load_dwordx4 a[36:39], v122, s[38:39] offset:0 sc1\n"
"v_exp_f32 v190, v190\n"
"v_exp_f32 v191, v191\n"
"v_exp_f32 v128, v128\n"
"v_exp_f32 v129, v129\n"
"v_smfmac_f32_32x32x32_bf16 v[208:223], v[88:91], v[184:191], v108\n"
"s_waitcnt vmcnt(10)\n"
"global_load_dwordx4 v[56:59], v7, s[36:37] offset:-2560 sc1\n"
"v_exp_f32 v130, v130\n"
"v_exp_f32 v131, v131\n"
"v_exp_f32 v132, v132\n"
"v_mfma_f32_32x32x4_2b_bf16 v[160:191], v[54:55], v[52:53], 0 cbsz:1 abid:0\n"
"v_exp_f32 v133, v133\n"
"v_exp_f32 v134, v134\n"
"v_exp_f32 v135, v135\n"
"v_exp_f32 v136, v136\n"
"v_exp_f32 v137, v137\n"
"v_exp_f32 v138, v138\n"
"v_exp_f32 v139, v139\n"
"v_exp_f32 v140, v140\n"
"v_exp_f32 v141, v141\n"
"v_smfmac_f32_32x32x32_bf16 v[224:239], v[92:95], v[128:135], v108\n"
"global_load_dwordx2 v[66:67], v4, s[36:37] offset:-1536 sc1\n"
"v_exp_f32 v142, v142\n"
"v_exp_f32 v143, v143\n"
"v_exp_f32 v144, v144\n"
"v_exp_f32 v145, v145\n"
"v_smfmac_f32_32x32x32_bf16 v[224:239], v[96:99], v[136:143], v108\n"
"global_load_dwordx4 v[60:63], v7, s[36:37] offset:0 sc1\n"
"v_exp_f32 v146, v146\n"
"v_exp_f32 v147, v147\n"
"v_exp_f32 v148, v148\n"
"v_exp_f32 v149, v149\n"
"v_mfma_f32_32x32x16_bf16 v[160:175], v[68:71], v[36:39], v[160:175]\n"
"v_exp_f32 v150, v150\n"
"v_exp_f32 v151, v151\n"
"v_exp_f32 v152, v152\n"
"v_exp_f32 v153, v153\n"
"v_smfmac_f32_32x32x32_bf16 v[240:255], v[92:95], v[144:151], v108\n"
"s_mov_b64 exec, s[48:49]\n"
"global_load_dwordx4 v[76:79], v5, s[36:37] offset:-1280 sc1\n"
"global_load_dwordx4 v[80:83], v5, s[36:37] offset:-640 sc1\n"
"global_load_dwordx4 v[84:87], v5, s[36:37] offset:1280 sc1\n"
"global_load_dwordx4 v[88:91], v5, s[36:37] offset:1920 sc1\n"
"s_mov_b64 exec, -1\n"
"s_add_u32 s36, s36, 5120\n"
"s_addc_u32 s37, s37, 0\n"
"v_exp_f32 v154, v154\n"
"v_exp_f32 v155, v155\n"
"v_exp_f32 v156, v156\n"
"v_exp_f32 v157, v157\n"
"v_mfma_f32_32x32x16_bf16 v[176:191], v[68:71], v[40:43], v[176:191]\n"
"global_load_dwordx4 a[40:43], v123, s[38:39] offset:0 sc1\n"
"v_exp_f32 v158, v158\n"
"v_exp_f32 v159, v159\n"
"v_exp_f32 v160, v160\n"
"v_exp_f32 v161, v161\n"
"v_smfmac_f32_32x32x32_bf16 v[240:255], v[96:99], v[152:159], v108\n"
"global_load_dwordx4 a[44:47], v124, s[38:39] offset:0 sc1\n"
"v_exp_f32 v162, v162\n"
"v_exp_f32 v163, v163\n"
"v_exp_f32 v164, v164\n"
"v_mfma_f32_32x32x4_2b_bf16 v[128:159], v[54:55], v[64:65], 0 cbsz:1 abid:1\n"
"v_exp_f32 v165, v165\n"
"v_exp_f32 v166, v166\n"
"v_exp_f32 v167, v167\n"
"v_exp_f32 v168, v168\n"
"v_exp_f32 v169, v169\n"
"v_exp_f32 v170, v170\n"
"v_exp_f32 v171, v171\n"
"v_exp_f32 v172, v172\n"
"v_exp_f32 v173, v173\n"
"v_smfmac_f32_32x32x32_bf16 v[192:207], v[92:95], v[160:167], v108\n"
"global_load_dwordx4 a[48:51], v125, s[38:39] offset:0 sc1\n"
"s_add_u32 s38, s38, 4096\n"
"s_addc_u32 s39, s39, 0\n"
"v_exp_f32 v174, v174\n"
"v_exp_f32 v175, v175\n"
"v_exp_f32 v176, v176\n"
"v_exp_f32 v177, v177\n"
"v_smfmac_f32_32x32x32_bf16 v[192:207], v[96:99], v[168:175], v108\n"
"global_load_dwordx4 a[52:55], v9, s[40:41] offset:-2048 sc1\n"
"v_exp_f32 v178, v178\n"
"v_exp_f32 v179, v179\n"
"v_exp_f32 v180, v180\n"
"v_exp_f32 v181, v181\n"
"v_mfma_f32_32x32x16_bf16 v[128:143], v[72:75], v[44:47], v[128:143]\n"
"v_exp_f32 v182, v182\n"
"v_exp_f32 v183, v183\n"
"v_exp_f32 v184, v184\n"
"v_exp_f32 v185, v185\n"
"v_smfmac_f32_32x32x32_bf16 v[208:223], v[92:95], v[176:183], v108\n"
"global_load_dwordx4 a[56:59], v9, s[40:41] offset:-1024 sc1\n"
"v_exp_f32 v186, v186\n"
"v_exp_f32 v187, v187\n"
"v_exp_f32 v188, v188\n"
"v_exp_f32 v189, v189\n"
"v_mfma_f32_32x32x16_bf16 v[144:159], v[72:75], v[48:51], v[144:159]\n"
"global_load_dwordx4 a[60:63], v9, s[40:41] offset:0 sc1\n"
"v_exp_f32 v190, v190\n"
"v_exp_f32 v191, v191\n"
"v_exp_f32 v128, v128\n"
"v_exp_f32 v129, v129\n"
"v_smfmac_f32_32x32x32_bf16 v[208:223], v[96:99], v[184:191], v108\n"
"global_load_dwordx4 a[64:67], v9, s[40:41] offset:1024 sc1\n"
"v_exp_f32 v130, v130\n"
"v_exp_f32 v131, v131\n"
"v_exp_f32 v132, v132\n"
"v_mfma_f32_32x32x4_2b_bf16 v[160:191], v[54:55], v[52:53], 0 cbsz:1 abid:1\n"
"v_exp_f32 v133, v133\n"
"v_exp_f32 v134, v134\n"
"v_exp_f32 v135, v135\n"
"v_exp_f32 v136, v136\n"
"v_exp_f32 v137, v137\n"
"v_exp_f32 v138, v138\n"
"v_exp_f32 v139, v139\n"
"v_exp_f32 v140, v140\n"
"v_exp_f32 v141, v141\n"
"v_smfmac_f32_32x32x32_bf16 v[224:239], v[100:103], v[128:135], v108\n"
"global_load_dwordx4 a[68:71], v9, s[40:41] offset:2048 sc1\n"
"s_add_u32 s40, s40, 5120\n"
"s_addc_u32 s41, s41, 0\n"
"v_exp_f32 v142, v142\n"
"v_exp_f32 v143, v143\n"
"v_exp_f32 v144, v144\n"
"v_exp_f32 v145, v145\n"
"v_smfmac_f32_32x32x32_bf16 v[224:239], v[104:107], v[136:143], v108\n"
"global_load_dwordx4 a[72:75], v122, s[38:39] offset:0 sc1\n"
"v_exp_f32 v146, v146\n"
"v_exp_f32 v147, v147\n"
"v_exp_f32 v148, v148\n"
"v_exp_f32 v149, v149\n"
"v_mfma_f32_32x32x16_bf16 v[160:175], v[72:75], v[36:39], v[160:175]\n"
"v_exp_f32 v150, v150\n"
"v_exp_f32 v151, v151\n"
"v_exp_f32 v152, v152\n"
"v_exp_f32 v153, v153\n"
"v_smfmac_f32_32x32x32_bf16 v[240:255], v[100:103], v[144:151], v108\n"
"global_load_dwordx4 a[76:79], v123, s[38:39] offset:0 sc1\n"
"v_exp_f32 v154, v154\n"
"v_exp_f32 v155, v155\n"
"v_exp_f32 v156, v156\n"
"s_waitcnt vmcnt(18)\n"
"v_mfma_f32_16x16x32_bf16 a[176:179], a[16:19], a[0:3], 0\n"
"v_exp_f32 v157, v157\n"
"v_mfma_f32_32x32x16_bf16 v[176:191], v[72:75], v[40:43], v[176:191]\n"
"global_load_dwordx4 a[80:83], v124, s[38:39] offset:0 sc1\n"
"v_exp_f32 v158, v158\n"
"v_exp_f32 v159, v159\n"
"v_exp_f32 v160, v160\n"
"v_mfma_f32_16x16x32_bf16 a[180:183], a[20:23], a[0:3], 0\n"
"v_exp_f32 v161, v161\n"
"v_smfmac_f32_32x32x32_bf16 v[240:255], v[104:107], v[152:159], v108\n"
"s_waitcnt vmcnt(15)\n"
"global_load_dwordx4 a[84:87], v125, s[38:39] offset:0 sc1\n"
"s_add_u32 s38, s38, 4096\n"
"s_addc_u32 s39, s39, 0\n"
"v_exp_f32 v162, v162\n"
"v_exp_f32 v163, v163\n"
"v_exp_f32 v164, v164\n"
"v_mfma_f32_32x32x4_2b_bf16 v[128:159], v[66:67], v[64:65], 0 cbsz:1 abid:0\n"
"v_exp_f32 v165, v165\n"
"v_exp_f32 v166, v166\n"
"v_exp_f32 v167, v167\n"
"v_exp_f32 v168, v168\n"
"v_exp_f32 v169, v169\n"
"v_exp_f32 v170, v170\n"
"v_exp_f32 v171, v171\n"
"v_exp_f32 v172, v172\n"
"v_exp_f32 v173, v173\n"
"v_smfmac_f32_32x32x32_bf16 v[192:207], v[100:103], v[160:167], v108\n"
"global_load_dwordx4 a[88:91], v9, s[40:41] offset:-2048 sc1\n"
"v_exp_f32 v174, v174\n"
"v_exp_f32 v175, v175\n"
"v_exp_f32 v176, v176\n"
"v_mfma_f32_16x16x32_bf16 a[184:187], a[24:27], a[0:3], 0\n"
"v_exp_f32 v177, v177\n"
"v_smfmac_f32_32x32x32_bf16 v[192:207], v[104:107], v[168:175], v108\n"
"global_load_dwordx4 a[92:95], v9, s[40:41] offset:-1024 sc1\n"
"v_exp_f32 v178, v178\n"
"v_exp_f32 v179, v179\n"
"v_exp_f32 v180, v180\n"
"v_mfma_f32_16x16x32_bf16 a[188:191], a[28:31], a[0:3], 0\n"
"v_exp_f32 v181, v181\n"
"v_mfma_f32_32x32x16_bf16 v[128:143], v[56:59], v[44:47], v[128:143]\n"
"v_exp_f32 v182, v182\n"
"v_exp_f32 v183, v183\n"
"v_exp_f32 v184, v184\n"
"v_mfma_f32_16x16x32_bf16 a[192:195], a[32:35], a[0:3], 0\n"
"v_exp_f32 v185, v185\n"
"v_smfmac_f32_32x32x32_bf16 v[208:223], v[100:103], v[176:183], v108\n"
"global_load_dwordx4 a[96:99], v9, s[40:41] offset:0 sc1\n"
"v_exp_f32 v186, v186\n"
"v_exp_f32 v187, v187\n"
"v_exp_f32 v188, v188\n"
"v_mfma_f32_16x16x32_bf16 a[196:199], a[16:19], a[4:7], 0\n"
"v_exp_f32 v189, v189\n"
"v_mfma_f32_32x32x16_bf16 v[144:159], v[56:59], v[48:51], v[144:159]\n"
"global_load_dwordx4 a[100:103], v9, s[40:41] offset:1024 sc1\n"
"v_exp_f32 v190, v190\n"
"v_exp_f32 v191, v191\n"
"v_exp_f32 v128, v128\n"
"v_mfma_f32_16x16x32_bf16 a[200:203], a[20:23], a[4:7], 0\n"
"v_exp_f32 v129, v129\n"
"v_smfmac_f32_32x32x32_bf16 v[208:223], v[104:107], v[184:191], v108\n"
"s_waitcnt vmcnt(16)\n"
"global_load_dwordx4 v[68:71], v7, s[36:37] offset:-2560 sc1\n"
"v_exp_f32 v130, v130\n"
"v_exp_f32 v131, v131\n"
"v_exp_f32 v132, v132\n"
"v_mfma_f32_32x32x4_2b_bf16 v[160:191], v[66:67], v[52:53], 0 cbsz:1 abid:0\n"
"v_exp_f32 v133, v133\n"
"v_exp_f32 v134, v134\n"
"v_exp_f32 v135, v135\n"
"v_exp_f32 v136, v136\n"
"v_exp_f32 v137, v137\n"
"v_exp_f32 v138, v138\n"
"v_exp_f32 v139, v139\n"
"v_exp_f32 v140, v140\n"
"v_exp_f32 v141, v141\n"
"v_smfmac_f32_32x32x32_bf16 v[224:239], v[76:79], v[128:135], v108\n"
"global_load_dwordx2 v[54:55], v4, s[36:37] offset:-1536 sc1\n"
"v_exp_f32 v142, v142\n"
"v_exp_f32 v143, v143\n"
"v_exp_f32 v144, v144\n"
"v_mfma_f32_16x16x32_bf16 a[204:207], a[24:27], a[4:7], 0\n"
"v_exp_f32 v145, v145\n"
"v_smfmac_f32_32x32x32_bf16 v[224:239], v[80:83], v[136:143], v108\n"
"global_load_dwordx4 v[72:75], v7, s[36:37] offset:0 sc1\n"
"v_exp_f32 v146, v146\n"
"v_exp_f32 v147, v147\n"
"v_exp_f32 v148, v148\n"
"v_mfma_f32_16x16x32_bf16 a[208:211], a[28:31], a[4:7], 0\n"
"v_exp_f32 v149, v149\n"
"v_mfma_f32_32x32x16_bf16 v[160:175], v[56:59], v[36:39], v[160:175]\n"
"v_exp_f32 v150, v150\n"
"v_exp_f32 v151, v151\n"
"v_exp_f32 v152, v152\n"
"v_mfma_f32_16x16x32_bf16 a[212:215], a[32:35], a[4:7], 0\n"
"v_exp_f32 v153, v153\n"
"v_smfmac_f32_32x32x32_bf16 v[240:255], v[76:79], v[144:151], v108\n"
"s_mov_b64 exec, s[48:49]\n"
"global_load_dwordx4 v[92:95], v5, s[36:37] offset:-1280 sc1\n"
"global_load_dwordx4 v[96:99], v5, s[36:37] offset:-640 sc1\n"
"global_load_dwordx4 v[100:103], v5, s[36:37] offset:1280 sc1\n"
"global_load_dwordx4 v[104:107], v5, s[36:37] offset:1920 sc1\n"
"s_mov_b64 exec, -1\n"
"s_add_u32 s36, s36, 5120\n"
"s_addc_u32 s37, s37, 0\n"
"v_exp_f32 v154, v154\n"
"v_exp_f32 v155, v155\n"
"v_exp_f32 v156, v156\n"
"v_mfma_f32_16x16x32_bf16 a[216:219], a[16:19], a[8:11], 0\n"
"v_exp_f32 v157, v157\n"
"v_mfma_f32_32x32x16_bf16 v[176:191], v[56:59], v[40:43], v[176:191]\n"
"global_load_dwordx4 a[104:107], v9, s[40:41] offset:2048 sc1\n"
"s_add_u32 s40, s40, 5120\n"
"s_addc_u32 s41, s41, 0\n"
"v_exp_f32 v158, v158\n"
"v_exp_f32 v159, v159\n"
"v_exp_f32 v160, v160\n"
"v_mfma_f32_16x16x32_bf16 a[220:223], a[20:23], a[8:11], 0\n"
"v_exp_f32 v161, v161\n"
"v_smfmac_f32_32x32x32_bf16 v[240:255], v[80:83], v[152:159], v108\n"
"global_load_dwordx4 a[108:111], v122, s[38:39] offset:0 sc1\n"
"v_exp_f32 v162, v162\n"
"v_exp_f32 v163, v163\n"
"v_exp_f32 v164, v164\n"
"v_mfma_f32_32x32x4_2b_bf16 v[128:159], v[66:67], v[64:65], 0 cbsz:1 abid:1\n"
"v_exp_f32 v165, v165\n"
"v_exp_f32 v166, v166\n"
"v_exp_f32 v167, v167\n"
"v_exp_f32 v168, v168\n"
"v_exp_f32 v169, v169\n"
"v_exp_f32 v170, v170\n"
"v_exp_f32 v171, v171\n"
"v_exp_f32 v172, v172\n"
"v_exp_f32 v173, v173\n"
"v_smfmac_f32_32x32x32_bf16 v[192:207], v[76:79], v[160:167], v108\n"
"global_load_dwordx4 a[112:115], v123, s[38:39] offset:0 sc1\n"
"v_exp_f32 v174, v174\n"
"v_exp_f32 v175, v175\n"
"v_exp_f32 v176, v176\n"
"v_mfma_f32_16x16x32_bf16 a[224:227], a[24:27], a[8:11], 0\n"
"v_exp_f32 v177, v177\n"
"v_smfmac_f32_32x32x32_bf16 v[192:207], v[80:83], v[168:175], v108\n"
"global_load_dwordx4 a[116:119], v124, s[38:39] offset:0 sc1\n"
"v_exp_f32 v178, v178\n"
"v_exp_f32 v179, v179\n"
"v_exp_f32 v180, v180\n"
"v_mfma_f32_16x16x32_bf16 a[228:231], a[28:31], a[8:11], 0\n"
"v_exp_f32 v181, v181\n"
"v_mfma_f32_32x32x16_bf16 v[128:143], v[60:63], v[44:47], v[128:143]\n"
"v_exp_f32 v182, v182\n"
"v_exp_f32 v183, v183\n"
"v_exp_f32 v184, v184\n"
"v_mfma_f32_16x16x32_bf16 a[232:235], a[32:35], a[8:11], 0\n"
"v_exp_f32 v185, v185\n"
"v_smfmac_f32_32x32x32_bf16 v[208:223], v[76:79], v[176:183], v108\n"
"global_load_dwordx4 a[120:123], v125, s[38:39] offset:0 sc1\n"
"s_add_u32 s38, s38, 4096\n"
"s_addc_u32 s39, s39, 0\n"
"v_exp_f32 v186, v186\n"
"v_exp_f32 v187, v187\n"
"v_exp_f32 v188, v188\n"
"v_mfma_f32_16x16x32_bf16 a[236:239], a[16:19], a[12:15], 0\n"
"v_exp_f32 v189, v189\n"
"v_mfma_f32_32x32x16_bf16 v[144:159], v[60:63], v[48:51], v[144:159]\n"
"global_load_dwordx4 a[124:127], v9, s[40:41] offset:-2048 sc1\n"
"v_exp_f32 v190, v190\n"
"v_exp_f32 v191, v191\n"
"v_exp_f32 v128, v128\n"
"v_mfma_f32_16x16x32_bf16 a[240:243], a[20:23], a[12:15], 0\n"
"v_exp_f32 v129, v129\n"
"v_smfmac_f32_32x32x32_bf16 v[208:223], v[80:83], v[184:191], v108\n"
"global_load_dwordx4 a[128:131], v9, s[40:41] offset:-1024 sc1\n"
"v_exp_f32 v130, v130\n"
"v_exp_f32 v131, v131\n"
"v_exp_f32 v132, v132\n"
"v_mfma_f32_32x32x4_2b_bf16 v[160:191], v[66:67], v[52:53], 0 cbsz:1 abid:1\n"
"v_exp_f32 v133, v133\n"
"v_exp_f32 v134, v134\n"
"v_exp_f32 v135, v135\n"
"v_exp_f32 v136, v136\n"
"v_exp_f32 v137, v137\n"
"v_exp_f32 v138, v138\n"
"v_exp_f32 v139, v139\n"
"v_exp_f32 v140, v140\n"
"v_exp_f32 v141, v141\n"
"v_smfmac_f32_32x32x32_bf16 v[224:239], v[84:87], v[128:135], v108\n"
"global_load_dwordx4 a[132:135], v9, s[40:41] offset:0 sc1\n"
"s_add_u32 s52, s35, 12\n"
"s_lshl_b32 s52, s52, 12\n"
"v_exp_f32 v142, v142\n"
"v_exp_f32 v143, v143\n"
"v_exp_f32 v144, v144\n"
"v_mfma_f32_16x16x32_bf16 a[244:247], a[24:27], a[12:15], 0\n"
"v_exp_f32 v145, v145\n"
"v_smfmac_f32_32x32x32_bf16 v[224:239], v[88:91], v[136:143], v108\n"
"global_load_dwordx4 a[136:139], v9, s[40:41] offset:1024 sc1\n"
"s_add_u32 s52, s52, s14\n"
"s_addc_u32 s53, s15, 0\n"
"v_exp_f32 v146, v146\n"
"v_exp_f32 v147, v147\n"
"v_exp_f32 v148, v148\n"
"v_mfma_f32_16x16x32_bf16 a[248:251], a[28:31], a[12:15], 0\n"
"v_exp_f32 v149, v149\n"
"v_mfma_f32_32x32x16_bf16 v[160:175], v[60:63], v[36:39], v[160:175]\n"
"v_exp_f32 v150, v150\n"
"v_exp_f32 v151, v151\n"
"v_exp_f32 v152, v152\n"
"v_mfma_f32_16x16x32_bf16 a[252:255], a[32:35], a[12:15], 0\n"
"v_exp_f32 v153, v153\n"
"v_smfmac_f32_32x32x32_bf16 v[240:255], v[84:87], v[144:151], v108\n"
"global_load_dwordx4 a[140:143], v9, s[40:41] offset:2048 sc1\n"
"s_add_u32 s40, s40, 5120\n"
"s_addc_u32 s41, s41, 0\n"
"v_exp_f32 v154, v154\n"
"v_exp_f32 v155, v155\n"
"v_exp_f32 v156, v156\n"
"s_waitcnt vmcnt(25)\n"
"v_mfma_f32_16x16x32_bf16 a[176:179], a[52:55], a[36:39], a[176:179]\n"
"v_exp_f32 v157, v157\n"
"v_mfma_f32_32x32x16_bf16 v[176:191], v[60:63], v[40:43], v[176:191]\n"
"global_load_dwordx4 a[144:147], v122, s[52:53] offset:0 sc1\n"
"v_exp_f32 v158, v158\n"
"v_exp_f32 v159, v159\n"
"v_exp_f32 v160, v160\n"
"v_mfma_f32_16x16x32_bf16 a[180:183], a[56:59], a[36:39], a[180:183]\n"
"v_exp_f32 v161, v161\n"
"v_smfmac_f32_32x32x32_bf16 v[240:255], v[88:91], v[152:159], v108\n"
"s_waitcnt vmcnt(15)\n"
"global_load_dwordx4 a[148:151], v123, s[52:53] offset:0 sc1\n"
"s_add_u32 s47, s35, 12\n"
"s_mul_i32 s47, s47, 5120\n"
"s_add_u32 s52, s47, s16\n"
"s_addc_u32 s53, s17, 0\n"
"v_exp_f32 v162, v162\n"
"v_exp_f32 v163, v163\n"
"v_exp_f32 v164, v164\n"
"v_mfma_f32_32x32x4_2b_bf16 v[128:159], v[54:55], v[64:65], 0 cbsz:1 abid:0\n"
"v_exp_f32 v165, v165\n"
"v_exp_f32 v166, v166\n"
"v_exp_f32 v167, v167\n"
"v_exp_f32 v168, v168\n"
"v_exp_f32 v169, v169\n"
"v_exp_f32 v170, v170\n"
"v_exp_f32 v171, v171\n"
"v_exp_f32 v172, v172\n"
"v_exp_f32 v173, v173\n"
"v_smfmac_f32_32x32x32_bf16 v[192:207], v[84:87], v[160:167], v108\n"
"global_load_dwordx4 a[152:155], v9, s[52:53] offset:-2048 sc1\n"
"v_exp_f32 v174, v174\n"
"v_exp_f32 v175, v175\n"
"v_exp_f32 v176, v176\n"
"v_mfma_f32_16x16x32_bf16 a[184:187], a[60:63], a[36:39], a[184:187]\n"
"v_exp_f32 v177, v177\n"
"v_smfmac_f32_32x32x32_bf16 v[192:207], v[88:91], v[168:175], v108\n"
"global_load_dwordx4 a[156:159], v9, s[52:53] offset:-1024 sc1\n"
"v_exp_f32 v178, v178\n"
"v_exp_f32 v179, v179\n"
"v_exp_f32 v180, v180\n"
"v_mfma_f32_16x16x32_bf16 a[188:191], a[64:67], a[36:39], a[188:191]\n"
"v_exp_f32 v181, v181\n"
"v_mfma_f32_32x32x16_bf16 v[128:143], v[68:71], v[44:47], v[128:143]\n"
"v_exp_f32 v182, v182\n"
"v_exp_f32 v183, v183\n"
"v_exp_f32 v184, v184\n"
"v_mfma_f32_16x16x32_bf16 a[192:195], a[68:71], a[36:39], a[192:195]\n"
"v_exp_f32 v185, v185\n"
"v_smfmac_f32_32x32x32_bf16 v[208:223], v[84:87], v[176:183], v108\n"
"global_load_dwordx4 a[160:163], v9, s[52:53] offset:0 sc1\n"
"v_exp_f32 v186, v186\n"
"v_exp_f32 v187, v187\n"
"v_exp_f32 v188, v188\n"
"v_mfma_f32_16x16x32_bf16 a[196:199], a[52:55], a[40:43], a[196:199]\n"
"v_exp_f32 v189, v189\n"
"v_mfma_f32_32x32x16_bf16 v[144:159], v[68:71], v[48:51], v[144:159]\n"
"global_load_dwordx4 a[164:167], v9, s[52:53] offset:1024 sc1\n"
"s_mul_i32 s47, s30, 15360\n"
"s_add_u32 m0, s47, 108544\n"
"v_exp_f32 v190, v190\n"
"v_exp_f32 v191, v191\n"
"v_exp_f32 v128, v128\n"
"v_mfma_f32_16x16x32_bf16 a[200:203], a[56:59], a[40:43], a[200:203]\n"
"v_exp_f32 v129, v129\n"
"v_smfmac_f32_32x32x32_bf16 v[208:223], v[88:91], v[184:191], v108\n"
"s_waitcnt vmcnt(16)\n"
"global_load_dwordx4 v[56:59], v7, s[36:37] offset:-2560 sc1\n"
"v_exp_f32 v130, v130\n"
"v_exp_f32 v131, v131\n"
"v_exp_f32 v132, v132\n"
"v_mfma_f32_32x32x4_2b_bf16 v[160:191], v[54:55], v[52:53], 0 cbsz:1 abid:0\n"
"v_exp_f32 v133, v133\n"
"v_exp_f32 v134, v134\n"
"v_exp_f32 v135, v135\n"
"v_exp_f32 v136, v136\n"
"v_exp_f32 v137, v137\n"
"v_exp_f32 v138, v138\n"
"v_exp_f32 v139, v139\n"
"v_exp_f32 v140, v140\n"
"v_exp_f32 v141, v141\n"
"v_smfmac_f32_32x32x32_bf16 v[224:239], v[92:95], v[128:135], v108\n"
"global_load_dwordx2 v[66:67], v4, s[36:37] offset:-1536 sc1\n"
"v_exp_f32 v142, v142\n"
"v_exp_f32 v143, v143\n"
"v_exp_f32 v144, v144\n"
"v_mfma_f32_16x16x32_bf16 a[204:207], a[60:63], a[40:43], a[204:207]\n"
"v_exp_f32 v145, v145\n"
"v_smfmac_f32_32x32x32_bf16 v[224:239], v[96:99], v[136:143], v108\n"
"global_load_dwordx4 v[60:63], v7, s[36:37] offset:0 sc1\n"
"v_exp_f32 v146, v146\n"
"v_exp_f32 v147, v147\n"
"v_exp_f32 v148, v148\n"
"v_mfma_f32_16x16x32_bf16 a[208:211], a[64:67], a[40:43], a[208:211]\n"
"v_exp_f32 v149, v149\n"
"v_mfma_f32_32x32x16_bf16 v[160:175], v[68:71], v[36:39], v[160:175]\n"
"v_exp_f32 v150, v150\n"
"v_exp_f32 v151, v151\n"
"v_exp_f32 v152, v152\n"
"v_mfma_f32_16x16x32_bf16 a[212:215], a[68:71], a[40:43], a[212:215]\n"
"v_exp_f32 v153, v153\n"
"v_smfmac_f32_32x32x32_bf16 v[240:255], v[92:95], v[144:151], v108\n"
"s_mov_b64 exec, s[48:49]\n"
"global_load_dwordx4 v[76:79], v5, s[36:37] offset:-1280 sc1\n"
"global_load_dwordx4 v[80:83], v5, s[36:37] offset:-640 sc1\n"
"global_load_dwordx4 v[84:87], v5, s[36:37] offset:1280 sc1\n"
"global_load_dwordx4 v[88:91], v5, s[36:37] offset:1920 sc1\n"
"s_mov_b64 exec, -1\n"
"s_add_u32 s36, s36, 5120\n"
"s_addc_u32 s37, s37, 0\n"
"v_exp_f32 v154, v154\n"
"v_exp_f32 v155, v155\n"
"v_exp_f32 v156, v156\n"
"v_mfma_f32_16x16x32_bf16 a[216:219], a[52:55], a[44:47], a[216:219]\n"
"v_exp_f32 v157, v157\n"
"v_mfma_f32_32x32x16_bf16 v[176:191], v[68:71], v[40:43], v[176:191]\n"
"global_load_dwordx4 a[168:171], v9, s[52:53] offset:2048 sc1\n"
"v_exp_f32 v158, v158\n"
"v_exp_f32 v159, v159\n"
"v_exp_f32 v160, v160\n"
"v_mfma_f32_16x16x32_bf16 a[220:223], a[56:59], a[44:47], a[220:223]\n"
"v_exp_f32 v161, v161\n"
"v_smfmac_f32_32x32x32_bf16 v[240:255], v[96:99], v[152:159], v108\n"
"global_load_lds_dwordx4 v122, s[38:39] offset:0 sc1\n"
"s_add_u32 m0, m0, 1024\n"
"v_exp_f32 v162, v162\n"
"v_exp_f32 v163, v163\n"
"v_exp_f32 v164, v164\n"
"v_mfma_f32_32x32x4_2b_bf16 v[128:159], v[54:55], v[64:65], 0 cbsz:1 abid:1\n"
"v_exp_f32 v165, v165\n"
"v_exp_f32 v166, v166\n"
"v_exp_f32 v167, v167\n"
"v_exp_f32 v168, v168\n"
"v_exp_f32 v169, v169\n"
"v_exp_f32 v170, v170\n"
"v_exp_f32 v171, v171\n"
"v_exp_f32 v172, v172\n"
"v_exp_f32 v173, v173\n"
"v_smfmac_f32_32x32x32_bf16 v[192:207], v[92:95], v[160:167], v108\n"
"global_load_lds_dwordx4 v123, s[38:39] offset:0 sc1\n"
"s_add_u32 m0, m0, 1024\n"
"v_exp_f32 v174, v174\n"
"v_exp_f32 v175, v175\n"
"v_exp_f32 v176, v176\n"
"v_mfma_f32_16x16x32_bf16 a[224:227], a[60:63], a[44:47], a[224:227]\n"
"v_exp_f32 v177, v177\n"
"v_smfmac_f32_32x32x32_bf16 v[192:207], v[96:99], v[168:175], v108\n"
"global_load_lds_dwordx4 v124, s[38:39] offset:0 sc1\n"
"s_add_u32 m0, m0, 1024\n"
"v_exp_f32 v178, v178\n"
"v_exp_f32 v179, v179\n"
"v_exp_f32 v180, v180\n"
"v_mfma_f32_16x16x32_bf16 a[228:231], a[64:67], a[44:47], a[228:231]\n"
"v_exp_f32 v181, v181\n"
"v_mfma_f32_32x32x16_bf16 v[128:143], v[72:75], v[44:47], v[128:143]\n"
"v_exp_f32 v182, v182\n"
"v_exp_f32 v183, v183\n"
"v_exp_f32 v184, v184\n"
"v_mfma_f32_16x16x32_bf16 a[232:235], a[68:71], a[44:47], a[232:235]\n"
"v_exp_f32 v185, v185\n"
"v_smfmac_f32_32x32x32_bf16 v[208:223], v[92:95], v[176:183], v108\n"
"global_load_lds_dwordx4 v125, s[38:39] offset:0 sc1\n"
"s_add_u32 m0, m0, 1024\n"
"s_sub_u32 s52, s40, 2048\n"
"s_subb_u32 s53, s41, 0\n"
"s_add_u32 s38, s38, 4096\n"
"s_addc_u32 s39, s39, 0\n"
"v_exp_f32 v186, v186\n"
"v_exp_f32 v187, v187\n"
"v_exp_f32 v188, v188\n"
"v_mfma_f32_16x16x32_bf16 a[236:239], a[52:55], a[48:51], a[236:239]\n"
"v_exp_f32 v189, v189\n"
"v_mfma_f32_32x32x16_bf16 v[144:159], v[72:75], v[48:51], v[144:159]\n"
"global_load_lds_dwordx4 v9, s[52:53] offset:0 sc1\n"
"v_exp_f32 v190, v190\n"
"v_exp_f32 v191, v191\n"
"v_exp_f32 v128, v128\n"
"v_mfma_f32_16x16x32_bf16 a[240:243], a[56:59], a[48:51], a[240:243]\n"
"v_exp_f32 v129, v129\n"
"v_smfmac_f32_32x32x32_bf16 v[208:223], v[96:99], v[184:191], v108\n"
"global_load_lds_dwordx4 v9, s[52:53] offset:1024 sc1\n"
"v_exp_f32 v130, v130\n"
"v_exp_f32 v131, v131\n"
"v_exp_f32 v132, v132\n"
"v_mfma_f32_32x32x4_2b_bf16 v[160:191], v[54:55], v[52:53], 0 cbsz:1 abid:1\n"
"v_exp_f32 v133, v133\n"
"v_exp_f32 v134, v134\n"
"v_exp_f32 v135, v135\n"
"v_exp_f32 v136, v136\n"
"v_exp_f32 v137, v137\n"
"v_exp_f32 v138, v138\n"
"v_exp_f32 v139, v139\n"
"v_exp_f32 v140, v140\n"
"v_exp_f32 v141, v141\n"
"v_smfmac_f32_32x32x32_bf16 v[224:239], v[100:103], v[128:135], v108\n"
"global_load_lds_dwordx4 v9, s[52:53] offset:2048 sc1\n"
"v_exp_f32 v142, v142\n"
"v_exp_f32 v143, v143\n"
"v_exp_f32 v144, v144\n"
"v_mfma_f32_16x16x32_bf16 a[244:247], a[60:63], a[48:51], a[244:247]\n"
"v_exp_f32 v145, v145\n"
"v_smfmac_f32_32x32x32_bf16 v[224:239], v[104:107], v[136:143], v108\n"
"global_load_lds_dwordx4 v9, s[52:53] offset:3072 sc1\n"
"s_add_u32 m0, m0, 2048\n"
"v_exp_f32 v146, v146\n"
"v_exp_f32 v147, v147\n"
"v_exp_f32 v148, v148\n"
"v_mfma_f32_16x16x32_bf16 a[248:251], a[64:67], a[48:51], a[248:251]\n"
"v_exp_f32 v149, v149\n"
"v_mfma_f32_32x32x16_bf16 v[160:175], v[72:75], v[36:39], v[160:175]\n"
"v_exp_f32 v150, v150\n"
"v_exp_f32 v151, v151\n"
"v_exp_f32 v152, v152\n"
"v_mfma_f32_16x16x32_bf16 a[252:255], a[68:71], a[48:51], a[252:255]\n"
"v_exp_f32 v153, v153\n"
"v_smfmac_f32_32x32x32_bf16 v[240:255], v[100:103], v[144:151], v108\n"
"global_load_lds_dwordx4 v9, s[40:41] offset:2048 sc1\n"
"s_add_u32 s40, s40, 5120\n"
"s_addc_u32 s41, s41, 0\n"
"s_mul_i32 m0, s30, 20480\n"
"v_exp_f32 v154, v154\n"
"v_exp_f32 v155, v155\n"
"v_exp_f32 v156, v156\n"
"s_waitcnt vmcnt(32)\n"
"v_mfma_f32_16x16x32_bf16 a[176:179], a[88:91], a[72:75], a[176:179]\n"
"v_exp_f32 v157, v157\n"
"v_mfma_f32_32x32x16_bf16 v[176:191], v[72:75], v[40:43], v[176:191]\n"
"global_load_dwordx4 a[0:3], v122, s[38:39] offset:0 sc1\n"
"v_exp_f32 v158, v158\n"
"v_exp_f32 v159, v159\n"
"v_exp_f32 v160, v160\n"
"v_mfma_f32_16x16x32_bf16 a[180:183], a[92:95], a[72:75], a[180:183]\n"
"v_exp_f32 v161, v161\n"
"v_smfmac_f32_32x32x32_bf16 v[240:255], v[104:107], v[152:159], v108\n"
"s_waitcnt vmcnt(15)\n"
"global_load_dwordx4 a[4:7], v123, s[38:39] offset:0 sc1\n"
"v_exp_f32 v162, v162\n"
"v_exp_f32 v163, v163\n"
"v_exp_f32 v164, v164\n"
"v_mfma_f32_32x32x4_2b_bf16 v[128:159], v[66:67], v[64:65], 0 cbsz:1 abid:0\n"
"v_exp_f32 v165, v165\n"
"v_exp_f32 v166, v166\n"
"v_exp_f32 v167, v167\n"
"v_exp_f32 v168, v168\n"
"v_exp_f32 v169, v169\n"
"v_exp_f32 v170, v170\n"
"v_exp_f32 v171, v171\n"
"v_exp_f32 v172, v172\n"
"v_exp_f32 v173, v173\n"
"v_smfmac_f32_32x32x32_bf16 v[192:207], v[100:103], v[160:167], v108\n"
"global_load_dwordx4 a[8:11], v124, s[38:39] offset:0 sc1\n"
"v_exp_f32 v174, v174\n"
"v_exp_f32 v175, v175\n"
"v_exp_f32 v176, v176\n"
"v_mfma_f32_16x16x32_bf16 a[184:187], a[96:99], a[72:75], a[184:187]\n"
"v_exp_f32 v177, v177\n"
"v_smfmac_f32_32x32x32_bf16 v[192:207], v[104:107], v[168:175], v108\n"
"global_load_dwordx4 a[12:15], v125, s[38:39] offset:0 sc1\n"
"s_add_u32 s38, s38, 4096\n"
"s_addc_u32 s39, s39, 0\n"
"v_exp_f32 v178, v178\n"
"v_exp_f32 v179, v179\n"
"v_exp_f32 v180, v180\n"
"v_mfma_f32_16x16x32_bf16 a[188:191], a[100:103], a[72:75], a[188:191]\n"
"v_exp_f32 v181, v181\n"
"v_mfma_f32_32x32x16_bf16 v[128:143], v[56:59], v[44:47], v[128:143]\n"
"v_exp_f32 v182, v182\n"
"v_exp_f32 v183, v183\n"
"v_exp_f32 v184, v184\n"
"v_mfma_f32_16x16x32_bf16 a[192:195], a[104:107], a[72:75], a[192:195]\n"
"v_exp_f32 v185, v185\n"
"v_smfmac_f32_32x32x32_bf16 v[208:223], v[100:103], v[176:183], v108\n"
"global_load_dwordx4 a[16:19], v9, s[40:41] offset:-2048 sc1\n"
"v_exp_f32 v186, v186\n"
"v_exp_f32 v187, v187\n"
"v_exp_f32 v188, v188\n"
"v_mfma_f32_16x16x32_bf16 a[196:199], a[88:91], a[76:79], a[196:199]\n"
"v_exp_f32 v189, v189\n"
"v_mfma_f32_32x32x16_bf16 v[144:159], v[56:59], v[48:51], v[144:159]\n"
"global_load_dwordx4 a[20:23], v9, s[40:41] offset:-1024 sc1\n"
"v_exp_f32 v190, v190\n"
"v_exp_f32 v191, v191\n"
"v_exp_f32 v128, v128\n"
"v_mfma_f32_16x16x32_bf16 a[200:203], a[92:95], a[76:79], a[200:203]\n"
"v_exp_f32 v129, v129\n"
"v_smfmac_f32_32x32x32_bf16 v[208:223], v[104:107], v[184:191], v108\n"
"s_waitcnt vmcnt(16)\n"
"global_load_dwordx4 v[68:71], v7, s[36:37] offset:-2560 sc1\n"
"v_exp_f32 v130, v130\n"
"v_exp_f32 v131, v131\n"
"v_exp_f32 v132, v132\n"
"v_mfma_f32_32x32x4_2b_bf16 v[160:191], v[66:67], v[52:53], 0 cbsz:1 abid:0\n"
"v_exp_f32 v133, v133\n"
"v_exp_f32 v134, v134\n"
"v_exp_f32 v135, v135\n"
"v_exp_f32 v136, v136\n"
"v_exp_f32 v137, v137\n"
"v_exp_f32 v138, v138\n"
"v_exp_f32 v139, v139\n"
"v_exp_f32 v140, v140\n"
"v_exp_f32 v141, v141\n"
"v_smfmac_f32_32x32x32_bf16 v[224:239], v[76:79], v[128:135], v108\n"
"global_load_dwordx2 v[54:55], v4, s[36:37] offset:-1536 sc1\n"
"v_exp_f32 v142, v142\n"
"v_exp_f32 v143, v143\n"
"v_exp_f32 v144, v144\n"
"v_mfma_f32_16x16x32_bf16 a[204:207], a[96:99], a[76:79], a[204:207]\n"
"v_exp_f32 v145, v145\n"
"v_smfmac_f32_32x32x32_bf16 v[224:239], v[80:83], v[136:143], v108\n"
"global_load_dwordx4 v[72:75], v7, s[36:37] offset:0 sc1\n"
"v_exp_f32 v146, v146\n"
"v_exp_f32 v147, v147\n"
"v_exp_f32 v148, v148\n"
"v_mfma_f32_16x16x32_bf16 a[208:211], a[100:103], a[76:79], a[208:211]\n"
"v_exp_f32 v149, v149\n"
"v_mfma_f32_32x32x16_bf16 v[160:175], v[56:59], v[36:39], v[160:175]\n"
"v_exp_f32 v150, v150\n"
"v_exp_f32 v151, v151\n"
"v_exp_f32 v152, v152\n"
"v_mfma_f32_16x16x32_bf16 a[212:215], a[104:107], a[76:79], a[212:215]\n"
"v_exp_f32 v153, v153\n"
"v_smfmac_f32_32x32x32_bf16 v[240:255], v[76:79], v[144:151], v108\n"
"s_mov_b64 exec, s[48:49]\n"
"global_load_dwordx4 v[92:95], v5, s[36:37] offset:-1280 sc1\n"
"global_load_dwordx4 v[96:99], v5, s[36:37] offset:-640 sc1\n"
"global_load_dwordx4 v[100:103], v5, s[36:37] offset:1280 sc1\n"
"global_load_dwordx4 v[104:107], v5, s[36:37] offset:1920 sc1\n"
"s_mov_b64 exec, -1\n"
"s_add_u32 s36, s36, 5120\n"
"s_addc_u32 s37, s37, 0\n"
"v_exp_f32 v154, v154\n"
"v_exp_f32 v155, v155\n"
"v_exp_f32 v156, v156\n"
"v_mfma_f32_16x16x32_bf16 a[216:219], a[88:91], a[80:83], a[216:219]\n"
"v_exp_f32 v157, v157\n"
"v_mfma_f32_32x32x16_bf16 v[176:191], v[56:59], v[40:43], v[176:191]\n"
"global_load_dwordx4 a[24:27], v9, s[40:41] offset:0 sc1\n"
"v_exp_f32 v158, v158\n"
"v_exp_f32 v159, v159\n"
"v_exp_f32 v160, v160\n"
"v_mfma_f32_16x16x32_bf16 a[220:223], a[92:95], a[80:83], a[220:223]\n"
"v_exp_f32 v161, v161\n"
"v_smfmac_f32_32x32x32_bf16 v[240:255], v[80:83], v[152:159], v108\n"
"global_load_dwordx4 a[28:31], v9, s[40:41] offset:1024 sc1\n"
"v_exp_f32 v162, v162\n"
"v_exp_f32 v163, v163\n"
"v_exp_f32 v164, v164\n"
"v_mfma_f32_32x32x4_2b_bf16 v[128:159], v[66:67], v[64:65], 0 cbsz:1 abid:1\n"
"v_exp_f32 v165, v165\n"
"v_exp_f32 v166, v166\n"
"v_exp_f32 v167, v167\n"
"v_exp_f32 v168, v168\n"
"v_exp_f32 v169, v169\n"
"v_exp_f32 v170, v170\n"
"v_exp_f32 v171, v171\n"
"v_exp_f32 v172, v172\n"
"v_exp_f32 v173, v173\n"
"v_smfmac_f32_32x32x32_bf16 v[192:207], v[76:79], v[160:167], v108\n"
"global_load_dwordx4 a[32:35], v9, s[40:41] offset:2048 sc1\n"
"s_add_u32 s40, s40, 5120\n"
"s_addc_u32 s41, s41, 0\n"
"v_exp_f32 v174, v174\n"
"v_exp_f32 v175, v175\n"
"v_exp_f32 v176, v176\n"
"v_mfma_f32_16x16x32_bf16 a[224:227], a[96:99], a[80:83], a[224:227]\n"
"v_exp_f32 v177, v177\n"
"v_smfmac_f32_32x32x32_bf16 v[192:207], v[80:83], v[168:175], v108\n"
"global_load_dwordx4 a[36:39], v122, s[38:39] offset:0 sc1\n"
"v_exp_f32 v178, v178\n"
"v_exp_f32 v179, v179\n"
"v_exp_f32 v180, v180\n"
"v_mfma_f32_16x16x32_bf16 a[228:231], a[100:103], a[80:83], a[228:231]\n"
"v_exp_f32 v181, v181\n"
"v_mfma_f32_32x32x16_bf16 v[128:143], v[60:63], v[44:47], v[128:143]\n"
"v_exp_f32 v182, v182\n"
"v_exp_f32 v183, v183\n"
"v_exp_f32 v184, v184\n"
"v_mfma_f32_16x16x32_bf16 a[232:235], a[104:107], a[80:83], a[232:235]\n"
"v_exp_f32 v185, v185\n"
"v_smfmac_f32_32x32x32_bf16 v[208:223], v[76:79], v[176:183], v108\n"
"global_load_dwordx4 a[40:43], v123, s[38:39] offset:0 sc1\n"
"v_exp_f32 v186, v186\n"
"v_exp_f32 v187, v187\n"
"v_exp_f32 v188, v188\n"
"v_mfma_f32_16x16x32_bf16 a[236:239], a[88:91], a[84:87], a[236:239]\n"
"v_exp_f32 v189, v189\n"
"v_mfma_f32_32x32x16_bf16 v[144:159], v[60:63], v[48:51], v[144:159]\n"
"global_load_dwordx4 a[44:47], v124, s[38:39] offset:0 sc1\n"
"v_exp_f32 v190, v190\n"
"v_exp_f32 v191, v191\n"
"v_exp_f32 v128, v128\n"
"v_mfma_f32_16x16x32_bf16 a[240:243], a[92:95], a[84:87], a[240:243]\n"
"v_exp_f32 v129, v129\n"
"v_smfmac_f32_32x32x32_bf16 v[208:223], v[80:83], v[184:191], v108\n"
"global_load_dwordx4 a[48:51], v125, s[38:39] offset:0 sc1\n"
"s_add_u32 s38, s38, 4096\n"
"s_addc_u32 s39, s39, 0\n"
"v_exp_f32 v130, v130\n"
"v_exp_f32 v131, v131\n"
"v_exp_f32 v132, v132\n"
"v_mfma_f32_32x32x4_2b_bf16 v[160:191], v[66:67], v[52:53], 0 cbsz:1 abid:1\n"
"v_exp_f32 v133, v133\n"
"v_exp_f32 v134, v134\n"
"v_exp_f32 v135, v135\n"
"v_exp_f32 v136, v136\n"
"v_exp_f32 v137, v137\n"
"v_exp_f32 v138, v138\n"
"v_exp_f32 v139, v139\n"
"v_exp_f32 v140, v140\n"
"v_exp_f32 v141, v141\n"
"v_smfmac_f32_32x32x32_bf16 v[224:239], v[84:87], v[128:135], v108\n"
"global_load_dwordx4 a[52:55], v9, s[40:41] offset:-2048 sc1\n"
"v_exp_f32 v142, v142\n"
"v_exp_f32 v143, v143\n"
"v_exp_f32 v144, v144\n"
"v_mfma_f32_16x16x32_bf16 a[244:247], a[96:99], a[84:87], a[244:247]\n"
"v_exp_f32 v145, v145\n"
"v_smfmac_f32_32x32x32_bf16 v[224:239], v[88:91], v[136:143], v108\n"
"global_load_dwordx4 a[56:59], v9, s[40:41] offset:-1024 sc1\n"
"v_exp_f32 v146, v146\n"
"v_exp_f32 v147, v147\n"
"v_exp_f32 v148, v148\n"
"v_mfma_f32_16x16x32_bf16 a[248:251], a[100:103], a[84:87], a[248:251]\n"
"v_exp_f32 v149, v149\n"
"v_mfma_f32_32x32x16_bf16 v[160:175], v[60:63], v[36:39], v[160:175]\n"
"v_exp_f32 v150, v150\n"
"v_exp_f32 v151, v151\n"
"v_exp_f32 v152, v152\n"
"v_mfma_f32_16x16x32_bf16 a[252:255], a[104:107], a[84:87], a[252:255]\n"
"v_exp_f32 v153, v153\n"
"v_smfmac_f32_32x32x32_bf16 v[240:255], v[84:87], v[144:151], v108\n"
"global_load_dwordx4 a[60:63], v9, s[40:41] offset:0 sc1\n"
"v_exp_f32 v154, v154\n"
"v_exp_f32 v155, v155\n"
"v_exp_f32 v156, v156\n"
"v_mfma_f32_16x16x32_bf16 a[176:179], a[124:127], a[108:111], a[176:179]\n"
"v_exp_f32 v157, v157\n"
"v_mfma_f32_32x32x16_bf16 v[176:191], v[60:63], v[40:43], v[176:191]\n"
"global_load_dwordx4 a[64:67], v9, s[40:41] offset:1024 sc1\n"
"v_exp_f32 v158, v158\n"
"v_exp_f32 v159, v159\n"
"v_exp_f32 v160, v160\n"
"v_mfma_f32_16x16x32_bf16 a[180:183], a[128:131], a[108:111], a[180:183]\n"
"v_exp_f32 v161, v161\n"
"v_smfmac_f32_32x32x32_bf16 v[240:255], v[88:91], v[152:159], v108\n"
"s_waitcnt vmcnt(15)\n"
"global_load_dwordx4 a[68:71], v9, s[40:41] offset:2048 sc1\n"
"s_add_u32 s40, s40, 5120\n"
"s_addc_u32 s41, s41, 0\n"
"v_exp_f32 v162, v162\n"
"v_exp_f32 v163, v163\n"
"v_exp_f32 v164, v164\n"
"v_mfma_f32_32x32x4_2b_bf16 v[128:159], v[54:55], v[64:65], 0 cbsz:1 abid:0\n"
"v_exp_f32 v165, v165\n"
"v_exp_f32 v166, v166\n"
"v_exp_f32 v167, v167\n"
"v_exp_f32 v168, v168\n"
"v_exp_f32 v169, v169\n"
"v_exp_f32 v170, v170\n"
"v_exp_f32 v171, v171\n"
"v_exp_f32 v172, v172\n"
"v_exp_f32 v173, v173\n"
"v_smfmac_f32_32x32x32_bf16 v[192:207], v[84:87], v[160:167], v108\n"
"global_load_dwordx4 a[72:75], v122, s[38:39] offset:0 sc1\n"
"v_exp_f32 v174, v174\n"
"v_exp_f32 v175, v175\n"
"v_exp_f32 v176, v176\n"
"v_mfma_f32_16x16x32_bf16 a[184:187], a[132:135], a[108:111], a[184:187]\n"
"v_exp_f32 v177, v177\n"
"v_smfmac_f32_32x32x32_bf16 v[192:207], v[88:91], v[168:175], v108\n"
"global_load_dwordx4 a[76:79], v123, s[38:39] offset:0 sc1\n"
"v_exp_f32 v178, v178\n"
"v_exp_f32 v179, v179\n"
"v_exp_f32 v180, v180\n"
"v_mfma_f32_16x16x32_bf16 a[188:191], a[136:139], a[108:111], a[188:191]\n"
"v_exp_f32 v181, v181\n"
"v_mfma_f32_32x32x16_bf16 v[128:143], v[68:71], v[44:47], v[128:143]\n"
"v_exp_f32 v182, v182\n"
"v_exp_f32 v183, v183\n"
"v_exp_f32 v184, v184\n"
"v_mfma_f32_16x16x32_bf16 a[192:195], a[140:143], a[108:111], a[192:195]\n"
"v_exp_f32 v185, v185\n"
"v_smfmac_f32_32x32x32_bf16 v[208:223], v[84:87], v[176:183], v108\n"
"global_load_dwordx4 a[80:83], v124, s[38:39] offset:0 sc1\n"
"v_exp_f32 v186, v186\n"
"v_exp_f32 v187, v187\n"
"v_exp_f32 v188, v188\n"
"v_mfma_f32_16x16x32_bf16 a[196:199], a[124:127], a[112:115], a[196:199]\n"
"v_exp_f32 v189, v189\n"
"v_mfma_f32_32x32x16_bf16 v[144:159], v[68:71], v[48:51], v[144:159]\n"
"global_load_dwordx4 a[84:87], v125, s[38:39] offset:0 sc1\n"
"s_add_u32 s38, s38, 4096\n"
"s_addc_u32 s39, s39, 0\n"
"v_exp_f32 v190, v190\n"
"v_exp_f32 v191, v191\n"
"v_exp_f32 v128, v128\n"
"v_mfma_f32_16x16x32_bf16 a[200:203], a[128:131], a[112:115], a[200:203]\n"
"v_exp_f32 v129, v129\n"
"v_smfmac_f32_32x32x32_bf16 v[208:223], v[88:91], v[184:191], v108\n"
"s_waitcnt vmcnt(16)\n"
"global_load_dwordx4 v[56:59], v7, s[36:37] offset:-2560 sc1\n"
"v_exp_f32 v130, v130\n"
"v_exp_f32 v131, v131\n"
"v_exp_f32 v132, v132\n"
"v_mfma_f32_32x32x4_2b_bf16 v[160:191], v[54:55], v[52:53], 0 cbsz:1 abid:0\n"
"v_exp_f32 v133, v133\n"
"v_exp_f32 v134, v134\n"
"v_exp_f32 v135, v135\n"
"v_exp_f32 v136, v136\n"
"v_exp_f32 v137, v137\n"
"v_exp_f32 v138, v138\n"
"v_exp_f32 v139, v139\n"
"v_exp_f32 v140, v140\n"
"v_exp_f32 v141, v141\n"
"v_smfmac_f32_32x32x32_bf16 v[224:239], v[92:95], v[128:135], v108\n"
"global_load_dwordx2 v[66:67], v4, s[36:37] offset:-1536 sc1\n"
"v_exp_f32 v142, v142\n"
"v_exp_f32 v143, v143\n"
"v_exp_f32 v144, v144\n"
"v_mfma_f32_16x16x32_bf16 a[204:207], a[132:135], a[112:115], a[204:207]\n"
"v_exp_f32 v145, v145\n"
"v_smfmac_f32_32x32x32_bf16 v[224:239], v[96:99], v[136:143], v108\n"
"global_load_dwordx4 v[60:63], v7, s[36:37] offset:0 sc1\n"
"v_exp_f32 v146, v146\n"
"v_exp_f32 v147, v147\n"
"v_exp_f32 v148, v148\n"
"v_mfma_f32_16x16x32_bf16 a[208:211], a[136:139], a[112:115], a[208:211]\n"
"v_exp_f32 v149, v149\n"
"v_mfma_f32_32x32x16_bf16 v[160:175], v[68:71], v[36:39], v[160:175]\n"
"v_exp_f32 v150, v150\n"
"v_exp_f32 v151, v151\n"
"v_exp_f32 v152, v152\n"
"v_mfma_f32_16x16x32_bf16 a[212:215], a[140:143], a[112:115], a[212:215]\n"
"v_exp_f32 v153, v153\n"
"v_smfmac_f32_32x32x32_bf16 v[240:255], v[92:95], v[144:151], v108\n"
"s_mov_b64 exec, s[48:49]\n"
"global_load_dwordx4 v[76:79], v5, s[36:37] offset:-1280 sc1\n"
"global_load_dwordx4 v[80:83], v5, s[36:37] offset:-640 sc1\n"
"global_load_dwordx4 v[84:87], v5, s[36:37] offset:1280 sc1\n"
"global_load_dwordx4 v[88:91], v5, s[36:37] offset:1920 sc1\n"
"s_mov_b64 exec, -1\n"
"s_add_u32 s36, s36, 5120\n"
"s_addc_u32 s37, s37, 0\n"
"v_exp_f32 v154, v154\n"
"v_exp_f32 v155, v155\n"
"v_exp_f32 v156, v156\n"
"v_mfma_f32_16x16x32_bf16 a[216:219], a[124:127], a[116:119], a[216:219]\n"
"v_exp_f32 v157, v157\n"
"v_mfma_f32_32x32x16_bf16 v[176:191], v[68:71], v[40:43], v[176:191]\n"
"global_load_dwordx4 a[88:91], v9, s[40:41] offset:-2048 sc1\n"
"v_exp_f32 v158, v158\n"
"v_exp_f32 v159, v159\n"
"v_exp_f32 v160, v160\n"
"v_mfma_f32_16x16x32_bf16 a[220:223], a[128:131], a[116:119], a[220:223]\n"
"v_exp_f32 v161, v161\n"
"v_smfmac_f32_32x32x32_bf16 v[240:255], v[96:99], v[152:159], v108\n"
"global_load_dwordx4 a[92:95], v9, s[40:41] offset:-1024 sc1\n"
"v_exp_f32 v162, v162\n"
"v_exp_f32 v163, v163\n"
"v_exp_f32 v164, v164\n"
"v_mfma_f32_32x32x4_2b_bf16 v[128:159], v[54:55], v[64:65], 0 cbsz:1 abid:1\n"
"v_exp_f32 v165, v165\n"
"v_exp_f32 v166, v166\n"
"v_exp_f32 v167, v167\n"
"v_exp_f32 v168, v168\n"
"v_exp_f32 v169, v169\n"
"v_exp_f32 v170, v170\n"
"v_exp_f32 v171, v171\n"
"v_exp_f32 v172, v172\n"
"v_exp_f32 v173, v173\n"
"v_smfmac_f32_32x32x32_bf16 v[192:207], v[92:95], v[160:167], v108\n"
"global_load_dwordx4 a[96:99], v9, s[40:41] offset:0 sc1\n"
"v_exp_f32 v174, v174\n"
"v_exp_f32 v175, v175\n"
"v_exp_f32 v176, v176\n"
"v_mfma_f32_16x16x32_bf16 a[224:227], a[132:135], a[116:119], a[224:227]\n"
"v_exp_f32 v177, v177\n"
"v_smfmac_f32_32x32x32_bf16 v[192:207], v[96:99], v[168:175], v108\n"
"global_load_dwordx4 a[100:103], v9, s[40:41] offset:1024 sc1\n"
"v_exp_f32 v178, v178\n"
"v_exp_f32 v179, v179\n"
"v_exp_f32 v180, v180\n"
"v_mfma_f32_16x16x32_bf16 a[228:231], a[136:139], a[116:119], a[228:231]\n"
"v_exp_f32 v181, v181\n"
"v_mfma_f32_32x32x16_bf16 v[128:143], v[72:75], v[44:47], v[128:143]\n"
"v_exp_f32 v182, v182\n"
"v_exp_f32 v183, v183\n"
"v_exp_f32 v184, v184\n"
"v_mfma_f32_16x16x32_bf16 a[232:235], a[140:143], a[116:119], a[232:235]\n"
"v_exp_f32 v185, v185\n"
"v_smfmac_f32_32x32x32_bf16 v[208:223], v[92:95], v[176:183], v108\n"
"global_load_dwordx4 a[104:107], v9, s[40:41] offset:2048 sc1\n"
"s_add_u32 s40, s40, 5120\n"
"s_addc_u32 s41, s41, 0\n"
"v_exp_f32 v186, v186\n"
"v_exp_f32 v187, v187\n"
"v_exp_f32 v188, v188\n"
"v_mfma_f32_16x16x32_bf16 a[236:239], a[124:127], a[120:123], a[236:239]\n"
"v_exp_f32 v189, v189\n"
"v_mfma_f32_32x32x16_bf16 v[144:159], v[72:75], v[48:51], v[144:159]\n"
"global_load_dwordx4 a[108:111], v122, s[38:39] offset:0 sc1\n"
"v_exp_f32 v190, v190\n"
"v_exp_f32 v191, v191\n"
"v_exp_f32 v128, v128\n"
"v_mfma_f32_16x16x32_bf16 a[240:243], a[128:131], a[120:123], a[240:243]\n"
"v_exp_f32 v129, v129\n"
"v_smfmac_f32_32x32x32_bf16 v[208:223], v[96:99], v[184:191], v108\n"
"global_load_dwordx4 a[112:115], v123, s[38:39] offset:0 sc1\n"
"v_exp_f32 v130, v130\n"
"v_exp_f32 v131, v131\n"
"v_exp_f32 v132, v132\n"
"v_mfma_f32_32x32x4_2b_bf16 v[160:191], v[54:55], v[52:53], 0 cbsz:1 abid:1\n"
"v_exp_f32 v133, v133\n"
"v_exp_f32 v134, v134\n"
"v_exp_f32 v135, v135\n"
"v_exp_f32 v136, v136\n"
"v_exp_f32 v137, v137\n"
"v_exp_f32 v138, v138\n"
"v_exp_f32 v139, v139\n"
"v_exp_f32 v140, v140\n"
"v_exp_f32 v141, v141\n"
"v_smfmac_f32_32x32x32_bf16 v[224:239], v[100:103], v[128:135], v108\n"
"global_load_dwordx4 a[116:119], v124, s[38:39] offset:0 sc1\n"
"v_exp_f32 v142, v142\n"
"v_exp_f32 v143, v143\n"
"v_exp_f32 v144, v144\n"
"v_mfma_f32_16x16x32_bf16 a[244:247], a[132:135], a[120:123], a[244:247]\n"
"v_exp_f32 v145, v145\n"
"v_smfmac_f32_32x32x32_bf16 v[224:239], v[104:107], v[136:143], v108\n"
"v_exp_f32 v146, v146\n"
"v_exp_f32 v147, v147\n"
"v_exp_f32 v148, v148\n"
"v_mfma_f32_16x16x32_bf16 a[248:251], a[136:139], a[120:123], a[248:251]\n"
"v_exp_f32 v149, v149\n"
"v_mfma_f32_32x32x16_bf16 v[160:175], v[72:75], v[36:39], v[160:175]\n"
"v_exp_f32 v150, v150\n"
"v_exp_f32 v151, v151\n"
"v_exp_f32 v152, v152\n"
"v_mfma_f32_16x16x32_bf16 a[252:255], a[140:143], a[120:123], a[252:255]\n"
"v_exp_f32 v153, v153\n"
"v_smfmac_f32_32x32x32_bf16 v[240:255], v[100:103], v[144:151], v108\n"
"global_load_dwordx4 a[120:123], v125, s[38:39] offset:0 sc1\n"
"s_add_u32 s38, s38, 4096\n"
"s_addc_u32 s39, s39, 0\n"
"v_exp_f32 v154, v154\n"
"v_exp_f32 v155, v155\n"
"v_exp_f32 v156, v156\n"
"s_waitcnt vmcnt(29)\n"
"v_mfma_f32_16x16x32_bf16 a[176:179], a[16:19], a[0:3], a[176:179]\n"
"v_exp_f32 v157, v157\n"
"v_mfma_f32_32x32x16_bf16 v[176:191], v[72:75], v[40:43], v[176:191]\n"
"global_load_dwordx4 a[124:127], v9, s[40:41] offset:-2048 sc1\n"
"v_exp_f32 v158, v158\n"
"v_exp_f32 v159, v159\n"
"v_exp_f32 v160, v160\n"
"v_mfma_f32_16x16x32_bf16 a[180:183], a[20:23], a[0:3], a[180:183]\n"
"v_exp_f32 v161, v161\n"
"v_smfmac_f32_32x32x32_bf16 v[240:255], v[104:107], v[152:159], v108\n"
"s_waitcnt vmcnt(14)\n"
"global_load_dwordx4 a[128:131], v9, s[40:41] offset:-1024 sc1\n"
"v_exp_f32 v162, v162\n"
"v_exp_f32 v163, v163\n"
"v_exp_f32 v164, v164\n"
"v_mfma_f32_32x32x4_2b_bf16 v[128:159], v[66:67], v[64:65], 0 cbsz:1 abid:0\n"
"v_exp_f32 v165, v165\n"
"v_exp_f32 v166, v166\n"
"v_exp_f32 v167, v167\n"
"v_exp_f32 v168, v168\n"
"v_exp_f32 v169, v169\n"
"v_exp_f32 v170, v170\n"
"v_exp_f32 v171, v171\n"
"v_exp_f32 v172, v172\n"
"v_exp_f32 v173, v173\n"
"v_smfmac_f32_32x32x32_bf16 v[192:207], v[100:103], v[160:167], v108\n"
"global_load_dwordx4 a[132:135], v9, s[40:41] offset:0 sc1\n"
"v_exp_f32 v174, v174\n"
"v_exp_f32 v175, v175\n"
"v_exp_f32 v176, v176\n"
"v_mfma_f32_16x16x32_bf16 a[184:187], a[24:27], a[0:3], a[184:187]\n"
"v_exp_f32 v177, v177\n"
"v_smfmac_f32_32x32x32_bf16 v[192:207], v[104:107], v[168:175], v108\n"
"global_load_dwordx4 a[136:139], v9, s[40:41] offset:1024 sc1\n"
"v_exp_f32 v178, v178\n"
"v_exp_f32 v179, v179\n"
"v_exp_f32 v180, v180\n"
"v_mfma_f32_16x16x32_bf16 a[188:191], a[28:31], a[0:3], a[188:191]\n"
"v_exp_f32 v181, v181\n"
"v_mfma_f32_32x32x16_bf16 v[128:143], v[56:59], v[44:47], v[128:143]\n"
"v_exp_f32 v182, v182\n"
"v_exp_f32 v183, v183\n"
"v_exp_f32 v184, v184\n"
"v_mfma_f32_16x16x32_bf16 a[192:195], a[32:35], a[0:3], a[192:195]\n"
"v_exp_f32 v185, v185\n"
"v_smfmac_f32_32x32x32_bf16 v[208:223], v[100:103], v[176:183], v108\n"
"global_load_dwordx4 a[140:143], v9, s[40:41] offset:2048 sc1\n"
"s_add_u32 s40, s40, 5120\n"
"s_addc_u32 s41, s41, 0\n"
"v_exp_f32 v186, v186\n"
"v_exp_f32 v187, v187\n"
"v_exp_f32 v188, v188\n"
"v_mfma_f32_16x16x32_bf16 a[196:199], a[16:19], a[4:7], a[196:199]\n"
"v_exp_f32 v189, v189\n"
"v_mfma_f32_32x32x16_bf16 v[144:159], v[56:59], v[48:51], v[144:159]\n"
"global_load_lds_dwordx4 v9, s[42:43] offset:0 sc1\n"
"v_exp_f32 v190, v190\n"
"v_exp_f32 v191, v191\n"
"v_exp_f32 v128, v128\n"
"v_mfma_f32_16x16x32_bf16 a[200:203], a[20:23], a[4:7], a[200:203]\n"
"v_exp_f32 v129, v129\n"
"v_smfmac_f32_32x32x32_bf16 v[208:223], v[104:107], v[184:191], v108\n"
"s_waitcnt vmcnt(15)\n"
"global_load_dwordx4 v[68:71], v7, s[36:37] offset:-2560 sc1\n"
"v_exp_f32 v130, v130\n"
"v_exp_f32 v131, v131\n"
"v_exp_f32 v132, v132\n"
"v_mfma_f32_32x32x4_2b_bf16 v[160:191], v[66:67], v[52:53], 0 cbsz:1 abid:0\n"
"v_exp_f32 v133, v133\n"
"v_exp_f32 v134, v134\n"
"v_exp_f32 v135, v135\n"
"v_exp_f32 v136, v136\n"
"v_exp_f32 v137, v137\n"
"v_exp_f32 v138, v138\n"
"v_exp_f32 v139, v139\n"
"v_exp_f32 v140, v140\n"
"v_exp_f32 v141, v141\n"
"v_smfmac_f32_32x32x32_bf16 v[224:239], v[76:79], v[128:135], v108\n"
"global_load_dwordx2 v[54:55], v4, s[36:37] offset:-1536 sc1\n"
"v_exp_f32 v142, v142\n"
"v_exp_f32 v143, v143\n"
"v_exp_f32 v144, v144\n"
"v_mfma_f32_16x16x32_bf16 a[204:207], a[24:27], a[4:7], a[204:207]\n"
"v_exp_f32 v145, v145\n"
"v_smfmac_f32_32x32x32_bf16 v[224:239], v[80:83], v[136:143], v108\n"
"global_load_dwordx4 v[72:75], v7, s[36:37] offset:0 sc1\n"
"v_exp_f32 v146, v146\n"
"v_exp_f32 v147, v147\n"
"v_exp_f32 v148, v148\n"
"v_mfma_f32_16x16x32_bf16 a[208:211], a[28:31], a[4:7], a[208:211]\n"
"v_exp_f32 v149, v149\n"
"v_mfma_f32_32x32x16_bf16 v[160:175], v[56:59], v[36:39], v[160:175]\n"
"v_exp_f32 v150, v150\n"
"v_exp_f32 v151, v151\n"
"v_exp_f32 v152, v152\n"
"v_mfma_f32_16x16x32_bf16 a[212:215], a[32:35], a[4:7], a[212:215]\n"
"v_exp_f32 v153, v153\n"
"v_smfmac_f32_32x32x32_bf16 v[240:255], v[76:79], v[144:151], v108\n"
"s_mov_b64 exec, s[48:49]\n"
"global_load_dwordx4 v[92:95], v5, s[36:37] offset:-1280 sc1\n"
"global_load_dwordx4 v[96:99], v5, s[36:37] offset:-640 sc1\n"
"global_load_dwordx4 v[100:103], v5, s[36:37] offset:1280 sc1\n"
"global_load_dwordx4 v[104:107], v5, s[36:37] offset:1920 sc1\n"
"s_mov_b64 exec, -1\n"
"s_add_u32 s36, s36, 5120\n"
"s_addc_u32 s37, s37, 0\n"
"v_exp_f32 v154, v154\n"
"v_exp_f32 v155, v155\n"
"v_exp_f32 v156, v156\n"
"v_mfma_f32_16x16x32_bf16 a[216:219], a[16:19], a[8:11], a[216:219]\n"
"v_exp_f32 v157, v157\n"
"v_mfma_f32_32x32x16_bf16 v[176:191], v[56:59], v[40:43], v[176:191]\n"
"global_load_lds_dwordx4 v9, s[42:43] offset:1024 sc1\n"
"v_exp_f32 v158, v158\n"
"v_exp_f32 v159, v159\n"
"v_exp_f32 v160, v160\n"
"v_mfma_f32_16x16x32_bf16 a[220:223], a[20:23], a[8:11], a[220:223]\n"
"v_exp_f32 v161, v161\n"
"v_smfmac_f32_32x32x32_bf16 v[240:255], v[80:83], v[152:159], v108\n"
"global_load_lds_dwordx4 v9, s[42:43] offset:2048 sc1\n"
"v_exp_f32 v162, v162\n"
"v_exp_f32 v163, v163\n"
"v_exp_f32 v164, v164\n"
"v_mfma_f32_32x32x4_2b_bf16 v[128:159], v[66:67], v[64:65], 0 cbsz:1 abid:1\n"
"v_exp_f32 v165, v165\n"
"v_exp_f32 v166, v166\n"
"v_exp_f32 v167, v167\n"
"v_exp_f32 v168, v168\n"
"v_exp_f32 v169, v169\n"
"v_exp_f32 v170, v170\n"
"v_exp_f32 v171, v171\n"
"v_exp_f32 v172, v172\n"
"v_exp_f32 v173, v173\n"
"v_smfmac_f32_32x32x32_bf16 v[192:207], v[76:79], v[160:167], v108\n"
"global_load_lds_dwordx4 v9, s[42:43] offset:3072 sc1\n"
"s_add_u32 s42, s42, 4096\n"
"s_addc_u32 s43, s43, 0\n"
"s_add_u32 m0, m0, 4096\n"
"v_exp_f32 v174, v174\n"
"v_exp_f32 v175, v175\n"
"v_exp_f32 v176, v176\n"
"v_mfma_f32_16x16x32_bf16 a[224:227], a[24:27], a[8:11], a[224:227]\n"
"v_exp_f32 v177, v177\n"
"v_smfmac_f32_32x32x32_bf16 v[192:207], v[80:83], v[168:175], v108\n"
"global_load_lds_dwordx4 v9, s[42:43] offset:0 sc1\n"
"v_exp_f32 v178, v178\n"
"v_exp_f32 v179, v179\n"
"v_exp_f32 v180, v180\n"
"v_mfma_f32_16x16x32_bf16 a[228:231], a[28:31], a[8:11], a[228:231]\n"
"v_exp_f32 v181, v181\n"
"v_mfma_f32_32x32x16_bf16 v[128:143], v[60:63], v[44:47], v[128:143]\n"
"v_exp_f32 v182, v182\n"
"v_exp_f32 v183, v183\n"
"v_exp_f32 v184, v184\n"
"v_mfma_f32_16x16x32_bf16 a[232:235], a[32:35], a[8:11], a[232:235]\n"
"v_exp_f32 v185, v185\n"
"v_smfmac_f32_32x32x32_bf16 v[208:223], v[76:79], v[176:183], v108\n"
"global_load_lds_dwordx4 v9, s[42:43] offset:1024 sc1\n"
"v_exp_f32 v186, v186\n"
"v_exp_f32 v187, v187\n"
"v_exp_f32 v188, v188\n"
"v_mfma_f32_16x16x32_bf16 a[236:239], a[16:19], a[12:15], a[236:239]\n"
"v_exp_f32 v189, v189\n"
"v_mfma_f32_32x32x16_bf16 v[144:159], v[60:63], v[48:51], v[144:159]\n"
"global_load_lds_dwordx4 v9, s[42:43] offset:2048 sc1\n"
"v_exp_f32 v190, v190\n"
"v_exp_f32 v191, v191\n"
"v_exp_f32 v128, v128\n"
"v_mfma_f32_16x16x32_bf16 a[240:243], a[20:23], a[12:15], a[240:243]\n"
"v_exp_f32 v129, v129\n"
"v_smfmac_f32_32x32x32_bf16 v[208:223], v[80:83], v[184:191], v108\n"
"global_load_lds_dwordx4 v9, s[42:43] offset:3072 sc1\n"
"s_add_u32 s42, s42, 4096\n"
"s_addc_u32 s43, s43, 0\n"
"s_add_u32 m0, m0, 4096\n"
"v_exp_f32 v130, v130\n"
"v_exp_f32 v131, v131\n"
"v_exp_f32 v132, v132\n"
"v_mfma_f32_32x32x4_2b_bf16 v[160:191], v[66:67], v[52:53], 0 cbsz:1 abid:1\n"
"v_exp_f32 v133, v133\n"
"v_exp_f32 v134, v134\n"
"v_exp_f32 v135, v135\n"
"v_exp_f32 v136, v136\n"
"v_exp_f32 v137, v137\n"
"v_exp_f32 v138, v138\n"
"v_exp_f32 v139, v139\n"
"v_exp_f32 v140, v140\n"
"v_exp_f32 v141, v141\n"
"v_smfmac_f32_32x32x32_bf16 v[224:239], v[84:87], v[128:135], v108\n"
"global_load_lds_dwordx4 v9, s[42:43] offset:0 sc1\n"
"v_exp_f32 v142, v142\n"
"v_exp_f32 v143, v143\n"
"v_exp_f32 v144, v144\n"
"v_mfma_f32_16x16x32_bf16 a[244:247], a[24:27], a[12:15], a[244:247]\n"
"v_exp_f32 v145, v145\n"
"v_smfmac_f32_32x32x32_bf16 v[224:239], v[88:91], v[136:143], v108\n"
"global_load_lds_dwordx4 v9, s[42:43] offset:1024 sc1\n"
"v_exp_f32 v146, v146\n"
"v_exp_f32 v147, v147\n"
"v_exp_f32 v148, v148\n"
"v_mfma_f32_16x16x32_bf16 a[248:251], a[28:31], a[12:15], a[248:251]\n"
"v_exp_f32 v149, v149\n"
"v_mfma_f32_32x32x16_bf16 v[160:175], v[60:63], v[36:39], v[160:175]\n"
"v_exp_f32 v150, v150\n"
"v_exp_f32 v151, v151\n"
"v_exp_f32 v152, v152\n"
"v_mfma_f32_16x16x32_bf16 a[252:255], a[32:35], a[12:15], a[252:255]\n"
"v_exp_f32 v153, v153\n"
"v_smfmac_f32_32x32x32_bf16 v[240:255], v[84:87], v[144:151], v108\n"
"global_load_lds_dwordx4 v9, s[42:43] offset:2048 sc1\n"
"v_exp_f32 v154, v154\n"
"v_exp_f32 v155, v155\n"
"v_exp_f32 v156, v156\n"
"v_mfma_f32_16x16x32_bf16 a[176:179], a[52:55], a[36:39], a[176:179]\n"
"v_exp_f32 v157, v157\n"
"v_mfma_f32_32x32x16_bf16 v[176:191], v[60:63], v[40:43], v[176:191]\n"
"global_load_lds_dwordx4 v9, s[42:43] offset:3072 sc1\n"
"s_add_u32 s42, s42, 4096\n"
"s_addc_u32 s43, s43, 0\n"
"s_add_u32 m0, m0, 4096\n"
"v_exp_f32 v158, v158\n"
"v_exp_f32 v159, v159\n"
"v_exp_f32 v160, v160\n"
"v_mfma_f32_16x16x32_bf16 a[180:183], a[56:59], a[36:39], a[180:183]\n"
"v_exp_f32 v161, v161\n"
"v_smfmac_f32_32x32x32_bf16 v[240:255], v[88:91], v[152:159], v108\n"
"s_waitcnt vmcnt(15)\n"
"global_load_lds_dwordx4 v9, s[42:43] offset:0 sc1\n"
"v_exp_f32 v162, v162\n"
"v_exp_f32 v163, v163\n"
"v_exp_f32 v164, v164\n"
"v_mfma_f32_32x32x4_2b_bf16 v[128:159], v[54:55], v[64:65], 0 cbsz:1 abid:0\n"
"v_exp_f32 v165, v165\n"
"v_exp_f32 v166, v166\n"
"v_exp_f32 v167, v167\n"
"v_exp_f32 v168, v168\n"
"v_exp_f32 v169, v169\n"
"v_exp_f32 v170, v170\n"
"v_exp_f32 v171, v171\n"
"v_exp_f32 v172, v172\n"
"v_exp_f32 v173, v173\n"
"v_smfmac_f32_32x32x32_bf16 v[192:207], v[84:87], v[160:167], v108\n"
"global_load_lds_dwordx4 v9, s[42:43] offset:1024 sc1\n"
"v_exp_f32 v174, v174\n"
"v_exp_f32 v175, v175\n"
"v_exp_f32 v176, v176\n"
"v_mfma_f32_16x16x32_bf16 a[184:187], a[60:63], a[36:39], a[184:187]\n"
"v_exp_f32 v177, v177\n"
"v_smfmac_f32_32x32x32_bf16 v[192:207], v[88:91], v[168:175], v108\n"
"global_load_lds_dwordx4 v9, s[42:43] offset:2048 sc1\n"
"v_exp_f32 v178, v178\n"
"v_exp_f32 v179, v179\n"
"v_exp_f32 v180, v180\n"
"v_mfma_f32_16x16x32_bf16 a[188:191], a[64:67], a[36:39], a[188:191]\n"
"v_exp_f32 v181, v181\n"
"v_mfma_f32_32x32x16_bf16 v[128:143], v[68:71], v[44:47], v[128:143]\n"
"v_exp_f32 v182, v182\n"
"v_exp_f32 v183, v183\n"
"v_exp_f32 v184, v184\n"
"v_mfma_f32_16x16x32_bf16 a[192:195], a[68:71], a[36:39], a[192:195]\n"
"v_exp_f32 v185, v185\n"
"v_smfmac_f32_32x32x32_bf16 v[208:223], v[84:87], v[176:183], v108\n"
"global_load_lds_dwordx4 v9, s[42:43] offset:3072 sc1\n"
"s_add_u32 s42, s42, 4096\n"
"s_addc_u32 s43, s43, 0\n"
"s_add_u32 m0, m0, 4096\n"
"v_exp_f32 v186, v186\n"
"v_exp_f32 v187, v187\n"
"v_exp_f32 v188, v188\n"
"v_mfma_f32_16x16x32_bf16 a[196:199], a[52:55], a[40:43], a[196:199]\n"
"v_exp_f32 v189, v189\n"
"v_mfma_f32_32x32x16_bf16 v[144:159], v[68:71], v[48:51], v[144:159]\n"
"global_load_lds_dwordx4 v9, s[42:43] offset:0 sc1\n"
"v_exp_f32 v190, v190\n"
"v_exp_f32 v191, v191\n"
"v_exp_f32 v128, v128\n"
"v_mfma_f32_16x16x32_bf16 a[200:203], a[56:59], a[40:43], a[200:203]\n"
"v_exp_f32 v129, v129\n"
"v_smfmac_f32_32x32x32_bf16 v[208:223], v[88:91], v[184:191], v108\n"
"s_waitcnt vmcnt(16)\n"
"global_load_dwordx4 v[56:59], v7, s[36:37] offset:-2560 sc1\n"
"v_exp_f32 v130, v130\n"
"v_exp_f32 v131, v131\n"
"v_exp_f32 v132, v132\n"
"v_mfma_f32_32x32x4_2b_bf16 v[160:191], v[54:55], v[52:53], 0 cbsz:1 abid:0\n"
"v_exp_f32 v133, v133\n"
"v_exp_f32 v134, v134\n"
"v_exp_f32 v135, v135\n"
"v_exp_f32 v136, v136\n"
"v_exp_f32 v137, v137\n"
"v_exp_f32 v138, v138\n"
"v_exp_f32 v139, v139\n"
"v_exp_f32 v140, v140\n"
"v_exp_f32 v141, v141\n"
"v_smfmac_f32_32x32x32_bf16 v[224:239], v[92:95], v[128:135], v108\n"
"global_load_dwordx2 v[66:67], v4, s[36:37] offset:-1536 sc1\n"
"v_exp_f32 v142, v142\n"
"v_exp_f32 v143, v143\n"
"v_exp_f32 v144, v144\n"
"v_mfma_f32_16x16x32_bf16 a[204:207], a[60:63], a[40:43], a[204:207]\n"
"v_exp_f32 v145, v145\n"
"v_smfmac_f32_32x32x32_bf16 v[224:239], v[96:99], v[136:143], v108\n"
"global_load_dwordx4 v[60:63], v7, s[36:37] offset:0 sc1\n"
"v_exp_f32 v146, v146\n"
"v_exp_f32 v147, v147\n"
"v_exp_f32 v148, v148\n"
"v_mfma_f32_16x16x32_bf16 a[208:211], a[64:67], a[40:43], a[208:211]\n"
"v_exp_f32 v149, v149\n"
"v_mfma_f32_32x32x16_bf16 v[160:175], v[68:71], v[36:39], v[160:175]\n"
"v_exp_f32 v150, v150\n"
"v_exp_f32 v151, v151\n"
"v_exp_f32 v152, v152\n"
"v_mfma_f32_16x16x32_bf16 a[212:215], a[68:71], a[40:43], a[212:215]\n"
"v_exp_f32 v153, v153\n"
"v_smfmac_f32_32x32x32_bf16 v[240:255], v[92:95], v[144:151], v108\n"
"s_mov_b64 exec, s[48:49]\n"
"global_load_dwordx4 v[76:79], v5, s[36:37] offset:-1280 sc1\n"
"global_load_dwordx4 v[80:83], v5, s[36:37] offset:-640 sc1\n"
"global_load_dwordx4 v[84:87], v5, s[36:37] offset:1280 sc1\n"
"global_load_dwordx4 v[88:91], v5, s[36:37] offset:1920 sc1\n"
"s_mov_b64 exec, -1\n"
"s_add_u32 s36, s36, 5120\n"
"s_addc_u32 s37, s37, 0\n"
"v_exp_f32 v154, v154\n"
"v_exp_f32 v155, v155\n"
"v_exp_f32 v156, v156\n"
"v_mfma_f32_16x16x32_bf16 a[216:219], a[52:55], a[44:47], a[216:219]\n"
"v_exp_f32 v157, v157\n"
"v_mfma_f32_32x32x16_bf16 v[176:191], v[68:71], v[40:43], v[176:191]\n"
"global_load_lds_dwordx4 v9, s[42:43] offset:1024 sc1\n"
"v_exp_f32 v158, v158\n"
"v_exp_f32 v159, v159\n"
"v_exp_f32 v160, v160\n"
"v_mfma_f32_16x16x32_bf16 a[220:223], a[56:59], a[44:47], a[220:223]\n"
"v_exp_f32 v161, v161\n"
"v_smfmac_f32_32x32x32_bf16 v[240:255], v[96:99], v[152:159], v108\n"
"global_load_lds_dwordx4 v9, s[42:43] offset:2048 sc1\n"
"v_exp_f32 v162, v162\n"
"v_exp_f32 v163, v163\n"
"v_exp_f32 v164, v164\n"
"v_mfma_f32_32x32x4_2b_bf16 v[128:159], v[54:55], v[64:65], 0 cbsz:1 abid:1\n"
"v_exp_f32 v165, v165\n"
"v_exp_f32 v166, v166\n"
"v_exp_f32 v167, v167\n"
"v_exp_f32 v168, v168\n"
"v_exp_f32 v169, v169\n"
"v_exp_f32 v170, v170\n"
"v_exp_f32 v171, v171\n"
"v_exp_f32 v172, v172\n"
"v_exp_f32 v173, v173\n"
"v_smfmac_f32_32x32x32_bf16 v[192:207], v[92:95], v[160:167], v108\n"
"global_load_lds_dwordx4 v9, s[42:43] offset:3072 sc1\n"
"s_mul_i32 s47, s30, -15360\n"
"s_add_u32 s47, s47, 65536\n"
"s_add_u32 s42, s42, s47\n"
"s_addc_u32 s43, s43, 0\n"
"s_add_u32 m0, m0, s47\n"
"v_exp_f32 v174, v174\n"
"v_exp_f32 v175, v175\n"
"v_exp_f32 v176, v176\n"
"v_mfma_f32_16x16x32_bf16 a[224:227], a[60:63], a[44:47], a[224:227]\n"
"v_exp_f32 v177, v177\n"
"v_smfmac_f32_32x32x32_bf16 v[192:207], v[96:99], v[168:175], v108\n"
"global_load_lds_dwordx4 v9, s[42:43] offset:0 sc1\n"
"v_exp_f32 v178, v178\n"
"v_exp_f32 v179, v179\n"
"v_exp_f32 v180, v180\n"
"v_mfma_f32_16x16x32_bf16 a[228:231], a[64:67], a[44:47], a[228:231]\n"
"v_exp_f32 v181, v181\n"
"v_mfma_f32_32x32x16_bf16 v[128:143], v[72:75], v[44:47], v[128:143]\n"
"v_exp_f32 v182, v182\n"
"v_exp_f32 v183, v183\n"
"v_exp_f32 v184, v184\n"
"v_mfma_f32_16x16x32_bf16 a[232:235], a[68:71], a[44:47], a[232:235]\n"
"v_exp_f32 v185, v185\n"
"v_smfmac_f32_32x32x32_bf16 v[208:223], v[92:95], v[176:183], v108\n"
"global_load_lds_dwordx4 v9, s[42:43] offset:1024 sc1\n"
"v_exp_f32 v186, v186\n"
"v_exp_f32 v187, v187\n"
"v_exp_f32 v188, v188\n"
"v_mfma_f32_16x16x32_bf16 a[236:239], a[52:55], a[48:51], a[236:239]\n"
"v_exp_f32 v189, v189\n"
"v_mfma_f32_32x32x16_bf16 v[144:159], v[72:75], v[48:51], v[144:159]\n"
"global_load_lds_dwordx4 v9, s[42:43] offset:2048 sc1\n"
"v_exp_f32 v190, v190\n"
"v_exp_f32 v191, v191\n"
"v_exp_f32 v128, v128\n"
"v_mfma_f32_16x16x32_bf16 a[240:243], a[56:59], a[48:51], a[240:243]\n"
"v_exp_f32 v129, v129\n"
"v_smfmac_f32_32x32x32_bf16 v[208:223], v[96:99], v[184:191], v108\n"
"global_load_lds_dwordx4 v9, s[42:43] offset:3072 sc1\n"
"s_add_u32 s42, s42, 4096\n"
"s_addc_u32 s43, s43, 0\n"
"s_add_u32 m0, m0, 4096\n"
"v_exp_f32 v130, v130\n"
"v_exp_f32 v131, v131\n"
"v_exp_f32 v132, v132\n"
"v_mfma_f32_32x32x4_2b_bf16 v[160:191], v[54:55], v[52:53], 0 cbsz:1 abid:1\n"
"v_exp_f32 v133, v133\n"
"v_exp_f32 v134, v134\n"
"v_exp_f32 v135, v135\n"
"v_exp_f32 v136, v136\n"
"v_exp_f32 v137, v137\n"
"v_exp_f32 v138, v138\n"
"v_exp_f32 v139, v139\n"
"v_exp_f32 v140, v140\n"
"v_exp_f32 v141, v141\n"
"v_smfmac_f32_32x32x32_bf16 v[224:239], v[100:103], v[128:135], v108\n"
"global_load_lds_dwordx4 v9, s[42:43] offset:0 sc1\n"
"v_exp_f32 v142, v142\n"
"v_exp_f32 v143, v143\n"
"v_exp_f32 v144, v144\n"
"v_mfma_f32_16x16x32_bf16 a[244:247], a[60:63], a[48:51], a[244:247]\n"
"v_exp_f32 v145, v145\n"
"v_smfmac_f32_32x32x32_bf16 v[224:239], v[104:107], v[136:143], v108\n"
"global_load_dwordx4 v[112:115], v0, s[28:29] offset:0\n"
"v_exp_f32 v146, v146\n"
"v_exp_f32 v147, v147\n"
"v_exp_f32 v148, v148\n"
"v_mfma_f32_16x16x32_bf16 a[248:251], a[64:67], a[48:51], a[248:251]\n"
"v_exp_f32 v149, v149\n"
"v_mfma_f32_32x32x16_bf16 v[160:175], v[72:75], v[36:39], v[160:175]\n"
"v_exp_f32 v150, v150\n"
"v_exp_f32 v151, v151\n"
"v_exp_f32 v152, v152\n"
"v_mfma_f32_16x16x32_bf16 a[252:255], a[68:71], a[48:51], a[252:255]\n"
"v_exp_f32 v153, v153\n"
"v_smfmac_f32_32x32x32_bf16 v[240:255], v[100:103], v[144:151], v108\n"
"global_load_dwordx4 v[116:119], v0, s[28:29] offset:64\n"
"v_exp_f32 v154, v154\n"
"v_exp_f32 v155, v155\n"
"v_exp_f32 v156, v156\n"
"v_exp_f32 v157, v157\n"
"v_mfma_f32_32x32x16_bf16 v[176:191], v[72:75], v[40:43], v[176:191]\n"
"global_load_dwordx2 v[120:121], v17, s[28:29] offset:0\n"
"v_exp_f32 v158, v158\n"
"v_exp_f32 v159, v159\n"
"v_exp_f32 v160, v160\n"
"v_exp_f32 v161, v161\n"
"v_smfmac_f32_32x32x32_bf16 v[240:255], v[104:107], v[152:159], v108\n"
"s_waitcnt vmcnt(15)\n"
"global_load_dwordx4 a[0:3], v122, s[38:39] offset:0 sc1\n"
"v_exp_f32 v162, v162\n"
"v_exp_f32 v163, v163\n"
"v_exp_f32 v164, v164\n"
"v_mfma_f32_32x32x4_2b_bf16 v[128:159], v[66:67], v[64:65], 0 cbsz:1 abid:0\n"
"v_exp_f32 v165, v165\n"
"v_exp_f32 v166, v166\n"
"v_exp_f32 v167, v167\n"
"v_exp_f32 v168, v168\n"
"v_exp_f32 v169, v169\n"
"v_exp_f32 v170, v170\n"
"v_exp_f32 v171, v171\n"
"v_exp_f32 v172, v172\n"
"v_exp_f32 v173, v173\n"
"v_smfmac_f32_32x32x32_bf16 v[192:207], v[100:103], v[160:167], v108\n"
"global_load_dword v109, v16, s[66:67] offset:0 sc1\n"
"v_exp_f32 v174, v174\n"
"v_exp_f32 v175, v175\n"
"v_exp_f32 v176, v176\n"
"v_exp_f32 v177, v177\n"
"v_smfmac_f32_32x32x32_bf16 v[192:207], v[104:107], v[168:175], v108\n"
"global_load_dwordx4 a[4:7], v123, s[38:39] offset:0 sc1\n"
"v_exp_f32 v178, v178\n"
"v_exp_f32 v179, v179\n"
"v_exp_f32 v180, v180\n"
"v_exp_f32 v181, v181\n"
"v_mfma_f32_32x32x16_bf16 v[128:143], v[56:59], v[44:47], v[128:143]\n"
"v_exp_f32 v182, v182\n"
"v_exp_f32 v183, v183\n"
"v_exp_f32 v184, v184\n"
"v_exp_f32 v185, v185\n"
"v_smfmac_f32_32x32x32_bf16 v[208:223], v[100:103], v[176:183], v108\n"
"global_load_dwordx4 a[8:11], v124, s[38:39] offset:0 sc1\n"
"v_exp_f32 v186, v186\n"
"v_exp_f32 v187, v187\n"
"v_exp_f32 v188, v188\n"
"v_exp_f32 v189, v189\n"
"v_mfma_f32_32x32x16_bf16 v[144:159], v[56:59], v[48:51], v[144:159]\n"
"global_load_dwordx4 a[12:15], v125, s[38:39] offset:0 sc1\n"
"s_add_u32 s38, s38, 4096\n"
"s_addc_u32 s39, s39, 0\n"
"v_exp_f32 v190, v190\n"
"v_exp_f32 v191, v191\n"
"v_exp_f32 v128, v128\n"
"v_exp_f32 v129, v129\n"
"v_smfmac_f32_32x32x32_bf16 v[208:223], v[104:107], v[184:191], v108\n"
"s_waitcnt vmcnt(16)\n"
"global_load_dwordx4 v[68:71], v7, s[36:37] offset:-2560 sc1\n"
"v_exp_f32 v130, v130\n"
"v_exp_f32 v131, v131\n"
"v_exp_f32 v132, v132\n"
"v_mfma_f32_32x32x4_2b_bf16 v[160:191], v[66:67], v[52:53], 0 cbsz:1 abid:0\n"
"v_exp_f32 v133, v133\n"
"v_exp_f32 v134, v134\n"
"v_exp_f32 v135, v135\n"
"v_exp_f32 v136, v136\n"
"v_exp_f32 v137, v137\n"
"v_exp_f32 v138, v138\n"
"v_exp_f32 v139, v139\n"
"v_exp_f32 v140, v140\n"
"v_exp_f32 v141, v141\n"
"v_smfmac_f32_32x32x32_bf16 v[224:239], v[76:79], v[128:135], v108\n"
"global_load_dwordx2 v[54:55], v4, s[36:37] offset:-1536 sc1\n"
"v_exp_f32 v142, v142\n"
"v_exp_f32 v143, v143\n"
"v_exp_f32 v144, v144\n"
"v_exp_f32 v145, v145\n"
"v_smfmac_f32_32x32x32_bf16 v[224:239], v[80:83], v[136:143], v108\n"
"global_load_dwordx4 v[72:75], v7, s[36:37] offset:0 sc1\n"
"v_exp_f32 v146, v146\n"
"v_exp_f32 v147, v147\n"
"v_exp_f32 v148, v148\n"
"v_exp_f32 v149, v149\n"
"v_mfma_f32_32x32x16_bf16 v[160:175], v[56:59], v[36:39], v[160:175]\n"
"v_exp_f32 v150, v150\n"
"v_exp_f32 v151, v151\n"
"v_exp_f32 v152, v152\n"
"v_exp_f32 v153, v153\n"
"v_smfmac_f32_32x32x32_bf16 v[240:255], v[76:79], v[144:151], v108\n"
"s_mov_b64 exec, s[48:49]\n"
"global_load_dwordx4 v[92:95], v5, s[36:37] offset:-1280 sc1\n"
"global_load_dwordx4 v[96:99], v5, s[36:37] offset:-640 sc1\n"
"global_load_dwordx4 v[100:103], v5, s[36:37] offset:1280 sc1\n"
"global_load_dwordx4 v[104:107], v5, s[36:37] offset:1920 sc1\n"
"s_mov_b64 exec, -1\n"
"s_add_u32 s36, s36, 5120\n"
"s_addc_u32 s37, s37, 0\n"
"v_exp_f32 v154, v154\n"
"v_exp_f32 v155, v155\n"
"v_exp_f32 v156, v156\n"
"v_exp_f32 v157, v157\n"
"v_mfma_f32_32x32x16_bf16 v[176:191], v[56:59], v[40:43], v[176:191]\n"
"global_load_dwordx4 a[16:19], v9, s[40:41] offset:-2048 sc1\n"
"v_exp_f32 v158, v158\n"
"v_exp_f32 v159, v159\n"
"v_exp_f32 v160, v160\n"
"v_exp_f32 v161, v161\n"
"v_smfmac_f32_32x32x32_bf16 v[240:255], v[80:83], v[152:159], v108\n"
"global_load_dwordx4 a[20:23], v9, s[40:41] offset:-1024 sc1\n"
"v_exp_f32 v162, v162\n"
"v_exp_f32 v163, v163\n"
"v_exp_f32 v164, v164\n"
"v_mfma_f32_32x32x4_2b_bf16 v[128:159], v[66:67], v[64:65], 0 cbsz:1 abid:1\n"
"v_exp_f32 v165, v165\n"
"v_exp_f32 v166, v166\n"
"v_exp_f32 v167, v167\n"
"v_exp_f32 v168, v168\n"
"v_exp_f32 v169, v169\n"
"v_exp_f32 v170, v170\n"
"v_exp_f32 v171, v171\n"
"v_exp_f32 v172, v172\n"
"v_exp_f32 v173, v173\n"
"v_smfmac_f32_32x32x32_bf16 v[192:207], v[76:79], v[160:167], v108\n"
"global_load_dwordx4 a[24:27], v9, s[40:41] offset:0 sc1\n"
"v_exp_f32 v174, v174\n"
"v_exp_f32 v175, v175\n"
"v_exp_f32 v176, v176\n"
"v_exp_f32 v177, v177\n"
"v_smfmac_f32_32x32x32_bf16 v[192:207], v[80:83], v[168:175], v108\n"
"global_load_dwordx4 a[28:31], v9, s[40:41] offset:1024 sc1\n"
"v_exp_f32 v178, v178\n"
"v_exp_f32 v179, v179\n"
"v_exp_f32 v180, v180\n"
"v_exp_f32 v181, v181\n"
"v_mfma_f32_32x32x16_bf16 v[128:143], v[60:63], v[44:47], v[128:143]\n"
"v_exp_f32 v182, v182\n"
"v_exp_f32 v183, v183\n"
"v_exp_f32 v184, v184\n"
"v_exp_f32 v185, v185\n"
"v_smfmac_f32_32x32x32_bf16 v[208:223], v[76:79], v[176:183], v108\n"
"global_load_dwordx4 a[32:35], v9, s[40:41] offset:2048 sc1\n"
"s_add_u32 s40, s40, 5120\n"
"s_addc_u32 s41, s41, 0\n"
"v_exp_f32 v186, v186\n"
"v_exp_f32 v187, v187\n"
"v_exp_f32 v188, v188\n"
"v_exp_f32 v189, v189\n"
"v_mfma_f32_32x32x16_bf16 v[144:159], v[60:63], v[48:51], v[144:159]\n"
"global_load_dwordx4 a[36:39], v122, s[38:39] offset:0 sc1\n"
"v_exp_f32 v190, v190\n"
"v_exp_f32 v191, v191\n"
"v_exp_f32 v128, v128\n"
"v_exp_f32 v129, v129\n"
"v_smfmac_f32_32x32x32_bf16 v[208:223], v[80:83], v[184:191], v108\n"
"global_load_dwordx4 a[40:43], v123, s[38:39] offset:0 sc1\n"
"v_exp_f32 v130, v130\n"
"v_exp_f32 v131, v131\n"
"v_exp_f32 v132, v132\n"
"v_mfma_f32_32x32x4_2b_bf16 v[160:191], v[66:67], v[52:53], 0 cbsz:1 abid:1\n"
"v_exp_f32 v133, v133\n"
"v_exp_f32 v134, v134\n"
"v_exp_f32 v135, v135\n"
"v_exp_f32 v136, v136\n"
"v_exp_f32 v137, v137\n"
"v_exp_f32 v138, v138\n"
"v_exp_f32 v139, v139\n"
"v_exp_f32 v140, v140\n"
"v_exp_f32 v141, v141\n"
"v_smfmac_f32_32x32x32_bf16 v[224:239], v[84:87], v[128:135], v108\n"
"global_load_dwordx4 a[44:47], v124, s[38:39] offset:0 sc1\n"
"v_exp_f32 v142, v142\n"
"v_exp_f32 v143, v143\n"
"v_exp_f32 v144, v144\n"
"v_exp_f32 v145, v145\n"
"v_smfmac_f32_32x32x32_bf16 v[224:239], v[88:91], v[136:143], v108\n"
"global_load_dwordx4 a[48:51], v125, s[38:39] offset:0 sc1\n"
"s_add_u32 s38, s38, 4096\n"
"s_addc_u32 s39, s39, 0\n"
"v_exp_f32 v146, v146\n"
"v_exp_f32 v147, v147\n"
"v_exp_f32 v148, v148\n"
"v_exp_f32 v149, v149\n"
"v_mfma_f32_32x32x16_bf16 v[160:175], v[60:63], v[36:39], v[160:175]\n"
"v_exp_f32 v150, v150\n"
"v_exp_f32 v151, v151\n"
"v_exp_f32 v152, v152\n"
"v_exp_f32 v153, v153\n"
"v_smfmac_f32_32x32x32_bf16 v[240:255], v[84:87], v[144:151], v108\n"
"global_load_dwordx4 a[52:55], v9, s[40:41] offset:-2048 sc1\n"
"v_exp_f32 v154, v154\n"
"v_exp_f32 v155, v155\n"
"v_exp_f32 v156, v156\n"
"v_exp_f32 v157, v157\n"
"v_mfma_f32_32x32x16_bf16 v[176:191], v[60:63], v[40:43], v[176:191]\n"
"global_load_dwordx4 a[56:59], v9, s[40:41] offset:-1024 sc1\n"
"v_exp_f32 v158, v158\n"
"v_exp_f32 v159, v159\n"
"v_exp_f32 v160, v160\n"
"v_exp_f32 v161, v161\n"
"v_smfmac_f32_32x32x32_bf16 v[240:255], v[88:91], v[152:159], v108\n"
"s_waitcnt vmcnt(15)\n"
"global_load_dwordx4 a[60:63], v9, s[40:41] offset:0 sc1\n"
"v_exp_f32 v162, v162\n"
"v_exp_f32 v163, v163\n"
"v_exp_f32 v164, v164\n"
"v_mfma_f32_32x32x4_2b_bf16 v[128:159], v[54:55], v[64:65], 0 cbsz:1 abid:0\n"
"v_exp_f32 v165, v165\n"
"v_exp_f32 v166, v166\n"
"v_exp_f32 v167, v167\n"
"v_exp_f32 v168, v168\n"
"v_exp_f32 v169, v169\n"
"v_exp_f32 v170, v170\n"
"v_exp_f32 v171, v171\n"
"v_exp_f32 v172, v172\n"
"v_exp_f32 v173, v173\n"
"v_smfmac_f32_32x32x32_bf16 v[192:207], v[84:87], v[160:167], v108\n"
"v_cmp_eq_u32 vcc, v109, v33\n"
"v_exp_f32 v174, v174\n"
"v_exp_f32 v175, v175\n"
"v_exp_f32 v176, v176\n"
"v_exp_f32 v177, v177\n"
"v_smfmac_f32_32x32x32_bf16 v[192:207], v[88:91], v[168:175], v108\n"
"s_cbranch_vccnz 22f\n"
"23:\n"
"global_load_dwordx4 a[64:67], v9, s[40:41] offset:1024 sc1\n"
"v_exp_f32 v178, v178\n"
"v_exp_f32 v179, v179\n"
"v_exp_f32 v180, v180\n"
"v_exp_f32 v181, v181\n"
"v_mfma_f32_32x32x16_bf16 v[128:143], v[68:71], v[44:47], v[128:143]\n"
"v_exp_f32 v182, v182\n"
"v_exp_f32 v183, v183\n"
"v_exp_f32 v184, v184\n"
"v_exp_f32 v185, v185\n"
"v_smfmac_f32_32x32x32_bf16 v[208:223], v[84:87], v[176:183], v108\n"
"global_load_dwordx4 a[68:71], v9, s[40:41] offset:2048 sc1\n"
"s_add_u32 s40, s40, 5120\n"
"s_addc_u32 s41, s41, 0\n"
"v_exp_f32 v186, v186\n"
"v_exp_f32 v187, v187\n"
"v_exp_f32 v188, v188\n"
"v_exp_f32 v189, v189\n"
"v_mfma_f32_32x32x16_bf16 v[144:159], v[68:71], v[48:51], v[144:159]\n"
"v_exp_f32 v190, v190\n"
"v_exp_f32 v191, v191\n"
"v_exp_f32 v128, v128\n"
"v_exp_f32 v129, v129\n"
"v_smfmac_f32_32x32x32_bf16 v[208:223], v[88:91], v[184:191], v108\n"
"s_waitcnt vmcnt(14)\n"
"v_exp_f32 v130, v130\n"
"v_exp_f32 v131, v131\n"
"v_exp_f32 v132, v132\n"
"v_mfma_f32_32x32x4_2b_bf16 v[160:191], v[54:55], v[52:53], 0 cbsz:1 abid:0\n"
"v_exp_f32 v133, v133\n"
"v_exp_f32 v134, v134\n"
"v_exp_f32 v135, v135\n"
"v_exp_f32 v136, v136\n"
"v_exp_f32 v137, v137\n"
"v_exp_f32 v138, v138\n"
"v_exp_f32 v139, v139\n"
"v_exp_f32 v140, v140\n"
"v_exp_f32 v141, v141\n"
"v_smfmac_f32_32x32x32_bf16 v[224:239], v[92:95], v[128:135], v108\n"
"global_load_dwordx4 v[76:79], v122, s[38:39] offset:0 sc1\n"
"v_exp_f32 v142, v142\n"
"v_exp_f32 v143, v143\n"
"v_exp_f32 v144, v144\n"
"v_exp_f32 v145, v145\n"
"v_smfmac_f32_32x32x32_bf16 v[224:239], v[96:99], v[136:143], v108\n"
"global_load_dwordx4 v[80:83], v123, s[38:39] offset:0 sc1\n"
"v_exp_f32 v146, v146\n"
"v_exp_f32 v147, v147\n"
"v_exp_f32 v148, v148\n"
"v_exp_f32 v149, v149\n"
"v_mfma_f32_32x32x16_bf16 v[160:175], v[68:71], v[36:39], v[160:175]\n"
"v_exp_f32 v150, v150\n"
"v_exp_f32 v151, v151\n"
"v_exp_f32 v152, v152\n"
"v_exp_f32 v153, v153\n"
"v_smfmac_f32_32x32x32_bf16 v[240:255], v[92:95], v[144:151], v108\n"
"global_load_dwordx4 v[84:87], v124, s[38:39] offset:0 sc1\n"
"v_exp_f32 v154, v154\n"
"v_exp_f32 v155, v155\n"
"v_exp_f32 v156, v156\n"
"v_exp_f32 v157, v157\n"
"v_mfma_f32_32x32x16_bf16 v[176:191], v[68:71], v[40:43], v[176:191]\n"
"global_load_dwordx4 v[88:91], v125, s[38:39] offset:0 sc1\n"
"s_add_u32 s38, s38, 4096\n"
"s_addc_u32 s39, s39, 0\n"
"v_exp_f32 v158, v158\n"
"v_exp_f32 v159, v159\n"
"v_exp_f32 v160, v160\n"
"v_exp_f32 v161, v161\n"
"v_smfmac_f32_32x32x32_bf16 v[240:255], v[96:99], v[152:159], v108\n"
"global_load_dwordx4 v[56:59], v9, s[40:41] offset:-2048 sc1\n"
"v_exp_f32 v162, v162\n"
"v_exp_f32 v163, v163\n"
"v_exp_f32 v164, v164\n"
"v_mfma_f32_32x32x4_2b_bf16 v[128:159], v[54:55], v[64:65], 0 cbsz:1 abid:1\n"
"v_exp_f32 v165, v165\n"
"v_exp_f32 v166, v166\n"
"v_exp_f32 v167, v167\n"
"v_exp_f32 v168, v168\n"
"v_exp_f32 v169, v169\n"
"v_exp_f32 v170, v170\n"
"v_exp_f32 v171, v171\n"
"v_exp_f32 v172, v172\n"
"v_exp_f32 v173, v173\n"
"v_smfmac_f32_32x32x32_bf16 v[192:207], v[92:95], v[160:167], v108\n"
"global_load_dwordx4 v[60:63], v9, s[40:41] offset:-1024 sc1\n"
"v_exp_f32 v174, v174\n"
"v_exp_f32 v175, v175\n"
"v_exp_f32 v176, v176\n"
"v_exp_f32 v177, v177\n"
"v_smfmac_f32_32x32x32_bf16 v[192:207], v[96:99], v[168:175], v108\n"
"v_exp_f32 v178, v178\n"
"v_exp_f32 v179, v179\n"
"v_exp_f32 v180, v180\n"
"v_exp_f32 v181, v181\n"
"v_mfma_f32_32x32x16_bf16 v[128:143], v[72:75], v[44:47], v[128:143]\n"
"v_exp_f32 v182, v182\n"
"v_exp_f32 v183, v183\n"
"v_exp_f32 v184, v184\n"
"v_exp_f32 v185, v185\n"
"v_smfmac_f32_32x32x32_bf16 v[208:223], v[92:95], v[176:183], v108\n"
"v_exp_f32 v186, v186\n"
"v_exp_f32 v187, v187\n"
"v_exp_f32 v188, v188\n"
"v_exp_f32 v189, v189\n"
"v_mfma_f32_32x32x16_bf16 v[144:159], v[72:75], v[48:51], v[144:159]\n"
"v_exp_f32 v190, v190\n"
"v_exp_f32 v191, v191\n"
"v_exp_f32 v128, v128\n"
"v_exp_f32 v129, v129\n"
"v_smfmac_f32_32x32x32_bf16 v[208:223], v[96:99], v[184:191], v108\n"
"global_load_dwordx4 v[44:47], v9, s[40:41] offset:0 sc1\n"
"v_exp_f32 v130, v130\n"
"v_exp_f32 v131, v131\n"
"v_exp_f32 v132, v132\n"
"v_mfma_f32_32x32x4_2b_bf16 v[160:191], v[54:55], v[52:53], 0 cbsz:1 abid:1\n"
"v_exp_f32 v133, v133\n"
"v_exp_f32 v134, v134\n"
"v_exp_f32 v135, v135\n"
"v_exp_f32 v136, v136\n"
"v_exp_f32 v137, v137\n"
"v_exp_f32 v138, v138\n"
"v_exp_f32 v139, v139\n"
"v_exp_f32 v140, v140\n"
"v_exp_f32 v141, v141\n"
"v_smfmac_f32_32x32x32_bf16 v[224:239], v[100:103], v[128:135], v108\n"
"global_load_dwordx4 v[48:51], v9, s[40:41] offset:1024 sc1\n"
"v_exp_f32 v142, v142\n"
"v_exp_f32 v143, v143\n"
"v_exp_f32 v144, v144\n"
"v_exp_f32 v145, v145\n"
"v_smfmac_f32_32x32x32_bf16 v[224:239], v[104:107], v[136:143], v108\n"
"global_load_dwordx4 v[64:67], v9, s[40:41] offset:2048 sc1\n"
"s_add_u32 s40, s40, 5120\n"
"s_addc_u32 s41, s41, 0\n"
"v_exp_f32 v146, v146\n"
"v_exp_f32 v147, v147\n"
"v_exp_f32 v148, v148\n"
"v_exp_f32 v149, v149\n"
"v_mfma_f32_32x32x16_bf16 v[160:175], v[72:75], v[36:39], v[160:175]\n"
"v_exp_f32 v150, v150\n"
"v_exp_f32 v151, v151\n"
"v_exp_f32 v152, v152\n"
"v_exp_f32 v153, v153\n"
"v_smfmac_f32_32x32x32_bf16 v[240:255], v[100:103], v[144:151], v108\n"
"v_exp_f32 v154, v154\n"
"v_exp_f32 v155, v155\n"
"v_exp_f32 v156, v156\n"
"v_exp_f32 v157, v157\n"
"v_mfma_f32_32x32x16_bf16 v[176:191], v[72:75], v[40:43], v[176:191]\n"
"v_exp_f32 v158, v158\n"
"v_exp_f32 v159, v159\n"
"v_exp_f32 v160, v160\n"
"ds_write_b128 v20, v[224:227] offset:0\n"
"v_exp_f32 v161, v161\n"
"v_smfmac_f32_32x32x32_bf16 v[240:255], v[104:107], v[152:159], v108\n"
"v_exp_f32 v162, v162\n"
"v_exp_f32 v163, v163\n"
"ds_write_b128 v20, v[228:231] offset:1024\n"
"v_exp_f32 v164, v164\n"
"v_exp_f32 v165, v165\n"
"v_exp_f32 v166, v166\n"
"ds_write_b128 v20, v[232:235] offset:2048\n"
"v_exp_f32 v167, v167\n"
"v_exp_f32 v168, v168\n"
"v_exp_f32 v169, v169\n"
"v_exp_f32 v170, v170\n"
"v_exp_f32 v171, v171\n"
"ds_write_b128 v20, v[240:243] offset:3072\n"
"v_exp_f32 v172, v172\n"
"v_exp_f32 v173, v173\n"
"global_load_dwordx4 a[172:175], v9, s[40:41] offset:-2048 sc1\n"
"v_smfmac_f32_32x32x32_bf16 v[192:207], v[100:103], v[160:167], v108\n"
"v_exp_f32 v174, v174\n"
"ds_write_b128 v20, v[244:247] offset:4096\n"
"v_exp_f32 v175, v175\n"
"v_exp_f32 v176, v176\n"
"v_exp_f32 v177, v177\n"
"ds_write_b128 v20, v[248:251] offset:5120\n"
"v_smfmac_f32_32x32x32_bf16 v[192:207], v[104:107], v[168:175], v108\n"
"v_exp_f32 v178, v178\n"
"v_exp_f32 v179, v179\n"
"v_exp_f32 v180, v180\n"
"v_exp_f32 v181, v181\n"
"s_waitcnt lgkmcnt(0)\n"
"s_barrier\n"
"ds_read_b128 v[136:139], v21 offset:0\n"
"v_exp_f32 v182, v182\n"
"ds_read_b128 v[140:143], v21 offset:1024\n"
"global_load_dwordx4 v[224:227], v9, s[40:41] offset:-1024 sc1\n"
"v_exp_f32 v183, v183\n"
"ds_read_b128 v[144:147], v21 offset:2048\n"
"v_exp_f32 v184, v184\n"
"ds_read_b128 v[148:151], v21 offset:3072\n"
"v_exp_f32 v185, v185\n"
"ds_read_b128 v[152:155], v21 offset:4096\n"
"v_smfmac_f32_32x32x32_bf16 v[208:223], v[100:103], v[176:183], v108\n"
"v_exp_f32 v186, v186\n"
"ds_read_b128 v[156:159], v21 offset:5120\n"
"v_exp_f32 v187, v187\n"
"v_exp_f32 v188, v188\n"
"v_exp_f32 v189, v189\n"
"v_exp_f32 v190, v190\n"
"v_exp_f32_e64 v191, v191\n"
"s_nop 1\n"
"v_smfmac_f32_32x32x32_bf16 v[208:223], v[104:107], v[184:191], v108\n"
"global_load_dwordx4 v[228:231], v9, s[40:41] offset:0 sc1\n"
"s_waitcnt lgkmcnt(3)\n"
"v_pk_add_f32 v[200:201], v[200:201], v[144:145]\n"
"v_pk_add_f32 v[202:203], v[202:203], v[146:147]\n"
"v_pk_add_f32 v[192:193], v[192:193], v[136:137]\n"
"v_pk_add_f32 v[194:195], v[194:195], v[138:139]\n"
"v_pk_add_f32 v[196:197], v[196:197], v[140:141]\n"
"v_pk_add_f32 v[198:199], v[198:199], v[142:143]\n"
"global_load_dwordx4 v[232:235], v9, s[40:41] offset:1024 sc1\n"
"global_load_dwordx4 v[236:239], v9, s[40:41] offset:2048 sc1\n"
"s_waitcnt lgkmcnt(0)\n"
"s_nop 0\n"
"v_pk_add_f32 v[216:217], v[216:217], v[156:157]\n"
"v_pk_add_f32 v[218:219], v[218:219], v[158:159]\n"
"v_pk_add_f32 v[208:209], v[208:209], v[148:149]\n"
"v_pk_add_f32 v[210:211], v[210:211], v[150:151]\n"
"v_pk_add_f32 v[212:213], v[212:213], v[152:153]\n"
"v_pk_add_f32 v[214:215], v[214:215], v[154:155]\n"
"v_permlane32_swap_b32 v200, v216\n"
"v_permlane32_swap_b32 v201, v217\n"
"v_permlane32_swap_b32 v202, v218\n"
"v_permlane32_swap_b32 v203, v219\n"
"v_rcp_f32_e64 v216, v216\n"
"s_nop 0\n"
"v_mul_f32 v162, 0x3f317218, v216\n"
"v_mul_f32 v164, 0x3f317218, v216\n"
"v_mul_f32 v216, 0x3f317218, v216\n"
"s_mov_b32 s36, 0x3fb8aa3b\n"
"v_pk_mul_f32 v[200:201], v[200:201], v[216:217] op_sel_hi:[1,0]\n"
"v_pk_mul_f32 v[202:203], v[202:203], v[216:217] op_sel_hi:[1,0]\n"
"v_permlane32_swap_b32 v162, v164\n"
"v_cvt_pk_bf16_f32 v184, v200, v201\n"
"v_cvt_pk_bf16_f32 v185, v202, v203\n"
"global_store_dwordx2 v6, v[184:185], s[20:21] offset:2048 sc1\n"
"v_pk_mul_f32 v[192:193], v[192:193], v[162:163] op_sel_hi:[1,0]\n"
"v_pk_mul_f32 v[194:195], v[194:195], v[162:163] op_sel_hi:[1,0]\n"
"v_pk_mul_f32 v[196:197], v[196:197], v[162:163] op_sel_hi:[1,0]\n"
"v_pk_mul_f32 v[198:199], v[198:199], v[162:163] op_sel_hi:[1,0]\n"
"v_cvt_pk_bf16_f32 v204, v192, v193\n"
"v_cvt_pk_bf16_f32 v205, v194, v195\n"
"v_cvt_pk_bf16_f32 v206, v196, v197\n"
"v_cvt_pk_bf16_f32 v207, v198, v199\n"
"global_store_dwordx4 v11, v[204:207], s[20:21] offset:0 sc1\n"
"v_pk_mul_f32 v[208:209], v[208:209], v[164:165] op_sel_hi:[1,0]\n"
"v_pk_mul_f32 v[210:211], v[210:211], v[164:165] op_sel_hi:[1,0]\n"
"v_pk_mul_f32 v[212:213], v[212:213], v[164:165] op_sel_hi:[1,0]\n"
"v_pk_mul_f32 v[214:215], v[214:215], v[164:165] op_sel_hi:[1,0]\n"
"v_cvt_pk_bf16_f32 v220, v208, v209\n"
"v_cvt_pk_bf16_f32 v221, v210, v211\n"
"v_cvt_pk_bf16_f32 v222, v212, v213\n"
"v_cvt_pk_bf16_f32 v223, v214, v215\n"
"global_store_dwordx4 v11, v[220:223], s[20:21] offset:1024 sc1\n"
"v_mfma_f32_16x16x32_bf16 a[236:239], a[88:91], a[84:87], a[236:239]\n"
"global_load_dwordx4 v[240:243], v125, s[38:39] offset:0 sc1\n"
"v_mfma_f32_16x16x32_bf16 a[240:243], a[92:95], a[84:87], a[240:243]\n"
"v_add_f32 v128, v192, v194\n"
"v_add_f32 v129, v193, v195\n"
"v_mfma_f32_16x16x32_bf16 a[244:247], a[96:99], a[84:87], a[244:247]\n"
"v_add_f32 v128, v128, v196\n"
"v_add_f32 v129, v129, v197\n"
"v_mfma_f32_16x16x32_bf16 a[248:251], a[100:103], a[84:87], a[248:251]\n"
"v_add_f32 v128, v128, v198\n"
"v_add_f32 v129, v129, v199\n"
"v_mfma_f32_16x16x32_bf16 a[252:255], a[104:107], a[84:87], a[252:255]\n"
"v_add_f32 v128, v128, v200\n"
"v_add_f32 v129, v129, v201\n"
"v_mfma_f32_16x16x32_bf16 a[236:239], a[124:127], a[120:123], a[236:239]\n"
"global_load_dwordx4 v[244:247], v124, s[38:39] offset:0 sc1\n"
"v_mfma_f32_16x16x32_bf16 a[240:243], a[128:131], a[120:123], a[240:243]\n"
"v_add_f32 v128, v128, v202\n"
"v_add_f32 v129, v129, v203\n"
"v_mfma_f32_16x16x32_bf16 a[244:247], a[132:135], a[120:123], a[244:247]\n"
"v_add_f32 v128, v128, v208\n"
"v_add_f32 v129, v129, v209\n"
"v_mfma_f32_16x16x32_bf16 a[248:251], a[136:139], a[120:123], a[248:251]\n"
"v_add_f32 v128, v128, v210\n"
"v_add_f32 v129, v129, v211\n"
"v_mfma_f32_16x16x32_bf16 a[252:255], a[140:143], a[120:123], a[252:255]\n"
"v_add_f32 v128, v128, v212\n"
"v_add_f32 v129, v129, v213\n"
"s_waitcnt vmcnt(28)\n"
"v_mfma_f32_16x16x32_bf16 a[236:239], a[16:19], a[12:15], a[236:239]\n"
"global_load_dwordx4 v[248:251], v123, s[38:39] offset:0 sc1\n"
"v_mfma_f32_16x16x32_bf16 a[240:243], a[20:23], a[12:15], a[240:243]\n"
"v_add_f32 v128, v128, v214\n"
"v_add_f32 v129, v129, v215\n"
"v_mfma_f32_16x16x32_bf16 a[244:247], a[24:27], a[12:15], a[244:247]\n"
"v_add_f32 v128, v128, v129\n"
"v_fma_f32 v128, 0, v128, v216\n"
"v_mfma_f32_16x16x32_bf16 a[248:251], a[28:31], a[12:15], a[248:251]\n"
"v_subrev_u32 v128, 0x2b800000, v128\n"
"v_cmp_lt_u32 vcc, 0x28000000, v128\n"
"v_mfma_f32_16x16x32_bf16 a[252:255], a[32:35], a[12:15], a[252:255]\n"
"global_load_dwordx4 v[252:255], v122, s[38:39] offset:0 sc1\n"
"s_waitcnt vmcnt(21)\n"
"v_mfma_f32_16x16x32_bf16 a[236:239], a[52:55], a[48:51], a[236:239]\n"
"s_nop 0\n"
"s_nop 0\n"
"v_mfma_f32_16x16x32_bf16 a[240:243], a[56:59], a[48:51], a[240:243]\n"
"s_nop 0\n"
"s_nop 0\n"
"s_cbranch_vccnz 30f\n"
"31:\n"
"v_mfma_f32_16x16x32_bf16 a[244:247], a[60:63], a[48:51], a[244:247]\n"
"ds_read_b128 v[92:95], v20 offset:6144\n"
"v_accvgpr_read_b32 v188, a176\n"
"v_mfma_f32_16x16x32_bf16 a[248:251], a[64:67], a[48:51], a[248:251]\n"
"ds_read_b128 v[96:99], v20 offset:7168\n"
"v_accvgpr_read_b32 v189, a177\n"
"v_mfma_f32_16x16x32_bf16 a[252:255], a[68:71], a[48:51], a[252:255]\n"
"global_load_dwordx4 v[208:211], v26, s[24:25] offset:0\n"
"v_mfma_f32_16x16x32_bf16 a[216:219], a[88:91], a[80:83], a[216:219]\n"
"ds_read_b128 v[100:103], v20 offset:8192\n"
"v_accvgpr_read_b32 v190, a178\n"
"v_mfma_f32_16x16x32_bf16 a[220:223], a[92:95], a[80:83], a[220:223]\n"
"ds_read_b128 v[104:107], v20 offset:9216\n"
"v_accvgpr_read_b32 v191, a179\n"
"v_mfma_f32_16x16x32_bf16 a[224:227], a[96:99], a[80:83], a[224:227]\n"
"ds_read_b128 v[68:71], v20 offset:10240\n"
"v_accvgpr_read_b32 v192, a180\n"
"v_mfma_f32_16x16x32_bf16 a[228:231], a[100:103], a[80:83], a[228:231]\n"
"global_load_dwordx4 v[212:215], v26, s[24:25] offset:64\n"
"v_mfma_f32_16x16x32_bf16 a[232:235], a[104:107], a[80:83], a[232:235]\n"
"ds_read_b128 v[72:75], v20 offset:11264\n"
"v_accvgpr_read_b32 v193, a181\n"
"v_mfma_f32_16x16x32_bf16 a[216:219], a[124:127], a[116:119], a[216:219]\n"
"ds_read_b128 v[36:39], v20 offset:12288\n"
"v_accvgpr_read_b32 v194, a182\n"
"v_mfma_f32_16x16x32_bf16 a[220:223], a[128:131], a[116:119], a[220:223]\n"
"ds_read_b128 v[40:43], v20 offset:13312\n"
"v_accvgpr_read_b32 v195, a183\n"
"v_mfma_f32_16x16x32_bf16 a[224:227], a[132:135], a[116:119], a[224:227]\n"
"ds_read_b128 v[52:55], v20 offset:14336\n"
"v_accvgpr_read_b32 v196, a184\n"
"v_mfma_f32_16x16x32_bf16 a[228:231], a[136:139], a[116:119], a[228:231]\n"
"global_load_dwordx2 v[216:217], v13, s[24:25] offset:0\n"
"v_mfma_f32_16x16x32_bf16 a[232:235], a[140:143], a[116:119], a[232:235]\n"
"ds_read_b128 a[84:87], v29 offset:0\n"
"v_accvgpr_read_b32 v197, a185\n"
"s_waitcnt vmcnt(15)\n"
"v_mfma_f32_16x16x32_bf16 a[236:239], v[56:59], v[88:91], a[236:239]\n"
"ds_read_b128 a[120:123], v29 offset:1024\n"
"v_accvgpr_read_b32 v198, a186\n"
"v_mfma_f32_16x16x32_bf16 a[240:243], v[60:63], v[88:91], a[240:243]\n"
"ds_read_b128 a[12:15], v29 offset:2048\n"
"v_accvgpr_read_b32 v199, a187\n"
"v_mfma_f32_16x16x32_bf16 a[244:247], v[44:47], v[88:91], a[244:247]\n"
"ds_read_b128 a[48:51], v29 offset:3072\n"
"v_accvgpr_read_b32 v200, a188\n"
"v_mfma_f32_16x16x32_bf16 a[248:251], v[48:51], v[88:91], a[248:251]\n"
"v_accvgpr_read_b32 v201, a189\n"
"v_accvgpr_read_b32 v202, a190\n"
"v_mfma_f32_16x16x32_bf16 a[252:255], v[64:67], v[88:91], a[252:255]\n"
"v_accvgpr_read_b32 v203, a191\n"
"v_accvgpr_read_b32 v204, a192\n"
"s_waitcnt lgkmcnt(4)\n"
"v_mfma_f32_16x16x32_bf16 a[236:239], v[68:71], v[104:107], a[236:239]\n"
"s_waitcnt lgkmcnt(0)\n"
"s_barrier\n"
"ds_read_b128 a[176:179], v29 offset:4096\n"
"v_accvgpr_read_b32 v205, a193\n"
"v_mfma_f32_16x16x32_bf16 a[240:243], v[72:75], v[104:107], a[240:243]\n"
"ds_read_b128 a[80:83], v29 offset:5120\n"
"v_accvgpr_read_b32 v206, a194\n"
"v_mfma_f32_16x16x32_bf16 a[244:247], v[36:39], v[104:107], a[244:247]\n"
"ds_read_b128 a[180:183], v29 offset:6144\n"
"v_accvgpr_read_b32 v207, a195\n"
"v_mfma_f32_16x16x32_bf16 a[248:251], v[40:43], v[104:107], a[248:251]\n"
"ds_read_b128 a[116:119], v29 offset:7168\n"
"v_lshlrev_b32 v126, 16, v112\n"
"v_mfma_f32_16x16x32_bf16 a[252:255], v[52:55], v[104:107], a[252:255]\n"
"ds_read_b128 a[184:187], v29 offset:8192\n"
"v_and_b32 v127, 0xffff0000, v112\n"
"v_mfma_f32_16x16x32_bf16 a[216:219], a[16:19], a[8:11], a[216:219]\n"
"ds_read_b128 v[88:91], v29 offset:9216\n"
"v_fmac_f32 v188, s36, v126\n"
"v_mfma_f32_16x16x32_bf16 a[220:223], a[20:23], a[8:11], a[220:223]\n"
"ds_read_b128 a[188:191], v29 offset:10240\n"
"v_fmac_f32 v189, s36, v127\n"
"v_mfma_f32_16x16x32_bf16 a[224:227], a[24:27], a[8:11], a[224:227]\n"
"ds_read_b128 a[192:195], v29 offset:11264\n"
"v_lshlrev_b32 v126, 16, v113\n"
"v_mfma_f32_16x16x32_bf16 a[228:231], a[28:31], a[8:11], a[228:231]\n"
"ds_read_b128 v[104:107], v29 offset:12288\n"
"v_and_b32 v127, 0xffff0000, v113\n"
"v_mfma_f32_16x16x32_bf16 a[232:235], a[32:35], a[8:11], a[232:235]\n"
"ds_read_b128 a[8:11], v29 offset:13312\n"
"v_fmac_f32 v190, s36, v126\n"
"s_waitcnt vmcnt(6)\n"
"v_mfma_f32_16x16x32_bf16 a[236:239], a[172:175], v[240:243], a[236:239]\n"
"v_fmac_f32 v191, s36, v127\n"
"v_lshlrev_b32 v126, 16, v114\n"
"v_mfma_f32_16x16x32_bf16 a[240:243], v[224:227], v[240:243], a[240:243]\n"
"v_and_b32 v127, 0xffff0000, v114\n"
"v_fmac_f32 v192, s36, v126\n"
"v_mfma_f32_16x16x32_bf16 a[244:247], v[228:231], v[240:243], a[244:247]\n"
"v_fmac_f32 v193, s36, v127\n"
"v_lshlrev_b32 v126, 16, v115\n"
"v_mfma_f32_16x16x32_bf16 a[248:251], v[232:235], v[240:243], a[248:251]\n"
"v_and_b32 v127, 0xffff0000, v115\n"
"v_fmac_f32 v194, s36, v126\n"
"v_mfma_f32_16x16x32_bf16 a[252:255], v[236:239], v[240:243], a[252:255]\n"
"ds_read_b128 v[240:243], v29 offset:14336\n"
"v_fmac_f32 v195, s36, v127\n"
"v_mfma_f32_16x16x32_bf16 a[216:219], a[52:55], a[44:47], a[216:219]\n"
"ds_write_b128 v19, a[236:239] offset:10240\n"
"v_lshlrev_b32 v126, 16, v116\n"
"v_mfma_f32_16x16x32_bf16 a[220:223], a[56:59], a[44:47], a[220:223]\n"
"v_and_b32 v127, 0xffff0000, v116\n"
"v_fmac_f32 v196, s36, v126\n"
"v_mfma_f32_16x16x32_bf16 a[224:227], a[60:63], a[44:47], a[224:227]\n"
"v_fmac_f32 v197, s36, v127\n"
"v_lshlrev_b32 v126, 16, v117\n"
"v_mfma_f32_16x16x32_bf16 a[228:231], a[64:67], a[44:47], a[228:231]\n"
"ds_write_b128 v19, a[240:243] offset:11264\n"
"v_and_b32 v127, 0xffff0000, v117\n"
"v_mfma_f32_16x16x32_bf16 a[232:235], a[68:71], a[44:47], a[232:235]\n"
"v_fmac_f32 v198, s36, v126\n"
"v_fmac_f32 v199, s36, v127\n"
"v_mfma_f32_16x16x32_bf16 a[216:219], v[68:71], v[100:103], a[216:219]\n"
"v_lshlrev_b32 v126, 16, v118\n"
"v_and_b32 v127, 0xffff0000, v118\n"
"v_mfma_f32_16x16x32_bf16 a[220:223], v[72:75], v[100:103], a[220:223]\n"
"ds_write_b128 v19, a[244:247] offset:12288\n"
"v_fmac_f32 v200, s36, v126\n"
"v_mfma_f32_16x16x32_bf16 a[224:227], v[36:39], v[100:103], a[224:227]\n"
"v_fmac_f32 v201, s36, v127\n"
"v_lshlrev_b32 v126, 16, v119\n"
"v_mfma_f32_16x16x32_bf16 a[228:231], v[40:43], v[100:103], a[228:231]\n"
"s_cmp_eq_u32 s30, 0\n"
"s_cselect_b64 exec, 15, 3\n"
"global_atomic_add v15, v110, s[66:67]\n"
"s_mov_b64 exec, -1\n"
"v_mfma_f32_16x16x32_bf16 a[232:235], v[52:55], v[100:103], a[232:235]\n"
"v_and_b32 v127, 0xffff0000, v119\n"
"v_fmac_f32 v202, s36, v126\n"
"v_mfma_f32_16x16x32_bf16 a[216:219], v[56:59], v[84:87], a[216:219]\n"
"ds_write_b128 v19, a[248:251] offset:13312\n"
"v_fmac_f32 v203, s36, v127\n"
"v_mfma_f32_16x16x32_bf16 a[220:223], v[60:63], v[84:87], a[220:223]\n"
"v_lshlrev_b32 v126, 16, v120\n"
"v_and_b32 v127, 0xffff0000, v120\n"
"v_mfma_f32_16x16x32_bf16 a[224:227], v[44:47], v[84:87], a[224:227]\n"
"v_fmac_f32 v204, s36, v126\n"
"v_fmac_f32 v205, s36, v127\n"
"v_mfma_f32_16x16x32_bf16 a[228:231], v[48:51], v[84:87], a[228:231]\n"
"ds_write_b128 v19, a[252:255] offset:14336\n"
"v_lshlrev_b32 v126, 16, v121\n"
"v_mfma_f32_16x16x32_bf16 a[232:235], v[64:67], v[84:87], a[232:235]\n"
"v_and_b32 v127, 0xffff0000, v121\n"
"v_fmac_f32 v206, s36, v126\n"
"s_waitcnt vmcnt(6)\n"
"v_mfma_f32_16x16x32_bf16 a[216:219], a[172:175], v[244:247], a[216:219]\n"
"v_fmac_f32 v207, s36, v127\n"
"v_mfma_f32_16x16x32_bf16 a[220:223], v[224:227], v[244:247], a[220:223]\n"
"v_mfma_f32_16x16x32_bf16 a[224:227], v[228:231], v[244:247], a[224:227]\n"
"v_mfma_f32_16x16x32_bf16 a[228:231], v[232:235], v[244:247], a[228:231]\n"
"v_mfma_f32_16x16x32_bf16 a[232:235], v[236:239], v[244:247], a[232:235]\n"
"s_waitcnt lgkmcnt(0)\n"
"s_barrier\n"
"ds_read_b128 v[168:171], v20 offset:10240\n"
"v_mfma_f32_16x16x32_bf16 a[196:199], a[88:91], a[76:79], a[196:199]\n"
"ds_read_b128 v[172:175], v20 offset:11264\n"
"v_mfma_f32_16x16x32_bf16 a[200:203], a[92:95], a[76:79], a[200:203]\n"
"ds_read_b128 v[176:179], v20 offset:12288\n"
"v_mfma_f32_16x16x32_bf16 a[204:207], a[96:99], a[76:79], a[204:207]\n"
"ds_read_b128 v[180:183], v20 offset:13312\n"
"v_mfma_f32_16x16x32_bf16 a[208:211], a[100:103], a[76:79], a[208:211]\n"
"ds_read_b128 v[184:187], v20 offset:14336\n"
"s_waitcnt lgkmcnt(4)\n"
"v_add_f32 v188, v188, v168\n"
"v_mfma_f32_16x16x32_bf16 a[212:215], a[104:107], a[76:79], a[212:215]\n"
"ds_write_b128 v21, a[216:219] offset:5120\n"
"v_add_f32 v189, v189, v169\n"
"v_mfma_f32_16x16x32_bf16 a[196:199], a[124:127], a[112:115], a[196:199]\n"
"v_add_f32 v190, v190, v170\n"
"v_add_f32 v191, v191, v171\n"
"v_mfma_f32_16x16x32_bf16 a[200:203], a[128:131], a[112:115], a[200:203]\n"
"s_waitcnt lgkmcnt(4)\n"
"v_add_f32 v192, v192, v172\n"
"v_add_f32 v193, v193, v173\n"
"v_mfma_f32_16x16x32_bf16 a[204:207], a[132:135], a[112:115], a[204:207]\n"
"ds_write_b128 v21, a[220:223] offset:6144\n"
"v_add_f32 v194, v194, v174\n"
"v_mfma_f32_16x16x32_bf16 a[208:211], a[136:139], a[112:115], a[208:211]\n"
"v_add_f32 v195, v195, v175\n"
"s_waitcnt lgkmcnt(4)\n"
"v_add_f32 v196, v196, v176\n"
"v_mfma_f32_16x16x32_bf16 a[212:215], a[140:143], a[112:115], a[212:215]\n"
"v_add_f32 v197, v197, v177\n"
"v_add_f32 v198, v198, v178\n"
"v_mfma_f32_16x16x32_bf16 a[196:199], a[16:19], a[4:7], a[196:199]\n"
"ds_write_b128 v21, a[224:227] offset:7168\n"
"v_add_f32 v199, v199, v179\n"
"v_mfma_f32_16x16x32_bf16 a[200:203], a[20:23], a[4:7], a[200:203]\n"
"s_waitcnt lgkmcnt(4)\n"
"v_add_f32 v200, v200, v180\n"
"v_add_f32 v201, v201, v181\n"
"v_mfma_f32_16x16x32_bf16 a[204:207], a[24:27], a[4:7], a[204:207]\n"
"v_add_f32 v202, v202, v182\n"
"v_add_f32 v203, v203, v183\n"
"v_mfma_f32_16x16x32_bf16 a[208:211], a[28:31], a[4:7], a[208:211]\n"
"s_waitcnt lgkmcnt(3)\n"
"v_add_f32 v204, v204, v184\n"
"v_add_f32 v205, v205, v185\n"
"v_mfma_f32_16x16x32_bf16 a[212:215], a[32:35], a[4:7], a[212:215]\n"
"ds_write_b128 v21, a[228:231] offset:8192\n"
"v_add_f32 v206, v206, v186\n"
"v_mfma_f32_16x16x32_bf16 a[196:199], a[52:55], a[40:43], a[196:199]\n"
"v_add_f32 v207, v207, v187\n"
"v_mfma_f32_16x16x32_bf16 a[200:203], a[56:59], a[40:43], a[200:203]\n"
"v_mfma_f32_16x16x32_bf16 a[204:207], a[60:63], a[40:43], a[204:207]\n"
"ds_write_b128 v21, a[232:235] offset:9216\n"
"v_mfma_f32_16x16x32_bf16 a[208:211], a[64:67], a[40:43], a[208:211]\n"
"v_mfma_f32_16x16x32_bf16 a[212:215], a[68:71], a[40:43], a[212:215]\n"
"v_mfma_f32_16x16x32_bf16 a[196:199], v[68:71], v[96:99], a[196:199]\n"
"v_mfma_f32_16x16x32_bf16 a[200:203], v[72:75], v[96:99], a[200:203]\n"
"v_mfma_f32_16x16x32_bf16 a[204:207], v[36:39], v[96:99], a[204:207]\n"
"s_mov_b32 exec_hi, 0\n"
"global_load_dword v109, v14, s[66:67] offset:0 sc1\n"
"s_mov_b32 exec_hi, -1\n"
"v_mfma_f32_16x16x32_bf16 a[208:211], v[40:43], v[96:99], a[208:211]\n"
"s_waitcnt lgkmcnt(0)\n"
"s_barrier\n"
"ds_read_b128 v[148:151], v20 offset:5120\n"
"v_mfma_f32_16x16x32_bf16 a[212:215], v[52:55], v[96:99], a[212:215]\n"
"ds_read_b128 v[152:155], v20 offset:6144\n"
"v_mfma_f32_16x16x32_bf16 a[196:199], v[56:59], v[80:83], a[196:199]\n"
"ds_read_b128 v[156:159], v20 offset:7168\n"
"v_mfma_f32_16x16x32_bf16 a[200:203], v[60:63], v[80:83], a[200:203]\n"
"ds_read_b128 v[160:163], v20 offset:8192\n"
"v_mfma_f32_16x16x32_bf16 a[204:207], v[44:47], v[80:83], a[204:207]\n"
"ds_read_b128 v[164:167], v20 offset:9216\n"
"s_waitcnt lgkmcnt(4)\n"
"v_add_f32 v188, v188, v148\n"
"v_mfma_f32_16x16x32_bf16 a[208:211], v[48:51], v[80:83], a[208:211]\n"
"ds_read_b128 a[236:239], v29 offset:15360\n"
"v_add_f32 v189, v189, v149\n"
"v_mfma_f32_16x16x32_bf16 a[212:215], v[64:67], v[80:83], a[212:215]\n"
"ds_read_b128 a[240:243], v29 offset:16384\n"
"v_add_f32 v190, v190, v150\n"
"s_waitcnt vmcnt(6)\n"
"v_mfma_f32_16x16x32_bf16 a[196:199], a[172:175], v[248:251], a[196:199]\n"
"ds_read_b128 a[44:47], v29 offset:17408\n"
"v_add_f32 v191, v191, v151\n"
"v_mfma_f32_16x16x32_bf16 a[200:203], v[224:227], v[248:251], a[200:203]\n"
"ds_read_b128 a[244:247], v29 offset:18432\n"
"s_waitcnt lgkmcnt(7)\n"
"v_add_f32 v192, v192, v152\n"
"v_mfma_f32_16x16x32_bf16 a[204:207], v[228:231], v[248:251], a[204:207]\n"
"ds_read_b128 v[100:103], v29 offset:19456\n"
"v_add_f32 v193, v193, v153\n"
"v_mfma_f32_16x16x32_bf16 a[208:211], v[232:235], v[248:251], a[208:211]\n"
"ds_read_b128 a[248:251], v29 offset:20480\n"
"v_add_f32 v194, v194, v154\n"
"v_mfma_f32_16x16x32_bf16 a[212:215], v[236:239], v[248:251], a[212:215]\n"
"ds_read_b128 a[252:255], v29 offset:21504\n"
"v_add_f32 v195, v195, v155\n"
"v_mfma_f32_16x16x32_bf16 a[196:199], a[152:155], a[148:151], a[196:199]\n"
"ds_read_b128 v[84:87], v29 offset:22528\n"
"s_waitcnt lgkmcnt(10)\n"
"v_add_f32 v196, v196, v156\n"
"v_mfma_f32_16x16x32_bf16 a[200:203], a[156:159], a[148:151], a[200:203]\n"
"ds_read_b128 v[244:247], v29 offset:23552\n"
"v_add_f32 v197, v197, v157\n"
"v_mfma_f32_16x16x32_bf16 a[204:207], a[160:163], a[148:151], a[204:207]\n"
"ds_read_b128 a[76:79], v29 offset:24576\n"
"v_add_f32 v198, v198, v158\n"
"v_mfma_f32_16x16x32_bf16 a[208:211], a[164:167], a[148:151], a[208:211]\n"
"ds_read_b128 a[216:219], v29 offset:25600\n"
"v_add_f32 v199, v199, v159\n"
"v_mfma_f32_16x16x32_bf16 a[212:215], a[168:171], a[148:151], a[212:215]\n"
"ds_read_b128 a[220:223], v29 offset:26624\n"
"s_waitcnt lgkmcnt(13)\n"
"v_add_f32 v200, v200, v160\n"
"v_mfma_f32_16x16x32_bf16 v[188:191], a[88:91], a[72:75], v[188:191]\n"
"ds_write_b128 v22, a[196:199] offset:0\n"
"v_add_f32 v201, v201, v161\n"
"v_mfma_f32_16x16x32_bf16 v[192:195], a[92:95], a[72:75], v[192:195]\n"
"v_add_f32 v202, v202, v162\n"
"v_add_f32 v203, v203, v163\n"
"v_mfma_f32_16x16x32_bf16 v[196:199], a[96:99], a[72:75], v[196:199]\n"
"s_waitcnt lgkmcnt(13)\n"
"v_add_f32 v204, v204, v164\n"
"v_add_f32 v205, v205, v165\n"
"v_mfma_f32_16x16x32_bf16 v[200:203], a[100:103], a[72:75], v[200:203]\n"
"ds_write_b128 v22, a[200:203] offset:1024\n"
"s_nop 0\n"
"v_mfma_f32_16x16x32_bf16 v[204:207], a[104:107], a[72:75], v[204:207]\n"
"s_nop 1\n"
"v_mfma_f32_16x16x32_bf16 v[188:191], a[124:127], a[108:111], v[188:191]\n"
"s_nop 1\n"
"v_mfma_f32_16x16x32_bf16 v[192:195], a[128:131], a[108:111], v[192:195]\n"
"s_waitcnt vmcnt(0)\n"
"v_cmp_eq_u32 vcc, v109, v34\n"
"ds_write_b128 v22, a[204:207] offset:2048\n"
"v_mfma_f32_16x16x32_bf16 v[196:199], a[132:135], a[108:111], v[196:199]\n"
"v_add_f32 v206, v206, v166\n"
"v_add_f32 v207, v207, v167\n"
"v_mfma_f32_16x16x32_bf16 v[200:203], a[136:139], a[108:111], v[200:203]\n"
"s_cmp_lg_u32 vcc_lo, 0\n"
"s_cbranch_scc1 40f\n"
"41:\n"
"global_load_dwordx4 a[112:115], v8, s[44:45] offset:-2560 sc1\n"
"v_mfma_f32_16x16x32_bf16 v[204:207], a[140:143], a[108:111], v[204:207]\n"
"v_lshlrev_b32 v148, 16, v208\n"
"v_and_b32 v149, 0xffff0000, v208\n"
"v_mfma_f32_16x16x32_bf16 v[188:191], a[16:19], a[0:3], v[188:191]\n"
"ds_write_b128 v22, a[208:211] offset:3072\n"
"v_lshlrev_b32 v150, 16, v209\n"
"v_mfma_f32_16x16x32_bf16 v[192:195], a[20:23], a[0:3], v[192:195]\n"
"v_and_b32 v151, 0xffff0000, v209\n"
"v_lshlrev_b32 v152, 16, v210\n"
"v_mfma_f32_16x16x32_bf16 v[196:199], a[24:27], a[0:3], v[196:199]\n"
"global_load_dwordx4 a[224:227], v24, s[44:45] offset:-2560 sc1\n"
"v_mfma_f32_16x16x32_bf16 v[200:203], a[28:31], a[0:3], v[200:203]\n"
"ds_write_b128 v22, a[212:215] offset:4096\n"
"v_and_b32 v153, 0xffff0000, v210\n"
"v_mfma_f32_16x16x32_bf16 v[204:207], a[32:35], a[0:3], v[204:207]\n"
"v_lshlrev_b32 v154, 16, v211\n"
"v_and_b32 v155, 0xffff0000, v211\n"
"v_mfma_f32_16x16x32_bf16 v[188:191], a[52:55], a[36:39], v[188:191]\n"
"v_lshlrev_b32 v156, 16, v212\n"
"v_and_b32 v157, 0xffff0000, v212\n"
"v_mfma_f32_16x16x32_bf16 v[192:195], a[56:59], a[36:39], v[192:195]\n"
"v_lshlrev_b32 v158, 16, v213\n"
"v_and_b32 v159, 0xffff0000, v213\n"
"v_mfma_f32_16x16x32_bf16 v[196:199], a[60:63], a[36:39], v[196:199]\n"
"global_load_dwordx4 a[4:7], v8, s[44:45] offset:2560 sc1\n"
"v_mfma_f32_16x16x32_bf16 v[200:203], a[64:67], a[36:39], v[200:203]\n"
"v_lshlrev_b32 v160, 16, v214\n"
"v_and_b32 v161, 0xffff0000, v214\n"
"v_mfma_f32_16x16x32_bf16 v[204:207], a[68:71], a[36:39], v[204:207]\n"
"s_waitcnt lgkmcnt(0)\n"
"s_barrier\n"
"ds_read_b128 v[128:131], v20 offset:0\n"
"v_lshlrev_b32 v162, 16, v215\n"
"v_mfma_f32_16x16x32_bf16 v[188:191], v[68:71], v[92:95], v[188:191]\n"
"ds_read_b128 v[132:135], v20 offset:1024\n"
"v_and_b32 v163, 0xffff0000, v215\n"
"v_mfma_f32_16x16x32_bf16 v[192:195], v[72:75], v[92:95], v[192:195]\n"
"global_load_dwordx4 a[228:231], v24, s[44:45] offset:2560 sc1\n"
"s_add_u32 s44, s22, 10240\n"
"s_addc_u32 s45, s23, 0\n"
"v_mfma_f32_16x16x32_bf16 v[196:199], v[36:39], v[92:95], v[196:199]\n"
"ds_read_b128 v[136:139], v20 offset:2048\n"
"v_lshlrev_b32 v164, 16, v216\n"
"v_mfma_f32_16x16x32_bf16 v[200:203], v[40:43], v[92:95], v[200:203]\n"
"ds_read_b128 v[140:143], v20 offset:3072\n"
"s_waitcnt lgkmcnt(3)\n"
"v_add_f32 v188, v188, v128\n"
"v_mfma_f32_16x16x32_bf16 v[204:207], v[52:55], v[92:95], v[204:207]\n"
"ds_read_b128 v[144:147], v20 offset:4096\n"
"s_waitcnt lgkmcnt(3)\n"
"v_add_f32 v192, v192, v132\n"
"v_mfma_f32_16x16x32_bf16 v[188:191], v[56:59], v[76:79], v[188:191]\n"
"ds_read_b128 a[232:235], v29 offset:27648\n"
"v_and_b32 v165, 0xffff0000, v216\n"
"v_mfma_f32_16x16x32_bf16 v[192:195], v[60:63], v[76:79], v[192:195]\n"
"global_load_dwordx4 a[40:43], v8, s[44:45] offset:-2560 sc1\n"
"v_mfma_f32_16x16x32_bf16 v[196:199], v[44:47], v[76:79], v[196:199]\n"
"ds_read_b128 v[96:99], v29 offset:28672\n"
"v_lshlrev_b32 v166, 16, v217\n"
"v_mfma_f32_16x16x32_bf16 v[200:203], v[48:51], v[76:79], v[200:203]\n"
"ds_read_b128 v[80:83], v29 offset:29696\n"
"v_add_f32 v189, v189, v129\n"
"v_mfma_f32_16x16x32_bf16 v[204:207], v[64:67], v[76:79], v[204:207]\n"
"ds_read_b128 v[248:251], v29 offset:30720\n"
"v_add_f32 v193, v193, v133\n"
"v_mfma_f32_16x16x32_bf16 v[188:191], a[172:175], v[252:255], v[188:191]\n"
"ds_read_b128 a[148:151], v29 offset:31744\n"
"s_waitcnt lgkmcnt(7)\n"
"v_add_f32 v196, v196, v136\n"
"v_mfma_f32_16x16x32_bf16 v[192:195], v[224:227], v[252:255], v[192:195]\n"
"global_load_dwordx4 a[88:91], v24, s[44:45] offset:-2560 sc1\n"
"v_mfma_f32_16x16x32_bf16 v[196:199], v[228:231], v[252:255], v[196:199]\n"
"ds_read_b128 a[196:199], v29 offset:32768\n"
"s_waitcnt lgkmcnt(6)\n"
"v_add_f32 v204, v204, v144\n"
"v_mfma_f32_16x16x32_bf16 v[200:203], v[232:235], v[252:255], v[200:203]\n"
"ds_read_b128 a[92:95], v29 offset:33792\n"
"v_add_f32 v190, v190, v130\n"
"v_mfma_f32_16x16x32_bf16 v[204:207], v[236:239], v[252:255], v[204:207]\n"
"ds_read_b128 a[96:99], v29 offset:34816\n"
"v_add_f32 v194, v194, v134\n"
"v_mfma_f32_16x16x32_bf16 v[188:191], a[152:155], a[144:147], v[188:191]\n"
"global_load_dwordx4 a[100:103], v8, s[44:45] offset:2560 sc1\n"
"v_mfma_f32_16x16x32_bf16 v[192:195], a[156:159], a[144:147], v[192:195]\n"
"ds_read_b128 a[200:203], v29 offset:35840\n"
"v_add_f32 v200, v200, v140\n"
"v_mfma_f32_16x16x32_bf16 v[196:199], a[160:163], a[144:147], v[196:199]\n"
"ds_read_b128 a[72:75], v29 offset:36864\n"
"v_add_f32 v205, v205, v145\n"
"v_mfma_f32_16x16x32_bf16 v[200:203], a[164:167], a[144:147], v[200:203]\n"
"ds_read_b128 a[104:107], v29 offset:37888\n"
"v_add_f32 v191, v191, v131\n"
"v_mfma_f32_16x16x32_bf16 v[204:207], a[168:171], a[144:147], v[204:207]\n"
"ds_read_b128 a[124:127], v29 offset:38912\n"
"v_add_f32 v195, v195, v135\n"
"s_waitcnt vmcnt(6)\n"
"v_mfma_f32_16x16x32_bf16 a[128:131], a[84:87], a[112:115], 0\n"
"global_load_dwordx4 a[140:143], v24, s[44:45] offset:2560 sc1\n"
"s_add_u32 s44, s22, 20480\n"
"s_addc_u32 s45, s23, 0\n"
"v_mfma_f32_16x16x32_bf16 a[204:207], a[120:123], a[112:115], 0\n"
"v_add_f32 v197, v197, v137\n"
"v_add_f32 v198, v198, v138\n"
"v_mfma_f32_16x16x32_bf16 a[132:135], a[12:15], a[112:115], 0\n"
"v_add_f32 v199, v199, v139\n"
"v_add_f32 v201, v201, v141\n"
"v_mfma_f32_16x16x32_bf16 a[136:139], a[48:51], a[112:115], 0\n"
"v_add_f32 v202, v202, v142\n"
"v_add_f32 v203, v203, v143\n"
"v_mfma_f32_16x16x32_bf16 a[108:111], a[176:179], a[112:115], 0\n"
"global_load_dwordx4 a[16:19], v8, s[44:45] offset:-2560 sc1\n"
"s_waitcnt vmcnt(7)\n"
"v_mfma_f32_16x16x32_bf16 v[108:111], a[84:87], a[224:227], 0\n"
"v_add_f32 v206, v206, v146\n"
"v_add_f32 v207, v207, v147\n"
"v_mfma_f32_16x16x32_bf16 v[112:115], a[120:123], a[224:227], 0\n"
"v_exp_f32 v128, -v188\n"
"v_mfma_f32_16x16x32_bf16 v[116:119], a[12:15], a[224:227], 0\n"
"v_exp_f32 v129, -v189\n"
"v_mfma_f32_16x16x32_bf16 v[120:123], a[48:51], a[224:227], 0\n"
"v_exp_f32 v130, -v190\n"
"v_mfma_f32_16x16x32_bf16 v[124:127], a[176:179], a[224:227], 0\n"
"global_load_dwordx4 a[208:211], v8, s[44:45] offset:2560 sc1\n"
"s_add_u32 s44, s22, 30720\n"
"s_addc_u32 s45, s23, 0\n"
"s_waitcnt vmcnt(7)\n"
"v_mfma_f32_16x16x32_bf16 a[128:131], a[80:83], a[4:7], a[128:131]\n"
"v_exp_f32 v131, -v191\n"
"v_mfma_f32_16x16x32_bf16 a[204:207], a[180:183], a[4:7], a[204:207]\n"
"v_exp_f32 v132, -v192\n"
"v_mfma_f32_16x16x32_bf16 a[132:135], a[116:119], a[4:7], a[132:135]\n"
"v_exp_f32 v133, -v193\n"
"v_mfma_f32_16x16x32_bf16 a[136:139], a[184:187], a[4:7], a[136:139]\n"
"v_exp_f32 v134, -v194\n"
"v_mfma_f32_16x16x32_bf16 a[108:111], v[88:91], a[4:7], a[108:111]\n"
"global_load_dwordx4 a[20:23], v8, s[44:45] offset:-2560 sc1\n"
"s_waitcnt vmcnt(7)\n"
"v_mfma_f32_16x16x32_bf16 v[108:111], a[80:83], a[228:231], v[108:111]\n"
"v_exp_f32 v135, -v195\n"
"v_mfma_f32_16x16x32_bf16 v[112:115], a[180:183], a[228:231], v[112:115]\n"
"v_exp_f32 v136, -v196\n"
"v_mfma_f32_16x16x32_bf16 v[116:119], a[116:119], a[228:231], v[116:119]\n"
"v_exp_f32 v137, -v197\n"
"v_mfma_f32_16x16x32_bf16 v[120:123], a[184:187], a[228:231], v[120:123]\n"
"global_load_dwordx4 a[24:27], v8, s[44:45] offset:2560 sc1\n"
"s_add_u32 s44, s22, 0\n"
"s_addc_u32 s45, s23, 0\n"
"v_mfma_f32_16x16x32_bf16 v[124:127], v[88:91], a[228:231], v[124:127]\n"
"v_exp_f32 v138, -v198\n"
"s_waitcnt vmcnt(7)\n"
"v_mfma_f32_16x16x32_bf16 a[128:131], a[188:191], a[40:43], a[128:131]\n"
"v_exp_f32 v139, -v199\n"
"v_mfma_f32_16x16x32_bf16 a[204:207], a[192:195], a[40:43], a[204:207]\n"
"v_exp_f32 v140, -v200\n"
"v_mfma_f32_16x16x32_bf16 a[132:135], v[104:107], a[40:43], a[132:135]\n"
"v_exp_f32 v141, -v201\n"
"v_mfma_f32_16x16x32_bf16 a[136:139], a[8:11], a[40:43], a[136:139]\n"
"global_load_dwordx2 a[28:29], v3, s[44:45] offset:-2560 sc1\n"
"v_mfma_f32_16x16x32_bf16 a[108:111], v[240:243], a[40:43], a[108:111]\n"
"v_exp_f32 v142, -v202\n"
"s_waitcnt vmcnt(7)\n"
"v_mfma_f32_16x16x32_bf16 v[108:111], a[188:191], a[88:91], v[108:111]\n"
"global_load_dwordx2 a[30:31], v3, s[44:45] offset:0 sc1\n"
"s_add_u32 s44, s22, 20480\n"
"s_addc_u32 s45, s23, 0\n"
"v_mfma_f32_16x16x32_bf16 v[112:115], a[192:195], a[88:91], v[112:115]\n"
"v_exp_f32 v143, -v203\n"
"v_mfma_f32_16x16x32_bf16 v[116:119], v[104:107], a[88:91], v[116:119]\n"
"v_exp_f32 v144, -v204\n"
"v_mfma_f32_16x16x32_bf16 v[120:123], a[8:11], a[88:91], v[120:123]\n"
"global_load_dwordx2 a[212:213], v3, s[44:45] offset:-2560 sc1\n"
"v_mfma_f32_16x16x32_bf16 v[124:127], v[240:243], a[88:91], v[124:127]\n"
"v_exp_f32 v145, -v205\n"
"s_waitcnt vmcnt(8)\n"
"v_mfma_f32_16x16x32_bf16 a[128:131], a[236:239], a[100:103], a[128:131]\n"
"v_exp_f32 v146, -v206\n"
"v_mfma_f32_16x16x32_bf16 a[204:207], a[240:243], a[100:103], a[204:207]\n"
"global_load_dwordx2 a[214:215], v3, s[44:45] offset:0 sc1\n"
"v_mfma_f32_16x16x32_bf16 a[132:135], a[44:47], a[100:103], a[132:135]\n"
"v_exp_f32 v147, -v207\n"
"v_mfma_f32_16x16x32_bf16 a[136:139], a[244:247], a[100:103], a[136:139]\n"
"v_add_f32 v128, 1.0, v128\n"
"v_add_f32 v129, 1.0, v129\n"
"v_mfma_f32_16x16x32_bf16 a[108:111], v[100:103], a[100:103], a[108:111]\n"
"global_load_dwordx4 a[0:3], v24, s[44:45] offset:-2560 sc1\n"
"s_waitcnt vmcnt(9)\n"
"v_mfma_f32_16x16x32_bf16 v[108:111], a[236:239], a[140:143], v[108:111]\n"
"v_rcp_f32 v128, v128\n"
"v_mfma_f32_16x16x32_bf16 v[112:115], a[240:243], a[140:143], v[112:115]\n"
"v_add_f32 v130, 1.0, v130\n"
"v_add_f32 v131, 1.0, v131\n"
"v_mfma_f32_16x16x32_bf16 v[116:119], a[44:47], a[140:143], v[116:119]\n"
"v_rcp_f32 v129, v129\n"
"v_mfma_f32_16x16x32_bf16 v[120:123], a[244:247], a[140:143], v[120:123]\n"
"global_load_dwordx4 a[32:35], v24, s[44:45] offset:2560 sc1\n"
"s_add_u32 s44, s22, 30720\n"
"s_addc_u32 s45, s23, 0\n"
"v_mfma_f32_16x16x32_bf16 v[124:127], v[100:103], a[140:143], v[124:127]\n"
"v_rcp_f32 v130, v130\n"
"s_waitcnt vmcnt(9)\n"
"v_mfma_f32_16x16x32_bf16 a[128:131], a[248:251], a[16:19], a[128:131]\n"
"ds_read_b128 a[52:55], v29 offset:39936\n"
"v_add_f32 v132, 1.0, v132\n"
"v_mfma_f32_16x16x32_bf16 a[204:207], a[252:255], a[16:19], a[204:207]\n"
"ds_read_b128 a[56:59], v23 offset:0\n"
"v_add_f32 v133, 1.0, v133\n"
"v_mfma_f32_16x16x32_bf16 a[132:135], v[84:87], a[16:19], a[132:135]\n"
"ds_read_b128 a[60:63], v23 offset:1024\n"
"v_add_f32 v134, 1.0, v134\n"
"v_mfma_f32_16x16x32_bf16 a[136:139], v[244:247], a[16:19], a[136:139]\n"
"global_load_dwordx4 a[64:67], v24, s[44:45] offset:-2560 sc1\n"
"v_mfma_f32_16x16x32_bf16 a[108:111], a[76:79], a[16:19], a[108:111]\n"
"ds_read_b128 a[36:39], v23 offset:2048\n"
"v_add_f32 v135, 1.0, v135\n"
"s_waitcnt vmcnt(9) lgkmcnt(13)\n"
"v_mfma_f32_16x16x32_bf16 a[128:131], a[216:219], a[208:211], a[128:131]\n"
"ds_read_b128 a[68:71], v23 offset:3072\n"
"v_add_f32 v136, 1.0, v136\n"
"v_mfma_f32_16x16x32_bf16 a[204:207], a[220:223], a[208:211], a[204:207]\n"
"ds_read_b128 v[68:71], v23 offset:4096\n"
"v_add_f32 v137, 1.0, v137\n"
"v_mfma_f32_16x16x32_bf16 a[132:135], a[232:235], a[208:211], a[132:135]\n"
"ds_read_b128 v[72:75], v23 offset:5120\n"
"v_add_f32 v138, 1.0, v138\n"
"v_mfma_f32_16x16x32_bf16 a[136:139], v[96:99], a[208:211], a[136:139]\n"
"global_load_dwordx4 v[36:39], v24, s[44:45] offset:2560 sc1\n"
"s_add_u32 s44, s22, 0\n"
"s_addc_u32 s45, s23, 0\n"
"v_mfma_f32_16x16x32_bf16 a[108:111], v[80:83], a[208:211], a[108:111]\n"
"ds_read_b128 v[40:43], v23 offset:6144\n"
"v_add_f32 v139, 1.0, v139\n"
"s_waitcnt vmcnt(9) lgkmcnt(12)\n"
"v_mfma_f32_16x16x32_bf16 a[128:131], v[248:251], a[20:23], a[128:131]\n"
"ds_read_b128 v[92:95], v23 offset:7168\n"
"v_add_f32 v140, 1.0, v140\n"
"v_mfma_f32_16x16x32_bf16 a[204:207], a[148:151], a[20:23], a[204:207]\n"
"ds_read_b128 v[52:55], v23 offset:8192\n"
"v_add_f32 v141, 1.0, v141\n"
"v_mfma_f32_16x16x32_bf16 a[132:135], a[196:199], a[20:23], a[132:135]\n"
"global_load_dwordx2 v[56:57], v25, s[44:45] offset:-2560 sc1\n"
"v_mfma_f32_16x16x32_bf16 a[136:139], a[92:95], a[20:23], a[136:139]\n"
"ds_read_b128 v[60:63], v23 offset:9216\n"
"v_add_f32 v142, 1.0, v142\n"
"v_mfma_f32_16x16x32_bf16 a[108:111], a[96:99], a[20:23], a[108:111]\n"
"v_rcp_f32 v131, v131\n"
"s_waitcnt vmcnt(9) lgkmcnt(10)\n"
"v_mfma_f32_16x16x32_bf16 a[128:131], a[200:203], a[24:27], a[128:131]\n"
"global_load_dwordx2 v[58:59], v25, s[44:45] offset:0 sc1\n"
"s_add_u32 s44, s22, 20480\n"
"s_addc_u32 s45, s23, 0\n"
"v_mfma_f32_16x16x32_bf16 a[204:207], a[72:75], a[24:27], a[204:207]\n"
"v_rcp_f32 v132, v132\n"
"v_mfma_f32_16x16x32_bf16 a[132:135], a[104:107], a[24:27], a[132:135]\n"
"v_rcp_f32 v133, v133\n"
"v_mfma_f32_16x16x32_bf16 a[136:139], a[124:127], a[24:27], a[136:139]\n"
"global_load_dwordx2 v[44:45], v25, s[44:45] offset:-2560 sc1\n"
"v_mfma_f32_16x16x32_bf16 a[108:111], a[52:55], a[24:27], a[108:111]\n"
"v_rcp_f32 v134, v134\n"
"s_waitcnt vmcnt(9) lgkmcnt(5)\n"
"v_mfma_f32_16x16x32_bf16 a[128:131], a[56:59], a[28:31], a[128:131]\n"
"global_load_dwordx2 v[46:47], v25, s[44:45] offset:0 sc1\n"
"v_mfma_f32_16x16x32_bf16 a[204:207], a[60:63], a[28:31], a[204:207]\n"
"v_rcp_f32 v135, v135\n"
"v_mfma_f32_16x16x32_bf16 a[132:135], a[36:39], a[28:31], a[132:135]\n"
"v_rcp_f32 v136, v136\n"
"v_mfma_f32_16x16x32_bf16 a[136:139], a[68:71], a[28:31], a[136:139]\n"
"v_rcp_f32 v137, v137\n"
"v_mfma_f32_16x16x32_bf16 a[108:111], v[68:71], a[28:31], a[108:111]\n"
"v_rcp_f32 v138, v138\n"
"s_waitcnt vmcnt(8) lgkmcnt(0)\n"
"v_mfma_f32_16x16x32_bf16 a[128:131], v[72:75], a[212:215], a[128:131]\n"
"v_rcp_f32 v139, v139\n"
"v_mfma_f32_16x16x32_bf16 a[204:207], v[40:43], a[212:215], a[204:207]\n"
"v_rcp_f32 v140, v140\n"
"v_mfma_f32_16x16x32_bf16 a[132:135], v[92:95], a[212:215], a[132:135]\n"
"v_rcp_f32 v141, v141\n"
"v_mfma_f32_16x16x32_bf16 a[136:139], v[52:55], a[212:215], a[136:139]\n"
"v_add_f32 v143, 1.0, v143\n"
"v_add_f32 v144, 1.0, v144\n"
"v_mfma_f32_16x16x32_bf16 a[108:111], v[60:63], a[212:215], a[108:111]\n"
"ds_write_b128 v20, a[128:131] offset:0\n"
"v_add_f32 v145, 1.0, v145\n"
"s_waitcnt vmcnt(7)\n"
"v_mfma_f32_16x16x32_bf16 v[108:111], a[248:251], a[0:3], v[108:111]\n"
"v_rcp_f32 v142, v142\n"
"v_mfma_f32_16x16x32_bf16 v[112:115], a[252:255], a[0:3], v[112:115]\n"
"v_rcp_f32 v143, v143\n"
"v_mfma_f32_16x16x32_bf16 v[116:119], v[84:87], a[0:3], v[116:119]\n"
"ds_write_b128 v20, a[204:207] offset:1024\n"
"v_add_f32 v146, 1.0, v146\n"
"v_mfma_f32_16x16x32_bf16 v[120:123], v[244:247], a[0:3], v[120:123]\n"
"v_rcp_f32 v144, v144\n"
"v_mfma_f32_16x16x32_bf16 v[124:127], a[76:79], a[0:3], v[124:127]\n"
"v_rcp_f32 v145, v145\n"
"s_waitcnt vmcnt(6)\n"
"v_mfma_f32_16x16x32_bf16 v[108:111], a[216:219], a[32:35], v[108:111]\n"
"ds_write_b128 v20, a[132:135] offset:2048\n"
"v_add_f32 v147, 1.0, v147\n"
"v_mfma_f32_16x16x32_bf16 v[112:115], a[220:223], a[32:35], v[112:115]\n"
"v_rcp_f32 v146, v146\n"
"v_mfma_f32_16x16x32_bf16 v[116:119], a[232:235], a[32:35], v[116:119]\n"
"v_rcp_f32 v147, v147\n"
"v_mfma_f32_16x16x32_bf16 v[120:123], v[96:99], a[32:35], v[120:123]\n"
"v_mul_f32 v188, v188, v128\n"
"v_mul_f32 v189, v189, v129\n"
"v_mfma_f32_16x16x32_bf16 v[124:127], v[80:83], a[32:35], v[124:127]\n"
"ds_write_b128 v20, a[136:139] offset:3072\n"
"v_mul_f32 v190, v190, v130\n"
"s_waitcnt vmcnt(5)\n"
"v_mfma_f32_16x16x32_bf16 v[108:111], v[248:251], a[64:67], v[108:111]\n"
"v_mul_f32 v191, v191, v131\n"
"v_mul_f32 v192, v192, v132\n"
"v_mfma_f32_16x16x32_bf16 v[112:115], a[148:151], a[64:67], v[112:115]\n"
"v_mul_f32 v193, v193, v133\n"
"v_mul_f32 v194, v194, v134\n"
"v_mfma_f32_16x16x32_bf16 v[116:119], a[196:199], a[64:67], v[116:119]\n"
"ds_write_b128 v20, a[108:111] offset:4096\n"
"v_mul_f32 v195, v195, v135\n"
"v_mfma_f32_16x16x32_bf16 v[120:123], a[92:95], a[64:67], v[120:123]\n"
"v_mul_f32 v196, v196, v136\n"
"v_mul_f32 v197, v197, v137\n"
"v_mfma_f32_16x16x32_bf16 v[124:127], a[96:99], a[64:67], v[124:127]\n"
"v_mul_f32 v198, v198, v138\n"
"v_mul_f32 v199, v199, v139\n"
"s_waitcnt vmcnt(4)\n"
"v_mfma_f32_16x16x32_bf16 v[108:111], a[200:203], v[36:39], v[108:111]\n"
"v_mul_f32 v200, v200, v140\n"
"v_mul_f32 v201, v201, v141\n"
"v_mfma_f32_16x16x32_bf16 v[112:115], a[72:75], v[36:39], v[112:115]\n"
"v_mul_f32 v202, v202, v142\n"
"v_mul_f32 v203, v203, v143\n"
"v_mfma_f32_16x16x32_bf16 v[116:119], a[104:107], v[36:39], v[116:119]\n"
"v_mul_f32 v204, v204, v144\n"
"v_mul_f32 v205, v205, v145\n"
"v_mfma_f32_16x16x32_bf16 v[120:123], a[124:127], v[36:39], v[120:123]\n"
"s_waitcnt lgkmcnt(0)\n"
"s_barrier\n"
"ds_read_b128 v[168:171], v22 offset:0\n"
"v_mul_f32 v206, v206, v146\n"
"v_mfma_f32_16x16x32_bf16 v[124:127], a[52:55], v[36:39], v[124:127]\n"
"ds_read_b128 v[172:175], v22 offset:1024\n"
"v_mul_f32 v207, v207, v147\n"
"s_waitcnt vmcnt(2)\n"
"v_mfma_f32_16x16x32_bf16 v[108:111], a[56:59], v[56:59], v[108:111]\n"
"ds_read_b128 v[176:179], v22 offset:2048\n"
"v_and_b32 v167, 0xffff0000, v217\n"
"v_mfma_f32_16x16x32_bf16 v[112:115], a[60:63], v[56:59], v[112:115]\n"
"ds_read_b128 v[180:183], v22 offset:3072\n"
"v_mfma_f32_16x16x32_bf16 v[116:119], a[36:39], v[56:59], v[116:119]\n"
"ds_read_b128 v[184:187], v22 offset:4096\n"
"s_waitcnt lgkmcnt(4)\n"
"v_fmac_f32 v148, v168, v188\n"
"v_mfma_f32_16x16x32_bf16 v[120:123], a[68:71], v[56:59], v[120:123]\n"
"v_fmac_f32 v149, v169, v189\n"
"v_fmac_f32 v150, v170, v190\n"
"v_mfma_f32_16x16x32_bf16 v[124:127], v[68:71], v[56:59], v[124:127]\n"
"v_fmac_f32 v151, v171, v191\n"
"s_waitcnt lgkmcnt(3)\n"
"v_fmac_f32 v152, v172, v192\n"
"s_waitcnt vmcnt(0)\n"
"v_mfma_f32_16x16x32_bf16 v[108:111], v[72:75], v[44:47], v[108:111]\n"
"v_fmac_f32 v153, v173, v193\n"
"v_fmac_f32 v154, v174, v194\n"
"v_mfma_f32_16x16x32_bf16 v[112:115], v[40:43], v[44:47], v[112:115]\n"
"v_fmac_f32 v155, v175, v195\n"
"s_waitcnt lgkmcnt(2)\n"
"v_fmac_f32 v156, v176, v196\n"
"v_mfma_f32_16x16x32_bf16 v[116:119], v[92:95], v[44:47], v[116:119]\n"
"v_fmac_f32 v157, v177, v197\n"
"v_fmac_f32 v158, v178, v198\n"
"v_mfma_f32_16x16x32_bf16 v[120:123], v[52:55], v[44:47], v[120:123]\n"
"v_fmac_f32 v159, v179, v199\n"
"s_waitcnt lgkmcnt(1)\n"
"v_fmac_f32 v160, v180, v200\n"
"v_mfma_f32_16x16x32_bf16 v[124:127], v[60:63], v[44:47], v[124:127]\n"
"v_fmac_f32 v161, v181, v201\n"
"v_fmac_f32 v162, v182, v202\n"
"v_fmac_f32 v163, v183, v203\n"
"s_waitcnt lgkmcnt(0)\n"
"v_fmac_f32 v164, v184, v204\n"
"v_fmac_f32 v165, v185, v205\n"
"v_fmac_f32 v166, v186, v206\n"
"v_fmac_f32 v167, v187, v207\n"
"v_pk_fma_f32 v[148:149], v[108:109], v[188:189], v[148:149]\n"
"v_pk_fma_f32 v[150:151], v[110:111], v[190:191], v[150:151]\n"
"v_pk_fma_f32 v[152:153], v[112:113], v[192:193], v[152:153]\n"
"v_pk_fma_f32 v[154:155], v[114:115], v[194:195], v[154:155]\n"
"v_pk_fma_f32 v[156:157], v[116:117], v[196:197], v[156:157]\n"
"v_pk_fma_f32 v[158:159], v[118:119], v[198:199], v[158:159]\n"
"v_pk_fma_f32 v[160:161], v[120:121], v[200:201], v[160:161]\n"
"v_pk_fma_f32 v[162:163], v[122:123], v[202:203], v[162:163]\n"
"v_pk_fma_f32 v[164:165], v[124:125], v[204:205], v[164:165]\n"
"v_pk_fma_f32 v[166:167], v[126:127], v[206:207], v[166:167]\n"
"v_cvt_pk_bf16_f32 v208, v148, v149\n"
"v_cvt_pk_bf16_f32 v209, v150, v151\n"
"v_cvt_pk_bf16_f32 v210, v152, v153\n"
"v_cvt_pk_bf16_f32 v211, v154, v155\n"
"global_store_dwordx4 v26, v[208:211], s[26:27] offset:0 \n"
"v_cvt_pk_bf16_f32 v212, v156, v157\n"
"v_cvt_pk_bf16_f32 v213, v158, v159\n"
"v_cvt_pk_bf16_f32 v214, v160, v161\n"
"v_cvt_pk_bf16_f32 v215, v162, v163\n"
"global_store_dwordx4 v26, v[212:215], s[26:27] offset:64 \n"
"v_cvt_pk_bf16_f32 v216, v164, v165\n"
"v_cvt_pk_bf16_f32 v217, v166, v167\n"
"global_store_dwordx2 v13, v[216:217], s[26:27] \n"
"s_endpgm\n"
"12:\n"
"9200:\n"
"s_cbranch_execz 9201f\n"
"s_nop 1\n"
"s_mov_b64 exec, 200977\n"
"global_load_dword v109, v1, s[66:67] offset:0 sc1\n"
"s_mov_b64 exec, -1\n"
"v_pk_fma_f32 v[166:167], v[198:199], v[198:199], v[184:185] op_sel:[0,0,1] op_sel_hi:[1,1,1] neg_lo:[1,0,0] neg_hi:[1,0,0]\n"
"v_pk_fma_f32 v[168:169], v[200:201], v[200:201], v[184:185] op_sel:[0,0,1] op_sel_hi:[1,1,1] neg_lo:[1,0,0] neg_hi:[1,0,0]\n"
"v_pk_fma_f32 v[170:171], v[202:203], v[202:203], v[184:185] op_sel:[0,0,1] op_sel_hi:[1,1,1] neg_lo:[1,0,0] neg_hi:[1,0,0]\n"
"v_pk_fma_f32 v[172:173], v[204:205], v[204:205], v[184:185] op_sel:[0,0,1] op_sel_hi:[1,1,1] neg_lo:[1,0,0] neg_hi:[1,0,0]\n"
"v_pk_fma_f32 v[174:175], v[206:207], v[206:207], v[184:185] op_sel:[0,0,1] op_sel_hi:[1,1,1] neg_lo:[1,0,0] neg_hi:[1,0,0]\n"
"v_pk_mul_f32 v[160:161], v[160:161], v[192:193]\n"
"v_pk_mul_f32 v[162:163], v[162:163], v[194:195]\n"
"v_pk_mul_f32 v[164:165], v[164:165], v[196:197]\n"
"v_pk_mul_f32 v[166:167], v[166:167], v[198:199]\n"
"v_pk_mul_f32 v[168:169], v[168:169], v[200:201]\n"
"v_pk_mul_f32 v[170:171], v[170:171], v[202:203]\n"
"v_pk_mul_f32 v[172:173], v[172:173], v[204:205]\n"
"v_pk_mul_f32 v[174:175], v[174:175], v[206:207]\n"
"v_mfma_f32_32x32x4_2b_bf16 v[224:255], v[186:187], v[186:187], 0 cbsz:1 abid:0\n"
"v_exp_f32 v160, v160\n"
"v_exp_f32 v161, v161\n"
"v_exp_f32 v162, v162\n"
"v_exp_f32 v163, v163\n"
"v_exp_f32 v164, v164\n"
"v_exp_f32 v165, v165\n"
"v_exp_f32 v166, v166\n"
"v_exp_f32 v167, v167\n"
"v_exp_f32 v168, v168\n"
"v_exp_f32 v169, v169\n"
"v_exp_f32 v170, v170\n"
"v_exp_f32 v171, v171\n"
"v_exp_f32 v172, v172\n"
"v_exp_f32 v173, v173\n"
"v_exp_f32 v174, v174\n"
"v_exp_f32 v175, v175\n"
"v_pk_fma_f32 v[160:161], v[160:161], v[190:191], v[190:191] op_sel_hi:[1,0,0]\n"
"v_pk_fma_f32 v[162:163], v[162:163], v[190:191], v[190:191] op_sel_hi:[1,0,0]\n"
"v_pk_fma_f32 v[164:165], v[164:165], v[190:191], v[190:191] op_sel_hi:[1,0,0]\n"
"v_pk_fma_f32 v[166:167], v[166:167], v[190:191], v[190:191] op_sel_hi:[1,0,0]\n"
"v_pk_fma_f32 v[168:169], v[168:169], v[190:191], v[190:191] op_sel_hi:[1,0,0]\n"
"v_pk_fma_f32 v[170:171], v[170:171], v[190:191], v[190:191] op_sel_hi:[1,0,0]\n"
"v_pk_fma_f32 v[172:173], v[172:173], v[190:191], v[190:191] op_sel_hi:[1,0,0]\n"
"v_pk_fma_f32 v[174:175], v[174:175], v[190:191], v[190:191] op_sel_hi:[1,0,0]\n"
"v_rcp_f32 v160, v160\n"
"v_rcp_f32 v161, v161\n"
"v_rcp_f32 v162, v162\n"
"v_rcp_f32 v163, v163\n"
"v_rcp_f32 v164, v164\n"
"v_rcp_f32 v165, v165\n"
"v_rcp_f32 v166, v166\n"
"v_rcp_f32 v167, v167\n"
"v_rcp_f32 v168, v168\n"
"v_rcp_f32 v169, v169\n"
"v_rcp_f32 v170, v170\n"
"v_rcp_f32 v171, v171\n"
"v_rcp_f32 v172, v172\n"
"v_rcp_f32 v173, v173\n"
"v_rcp_f32 v174, v174\n"
"v_rcp_f32 v175, v175\n"
"v_pk_mul_f32 v[160:161], v[160:161], v[192:193]\n"
"v_pk_mul_f32 v[162:163], v[162:163], v[194:195]\n"
"v_pk_mul_f32 v[164:165], v[164:165], v[196:197]\n"
"v_pk_mul_f32 v[166:167], v[166:167], v[198:199]\n"
"v_pk_mul_f32 v[168:169], v[168:169], v[200:201]\n"
"v_pk_mul_f32 v[170:171], v[170:171], v[202:203]\n"
"v_pk_mul_f32 v[172:173], v[172:173], v[204:205]\n"
"v_pk_mul_f32 v[174:175], v[174:175], v[206:207]\n"
"v_cvt_pk_bf16_f32 v160, v160, v161\n"
"v_cvt_pk_bf16_f32 v162, v162, v163\n"
"v_cvt_pk_bf16_f32 v164, v164, v165\n"
"v_cvt_pk_bf16_f32 v166, v166, v167\n"
"v_cvt_pk_bf16_f32 v168, v168, v169\n"
"v_cvt_pk_bf16_f32 v170, v170, v171\n"
"v_cvt_pk_bf16_f32 v172, v172, v173\n"
"v_cvt_pk_bf16_f32 v174, v174, v175\n"
"v_accvgpr_write_b32 a96, v160\n"
"v_accvgpr_write_b32 a97, v162\n"
"v_accvgpr_write_b32 a100, v164\n"
"v_accvgpr_write_b32 a101, v166\n"
"v_accvgpr_write_b32 a104, v168\n"
"v_accvgpr_write_b32 a105, v170\n"
"v_accvgpr_write_b32 a108, v172\n"
"v_accvgpr_write_b32 a109, v174\n"
"v_pk_fma_f32 v[160:161], v[180:181], v[148:149], v[132:133] op_sel:[0,0,0] op_sel_hi:[0,1,1]\n"
"v_pk_fma_f32 v[162:163], v[180:181], v[150:151], v[134:135] op_sel:[0,0,0] op_sel_hi:[0,1,1]\n"
"v_pk_fma_f32 v[164:165], v[180:181], v[148:149], v[132:133] op_sel:[1,0,0] op_sel_hi:[1,1,1]\n"
"v_pk_fma_f32 v[166:167], v[180:181], v[150:151], v[134:135] op_sel:[1,0,0] op_sel_hi:[1,1,1]\n"
"v_pk_fma_f32 v[168:169], v[182:183], v[148:149], v[132:133] op_sel:[0,0,0] op_sel_hi:[0,1,1]\n"
"v_pk_fma_f32 v[170:171], v[182:183], v[150:151], v[134:135] op_sel:[0,0,0] op_sel_hi:[0,1,1]\n"
"v_pk_fma_f32 v[172:173], v[182:183], v[148:149], v[132:133] op_sel:[1,0,0] op_sel_hi:[1,1,1]\n"
"v_pk_fma_f32 v[174:175], v[182:183], v[150:151], v[134:135] op_sel:[1,0,0] op_sel_hi:[1,1,1]\n"
"v_pk_fma_f32 v[208:209], v[208:209], v[176:177], v[160:161] op_sel:[0,0,0] op_sel_hi:[1,0,1]\n"
"v_pk_fma_f32 v[210:211], v[210:211], v[176:177], v[162:163] op_sel:[0,0,0] op_sel_hi:[1,0,1]\n"
"v_pk_fma_f32 v[212:213], v[212:213], v[176:177], v[164:165] op_sel:[0,1,0] op_sel_hi:[1,1,1]\n"
"v_pk_fma_f32 v[214:215], v[214:215], v[176:177], v[166:167] op_sel:[0,1,0] op_sel_hi:[1,1,1]\n"
"v_pk_fma_f32 v[216:217], v[216:217], v[178:179], v[168:169] op_sel:[0,0,0] op_sel_hi:[1,0,1]\n"
"v_pk_fma_f32 v[218:219], v[218:219], v[178:179], v[170:171] op_sel:[0,0,0] op_sel_hi:[1,0,1]\n"
"v_pk_fma_f32 v[220:221], v[220:221], v[178:179], v[172:173] op_sel:[0,1,0] op_sel_hi:[1,1,1]\n"
"v_pk_fma_f32 v[222:223], v[222:223], v[178:179], v[174:175] op_sel:[0,1,0] op_sel_hi:[1,1,1]\n"
"v_pk_fma_f32 v[160:161], v[208:209], v[208:209], v[184:185] op_sel:[0,0,1] op_sel_hi:[1,1,1] neg_lo:[1,0,0] neg_hi:[1,0,0]\n"
"v_pk_fma_f32 v[162:163], v[210:211], v[210:211], v[184:185] op_sel:[0,0,1] op_sel_hi:[1,1,1] neg_lo:[1,0,0] neg_hi:[1,0,0]\n"
"v_pk_fma_f32 v[164:165], v[212:213], v[212:213], v[184:185] op_sel:[0,0,1] op_sel_hi:[1,1,1] neg_lo:[1,0,0] neg_hi:[1,0,0]\n"
"v_pk_fma_f32 v[166:167], v[214:215], v[214:215], v[184:185] op_sel:[0,0,1] op_sel_hi:[1,1,1] neg_lo:[1,0,0] neg_hi:[1,0,0]\n"
"v_pk_fma_f32 v[168:169], v[216:217], v[216:217], v[184:185] op_sel:[0,0,1] op_sel_hi:[1,1,1] neg_lo:[1,0,0] neg_hi:[1,0,0]\n"
"v_pk_fma_f32 v[170:171], v[218:219], v[218:219], v[184:185] op_sel:[0,0,1] op_sel_hi:[1,1,1] neg_lo:[1,0,0] neg_hi:[1,0,0]\n"
"v_pk_fma_f32 v[172:173], v[220:221], v[220:221], v[184:185] op_sel:[0,0,1] op_sel_hi:[1,1,1] neg_lo:[1,0,0] neg_hi:[1,0,0]\n"
"v_pk_fma_f32 v[174:175], v[222:223], v[222:223], v[184:185] op_sel:[0,0,1] op_sel_hi:[1,1,1] neg_lo:[1,0,0] neg_hi:[1,0,0]\n"
"v_pk_mul_f32 v[160:161], v[160:161], v[208:209]\n"
"v_pk_mul_f32 v[162:163], v[162:163], v[210:211]\n"
"v_pk_mul_f32 v[164:165], v[164:165], v[212:213]\n"
"v_pk_mul_f32 v[166:167], v[166:167], v[214:215]\n"
"v_pk_mul_f32 v[168:169], v[168:169], v[216:217]\n"
"v_pk_mul_f32 v[170:171], v[170:171], v[218:219]\n"
"v_pk_mul_f32 v[172:173], v[172:173], v[220:221]\n"
"v_pk_mul_f32 v[174:175], v[174:175], v[222:223]\n"
"v_exp_f32 v160, v160\n"
"v_exp_f32 v161, v161\n"
"v_exp_f32 v162, v162\n"
"v_exp_f32 v163, v163\n"
"v_exp_f32 v164, v164\n"
"v_exp_f32 v165, v165\n"
"v_exp_f32 v166, v166\n"
"v_exp_f32 v167, v167\n"
"v_exp_f32 v168, v168\n"
"v_exp_f32 v169, v169\n"
"v_exp_f32 v170, v170\n"
"v_exp_f32 v171, v171\n"
"v_exp_f32 v172, v172\n"
"v_exp_f32 v173, v173\n"
"v_exp_f32 v174, v174\n"
"v_exp_f32 v175, v175\n"
"v_pk_fma_f32 v[160:161], v[160:161], v[190:191], v[190:191] op_sel_hi:[1,0,0]\n"
"v_pk_fma_f32 v[162:163], v[162:163], v[190:191], v[190:191] op_sel_hi:[1,0,0]\n"
"v_pk_fma_f32 v[164:165], v[164:165], v[190:191], v[190:191] op_sel_hi:[1,0,0]\n"
"v_pk_fma_f32 v[166:167], v[166:167], v[190:191], v[190:191] op_sel_hi:[1,0,0]\n"
"v_pk_fma_f32 v[168:169], v[168:169], v[190:191], v[190:191] op_sel_hi:[1,0,0]\n"
"v_pk_fma_f32 v[170:171], v[170:171], v[190:191], v[190:191] op_sel_hi:[1,0,0]\n"
"v_pk_fma_f32 v[172:173], v[172:173], v[190:191], v[190:191] op_sel_hi:[1,0,0]\n"
"v_pk_fma_f32 v[174:175], v[174:175], v[190:191], v[190:191] op_sel_hi:[1,0,0]\n"
"v_mfma_f32_32x32x16_bf16 v[192:207], v[186:189], v[186:189], 0\n"
"v_rcp_f32 v160, v160\n"
"v_rcp_f32 v161, v161\n"
"v_rcp_f32 v162, v162\n"
"v_rcp_f32 v163, v163\n"
"v_rcp_f32 v164, v164\n"
"v_rcp_f32 v165, v165\n"
"v_rcp_f32 v166, v166\n"
"v_rcp_f32 v167, v167\n"
"v_rcp_f32 v168, v168\n"
"v_rcp_f32 v169, v169\n"
"v_rcp_f32 v170, v170\n"
"v_rcp_f32 v171, v171\n"
"v_rcp_f32 v172, v172\n"
"v_rcp_f32 v173, v173\n"
"v_rcp_f32 v174, v174\n"
"v_rcp_f32 v175, v175\n"
"v_pk_mul_f32 v[160:161], v[160:161], v[208:209]\n"
"v_pk_mul_f32 v[162:163], v[162:163], v[210:211]\n"
"v_pk_mul_f32 v[164:165], v[164:165], v[212:213]\n"
"v_pk_mul_f32 v[166:167], v[166:167], v[214:215]\n"
"v_pk_mul_f32 v[168:169], v[168:169], v[216:217]\n"
"v_pk_mul_f32 v[170:171], v[170:171], v[218:219]\n"
"v_pk_mul_f32 v[172:173], v[172:173], v[220:221]\n"
"9201:\n"
"s_cbranch_execz 97b\n"
"s_nop 0\n"
"v_pk_mul_f32 v[174:175], v[174:175], v[222:223]\n"
"v_cvt_pk_bf16_f32 v160, v160, v161\n"
"v_cvt_pk_bf16_f32 v162, v162, v163\n"
"v_cvt_pk_bf16_f32 v164, v164, v165\n"
"v_cvt_pk_bf16_f32 v166, v166, v167\n"
"v_cvt_pk_bf16_f32 v168, v168, v169\n"
"v_cvt_pk_bf16_f32 v170, v170, v171\n"
"v_cvt_pk_bf16_f32 v172, v172, v173\n"
"v_cvt_pk_bf16_f32 v174, v174, v175\n"
"v_accvgpr_write_b32 a98, v160\n"
"v_accvgpr_write_b32 a99, v162\n"
"v_accvgpr_write_b32 a102, v164\n"
"v_accvgpr_write_b32 a103, v166\n"
"v_accvgpr_write_b32 a106, v168\n"
"v_accvgpr_write_b32 a107, v170\n"
"v_accvgpr_write_b32 a110, v172\n"
"v_accvgpr_write_b32 a111, v174\n"
"14:\n"
"s_waitcnt vmcnt(0) lgkmcnt(0)\n"
"v_cmp_eq_u32 vcc, v109, v35\n"
"s_nop 4\n"
"s_and_b32 s47, vcc_lo, 200977\n"
"s_cbranch_scc1 10f\n"
"11:\n"
"global_load_dwordx2 v[66:67], v4, s[36:37] offset:-1536 sc1\n"
"global_load_dwordx2 v[64:65], v10, s[6:7] offset:1024 sc1\n"
"global_load_dwordx4 v[56:59], v7, s[36:37] offset:-2560 sc1\n"
"global_load_dwordx4 v[44:47], v9, s[6:7] offset:0 sc1\n"
"global_load_dwordx4 v[48:51], v9, s[6:7] offset:1280 sc1\n"
"global_load_dwordx2 v[52:53], v10, s[0:1] offset:1024 sc1\n"
"s_mov_b64 exec, s[48:49]\n"
"global_load_dwordx4 v[76:79], v5, s[36:37] offset:-1280 sc1\n"
"s_mov_b64 exec, -1\n"
"s_mov_b64 exec, s[48:49]\n"
"global_load_dwordx4 v[80:83], v5, s[36:37] offset:-640 sc1\n"
"s_mov_b64 exec, -1\n"
"global_load_dwordx4 v[36:39], v9, s[0:1] offset:0 sc1\n"
"global_load_dwordx4 v[40:43], v9, s[0:1] offset:1280 sc1\n"
"global_load_dwordx4 v[60:63], v7, s[36:37] offset:0 sc1\n"
"s_mov_b64 exec, s[48:49]\n"
"global_load_dwordx4 v[84:87], v5, s[36:37] offset:1280 sc1\n"
"s_mov_b64 exec, -1\n"
"s_mov_b64 exec, s[48:49]\n"
"global_load_dwordx4 v[88:91], v5, s[36:37] offset:1920 sc1\n"
"s_mov_b64 exec, -1\n"
"s_add_u32 s36, s36, 5120\n"
"s_addc_u32 s37, s37, 0\n"
"s_waitcnt vmcnt(11)\n"
"v_mfma_f32_32x32x4_2b_bf16 v[128:159], v[66:67], v[64:65], 0 cbsz:1 abid:0\n"
"s_waitcnt vmcnt(9)\n"
"s_nop 15\n"
"s_nop 0\n"
"v_mfma_f32_32x32x16_bf16 v[128:143], v[56:59], v[44:47], v[128:143]\n"
"s_waitcnt vmcnt(8)\n"
"v_mfma_f32_32x32x16_bf16 v[144:159], v[56:59], v[48:51], v[144:159]\n"
"s_nop 15\n"
"s_branch 13b\n"
"50:\n"
"9501:\n"
"s_cbranch_execz 97b\n"
"s_barrier\n"
"s_mov_b64 exec, 200977\n"
"global_load_dword v109, v1, s[66:67] offset:0 sc1\n"
"s_mov_b64 exec, -1\n"
"s_mov_b64 s[36:37], s[12:13]\n"
"s_mov_b64 s[38:39], s[14:15]\n"
"s_mov_b64 s[40:41], s[16:17]\n"
"s_mov_b64 s[42:43], s[18:19]\n"
"s_mov_b64 s[44:45], s[22:23]\n"
"s_cmp_eq_u32 s30, 0\n"
"s_cselect_b64 s[50:51], 4, 0\n"
"s_mov_b32 s48, 0xfffff\n"
"s_mov_b32 s49, 0xfffff\n"
"s_movk_i32 s6, 2560\n"
"s_cmp_lt_u32 s30, 2\n"
"s_cselect_b32 s6, s6, -2560\n"
"s_cselect_b32 s7, 0, -1\n"
"s_add_u32 s6, s6, s0\n"
"s_addc_u32 s7, s7, s1\n"
"s_lshl_b32 s35, s30, 3\n"
"s_lshr_b32 s35, 0x020e1c28, s35\n"
"s_and_b32 s35, s35, 63\n"
"s_mov_b64 s[52:53], 1\n"
"v_mov_b32 v108, 0xdddddddd\n"
"v_mov_b32 v186, 0\n"
"v_mov_b32 v187, 0\n"
"v_mov_b32 v188, 0\n"
"v_mov_b32 v189, 0\n"
"v_mov_b32 v76, v12\n"
"v_mov_b32 v77, v12\n"
"v_mov_b32 v78, v12\n"
"v_mov_b32 v79, v12\n"
"v_mov_b32 v80, v12\n"
"v_mov_b32 v81, v12\n"
"v_mov_b32 v82, v12\n"
"v_mov_b32 v83, v12\n"
"v_mov_b32 v84, v12\n"
"v_mov_b32 v85, v12\n"
"v_mov_b32 v86, v12\n"
"v_mov_b32 v87, v12\n"
"v_mov_b32 v88, v12\n"
"v_mov_b32 v89, v12\n"
"v_mov_b32 v90, v12\n"
"v_mov_b32 v91, v12\n"
"v_mov_b32 v92, v12\n"
"v_mov_b32 v93, v12\n"
"v_mov_b32 v94, v12\n"
"v_mov_b32 v95, v12\n"
"v_mov_b32 v96, v12\n"
"v_mov_b32 v97, v12\n"
"v_mov_b32 v98, v12\n"
"v_mov_b32 v99, v12\n"
"v_mov_b32 v100, v12\n"
"v_mov_b32 v101, v12\n"
"v_mov_b32 v102, v12\n"
"v_mov_b32 v103, v12\n"
"v_mov_b32 v104, v12\n"
"v_mov_b32 v105, v12\n"
"v_mov_b32 v106, v12\n"
"v_mov_b32 v107, v12\n"
"v_xor_b32 v122, 0, v28\n"
"v_xor_b32 v123, 1024, v28\n"
"v_xor_b32 v124, 2048, v28\n"
"v_xor_b32 v125, 3072, v28\n"
"v_mov_b32 v192, 0\n"
"v_mov_b32 v193, 0\n"
"v_mov_b32 v194, 0\n"
"v_mov_b32 v195, 0\n"
"v_mov_b32 v196, 0\n"
"v_mov_b32 v197, 0\n"
"v_mov_b32 v198, 0\n"
"v_mov_b32 v199, 0\n"
"v_mov_b32 v200, 0\n"
"v_mov_b32 v201, 0\n"
"v_mov_b32 v202, 0\n"
"v_mov_b32 v203, 0\n"
"v_mov_b32 v204, 0\n"
"v_mov_b32 v205, 0\n"
"v_mov_b32 v206, 0\n"
"v_mov_b32 v207, 0\n"
"v_mov_b32 v224, 0\n"
"v_mov_b32 v225, 0\n"
"v_mov_b32 v226, 0\n"
"v_mov_b32 v227, 0\n"
"v_mov_b32 v228, 0\n"
"v_mov_b32 v229, 0\n"
"v_mov_b32 v230, 0\n"
"v_mov_b32 v231, 0\n"
"v_mov_b32 v232, 0\n"
"v_mov_b32 v233, 0\n"
"v_mov_b32 v234, 0\n"
"v_mov_b32 v235, 0\n"
"v_mov_b32 v236, 0\n"
"v_mov_b32 v237, 0\n"
"v_mov_b32 v238, 0\n"
"v_mov_b32 v239, 0\n"
"v_mov_b32 v240, 0\n"
"v_mov_b32 v241, 0\n"
"v_mov_b32 v242, 0\n"
"v_mov_b32 v243, 0\n"
"v_mov_b32 v244, 0\n"
"v_mov_b32 v245, 0\n"
"v_mov_b32 v246, 0\n"
"v_mov_b32 v247, 0\n"
"v_mov_b32 v248, 0\n"
"v_mov_b32 v249, 0\n"
"v_mov_b32 v250, 0\n"
"v_mov_b32 v251, 0\n"
"v_mov_b32 v252, 0\n"
"v_mov_b32 v253, 0\n"
"v_mov_b32 v254, 0\n"
"v_mov_b32 v255, 0\n"
"s_branch 14b\n"
"10:\n"
"s_mov_b32 s46, 0\n"
"101:\n"
"s_sleep 2\n"
"s_mov_b64 exec, 200977\n"
"global_load_dword v109, v1, s[66:67] offset:0 sc1\n"
"s_mov_b64 exec, -1\n"
"s_add_u32 s46, s46, 1\n"
"s_and_b32 s47, s46, 15\n"
"s_cbranch_scc1 102f\n"
"s_mov_b64 exec, 4\n"
"global_atomic_add v15, v110, s[66:67]\n"
"s_mov_b64 exec, -1\n"
"102:\n"
"s_cmp_gt_u32 s46, 0x100000\n"
"s_cbranch_scc1 11b\n"
"s_waitcnt vmcnt(0)\n"
"v_cmp_eq_u32 vcc, v109, v35\n"
"s_nop 4\n"
"s_and_b32 s47, vcc_lo, 200977\n"
"s_cbranch_scc1 101b\n"
"s_branch 11b\n"
"24:\n"
"s_mov_b32 s46, 0\n"
"241:\n"
"s_sleep 2\n"
"global_load_dword v109, v1, s[66:67] offset:0 sc1\n"
"s_add_u32 s46, s46, 1\n"
"s_and_b32 s47, s46, 15\n"
"s_cbranch_scc1 242f\n"
"s_mov_b64 exec, 4\n"
"global_atomic_add v15, v110, s[66:67]\n"
"s_mov_b64 exec, -1\n"
"242:\n"
"s_cmp_gt_u32 s46, 0x100000\n"
"s_cbranch_scc1 25b\n"
"s_waitcnt vmcnt(0)\n"
"v_cmp_eq_u32 vcc, v109, v35\n"
"s_nop 4\n"
"s_cmp_lg_u64 vcc, 0\n"
"s_cbranch_scc1 241b\n"
"s_branch 25b\n"
"20:\n"
"s_mov_b32 s46, 0\n"
"201:\n"
"s_sleep 4\n"
"s_mov_b32 exec_lo, 0\n"
"global_load_dword v109, v14, s[66:67] offset:0 sc1\n"
"s_mov_b32 exec_lo, -1\n"
"s_add_u32 s46, s46, 1\n"
"s_and_b32 s47, s46, 15\n"
"s_cbranch_scc1 202f\n"
"s_mov_b64 exec, 14\n"
"global_atomic_add v15, v110, s[66:67]\n"
"s_mov_b64 exec, -1\n"
"202:\n"
"s_cmp_gt_u32 s46, 0x100000\n"
"s_cbranch_scc1 21b\n"
"s_waitcnt vmcnt(0)\n"
"v_cmp_eq_u32 vcc, v109, v34\n"
"s_nop 4\n"
"s_cmp_lg_u32 vcc_hi, 0\n"
"s_cbranch_scc1 201b\n"
"s_branch 21b\n"
"22:\n"
"s_mov_b32 s46, 0\n"
"221:\n"
"s_sleep 8\n"
"global_load_dword v109, v16, s[66:67] offset:0 sc1\n"
"s_add_u32 s46, s46, 1\n"
"s_and_b32 s47, s46, 15\n"
"s_cbranch_scc1 222f\n"
"s_mov_b64 exec, 14\n"
"global_atomic_add v15, v110, s[66:67]\n"
"s_mov_b64 exec, -1\n"
"222:\n"
"s_cmp_gt_u32 s46, 0x100000\n"
"s_cbranch_scc1 23b\n"
"s_waitcnt vmcnt(0)\n"
"v_cmp_eq_u32 vcc, v109, v33\n"
"s_nop 4\n"
"s_cmp_lg_u64 vcc, 0\n"
"s_cbranch_scc1 221b\n"
"s_branch 23b\n"
"40:\n"
"s_mov_b32 s46, 0\n"
"s_mov_b32 exec_hi, 0\n"
"global_load_dword v126, v14, s[66:67] offset:0 sc1\n"
"s_mov_b32 exec_hi, -1\n"
"401:\n"
"s_sleep 3\n"
"s_mov_b32 exec_hi, 0\n"
"global_load_dword v109, v14, s[66:67] offset:0 sc1\n"
"s_mov_b32 exec_hi, -1\n"
"s_waitcnt vmcnt(1)\n"
"v_cmp_eq_u32 vcc, v126, v34\n"
"s_nop 4\n"
"s_cmp_lg_u32 vcc_lo, 0\n"
"s_cbranch_scc0 41b\n"
"s_sleep 3\n"
"s_mov_b32 exec_hi, 0\n"
"global_load_dword v126, v14, s[66:67] offset:0 sc1\n"
"s_mov_b32 exec_hi, -1\n"
"s_waitcnt vmcnt(1)\n"
"v_cmp_eq_u32 vcc, v109, v34\n"
"s_nop 4\n"
"s_cmp_lg_u32 vcc_lo, 0\n"
"s_cbranch_scc0 41b\n"
"s_add_u32 s46, s46, 1\n"
"s_cmp_gt_u32 s46, 0x100000\n"
"s_cbranch_scc0 401b\n"
"s_branch 41b\n"
"8:\n"
"s_mov_b32 s46, 0\n"
"81:\n"
"s_sleep 4\n"
"s_mov_b32 exec_lo, 0\n"
"global_load_dword v109, v1, s[66:67] offset:0 sc1\n"
"s_mov_b32 exec_lo, -1\n"
"s_add_u32 s46, s46, 1\n"
"s_cmp_gt_u32 s46, 0x100000\n"
"s_cbranch_scc1 9b\n"
"s_waitcnt vmcnt(0)\n"
"v_cmp_eq_u32 vcc, v109, v35\n"
"s_nop 4\n"
"s_bitcmp1_b32 vcc_hi, 31\n"
"s_cbranch_scc1 81b\n"
"s_branch 9b\n"
"30:\n"
"v_mov_b32 v126, 0xff800000\n"
"v_mov_b32 v127, 0xff800000\n"
"s_lshr_b32 s47, s30, 1\n"
"s_mul_i32 s47, s47, 81920\n"
"s_mov_b64 s[36:37], s[12:13]\n"
"s_sub_u32 s36, s36, s47\n"
"s_subb_u32 s37, s37, 0\n"
"s_mov_b32 s47, 32\n"
"32:\n"
"s_nop 15\n"
"s_nop 3\n"
"global_load_dwordx4 v[68:71], v7, s[36:37] offset:-2560 sc1\n"
"global_load_dwordx2 v[54:55], v4, s[36:37] offset:-1536 sc1\n"
"global_load_dwordx4 v[72:75], v7, s[36:37] offset:0 sc1\n"
"s_waitcnt vmcnt(1)\n"
"v_mfma_f32_32x32x4_2b_bf16 v[160:191], v[54:55], v[52:53], 0 cbsz:1 abid:0\n"
"s_nop 15\n"
"s_nop 1\n"
"v_mfma_f32_32x32x16_bf16 v[160:175], v[68:71], v[36:39], v[160:175]\n"
"v_mfma_f32_32x32x16_bf16 v[176:191], v[68:71], v[40:43], v[176:191]\n"
"s_nop 10\n"
"s_nop 0\n"
"v_max3_f32 v126, v160, v161, v126\n"
"v_max3_f32 v126, v162, v163, v126\n"
"v_max3_f32 v126, v164, v165, v126\n"
"v_max3_f32 v126, v166, v167, v126\n"
"v_max3_f32 v126, v168, v169, v126\n"
"v_max3_f32 v126, v170, v171, v126\n"
"v_max3_f32 v126, v172, v173, v126\n"
"v_max3_f32 v126, v174, v175, v126\n"
"v_max3_f32 v127, v176, v177, v127\n"
"v_max3_f32 v127, v178, v179, v127\n"
"v_max3_f32 v127, v180, v181, v127\n"
"v_max3_f32 v127, v182, v183, v127\n"
"v_max3_f32 v127, v184, v185, v127\n"
"v_max3_f32 v127, v186, v187, v127\n"
"v_max3_f32 v127, v188, v189, v127\n"
"v_max3_f32 v127, v190, v191, v127\n"
"v_mfma_f32_32x32x4_2b_bf16 v[160:191], v[54:55], v[52:53], 0 cbsz:1 abid:1\n"
"s_waitcnt vmcnt(0)\n"
"s_nop 15\n"
"s_nop 0\n"
"v_mfma_f32_32x32x16_bf16 v[160:175], v[72:75], v[36:39], v[160:175]\n"
"v_mfma_f32_32x32x16_bf16 v[176:191], v[72:75], v[40:43], v[176:191]\n"
"s_nop 10\n"
"v_max3_f32 v126, v160, v161, v126\n"
"v_max3_f32 v126, v162, v163, v126\n"
"v_max3_f32 v126, v164, v165, v126\n"
"v_max3_f32 v126, v166, v167, v126\n"
"v_max3_f32 v126, v168, v169, v126\n"
"v_max3_f32 v126, v170, v171, v126\n"
"v_max3_f32 v126, v172, v173, v126\n"
"v_max3_f32 v126, v174, v175, v126\n"
"v_max3_f32 v127, v176, v177, v127\n"
"v_max3_f32 v127, v178, v179, v127\n"
"v_max3_f32 v127, v180, v181, v127\n"
"v_max3_f32 v127, v182, v183, v127\n"
"v_max3_f32 v127, v184, v185, v127\n"
"v_max3_f32 v127, v186, v187, v127\n"
"v_max3_f32 v127, v188, v189, v127\n"
"v_max3_f32 v127, v190, v191, v127\n"
"s_add_u32 s36, s36, 5120\n"
"s_addc_u32 s37, s37, 0\n"
"s_sub_u32 s47, s47, 1\n"
"s_cmp_lg_u32 s47, 0\n"
"s_cbranch_scc1 32b\n"
"s_nop 15\n"
"v_mov_b32 v109, v126\n"
"s_nop 1\n"
"v_permlane32_swap_b32 v126, v109\n"
"v_max_f32 v126, v126, v109\n"
"v_mov_b32 v192, 0\n"
"v_mov_b32 v193, 0\n"
"v_mov_b32 v194, 0\n"
"v_mov_b32 v195, 0\n"
"v_mov_b32 v196, 0\n"
"v_mov_b32 v197, 0\n"
"v_mov_b32 v198, 0\n"
"v_mov_b32 v199, 0\n"
"v_mov_b32 v200, 0\n"
"v_mov_b32 v201, 0\n"
"v_mov_b32 v202, 0\n"
"v_mov_b32 v203, 0\n"
"v_mov_b32 v204, 0\n"
"v_mov_b32 v205, 0\n"
"v_mov_b32 v206, 0\n"
"v_mov_b32 v207, 0\n"
"v_mov_b32 v109, v127\n"
"s_nop 1\n"
"v_permlane32_swap_b32 v127, v109\n"
"v_max_f32 v127, v127, v109\n"
"v_mov_b32 v208, 0\n"
"v_mov_b32 v209, 0\n"
"v_mov_b32 v210, 0\n"
"v_mov_b32 v211, 0\n"
"v_mov_b32 v212, 0\n"
"v_mov_b32 v213, 0\n"
"v_mov_b32 v214, 0\n"
"v_mov_b32 v215, 0\n"
"v_mov_b32 v216, 0\n"
"v_mov_b32 v217, 0\n"
"v_mov_b32 v218, 0\n"
"v_mov_b32 v219, 0\n"
"v_mov_b32 v220, 0\n"
"v_mov_b32 v221, 0\n"
"v_mov_b32 v222, 0\n"
"v_mov_b32 v223, 0\n"
"s_lshr_b32 s47, s30, 1\n"
"s_mul_i32 s47, s47, 81920\n"
"s_mov_b64 s[36:37], s[12:13]\n"
"s_sub_u32 s36, s36, s47\n"
"s_subb_u32 s37, s37, 0\n"
"s_mov_b32 s47, 32\n"
"33:\n"
"s_nop 15\n"
"s_nop 3\n"
"global_load_dwordx4 v[68:71], v7, s[36:37] offset:-2560 sc1\n"
"global_load_dwordx2 v[54:55], v4, s[36:37] offset:-1536 sc1\n"
"global_load_dwordx4 v[72:75], v7, s[36:37] offset:0 sc1\n"
"s_mov_b64 exec, s[48:49]\n"
"global_load_dwordx4 v[92:95], v5, s[36:37] offset:-1280 sc1\n"
"global_load_dwordx4 v[96:99], v5, s[36:37] offset:-640 sc1\n"
"global_load_dwordx4 v[100:103], v5, s[36:37] offset:1280 sc1\n"
"global_load_dwordx4 v[104:107], v5, s[36:37] offset:1920 sc1\n"
"s_mov_b64 exec, -1\n"
"s_waitcnt vmcnt(5)\n"
"v_mfma_f32_32x32x4_2b_bf16 v[160:191], v[54:55], v[52:53], 0 cbsz:1 abid:0\n"
"s_nop 15\n"
"s_nop 1\n"
"v_mfma_f32_32x32x16_bf16 v[160:175], v[68:71], v[36:39], v[160:175]\n"
"v_mfma_f32_32x32x16_bf16 v[176:191], v[68:71], v[40:43], v[176:191]\n"
"s_nop 10\n"
"v_sub_f32 v160, v160, v126\n"
"v_sub_f32 v161, v161, v126\n"
"v_sub_f32 v162, v162, v126\n"
"v_sub_f32 v163, v163, v126\n"
"v_sub_f32 v164, v164, v126\n"
"v_sub_f32 v165, v165, v126\n"
"v_sub_f32 v166, v166, v126\n"
"v_sub_f32 v167, v167, v126\n"
"v_sub_f32 v168, v168, v126\n"
"v_sub_f32 v169, v169, v126\n"
"v_sub_f32 v170, v170, v126\n"
"v_sub_f32 v171, v171, v126\n"
"v_sub_f32 v172, v172, v126\n"
"v_sub_f32 v173, v173, v126\n"
"v_sub_f32 v174, v174, v126\n"
"v_sub_f32 v175, v175, v126\n"
"v_sub_f32 v176, v176, v127\n"
"v_sub_f32 v177, v177, v127\n"
"v_sub_f32 v178, v178, v127\n"
"v_sub_f32 v179, v179, v127\n"
"v_sub_f32 v180, v180, v127\n"
"v_sub_f32 v181, v181, v127\n"
"v_sub_f32 v182, v182, v127\n"
"v_sub_f32 v183, v183, v127\n"
"v_sub_f32 v184, v184, v127\n"
"v_sub_f32 v185, v185, v127\n"
"v_sub_f32 v186, v186, v127\n"
"v_sub_f32 v187, v187, v127\n"
"v_sub_f32 v188, v188, v127\n"
"v_sub_f32 v189, v189, v127\n"
"v_sub_f32 v190, v190, v127\n"
"v_sub_f32 v191, v191, v127\n"
"v_exp_f32 v160, v160\n"
"v_exp_f32 v161, v161\n"
"v_exp_f32 v162, v162\n"
"v_exp_f32 v163, v163\n"
"v_exp_f32 v164, v164\n"
"v_exp_f32 v165, v165\n"
"v_exp_f32 v166, v166\n"
"v_exp_f32 v167, v167\n"
"v_exp_f32 v168, v168\n"
"v_exp_f32 v169, v169\n"
"v_exp_f32 v170, v170\n"
"v_exp_f32 v171, v171\n"
"v_exp_f32 v172, v172\n"
"v_exp_f32 v173, v173\n"
"v_exp_f32 v174, v174\n"
"v_exp_f32 v175, v175\n"
"v_exp_f32 v176, v176\n"
"v_exp_f32 v177, v177\n"
"v_exp_f32 v178, v178\n"
"v_exp_f32 v179, v179\n"
"v_exp_f32 v180, v180\n"
"v_exp_f32 v181, v181\n"
"v_exp_f32 v182, v182\n"
"v_exp_f32 v183, v183\n"
"v_exp_f32 v184, v184\n"
"v_exp_f32 v185, v185\n"
"v_exp_f32 v186, v186\n"
"v_exp_f32 v187, v187\n"
"v_exp_f32 v188, v188\n"
"v_exp_f32 v189, v189\n"
"v_exp_f32 v190, v190\n"
"v_exp_f32 v191, v191\n"
"s_waitcnt vmcnt(3)\n"
"v_smfmac_f32_32x32x32_bf16 v[192:207], v[92:95], v[160:167], v108\n"
"s_waitcnt vmcnt(2)\n"
"v_smfmac_f32_32x32x32_bf16 v[192:207], v[96:99], v[168:175], v108\n"
"v_smfmac_f32_32x32x32_bf16 v[208:223], v[92:95], v[176:183], v108\n"
"v_smfmac_f32_32x32x32_bf16 v[208:223], v[96:99], v[184:191], v108\n"
"v_mfma_f32_32x32x4_2b_bf16 v[160:191], v[54:55], v[52:53], 0 cbsz:1 abid:1\n"
"s_nop 15\n"
"s_nop 1\n"
"v_mfma_f32_32x32x16_bf16 v[160:175], v[72:75], v[36:39], v[160:175]\n"
"v_mfma_f32_32x32x16_bf16 v[176:191], v[72:75], v[40:43], v[176:191]\n"
"s_nop 10\n"
"v_sub_f32 v160, v160, v126\n"
"v_sub_f32 v161, v161, v126\n"
"v_sub_f32 v162, v162, v126\n"
"v_sub_f32 v163, v163, v126\n"
"v_sub_f32 v164, v164, v126\n"
"v_sub_f32 v165, v165, v126\n"
"v_sub_f32 v166, v166, v126\n"
"v_sub_f32 v167, v167, v126\n"
"v_sub_f32 v168, v168, v126\n"
"v_sub_f32 v169, v169, v126\n"
"v_sub_f32 v170, v170, v126\n"
"v_sub_f32 v171, v171, v126\n"
"v_sub_f32 v172, v172, v126\n"
"v_sub_f32 v173, v173, v126\n"
"v_sub_f32 v174, v174, v126\n"
"v_sub_f32 v175, v175, v126\n"
"v_sub_f32 v176, v176, v127\n"
"v_sub_f32 v177, v177, v127\n"
"v_sub_f32 v178, v178, v127\n"
"v_sub_f32 v179, v179, v127\n"
"v_sub_f32 v180, v180, v127\n"
"v_sub_f32 v181, v181, v127\n"
"v_sub_f32 v182, v182, v127\n"
"v_sub_f32 v183, v183, v127\n"
"v_sub_f32 v184, v184, v127\n"
"v_sub_f32 v185, v185, v127\n"
"v_sub_f32 v186, v186, v127\n"
"v_sub_f32 v187, v187, v127\n"
"v_sub_f32 v188, v188, v127\n"
"v_sub_f32 v189, v189, v127\n"
"v_sub_f32 v190, v190, v127\n"
"v_sub_f32 v191, v191, v127\n"
"v_exp_f32 v160, v160\n"
"v_exp_f32 v161, v161\n"
"v_exp_f32 v162, v162\n"
"v_exp_f32 v163, v163\n"
"v_exp_f32 v164, v164\n"
"v_exp_f32 v165, v165\n"
"v_exp_f32 v166, v166\n"
"v_exp_f32 v167, v167\n"
"v_exp_f32 v168, v168\n"
"v_exp_f32 v169, v169\n"
"v_exp_f32 v170, v170\n"
"v_exp_f32 v171, v171\n"
"v_exp_f32 v172, v172\n"
"v_exp_f32 v173, v173\n"
"v_exp_f32 v174, v174\n"
"v_exp_f32 v175, v175\n"
"v_exp_f32 v176, v176\n"
"v_exp_f32 v177, v177\n"
"v_exp_f32 v178, v178\n"
"v_exp_f32 v179, v179\n"
"v_exp_f32 v180, v180\n"
"v_exp_f32 v181, v181\n"
"v_exp_f32 v182, v182\n"
"v_exp_f32 v183, v183\n"
"v_exp_f32 v184, v184\n"
"v_exp_f32 v185, v185\n"
"v_exp_f32 v186, v186\n"
"v_exp_f32 v187, v187\n"
"v_exp_f32 v188, v188\n"
"v_exp_f32 v189, v189\n"
"v_exp_f32 v190, v190\n"
"v_exp_f32 v191, v191\n"
"s_waitcnt vmcnt(1)\n"
"v_smfmac_f32_32x32x32_bf16 v[192:207], v[100:103], v[160:167], v108\n"
"s_waitcnt vmcnt(0)\n"
"v_smfmac_f32_32x32x32_bf16 v[192:207], v[104:107], v[168:175], v108\n"
"v_smfmac_f32_32x32x32_bf16 v[208:223], v[100:103], v[176:183], v108\n"
"v_smfmac_f32_32x32x32_bf16 v[208:223], v[104:107], v[184:191], v108\n"
"s_add_u32 s36, s36, 5120\n"
"s_addc_u32 s37, s37, 0\n"
"s_sub_u32 s47, s47, 1\n"
"s_cmp_lg_u32 s47, 0\n"
"s_cbranch_scc1 33b\n"
"s_nop 15\n"
"v_permlane32_swap_b32 v200, v216\n"
"v_permlane32_swap_b32 v201, v217\n"
"v_permlane32_swap_b32 v202, v218\n"
"v_permlane32_swap_b32 v203, v219\n"
"s_nop 1\n"
"v_rcp_f32 v216, v216\n"
"s_nop 0\n"
"v_mul_f32 v162, 0x3f317218, v216\n"
"v_mul_f32 v164, 0x3f317218, v216\n"
"v_mul_f32 v216, 0x3f317218, v216\n"
"s_mov_b32 s36, 0x3fb8aa3b\n"
"v_pk_mul_f32 v[200:201], v[200:201], v[216:217] op_sel_hi:[1,0]\n"
"v_pk_mul_f32 v[202:203], v[202:203], v[216:217] op_sel_hi:[1,0]\n"
"v_permlane32_swap_b32 v162, v164\n"
"v_cvt_pk_bf16_f32 v184, v200, v201\n"
"v_cvt_pk_bf16_f32 v185, v202, v203\n"
"global_store_dwordx2 v6, v[184:185], s[20:21] offset:2048 sc1\n"
"v_pk_mul_f32 v[192:193], v[192:193], v[162:163] op_sel_hi:[1,0]\n"
"v_pk_mul_f32 v[194:195], v[194:195], v[162:163] op_sel_hi:[1,0]\n"
"v_pk_mul_f32 v[196:197], v[196:197], v[162:163] op_sel_hi:[1,0]\n"
"v_pk_mul_f32 v[198:199], v[198:199], v[162:163] op_sel_hi:[1,0]\n"
"v_cvt_pk_bf16_f32 v204, v192, v193\n"
"v_cvt_pk_bf16_f32 v205, v194, v195\n"
"v_cvt_pk_bf16_f32 v206, v196, v197\n"
"v_cvt_pk_bf16_f32 v207, v198, v199\n"
"global_store_dwordx4 v11, v[204:207], s[20:21] offset:0 sc1\n"
"v_pk_mul_f32 v[208:209], v[208:209], v[164:165] op_sel_hi:[1,0]\n"
"v_pk_mul_f32 v[210:211], v[210:211], v[164:165] op_sel_hi:[1,0]\n"
"v_pk_mul_f32 v[212:213], v[212:213], v[164:165] op_sel_hi:[1,0]\n"
"v_pk_mul_f32 v[214:215], v[214:215], v[164:165] op_sel_hi:[1,0]\n"
"v_cvt_pk_bf16_f32 v220, v208, v209\n"
"v_cvt_pk_bf16_f32 v221, v210, v211\n"
"v_cvt_pk_bf16_f32 v222, v212, v213\n"
"v_cvt_pk_bf16_f32 v223, v214, v215\n"
"global_store_dwordx4 v11, v[220:223], s[20:21] offset:1024 sc1\n"
"s_waitcnt vmcnt(0)\n"
"s_branch 31b\n"
"60:\n"
"9500:\n"
"s_cbranch_execz 9501b\n"
"s_mov_b32 s36, s86\n"
"s_add_u32 s36, s36, 1\n"
"s_cmp_eq_u32 s36, 10\n"
"s_cselect_b32 s36, 0, s36\n"
"s_mul_i32 s37, s36, 245760\n"
"s_add_u32 s40, s82, s37\n"
"s_addc_u32 s41, s83, 0\n"
"s_lshl_b32 s37, s36, 7\n"
"s_add_u32 s38, s80, s37\n"
"s_addc_u32 s39, s81, 0\n"
"v_mov_b32 v56, 0x3f803f80\n"
"v_mov_b32 v57, 0x3f803f80\n"
"v_mov_b32 v58, 0x3f803f80\n"
"v_mov_b32 v59, 0x3f803f80\n"
"s_add_u32 m0, s89, 16384\n"
"s_nop 0\n"
"global_load_lds_dwordx4 v84, s[40:41] offset:0\n"
"s_and_b32 s3, s2, 3\n"
"s_lshl_b32 s3, s3, 3\n"
"s_bfe_u32 s4, s2, 0x10001\n"
"s_lshl_b32 s9, s2, 2\n"
"s_sub_i32 s10, s3, s4\n"
"s_lshr_b32 s14, s2, 3\n"
"s_and_b32 s3, s9, 4\n"
"s_lshr_b32 s18, s2, 5\n"
"s_add_i32 s3, s3, s14\n"
"s_add_i32 s10, s10, s18\n"
"s_and_b32 s3, s3, 32\n"
"s_or_b32 s4, s10, s3\n"
"s_lshl_b32 s3, s2, 1\n"
"s_and_b32 s3, s3, 12\n"
"s_bfe_u32 s7, s2, 0x20003\n"
"s_add_i32 s6, s3, s14\n"
"global_load_lds_dwordx4 v85, s[40:41] offset:1024\n"
"s_add_u32 m0, s88, 8192\n"
"s_and_b32 s3, s2, 4\n"
"s_lshr_b32 s13, s30, 1\n"
"s_sub_i32 s11, s6, 28\n"
"s_or_b32 s8, s7, s3\n"
"s_lshl_b32 s5, s8, 2\n"
"s_and_b32 s22, s30, 1\n"
"s_lshl_b32 s3, s11, 11\n"
"s_lshl_b32 s16, s13, 10\n"
"s_add_i32 s5, s5, s30\n"
"s_add_i32 s3, s3, s16\n"
"s_lshl_b32 s16, s22, 7\n"
"s_lshl_b32 s15, s10, 3\n"
"s_or_b32 s3, s3, s16\n"
"s_lshl_b32 s16, s5, 10\n"
"s_cmp_gt_u32 s4, 31\n"
"s_cselect_b32 s3, s3, s16\n"
"global_load_lds_dwordx4 v76, s[38:39] offset:0\n"
"s_add_u32 s36, s36, 1\n"
"s_cmp_eq_u32 s36, 10\n"
"s_cselect_b32 s36, 0, s36\n"
"s_addk_i32 s6, 0xd4\n"
"s_or_b32 s15, s15, s8\n"
"s_cmp_gt_u32 s4, 31\n"
"s_cselect_b32 s6, s6, s15\n"
"s_add_i32 s8, s15, 0x1fffff38\n"
"s_cmp_gt_u32 s4, 31\n"
"s_cselect_b32 s8, s11, s8\n"
"s_addk_i32 s15, 0xff58\n"
"s_cmp_gt_u32 s4, 31\n"
"s_mul_hi_u32 s16, s10, 0xcccccccd\n"
"s_cselect_b32 s31, s11, s15\n"
"s_lshr_b32 s11, s16, 2\n"
"s_lshr_b32 s15, s16, 3\n"
"s_sub_i32 s11, s10, s11\n"
"s_mul_i32 s15, s15, 10\n"
"s_and_b32 s11, s11, 1\n"
"global_load_lds_dwordx4 v77, s[38:39] offset:1024\n"
"s_mul_i32 s37, s36, 245760\n"
"s_add_u32 s40, s82, s37\n"
"s_addc_u32 s41, s83, 0\n"
"s_sub_i32 s16, s10, s15\n"
"s_or_b32 s17, s11, 10\n"
"s_lshl_b32 s15, s16, 4\n"
"s_cmp_lt_u32 s10, 10\n"
"s_cselect_b64 vcc, -1, 0\n"
"s_movk_i32 s19, 0xa00\n"
"s_and_b64 s[20:21], vcc, exec\n"
"s_cselect_b32 s54, 0x500, s19\n"
"s_mov_b32 s19, 0x58000\n"
"s_cselect_b32 s21, s19, 0x2d8000\n"
"s_lshl_b32 s55, s54, 6\n"
"s_and_b32 s25, s30, 2\n"
"v_mov_b32_e32 v2, 0x3ea52b5b\n"
"s_cmp_lt_u32 s10, 20\n"
"v_cndmask_b32_e32 v2, 1.0, v2, vcc\n"
"s_cselect_b64 vcc, -1, 0\n"
"global_load_lds_dwordx4 v78, s[38:39] offset:2048\n"
"s_xor_b32 s20, s30, 2\n"
"s_lshr_b32 s58, s20, 1\n"
"s_lshl_b32 s19, s13, 3\n"
"s_lshl_b32 s23, s58, 3\n"
"s_sub_i32 s56, s17, s19\n"
"s_lshl_b32 s57, s25, 4\n"
"s_xor_b32 s19, s30, 1\n"
"s_sub_i32 s59, s17, s23\n"
"s_cmp_gt_u32 s20, 1\n"
"s_cselect_b32 s62, s1, s0\n"
"s_xor_b32 s24, s30, 3\n"
"s_lshr_b32 s63, s24, 1\n"
"s_lshl_b32 s23, s63, 3\n"
"s_sub_i32 s64, s17, s23\n"
"s_cmp_gt_u32 s24, 1\n"
"s_cselect_b32 s65, s1, s0\n"
"global_load_lds_dwordx4 v79, s[38:39] offset:3072\n"
"s_add_u32 m0, s88, 12288\n"
"s_and_b32 s17, s9, 28\n"
"s_mul_i32 s9, s5, s54\n"
"s_or_b32 s23, s17, s7\n"
"s_lshr_b32 s7, s2, 6\n"
"s_lshl_b32 s78, s25, 8\n"
"s_lshl_b32 s33, s30, 10\n"
"s_lshl_b32 s34, s30, 8\n"
"s_lshl_b32 s9, s9, 1\n"
"s_add_i32 s17, s17, s7\n"
"s_or_b32 s35, s78, 0x25000\n"
"s_add_i32 s51, s33, 0x24000\n"
"s_add_i32 s52, s34, 0x25000\n"
"s_xor_b32 s53, s78, 0x25200\n"
"s_add_i32 s9, s9, s21\n"
"s_cmp_eq_u32 s11, 0\n"
"s_cselect_b32 s50, -1, 0xffff0000\n"
"global_load_lds_dwordx4 v80, s[38:39] offset:0\n"
"s_lshr_b32 s11, s2, 1\n"
"s_lshl_b32 s21, s22, 1\n"
"s_and_b32 s11, s11, 28\n"
"s_add_i32 s27, s21, s13\n"
"s_add_i32 s27, s27, s11\n"
"s_lshl_b32 s11, s23, 6\n"
"s_lshl_b32 s21, s30, 4\n"
"s_add_i32 s11, s11, s21\n"
"s_lshl_b32 s21, s2, 15\n"
"s_lshl_b32 s26, s30, 13\n"
"s_add_i32 s21, s26, s21\n"
"s_mul_i32 s26, s5, 0x36000\n"
"s_lshl_b32 s28, s10, 13\n"
"s_add_i32 s29, s28, s26\n"
"s_mul_i32 s26, s30, 40\n"
"s_add_i32 s8, s26, s8\n"
"global_load_lds_dwordx4 v81, s[38:39] offset:1024\n"
"s_mul_i32 s26, s11, 0x500\n"
"s_mul_i32 s28, s18, 0xa0\n"
"s_lshl_b32 s5, s5, 5\n"
"s_add_i32 s26, s26, s28\n"
"s_add_i32 s21, s21, 0x13fc000\n"
"s_add_i32 s29, s29, 0x7d8000\n"
"s_addk_i32 s8, 0x400\n"
"s_add_i32 s5, s5, s10\n"
"s_cmp_gt_u32 s4, 26\n"
"s_cselect_b32 s4, s21, s29\n"
"s_cselect_b32 s8, s8, s5\n"
"s_add_u32 s4, s66, s4\n"
"s_addc_u32 s5, s67, 0\n"
"s_lshl_b32 s21, s27, 5\n"
"s_mul_i32 s11, s7, 40\n"
"s_add_i32 s86, s7, 0xfffffee0\n"
"s_waitcnt vmcnt(15)\n"
"s_barrier\n"
"ds_read_b128 a[0:3], v52 offset:0\n"
"ds_read_b64_tr_b16 a[16:17], v36 offset:0\n"
"ds_read_b64_tr_b16 a[18:19], v36 offset:512\n"
"ds_read_b64_tr_b16 a[20:21], v37 offset:0\n"
"ds_read_b64_tr_b16 a[22:23], v37 offset:512\n"
"ds_read_b64_tr_b16 a[24:25], v38 offset:0\n"
"ds_read_b64_tr_b16 a[26:27], v38 offset:512\n"
"ds_read_b64_tr_b16 a[28:29], v39 offset:0\n"
"ds_read_b64_tr_b16 a[30:31], v39 offset:512\n"
"s_waitcnt lgkmcnt(6)\n"
"v_mfma_f32_16x16x32_bf16 v[112:115], a[16:19], v[56:59], 0\n"
"global_load_lds_dwordx4 v82, s[38:39] offset:2048\n"
"ds_read_b128 a[48:51], v53 offset:0\n"
"s_waitcnt lgkmcnt(5)\n"
"v_mfma_f32_16x16x32_bf16 v[116:119], a[20:23], v[56:59], 0\n"
"ds_read_b64_tr_b16 a[64:65], v40 offset:0\n"
"ds_read_b64_tr_b16 a[66:67], v40 offset:512\n"
"v_mfma_f32_16x16x32_bf16 v[128:131], a[0:3], a[16:19], 0\n"
"s_waitcnt vmcnt(13)\n"
"ds_read_b128 a[4:7], v52 offset:2048\n"
"ds_read_b64_tr_b16 a[68:69], v41 offset:0\n"
"v_mfma_f32_16x16x32_bf16 v[144:147], a[0:3], a[20:23], 0\n"
"global_load_lds_dwordx4 v83, s[38:39] offset:3072\n"
"s_lshl_b32 s37, s36, 7\n"
"s_add_u32 s38, s80, s37\n"
"s_addc_u32 s39, s81, 0\n"
"s_add_u32 m0, s89, 32768\n"
"ds_read_b64_tr_b16 a[70:71], v41 offset:512\n"
"s_waitcnt lgkmcnt(8)\n"
"v_mfma_f32_16x16x32_bf16 v[160:163], a[0:3], a[24:27], 0\n"
"ds_read_b64_tr_b16 a[72:73], v42 offset:0\n"
"s_lshl_b32 s87, s23, 5\n"
"s_lshl_b32 s7, s22, 4\n"
"s_add_i32 s21, s21, s17\n"
"s_or_b32 s90, s87, s7\n"
"s_lshl_b32 s7, s21, 4\n"
"s_waitcnt lgkmcnt(7)\n"
"v_mfma_f32_16x16x32_bf16 v[176:179], a[0:3], a[28:31], 0\n"
"ds_read_b64_tr_b16 a[74:75], v42 offset:512\n"
"ds_read_b64_tr_b16 a[76:77], v43 offset:0\n"
"s_waitcnt lgkmcnt(5)\n"
"v_mfma_f32_16x16x32_bf16 v[132:135], a[4:7], a[16:19], 0\n"
"s_waitcnt vmcnt(12)\n"
"ds_read_b128 a[8:11], v52 offset:4096\n"
"ds_read_b64_tr_b16 a[78:79], v43 offset:512\n"
"v_mfma_f32_16x16x32_bf16 v[148:151], a[4:7], a[20:23], 0\n"
"global_load_lds_dwordx4 v84, s[40:41] offset:0\n"
"ds_read_b128 a[52:55], v53 offset:2048\n"
"v_mfma_f32_16x16x32_bf16 v[164:167], a[4:7], a[24:27], 0\n"
"ds_read_b128 a[56:59], v53 offset:4096\n"
"s_add_i32 s91, s7, 0x2f000\n"
"s_lshl_b32 s7, s8, 3\n"
"s_lshl_b32 s2, s2, 3\n"
"s_mul_i32 s29, s30, 13\n"
"s_add_i32 s79, s11, 0x400\n"
"v_mfma_f32_16x16x32_bf16 v[180:183], a[4:7], a[28:31], 0\n"
"s_add_i32 s92, s7, 0xa000\n"
"s_lshl_b32 s93, s6, 7\n"
"s_add_i32 s94, s2, 0x8000\n"
"s_add_i32 s3, s3, 0x50000\n"
"s_or_b32 s95, s22, 52\n"
"s_lshr_b32 s96, s29, 1\n"
"s_waitcnt lgkmcnt(3)\n"
"v_mfma_f32_16x16x32_bf16 v[136:139], a[8:11], a[16:19], 0\n"
"s_waitcnt vmcnt(11)\n"
"ds_read_b128 a[12:15], v52 offset:6144\n"
"ds_read_b128 a[60:63], v53 offset:6144\n"
"v_mfma_f32_16x16x32_bf16 v[152:155], a[8:11], a[20:23], 0\n"
"global_load_lds_dwordx4 v85, s[40:41] offset:1024\n"
"s_add_u32 m0, s88, 16384\n"
"s_add_u32 s2, s66, s3\n"
"s_addc_u32 s3, s67, 0\n"
"s_add_u32 s6, s66, s9\n"
"s_addc_u32 s7, s67, 0\n"
"s_add_i32 s9, s9, s54\n"
"v_mfma_f32_16x16x32_bf16 v[168:171], a[8:11], a[24:27], 0\n"
"s_add_u32 s8, s66, s9\n"
"s_addc_u32 s9, s67, 0\n"
"s_min_u32 s10, s10, 26\n"
"s_lshl_b32 s10, s10, 7\n"
"v_bfe_u32 v3, v0, 4, 2\n"
"v_mfma_f32_16x16x32_bf16 v[184:187], a[8:11], a[28:31], 0\n"
"s_add_u32 s10, s70, s10\n"
"v_lshl_or_b32 v4, s58, 2, v3\n"
"s_addc_u32 s11, s71, 0\n"
"v_lshrrev_b32_e64 v4, v4, s12\n"
"s_waitcnt lgkmcnt(1)\n"
"v_mfma_f32_16x16x32_bf16 v[140:143], a[12:15], a[16:19], 0\n"
"global_load_lds_dwordx4 v76, s[38:39] offset:0\n"
"s_add_u32 s36, s36, 1\n"
"s_cmp_eq_u32 s36, 10\n"
"s_cselect_b32 s36, 0, s36\n"
"s_cmp_gt_u32 s30, 1\n"
"v_lshlrev_b32_e32 v4, 9, v4\n"
"s_cselect_b32 s54, s1, s0\n"
"v_mfma_f32_16x16x32_bf16 v[156:159], a[12:15], a[20:23], 0\n"
"v_and_b32_e32 v4, 0x200, v4\n"
"v_mov_b32_e32 v5, 0x400\n"
"v_mfma_f32_16x16x32_bf16 v[172:175], a[12:15], a[24:27], 0\n"
"v_cmp_gt_u32_e64 s[0:1], s59, v3\n"
"v_lshlrev_b32_e32 v10, 2, v3\n"
"v_mfma_f32_16x16x32_bf16 v[188:191], a[12:15], a[28:31], 0\n"
"global_load_lds_dwordx4 v77, s[38:39] offset:1024\n"
"s_mul_i32 s37, s36, 245760\n"
"s_add_u32 s40, s82, s37\n"
"s_addc_u32 s41, s83, 0\n"
"v_lshl_or_b32 v6, s13, 2, v3\n"
"s_waitcnt vmcnt(12) lgkmcnt(0)\n"
"s_barrier\n"
"v_mfma_f32_16x16x32_bf16 v[128:131], a[48:51], a[64:67], v[128:131]\n"
"s_waitcnt vmcnt(10)\n"
"ds_read_b128 a[0:3], v52 offset:8192\n"
"v_cndmask_b32_e64 v4, v5, v4, s[0:1]\n"
"v_mfma_f32_16x16x32_bf16 v[144:147], a[48:51], a[68:71], v[144:147]\n"
"ds_read_b64_tr_b16 a[16:17], v36 offset:16384\n"
"ds_read_b64_tr_b16 a[18:19], v36 offset:16896\n"
"v_mfma_f32_16x16x32_bf16 v[160:163], a[48:51], a[72:75], v[160:163]\n"
"global_load_lds_dwordx4 v78, s[38:39] offset:2048\n"
"ds_read_b64_tr_b16 a[20:21], v37 offset:16384\n"
"v_mfma_f32_16x16x32_bf16 v[176:179], a[48:51], a[76:79], v[176:179]\n"
"ds_read_b64_tr_b16 a[22:23], v37 offset:16896\n"
"v_cmp_gt_u32_e64 s[0:1], s56, v3\n"
"v_mfma_f32_16x16x32_bf16 v[132:135], a[52:55], a[64:67], v[132:135]\n"
"ds_read_b64_tr_b16 a[24:25], v38 offset:16384\n"
"v_lshrrev_b32_e64 v11, v10, s54\n"
"v_mfma_f32_16x16x32_bf16 v[148:151], a[52:55], a[68:71], v[148:151]\n"
"global_load_lds_dwordx4 v79, s[38:39] offset:3072\n"
"s_add_u32 m0, s88, 20480\n"
"ds_read_b64_tr_b16 a[26:27], v38 offset:16896\n"
"v_mfma_f32_16x16x32_bf16 v[164:167], a[52:55], a[72:75], v[164:167]\n"
"ds_read_b64_tr_b16 a[28:29], v39 offset:16384\n"
"v_lshrrev_b32_e64 v6, v6, s12\n"
"v_mfma_f32_16x16x32_bf16 v[180:183], a[52:55], a[76:79], v[180:183]\n"
"ds_read_b64_tr_b16 a[30:31], v39 offset:16896\n"
"v_cndmask_b32_e64 v8, 4, 0, s[0:1]\n"
"v_mfma_f32_16x16x32_bf16 v[136:139], a[56:59], a[64:67], v[136:139]\n"
"global_load_lds_dwordx4 v80, s[38:39] offset:0\n"
"v_and_or_b32 v11, v11, 15, s15\n"
"v_mfma_f32_16x16x32_bf16 v[152:155], a[56:59], a[68:71], v[152:155]\n"
"s_waitcnt vmcnt(11)\n"
"ds_read_b128 a[4:7], v52 offset:10240\n"
"v_lshlrev_b32_e32 v6, 9, v6\n"
"v_mfma_f32_16x16x32_bf16 v[168:171], a[56:59], a[72:75], v[168:171]\n"
"s_waitcnt vmcnt(9)\n"
"ds_read_b128 a[8:11], v52 offset:12288\n"
"v_or_b32_e32 v8, v8, v3\n"
"v_mfma_f32_16x16x32_bf16 v[184:187], a[56:59], a[76:79], v[184:187]\n"
"global_load_lds_dwordx4 v81, s[38:39] offset:1024\n"
"s_waitcnt vmcnt(8)\n"
"ds_read_b128 a[12:15], v52 offset:14336\n"
"v_mfma_f32_16x16x32_bf16 v[140:143], a[60:63], a[64:67], v[140:143]\n"
"v_mul_lo_u16_e32 v11, 0xcd, v11\n"
"v_and_b32_e32 v6, 0x200, v6\n"
"v_mfma_f32_16x16x32_bf16 v[156:159], a[60:63], a[68:71], v[156:159]\n"
"v_lshlrev_b32_e32 v8, 2, v8\n"
"v_lshrrev_b16_e32 v11, 10, v11\n"
"v_mfma_f32_16x16x32_bf16 v[172:175], a[60:63], a[72:75], v[172:175]\n"
"global_load_lds_dwordx4 v82, s[38:39] offset:2048\n"
"v_and_b32_e32 v13, 15, v0\n"
"v_mfma_f32_16x16x32_bf16 v[188:191], a[60:63], a[76:79], v[188:191]\n"
"v_cndmask_b32_e64 v7, v5, v6, s[0:1]\n"
"v_lshrrev_b32_e64 v8, v8, s54\n"
"s_waitcnt lgkmcnt(0)\n"
"v_mfma_f32_16x16x32_bf16 v[112:115], a[16:19], v[56:59], v[112:115]\n"
"ds_read_b128 a[48:51], v53 offset:8192\n"
"v_mul_u32_u24_e32 v11, s55, v11\n"
"v_mfma_f32_16x16x32_bf16 v[116:119], a[20:23], v[56:59], v[116:119]\n"
"global_load_lds_dwordx4 v83, s[38:39] offset:3072\n"
"s_lshl_b32 s37, s36, 7\n"
"s_add_u32 s38, s80, s37\n"
"s_addc_u32 s39, s81, 0\n"
"s_add_u32 m0, s89, 0\n"
"ds_read_b64_tr_b16 a[64:65], v40 offset:16384\n"
"v_mfma_f32_16x16x32_bf16 v[128:131], a[0:3], a[16:19], v[128:131]\n"
"ds_read_b64_tr_b16 a[66:67], v40 offset:16896\n"
"v_lshlrev_b32_e32 v14, 4, v13\n"
"v_mfma_f32_16x16x32_bf16 v[144:147], a[0:3], a[20:23], v[144:147]\n"
"ds_read_b64_tr_b16 a[68:69], v41 offset:16384\n"
"v_or3_b32 v7, v11, v7, v14\n"
"v_mfma_f32_16x16x32_bf16 v[160:163], a[0:3], a[24:27], v[160:163]\n"
"global_load_lds_dwordx4 v84, s[40:41] offset:0\n"
"ds_read_b64_tr_b16 a[70:71], v41 offset:16896\n"
"v_mfma_f32_16x16x32_bf16 v[176:179], a[0:3], a[28:31], v[176:179]\n"
"ds_read_b64_tr_b16 a[72:73], v42 offset:16384\n"
"ds_read_b64_tr_b16 a[74:75], v42 offset:16896\n"
"v_mfma_f32_16x16x32_bf16 v[132:135], a[4:7], a[16:19], v[132:135]\n"
"ds_read_b64_tr_b16 a[76:77], v43 offset:16384\n"
"v_lshl_or_b32 v11, s16, 6, v13\n"
"v_mfma_f32_16x16x32_bf16 v[148:151], a[4:7], a[20:23], v[148:151]\n"
"global_load_lds_dwordx4 v85, s[40:41] offset:1024\n"
"s_add_u32 m0, s88, 0\n"
"ds_read_b64_tr_b16 a[78:79], v43 offset:16896\n"
"v_mfma_f32_16x16x32_bf16 v[164:167], a[4:7], a[24:27], v[164:167]\n"
"ds_read_b128 a[52:55], v53 offset:10240\n"
"v_and_or_b32 v8, v8, 15, s15\n"
"v_mfma_f32_16x16x32_bf16 v[180:183], a[4:7], a[28:31], v[180:183]\n"
"ds_read_b128 a[56:59], v53 offset:12288\n"
"ds_read_b128 a[60:63], v53 offset:14336\n"
"v_mfma_f32_16x16x32_bf16 v[136:139], a[8:11], a[16:19], v[136:139]\n"
"global_load_lds_dwordx4 v76, s[38:39] offset:0\n"
"s_add_u32 s36, s36, 1\n"
"s_cmp_eq_u32 s36, 10\n"
"s_cselect_b32 s36, 0, s36\n"
"v_lshl_or_b32 v12, s63, 2, v3\n"
"v_mfma_f32_16x16x32_bf16 v[152:155], a[8:11], a[20:23], v[152:155]\n"
"v_or_b32_e32 v15, s57, v11\n"
"v_mul_lo_u16_e32 v8, 0xcd, v8\n"
"v_mfma_f32_16x16x32_bf16 v[168:171], a[8:11], a[24:27], v[168:171]\n"
"v_or_b32_e32 v6, 0x100, v6\n"
"v_lshrrev_b32_e64 v12, v12, s12\n"
"v_mfma_f32_16x16x32_bf16 v[184:187], a[8:11], a[28:31], v[184:187]\n"
"global_load_lds_dwordx4 v77, s[38:39] offset:1024\n"
"s_mul_i32 s37, s36, 245760\n"
"s_add_u32 s40, s82, s37\n"
"s_addc_u32 s41, s83, 0\n"
"v_mul_u32_u24_e32 v16, 0xccd, v15\n"
"s_mov_b32 s12, 0x28000\n"
"v_mfma_f32_16x16x32_bf16 v[140:143], a[12:15], a[16:19], v[140:143]\n"
"v_mov_b32_e32 v18, 20\n"
"v_lshrrev_b16_e32 v8, 10, v8\n"
"v_mfma_f32_16x16x32_bf16 v[156:159], a[12:15], a[20:23], v[156:159]\n"
"v_cndmask_b32_e64 v6, v5, v6, s[0:1]\n"
"v_mov_b32_e32 v9, 0x500\n"
"s_movk_i32 s0, 0x140\n"
"v_mfma_f32_16x16x32_bf16 v[172:175], a[12:15], a[24:27], v[172:175]\n"
"global_load_lds_dwordx4 v78, s[38:39] offset:2048\n"
"v_mul_u32_u24_sdwa v17, v16, s12 dst_sel:DWORD dst_unused:UNUSED_PAD src0_sel:WORD_1 src1_sel:DWORD\n"
"v_mfma_f32_16x16x32_bf16 v[188:191], a[12:15], a[28:31], v[188:191]\n"
"v_mul_lo_u16_sdwa v16, v16, v18 dst_sel:DWORD dst_unused:UNUSED_PAD src0_sel:WORD_1 src1_sel:DWORD\n"
"v_mul_u32_u24_e32 v8, s55, v8\n"
"s_waitcnt vmcnt(13) lgkmcnt(0)\n"
"s_barrier\n"
"v_mfma_f32_16x16x32_bf16 v[128:131], a[48:51], a[64:67], v[128:131]\n"
"s_waitcnt vmcnt(11)\n"
"ds_read_b128 a[0:3], v52 offset:16384\n"
"v_mad_u32_u24 v9, v3, s0, v9\n"
"v_mfma_f32_16x16x32_bf16 v[144:147], a[48:51], a[68:71], v[144:147]\n"
"global_load_lds_dwordx4 v79, s[38:39] offset:3072\n"
"s_add_u32 m0, s88, 4096\n"
"ds_read_b64_tr_b16 a[16:17], v36 offset:32768\n"
"v_mfma_f32_16x16x32_bf16 v[160:163], a[48:51], a[72:75], v[160:163]\n"
"ds_read_b64_tr_b16 a[18:19], v36 offset:33280\n"
"v_sub_u16_e32 v16, v15, v16\n"
"v_mfma_f32_16x16x32_bf16 v[176:179], a[48:51], a[76:79], v[176:179]\n"
"ds_read_b64_tr_b16 a[20:21], v37 offset:32768\n"
"v_or3_b32 v6, v8, v6, v14\n"
"v_mfma_f32_16x16x32_bf16 v[132:135], a[52:55], a[64:67], v[132:135]\n"
"global_load_lds_dwordx4 v80, s[38:39] offset:0\n"
"ds_read_b64_tr_b16 a[22:23], v37 offset:33280\n"
"v_mfma_f32_16x16x32_bf16 v[148:151], a[52:55], a[68:71], v[148:151]\n"
"ds_read_b64_tr_b16 a[24:25], v38 offset:32768\n"
"ds_read_b64_tr_b16 a[26:27], v38 offset:33280\n"
"v_mfma_f32_16x16x32_bf16 v[164:167], a[52:55], a[72:75], v[164:167]\n"
"ds_read_b64_tr_b16 a[28:29], v39 offset:32768\n"
"v_or_b32_e32 v8, 16, v15\n"
"v_mfma_f32_16x16x32_bf16 v[180:183], a[52:55], a[76:79], v[180:183]\n"
"global_load_lds_dwordx4 v81, s[38:39] offset:1024\n"
"ds_read_b64_tr_b16 a[30:31], v39 offset:33280\n"
"v_mfma_f32_16x16x32_bf16 v[136:139], a[56:59], a[64:67], v[136:139]\n"
"v_lshlrev_b16_e32 v16, 4, v16\n"
"v_or_b32_e32 v17, v9, v17\n"
"v_mfma_f32_16x16x32_bf16 v[152:155], a[56:59], a[68:71], v[152:155]\n"
"s_waitcnt vmcnt(12)\n"
"ds_read_b128 a[4:7], v52 offset:18432\n"
"v_mul_u32_u24_e32 v15, 0xccd, v8\n"
"v_mfma_f32_16x16x32_bf16 v[168:171], a[56:59], a[72:75], v[168:171]\n"
"global_load_lds_dwordx4 v82, s[38:39] offset:2048\n"
"s_waitcnt vmcnt(11)\n"
"ds_read_b128 a[8:11], v52 offset:20480\n"
"v_mfma_f32_16x16x32_bf16 v[184:187], a[56:59], a[76:79], v[184:187]\n"
"s_waitcnt vmcnt(9)\n"
"ds_read_b128 a[12:15], v52 offset:22528\n"
"v_add_u32_e32 v16, v17, v16\n"
"v_mfma_f32_16x16x32_bf16 v[140:143], a[60:63], a[64:67], v[140:143]\n"
"v_mul_lo_u16_sdwa v17, v15, v18 dst_sel:DWORD dst_unused:UNUSED_PAD src0_sel:WORD_1 src1_sel:DWORD\n"
"v_sub_u16_e32 v8, v8, v17\n"
"v_mfma_f32_16x16x32_bf16 v[156:159], a[60:63], a[68:71], v[156:159]\n"
"global_load_lds_dwordx4 v83, s[38:39] offset:3072\n"
"s_lshl_b32 s37, s36, 7\n"
"s_add_u32 s38, s80, s37\n"
"s_addc_u32 s39, s81, 0\n"
"s_add_u32 m0, s89, 16384\n"
"v_mul_u32_u24_sdwa v15, v15, s12 dst_sel:DWORD dst_unused:UNUSED_PAD src0_sel:WORD_1 src1_sel:DWORD\n"
"v_mfma_f32_16x16x32_bf16 v[172:175], a[60:63], a[72:75], v[172:175]\n"
"v_lshlrev_b16_e32 v8, 4, v8\n"
"v_or_b32_e32 v15, v9, v15\n"
"v_mfma_f32_16x16x32_bf16 v[188:191], a[60:63], a[76:79], v[188:191]\n"
"v_lshrrev_b32_e64 v10, v10, s62\n"
"v_add_u32_e32 v8, v15, v8\n"
"s_waitcnt lgkmcnt(0)\n"
"v_mfma_f32_16x16x32_bf16 v[112:115], a[16:19], v[56:59], v[112:115]\n"
"global_load_lds_dwordx4 v84, s[40:41] offset:0\n"
"ds_read_b128 a[48:51], v53 offset:16384\n"
"v_mfma_f32_16x16x32_bf16 v[116:119], a[20:23], v[56:59], v[116:119]\n"
"ds_read_b64_tr_b16 a[64:65], v40 offset:32768\n"
"ds_read_b64_tr_b16 a[66:67], v40 offset:33280\n"
"v_mfma_f32_16x16x32_bf16 v[128:131], a[0:3], a[16:19], v[128:131]\n"
"ds_read_b64_tr_b16 a[68:69], v41 offset:32768\n"
"v_cndmask_b32_e32 v61, v8, v6, vcc\n"
"v_mfma_f32_16x16x32_bf16 v[144:147], a[0:3], a[20:23], v[144:147]\n"
"global_load_lds_dwordx4 v85, s[40:41] offset:1024\n"
"s_add_u32 m0, s88, 8192\n"
"ds_read_b64_tr_b16 a[70:71], v41 offset:33280\n"
"v_mfma_f32_16x16x32_bf16 v[160:163], a[0:3], a[24:27], v[160:163]\n"
"ds_read_b64_tr_b16 a[72:73], v42 offset:32768\n"
"v_and_or_b32 v6, v10, 15, s15\n"
"v_mfma_f32_16x16x32_bf16 v[176:179], a[0:3], a[28:31], v[176:179]\n"
"ds_read_b64_tr_b16 a[74:75], v42 offset:33280\n"
"ds_read_b64_tr_b16 a[76:77], v43 offset:32768\n"
"v_mfma_f32_16x16x32_bf16 v[132:135], a[4:7], a[16:19], v[132:135]\n"
"global_load_lds_dwordx4 v76, s[38:39] offset:0\n"
"s_add_u32 s36, s36, 1\n"
"s_cmp_eq_u32 s36, 10\n"
"s_cselect_b32 s36, 0, s36\n"
"ds_read_b64_tr_b16 a[78:79], v43 offset:33280\n"
"v_mfma_f32_16x16x32_bf16 v[148:151], a[4:7], a[20:23], v[148:151]\n"
"ds_read_b128 a[52:55], v53 offset:18432\n"
"v_mul_lo_u16_e32 v6, 0xcd, v6\n"
"v_mfma_f32_16x16x32_bf16 v[164:167], a[4:7], a[24:27], v[164:167]\n"
"ds_read_b128 a[56:59], v53 offset:20480\n"
"v_lshrrev_b16_e32 v6, 10, v6\n"
"v_mfma_f32_16x16x32_bf16 v[180:183], a[4:7], a[28:31], v[180:183]\n"
"global_load_lds_dwordx4 v77, s[38:39] offset:1024\n"
"s_mul_i32 s37, s36, 245760\n"
"s_add_u32 s40, s82, s37\n"
"s_addc_u32 s41, s83, 0\n"
"ds_read_b128 a[60:63], v53 offset:22528\n"
"v_mfma_f32_16x16x32_bf16 v[136:139], a[8:11], a[16:19], v[136:139]\n"
"v_mul_u32_u24_e32 v6, s55, v6\n"
"v_or3_b32 v4, v6, v4, v14\n"
"v_mfma_f32_16x16x32_bf16 v[152:155], a[8:11], a[20:23], v[152:155]\n"
"v_bitop3_b32 v6, s57, 32, v11 bitop3:0x36\n"
"v_cndmask_b32_e32 v60, v16, v7, vcc\n"
"v_mfma_f32_16x16x32_bf16 v[168:171], a[8:11], a[24:27], v[168:171]\n"
"global_load_lds_dwordx4 v78, s[38:39] offset:2048\n"
"v_mul_u32_u24_e32 v7, 0xccd, v6\n"
"v_mfma_f32_16x16x32_bf16 v[184:187], a[8:11], a[28:31], v[184:187]\n"
"v_mul_lo_u16_sdwa v8, v7, v18 dst_sel:DWORD dst_unused:UNUSED_PAD src0_sel:WORD_1 src1_sel:DWORD\n"
"v_sub_u16_e32 v6, v6, v8\n"
"v_mfma_f32_16x16x32_bf16 v[140:143], a[12:15], a[16:19], v[140:143]\n"
"v_mul_u32_u24_sdwa v7, v7, s12 dst_sel:DWORD dst_unused:UNUSED_PAD src0_sel:WORD_1 src1_sel:DWORD\n"
"v_lshlrev_b16_e32 v6, 4, v6\n"
"v_mfma_f32_16x16x32_bf16 v[156:159], a[12:15], a[20:23], v[156:159]\n"
"global_load_lds_dwordx4 v79, s[38:39] offset:3072\n"
"s_add_u32 m0, s88, 12288\n"
"v_or_b32_e32 v7, v9, v7\n"
"v_mfma_f32_16x16x32_bf16 v[172:175], a[12:15], a[24:27], v[172:175]\n"
"v_add_u32_e32 v6, v7, v6\n"
"v_cndmask_b32_e32 v62, v6, v4, vcc\n"
"v_mfma_f32_16x16x32_bf16 v[188:191], a[12:15], a[28:31], v[188:191]\n"
"v_lshlrev_b32_e32 v4, 9, v12\n"
"v_and_b32_e32 v4, 0x200, v4\n"
"s_waitcnt vmcnt(14) lgkmcnt(0)\n"
"s_barrier\n"
"v_mfma_f32_16x16x32_bf16 v[128:131], a[48:51], a[64:67], v[128:131]\n"
"global_load_lds_dwordx4 v80, s[38:39] offset:0\n"
"s_waitcnt vmcnt(13)\n"
"ds_read_b128 a[0:3], v52 offset:0\n"
"v_mfma_f32_16x16x32_bf16 v[144:147], a[48:51], a[68:71], v[144:147]\n"
"ds_read_b64_tr_b16 a[16:17], v36 offset:0\n"
"ds_read_b64_tr_b16 a[18:19], v36 offset:512\n"
"v_mfma_f32_16x16x32_bf16 v[160:163], a[48:51], a[72:75], v[160:163]\n"
"ds_read_b64_tr_b16 a[20:21], v37 offset:0\n"
"v_or_b32_e32 v4, 0x100, v4\n"
"v_mfma_f32_16x16x32_bf16 v[176:179], a[48:51], a[76:79], v[176:179]\n"
"global_load_lds_dwordx4 v81, s[38:39] offset:1024\n"
"ds_read_b64_tr_b16 a[22:23], v37 offset:512\n"
"v_mfma_f32_16x16x32_bf16 v[132:135], a[52:55], a[64:67], v[132:135]\n"
"ds_read_b64_tr_b16 a[24:25], v38 offset:0\n"
"v_cmp_gt_u32_e64 s[0:1], s64, v3\n"
"v_mfma_f32_16x16x32_bf16 v[148:151], a[52:55], a[68:71], v[148:151]\n"
"ds_read_b64_tr_b16 a[26:27], v38 offset:512\n"
"ds_read_b64_tr_b16 a[28:29], v39 offset:0\n"
"v_mfma_f32_16x16x32_bf16 v[164:167], a[52:55], a[72:75], v[164:167]\n"
"global_load_lds_dwordx4 v82, s[38:39] offset:2048\n"
"ds_read_b64_tr_b16 a[30:31], v39 offset:512\n"
"v_mfma_f32_16x16x32_bf16 v[180:183], a[52:55], a[76:79], v[180:183]\n"
"v_mov_b32_e32 v15, s95\n"
"s_cmp_gt_u32 s30, 1\n"
"s_cselect_b32 s58, 0, s50\n"
"v_cndmask_b32_e64 v4, v5, v4, s[0:1]\n"
"v_mfma_f32_16x16x32_bf16 v[136:139], a[56:59], a[64:67], v[136:139]\n"
"v_cndmask_b32_e64 v5, 4, 0, s[0:1]\n"
"v_or_b32_e32 v5, v5, v3\n"
"v_mfma_f32_16x16x32_bf16 v[152:155], a[56:59], a[68:71], v[152:155]\n"
"global_load_lds_dwordx4 v83, s[38:39] offset:3072\n"
"s_lshl_b32 s37, s36, 7\n"
"s_add_u32 s38, s80, s37\n"
"s_addc_u32 s39, s81, 0\n"
"s_add_u32 m0, s89, 32768\n"
"s_waitcnt vmcnt(14)\n"
"ds_read_b128 a[4:7], v52 offset:2048\n"
"v_mfma_f32_16x16x32_bf16 v[168:171], a[56:59], a[72:75], v[168:171]\n"
"s_waitcnt vmcnt(12)\n"
"ds_read_b128 a[8:11], v52 offset:4096\n"
"v_lshlrev_b32_e32 v5, 2, v5\n"
"v_mfma_f32_16x16x32_bf16 v[184:187], a[56:59], a[76:79], v[184:187]\n"
"s_waitcnt vmcnt(10)\n"
"ds_read_b128 a[12:15], v52 offset:6144\n"
"v_lshrrev_b32_e64 v5, v5, s65\n"
"v_mfma_f32_16x16x32_bf16 v[140:143], a[60:63], a[64:67], v[140:143]\n"
"global_load_lds_dwordx4 v84, s[40:41] offset:0\n"
"v_and_or_b32 v5, v5, 15, s15\n"
"v_mfma_f32_16x16x32_bf16 v[156:159], a[60:63], a[68:71], v[156:159]\n"
"v_mul_lo_u16_e32 v5, 0xcd, v5\n"
"v_lshrrev_b16_e32 v5, 10, v5\n"
"v_mfma_f32_16x16x32_bf16 v[172:175], a[60:63], a[72:75], v[172:175]\n"
"v_mul_u32_u24_e32 v5, s55, v5\n"
"v_or3_b32 v4, v5, v4, v14\n"
"v_mfma_f32_16x16x32_bf16 v[188:191], a[60:63], a[76:79], v[188:191]\n"
"global_load_lds_dwordx4 v85, s[40:41] offset:1024\n"
"s_add_u32 m0, s88, 16384\n"
"v_bitop3_b32 v5, s57, 48, v11 bitop3:0x36\n"
"s_waitcnt lgkmcnt(0)\n"
"v_mfma_f32_16x16x32_bf16 v[112:115], a[16:19], v[56:59], v[112:115]\n"
"ds_read_b128 a[48:51], v53 offset:0\n"
"v_mul_u32_u24_e32 v6, 0xccd, v5\n"
"v_mfma_f32_16x16x32_bf16 v[116:119], a[20:23], v[56:59], v[116:119]\n"
"ds_read_b64_tr_b16 a[64:65], v40 offset:0\n"
"ds_read_b64_tr_b16 a[66:67], v40 offset:512\n"
"v_mfma_f32_16x16x32_bf16 v[128:131], a[0:3], a[16:19], v[128:131]\n"
"global_load_lds_dwordx4 v76, s[38:39] offset:0\n"
"s_add_u32 s36, s36, 1\n"
"s_cmp_eq_u32 s36, 10\n"
"s_cselect_b32 s36, 0, s36\n"
"ds_read_b64_tr_b16 a[68:69], v41 offset:0\n"
"v_mfma_f32_16x16x32_bf16 v[144:147], a[0:3], a[20:23], v[144:147]\n"
"ds_read_b64_tr_b16 a[70:71], v41 offset:512\n"
"v_mul_lo_u16_sdwa v7, v6, v18 dst_sel:DWORD dst_unused:UNUSED_PAD src0_sel:WORD_1 src1_sel:DWORD\n"
"v_mfma_f32_16x16x32_bf16 v[160:163], a[0:3], a[24:27], v[160:163]\n"
"ds_read_b64_tr_b16 a[72:73], v42 offset:0\n"
"v_sub_u16_e32 v5, v5, v7\n"
"v_mfma_f32_16x16x32_bf16 v[176:179], a[0:3], a[28:31], v[176:179]\n"
"global_load_lds_dwordx4 v77, s[38:39] offset:1024\n"
"s_mul_i32 s37, s36, 245760\n"
"s_add_u32 s40, s82, s37\n"
"s_addc_u32 s41, s83, 0\n"
"ds_read_b64_tr_b16 a[74:75], v42 offset:512\n"
"v_mfma_f32_16x16x32_bf16 v[132:135], a[4:7], a[16:19], v[132:135]\n"
"ds_read_b64_tr_b16 a[76:77], v43 offset:0\n"
"v_mul_u32_u24_sdwa v6, v6, s12 dst_sel:DWORD dst_unused:UNUSED_PAD src0_sel:WORD_1 src1_sel:DWORD\n"
"v_mfma_f32_16x16x32_bf16 v[148:151], a[4:7], a[20:23], v[148:151]\n"
"ds_read_b64_tr_b16 a[78:79], v43 offset:512\n"
"v_or_b32_e32 v6, v9, v6\n"
"v_mfma_f32_16x16x32_bf16 v[164:167], a[4:7], a[24:27], v[164:167]\n"
"global_load_lds_dwordx4 v78, s[38:39] offset:2048\n"
"ds_read_b128 a[52:55], v53 offset:2048\n"
"v_mfma_f32_16x16x32_bf16 v[180:183], a[4:7], a[28:31], v[180:183]\n"
"ds_read_b128 a[56:59], v53 offset:4096\n"
"ds_read_b128 a[60:63], v53 offset:6144\n"
"v_mfma_f32_16x16x32_bf16 v[136:139], a[8:11], a[16:19], v[136:139]\n"
"v_lshlrev_b16_e32 v5, 4, v5\n"
"v_add_u32_e32 v5, v6, v5\n"
"v_mfma_f32_16x16x32_bf16 v[152:155], a[8:11], a[20:23], v[152:155]\n"
"global_load_lds_dwordx4 v79, s[38:39] offset:3072\n"
"s_add_u32 m0, s88, 20480\n"
"v_cndmask_b32_e32 v63, v5, v4, vcc\n"
"v_mfma_f32_16x16x32_bf16 v[168:171], a[8:11], a[24:27], v[168:171]\n"
"v_lshrrev_b32_e32 v4, 1, v0\n"
"v_and_b32_e32 v5, 3, v0\n"
"v_mfma_f32_16x16x32_bf16 v[184:187], a[8:11], a[28:31], v[184:187]\n"
"v_and_or_b32 v4, v4, 12, v5\n"
"v_bfe_u32 v5, v0, 5, 1\n"
"v_mfma_f32_16x16x32_bf16 v[140:143], a[12:15], a[16:19], v[140:143]\n"
"global_load_lds_dwordx4 v80, s[38:39] offset:0\n"
"v_bfe_u32 v6, v0, 2, 1\n"
"v_mfma_f32_16x16x32_bf16 v[156:159], a[12:15], a[20:23], v[156:159]\n"
"v_lshlrev_b32_e32 v4, 4, v4\n"
"v_lshlrev_b32_e32 v7, 9, v5\n"
"v_mfma_f32_16x16x32_bf16 v[172:175], a[12:15], a[24:27], v[172:175]\n"
"v_lshlrev_b32_e32 v8, 8, v6\n"
"v_or3_b32 v7, v7, v8, v4\n"
"v_mfma_f32_16x16x32_bf16 v[188:191], a[12:15], a[28:31], v[188:191]\n"
"global_load_lds_dwordx4 v81, s[38:39] offset:1024\n"
"v_lshlrev_b32_e32 v6, 3, v6\n"
"s_waitcnt vmcnt(16) lgkmcnt(0)\n"
"s_barrier\n"
"v_mfma_f32_16x16x32_bf16 v[128:131], a[48:51], a[64:67], v[128:131]\n"
"s_waitcnt vmcnt(14)\n"
"ds_read_b128 a[0:3], v52 offset:8192\n"
"v_mul_u32_u24_e32 v8, 0xa00, v5\n"
"v_mfma_f32_16x16x32_bf16 v[144:147], a[48:51], a[68:71], v[144:147]\n"
"ds_read_b64_tr_b16 a[16:17], v36 offset:16384\n"
"ds_read_b64_tr_b16 a[18:19], v36 offset:16896\n"
"v_mfma_f32_16x16x32_bf16 v[160:163], a[48:51], a[72:75], v[160:163]\n"
"global_load_lds_dwordx4 v82, s[38:39] offset:2048\n"
"ds_read_b64_tr_b16 a[20:21], v37 offset:16384\n"
"v_mfma_f32_16x16x32_bf16 v[176:179], a[48:51], a[76:79], v[176:179]\n"
"ds_read_b64_tr_b16 a[22:23], v37 offset:16896\n"
"v_or3_b32 v4, v4, v6, v8\n"
"v_mfma_f32_16x16x32_bf16 v[132:135], a[52:55], a[64:67], v[132:135]\n"
"ds_read_b64_tr_b16 a[24:25], v38 offset:16384\n"
"v_and_b32_e32 v6, 63, v0\n"
"v_mfma_f32_16x16x32_bf16 v[148:151], a[52:55], a[68:71], v[148:151]\n"
"global_load_lds_dwordx4 v83, s[38:39] offset:3072\n"
"s_lshl_b32 s37, s36, 7\n"
"s_add_u32 s38, s80, s37\n"
"s_addc_u32 s39, s81, 0\n"
"s_add_u32 m0, s89, 0\n"
"ds_read_b64_tr_b16 a[26:27], v38 offset:16896\n"
"v_mfma_f32_16x16x32_bf16 v[164:167], a[52:55], a[72:75], v[164:167]\n"
"ds_read_b64_tr_b16 a[28:29], v39 offset:16384\n"
"v_lshlrev_b32_e32 v9, 4, v6\n"
"v_mfma_f32_16x16x32_bf16 v[180:183], a[52:55], a[76:79], v[180:183]\n"
"ds_read_b64_tr_b16 a[30:31], v39 offset:16896\n"
"v_and_b32_e32 v10, 0x100, v9\n"
"v_mfma_f32_16x16x32_bf16 v[136:139], a[56:59], a[64:67], v[136:139]\n"
"global_load_lds_dwordx4 v84, s[40:41] offset:0\n"
"v_bfe_u32 v11, v0, 4, 1\n"
"v_mfma_f32_16x16x32_bf16 v[152:155], a[56:59], a[68:71], v[152:155]\n"
"s_waitcnt vmcnt(15)\n"
"ds_read_b128 a[4:7], v52 offset:10240\n"
"v_or3_b32 v8, v14, v10, v8\n"
"v_mfma_f32_16x16x32_bf16 v[168:171], a[56:59], a[72:75], v[168:171]\n"
"s_waitcnt vmcnt(13)\n"
"ds_read_b128 a[8:11], v52 offset:12288\n"
"v_mul_u32_u24_e32 v10, 0x500, v5\n"
"v_mfma_f32_16x16x32_bf16 v[184:187], a[56:59], a[76:79], v[184:187]\n"
"global_load_lds_dwordx4 v85, s[40:41] offset:1024\n"
"s_add_u32 m0, s88, 0\n"
"s_waitcnt vmcnt(12)\n"
"ds_read_b128 a[12:15], v52 offset:14336\n"
"v_mfma_f32_16x16x32_bf16 v[140:143], a[60:63], a[64:67], v[140:143]\n"
"v_lshlrev_b32_e32 v12, 3, v11\n"
"v_or3_b32 v10, v12, v10, v14\n"
"v_mfma_f32_16x16x32_bf16 v[156:159], a[60:63], a[68:71], v[156:159]\n"
"v_lshlrev_b32_e32 v11, 9, v11\n"
"v_lshlrev_b32_e32 v12, 8, v5\n"
"v_mfma_f32_16x16x32_bf16 v[172:175], a[60:63], a[72:75], v[172:175]\n"
"global_load_lds_dwordx4 v76, s[38:39] offset:0\n"
"s_add_u32 s36, s36, 1\n"
"s_cmp_eq_u32 s36, 10\n"
"s_cselect_b32 s36, 0, s36\n"
"v_or3_b32 v11, v11, v12, v14\n"
"v_mfma_f32_16x16x32_bf16 v[188:191], a[60:63], a[76:79], v[188:191]\n"
"v_lshlrev_b32_e32 v12, 2, v13\n"
"v_mov_b32_e32 v14, 0x25200\n"
"s_waitcnt lgkmcnt(0)\n"
"v_mfma_f32_16x16x32_bf16 v[112:115], a[16:19], v[56:59], v[112:115]\n"
"ds_read_b128 a[48:51], v53 offset:8192\n"
"v_bitop3_b32 v70, s78, v12, v14 bitop3:0xde\n"
"v_mfma_f32_16x16x32_bf16 v[116:119], a[20:23], v[56:59], v[116:119]\n"
"global_load_lds_dwordx4 v77, s[38:39] offset:1024\n"
"s_mul_i32 s37, s36, 245760\n"
"s_add_u32 s40, s82, s37\n"
"s_addc_u32 s41, s83, 0\n"
"ds_read_b64_tr_b16 a[64:65], v40 offset:16384\n"
"v_mfma_f32_16x16x32_bf16 v[128:131], a[0:3], a[16:19], v[128:131]\n"
"ds_read_b64_tr_b16 a[66:67], v40 offset:16896\n"
"v_or_b32_e32 v64, s51, v12\n"
"v_mfma_f32_16x16x32_bf16 v[144:147], a[0:3], a[20:23], v[144:147]\n"
"ds_read_b64_tr_b16 a[68:69], v41 offset:16384\n"
"v_or_b32_e32 v69, s35, v12\n"
"v_mfma_f32_16x16x32_bf16 v[160:163], a[0:3], a[24:27], v[160:163]\n"
"global_load_lds_dwordx4 v78, s[38:39] offset:2048\n"
"ds_read_b64_tr_b16 a[70:71], v41 offset:16896\n"
"v_mfma_f32_16x16x32_bf16 v[176:179], a[0:3], a[28:31], v[176:179]\n"
"ds_read_b64_tr_b16 a[72:73], v42 offset:16384\n"
"ds_read_b64_tr_b16 a[74:75], v42 offset:16896\n"
"v_mfma_f32_16x16x32_bf16 v[132:135], a[4:7], a[16:19], v[132:135]\n"
"ds_read_b64_tr_b16 a[76:77], v43 offset:16384\n"
"v_and_b32_e32 v12, 31, v0\n"
"s_mul_i32 s0, s30, 5\n"
"v_mfma_f32_16x16x32_bf16 v[148:151], a[4:7], a[20:23], v[148:151]\n"
"global_load_lds_dwordx4 v79, s[38:39] offset:3072\n"
"s_add_u32 m0, s88, 4096\n"
"ds_read_b64_tr_b16 a[78:79], v43 offset:16896\n"
"v_mfma_f32_16x16x32_bf16 v[164:167], a[4:7], a[24:27], v[164:167]\n"
"ds_read_b128 a[52:55], v53 offset:10240\n"
"v_min_u32_e32 v14, 26, v12\n"
"v_mfma_f32_16x16x32_bf16 v[180:183], a[4:7], a[28:31], v[180:183]\n"
"ds_read_b128 a[56:59], v53 offset:12288\n"
"ds_read_b128 a[60:63], v53 offset:14336\n"
"v_mfma_f32_16x16x32_bf16 v[136:139], a[8:11], a[16:19], v[136:139]\n"
"global_load_lds_dwordx4 v80, s[38:39] offset:0\n"
"v_add3_u32 v14, s0, 32, v14\n"
"v_mfma_f32_16x16x32_bf16 v[152:155], a[8:11], a[20:23], v[152:155]\n"
"v_cmp_eq_u32_e32 vcc, 21, v12\n"
"v_cmp_gt_u32_e64 s[0:1], 21, v12\n"
"v_mfma_f32_16x16x32_bf16 v[168:171], a[8:11], a[24:27], v[168:171]\n"
"v_mul_u32_u24_e32 v5, 20, v5\n"
"v_cndmask_b32_e32 v14, v14, v15, vcc\n"
"v_mfma_f32_16x16x32_bf16 v[184:187], a[8:11], a[28:31], v[184:187]\n"
"global_load_lds_dwordx4 v81, s[38:39] offset:1024\n"
"v_add_u32_e32 v15, s96, v12\n"
"v_mfma_f32_16x16x32_bf16 v[140:143], a[12:15], a[16:19], v[140:143]\n"
"v_cmp_gt_u32_e32 vcc, 7, v12\n"
"v_mov_b32_e32 v19, 0x3f803f80\n"
"v_mfma_f32_16x16x32_bf16 v[156:159], a[12:15], a[20:23], v[156:159]\n"
"v_add_lshl_u32 v5, v5, v12, 4\n"
"v_cndmask_b32_e32 v15, 26, v15, vcc\n"
"v_mfma_f32_16x16x32_bf16 v[172:175], a[12:15], a[24:27], v[172:175]\n"
"global_load_lds_dwordx4 v82, s[38:39] offset:2048\n"
"v_subrev_co_u32_e32 v16, vcc, 8, v12\n"
"v_mfma_f32_16x16x32_bf16 v[188:191], a[12:15], a[28:31], v[188:191]\n"
"v_add_u32_e32 v16, s29, v16\n"
"v_cndmask_b32_e64 v14, v14, v16, s[0:1]\n"
"s_movk_i32 s0, 0x48\n"
"s_waitcnt vmcnt(17) lgkmcnt(0)\n"
"s_barrier\n"
"v_mfma_f32_16x16x32_bf16 v[128:131], a[48:51], a[64:67], v[128:131]\n"
"s_waitcnt vmcnt(15)\n"
"ds_read_b128 a[0:3], v52 offset:16384\n"
"v_add_u32_e32 v16, 0x1fffffe0, v14\n"
"v_mfma_f32_16x16x32_bf16 v[144:147], a[48:51], a[68:71], v[144:147]\n"
"global_load_lds_dwordx4 v83, s[38:39] offset:3072\n"
"s_lshl_b32 s37, s36, 7\n"
"s_add_u32 s38, s80, s37\n"
"s_addc_u32 s39, s81, 0\n"
"s_add_u32 m0, s89, 16384\n"
"ds_read_b64_tr_b16 a[16:17], v36 offset:32768\n"
"v_mfma_f32_16x16x32_bf16 v[160:163], a[48:51], a[72:75], v[160:163]\n"
"ds_read_b64_tr_b16 a[18:19], v36 offset:33280\n"
"v_lshl_add_u32 v17, v14, 2, s86\n"
"v_mfma_f32_16x16x32_bf16 v[176:179], a[48:51], a[76:79], v[176:179]\n"
"ds_read_b64_tr_b16 a[20:21], v37 offset:32768\n"
"v_cmp_gt_u32_e64 s[0:1], s0, v14\n"
"v_mfma_f32_16x16x32_bf16 v[132:135], a[52:55], a[64:67], v[132:135]\n"
"global_load_lds_dwordx4 v84, s[40:41] offset:0\n"
"ds_read_b64_tr_b16 a[22:23], v37 offset:33280\n"
"v_mfma_f32_16x16x32_bf16 v[148:151], a[52:55], a[68:71], v[148:151]\n"
"ds_read_b64_tr_b16 a[24:25], v38 offset:32768\n"
"ds_read_b64_tr_b16 a[26:27], v38 offset:33280\n"
"v_mfma_f32_16x16x32_bf16 v[164:167], a[52:55], a[72:75], v[164:167]\n"
"ds_read_b64_tr_b16 a[28:29], v39 offset:32768\n"
"v_add_u32_e32 v15, s87, v15\n"
"s_mul_i32 s16, s18, 54\n"
"v_mfma_f32_16x16x32_bf16 v[180:183], a[52:55], a[76:79], v[180:183]\n"
"global_load_lds_dwordx4 v85, s[40:41] offset:1024\n"
"s_add_u32 m0, s88, 8192\n"
"ds_read_b64_tr_b16 a[30:31], v39 offset:33280\n"
"v_mfma_f32_16x16x32_bf16 v[136:139], a[56:59], a[64:67], v[136:139]\n"
"v_cndmask_b32_e64 v16, v17, v16, s[0:1]\n"
"v_and_b32_e32 v17, 15, v14\n"
"v_mfma_f32_16x16x32_bf16 v[152:155], a[56:59], a[68:71], v[152:155]\n"
"s_waitcnt vmcnt(16)\n"
"ds_read_b128 a[4:7], v52 offset:18432\n"
"v_cmp_gt_u32_e64 s[0:1], 48, v14\n"
"v_mfma_f32_16x16x32_bf16 v[168:171], a[56:59], a[72:75], v[168:171]\n"
"global_load_lds_dwordx4 v76, s[38:39] offset:0\n"
"s_add_u32 s36, s36, 1\n"
"s_cmp_eq_u32 s36, 10\n"
"s_cselect_b32 s36, 0, s36\n"
"s_waitcnt vmcnt(15)\n"
"ds_read_b128 a[8:11], v52 offset:20480\n"
"v_mfma_f32_16x16x32_bf16 v[184:187], a[56:59], a[76:79], v[184:187]\n"
"s_waitcnt vmcnt(13)\n"
"ds_read_b128 a[12:15], v52 offset:22528\n"
"v_lshl_add_u32 v24, s30, 9, v8\n"
"v_mfma_f32_16x16x32_bf16 v[140:143], a[60:63], a[64:67], v[140:143]\n"
"v_lshl_add_u32 v8, s19, 9, v8\n"
"v_cndmask_b32_e64 v14, v16, v17, s[0:1]\n"
"v_mfma_f32_16x16x32_bf16 v[156:159], a[60:63], a[68:71], v[156:159]\n"
"global_load_lds_dwordx4 v77, s[38:39] offset:1024\n"
"s_mul_i32 s37, s36, 245760\n"
"s_add_u32 s40, s82, s37\n"
"s_addc_u32 s41, s83, 0\n"
"v_add_u32_e32 v14, s79, v14\n"
"v_mfma_f32_16x16x32_bf16 v[172:175], a[60:63], a[72:75], v[172:175]\n"
"v_cndmask_b32_e32 v14, v14, v15, vcc\n"
"v_or_b32_e32 v15, s90, v13\n"
"v_mfma_f32_16x16x32_bf16 v[188:191], a[60:63], a[76:79], v[188:191]\n"
"v_mov_b32_e32 v16, 0x2f000\n"
"v_lshl_add_u32 v15, v15, 4, v16\n"
"s_waitcnt lgkmcnt(0)\n"
"v_mfma_f32_16x16x32_bf16 v[112:115], a[16:19], v[56:59], v[112:115]\n"
"global_load_lds_dwordx4 v78, s[38:39] offset:2048\n"
"ds_read_b128 a[48:51], v53 offset:16384\n"
"v_mfma_f32_16x16x32_bf16 v[116:119], a[20:23], v[56:59], v[116:119]\n"
"ds_read_b64_tr_b16 a[64:65], v40 offset:32768\n"
"ds_read_b64_tr_b16 a[66:67], v40 offset:33280\n"
"v_mfma_f32_16x16x32_bf16 v[128:131], a[0:3], a[16:19], v[128:131]\n"
"ds_read_b64_tr_b16 a[68:69], v41 offset:32768\n"
"v_mov_b32_e32 v16, 0xa000\n"
"v_mfma_f32_16x16x32_bf16 v[144:147], a[0:3], a[20:23], v[144:147]\n"
"global_load_lds_dwordx4 v79, s[38:39] offset:3072\n"
"s_add_u32 m0, s88, 12288\n"
"ds_read_b64_tr_b16 a[70:71], v41 offset:33280\n"
"v_mfma_f32_16x16x32_bf16 v[160:163], a[0:3], a[24:27], v[160:163]\n"
"ds_read_b64_tr_b16 a[72:73], v42 offset:32768\n"
"v_lshl_add_u32 v14, v14, 3, v16\n"
"v_mfma_f32_16x16x32_bf16 v[176:179], a[0:3], a[28:31], v[176:179]\n"
"ds_read_b64_tr_b16 a[74:75], v42 offset:33280\n"
"ds_read_b64_tr_b16 a[76:77], v43 offset:32768\n"
"v_mfma_f32_16x16x32_bf16 v[132:135], a[4:7], a[16:19], v[132:135]\n"
"global_load_lds_dwordx4 v80, s[38:39] offset:0\n"
"ds_read_b64_tr_b16 a[78:79], v43 offset:33280\n"
"v_mfma_f32_16x16x32_bf16 v[148:151], a[4:7], a[20:23], v[148:151]\n"
"ds_read_b128 a[52:55], v53 offset:18432\n"
"v_cmp_gt_u32_e32 vcc, 32, v6\n"
"v_mfma_f32_16x16x32_bf16 v[164:167], a[4:7], a[24:27], v[164:167]\n"
"ds_read_b128 a[56:59], v53 offset:20480\n"
"v_mov_b32_e32 v16, s94\n"
"v_mfma_f32_16x16x32_bf16 v[180:183], a[4:7], a[28:31], v[180:183]\n"
"global_load_lds_dwordx4 v81, s[38:39] offset:1024\n"
"ds_read_b128 a[60:63], v53 offset:22528\n"
"v_mfma_f32_16x16x32_bf16 v[136:139], a[8:11], a[16:19], v[136:139]\n"
"v_mov_b32_e32 v17, s93\n"
"v_cndmask_b32_e32 v14, v14, v15, vcc\n"
"v_mfma_f32_16x16x32_bf16 v[152:155], a[8:11], a[20:23], v[152:155]\n"
"v_and_b32_e32 v15, 1, v0\n"
"v_cmp_eq_u32_e32 vcc, 0, v15\n"
"s_mul_i32 s1, s19, 0x3c00\n"
"s_mul_i32 s18, s18, 0x19000\n"
"v_mfma_f32_16x16x32_bf16 v[168:171], a[8:11], a[24:27], v[168:171]\n"
"global_load_lds_dwordx4 v82, s[38:39] offset:2048\n"
"v_cndmask_b32_e32 v15, v16, v17, vcc\n"
"v_mfma_f32_16x16x32_bf16 v[184:187], a[8:11], a[28:31], v[184:187]\n"
"v_mov_b32_e32 v16, s92\n"
"v_mov_b32_e32 v17, s91\n"
"v_mfma_f32_16x16x32_bf16 v[140:143], a[12:15], a[16:19], v[140:143]\n"
"v_cndmask_b32_e32 v16, v16, v17, vcc\n"
"v_and_b32_e32 v17, 2, v0\n"
"v_mfma_f32_16x16x32_bf16 v[156:159], a[12:15], a[20:23], v[156:159]\n"
"global_load_lds_dwordx4 v83, s[38:39] offset:3072\n"
"s_lshl_b32 s37, s36, 7\n"
"s_add_u32 s38, s80, s37\n"
"s_addc_u32 s39, s81, 0\n"
"s_add_u32 m0, s89, 32768\n"
"v_cmp_eq_u32_e32 vcc, 0, v17\n"
"v_mfma_f32_16x16x32_bf16 v[172:175], a[12:15], a[24:27], v[172:175]\n"
"v_lshlrev_b32_e32 v17, 3, v3\n"
"v_mul_u32_u24_e32 v3, 0x1400, v3\n"
"v_mfma_f32_16x16x32_bf16 v[188:191], a[12:15], a[28:31], v[188:191]\n"
"v_cndmask_b32_e32 v15, v15, v16, vcc\n"
"v_mov_b32_e32 v16, 0x8000\n"
"s_waitcnt vmcnt(18) lgkmcnt(0)\n"
"s_barrier\n"
"v_lshl_add_u32 v16, v0, 3, v16\n"
"v_and_b32_e32 v0, 48, v0\n"
"v_or_b32_e32 v67, s35, v0\n"
"s_cmp_gt_u32 s30, 1\n"
"s_cselect_b32 s35, s50, 0\n"
"s_lshl_b32 s0, s14, 10\n"
"v_cmp_eq_u32_e32 vcc, 20, v12\n"
"s_add_i32 s34, s0, s34\n"
"s_mul_i32 s0, s30, 0x3c00\n"
"v_cndmask_b32_e32 v12, 0, v19, vcc\n"
"v_or_b32_e32 v19, 0x19000, v9\n"
"v_add_u32_e32 v20, s0, v19\n"
"s_mul_i32 s0, s22, 0x2800\n"
"v_or_b32_e32 v23, s0, v9\n"
"s_lshl_b32 s0, s17, 5\n"
"s_add_i32 s0, s27, s0\n"
"s_mulk_i32 s0, 0xa00\n"
"s_lshl_b32 s50, s13, 12\n"
"s_add_i32 s0, s0, 0x58000\n"
"s_add_u32 s0, s66, s0\n"
"v_add_u32_e32 v22, s1, v19\n"
"s_addc_u32 s1, s67, 0\n"
"s_lshl_b32 s12, s17, 1\n"
"s_add_i32 s12, s12, s13\n"
"s_mul_i32 s12, s12, 0x14000\n"
"s_add_i32 s12, s12, 0x2d8a00\n"
"s_add_u32 s12, s66, s12\n"
"s_mul_i32 s14, s23, 54\n"
"s_addc_u32 s13, s67, 0\n"
"s_add_i32 s14, s14, s29\n"
"s_lshl_b32 s14, s14, 12\n"
"s_add_i32 s14, s14, 0x7d8000\n"
"s_add_u32 s14, s66, s14\n"
"s_addc_u32 s15, s67, 0\n"
"s_add_i32 s16, s16, s29\n"
"v_lshl_or_b32 v3, v13, 3, v3\n"
"s_mulk_i32 s16, 0x1400\n"
"v_add_u32_e32 v3, 0x800, v3\n"
"s_add_i32 s16, s16, 0x1118800\n"
"v_lshl_add_u32 v25, s30, 7, v3\n"
"v_lshl_add_u32 v3, s19, 7, v3\n"
"s_add_u32 s16, s66, s16\n"
"s_mul_i32 s19, s30, 0x5000\n"
"s_addc_u32 s17, s67, 0\n"
"s_add_i32 s18, s19, s18\n"
"s_mulk_i32 s20, 0x3c00\n"
"s_add_i32 s18, s18, 0x1334000\n"
"v_add_u32_e32 v21, s20, v19\n"
"s_add_u32 s18, s66, s18\n"
"s_mul_i32 s20, s21, 0xa00\n"
"s_addc_u32 s19, s67, 0\n"
"s_add_i32 s20, s20, 0xe98000\n"
"s_add_u32 s20, s66, s20\n"
"s_addc_u32 s21, s67, 0\n"
"s_lshl_b32 s23, s23, 1\n"
"v_or_b32_e32 v65, s51, v0\n"
"s_mul_i32 s51, s22, 0xa000\n"
"s_or_b32 s22, s23, s22\n"
"s_mul_i32 s22, s22, 0xa000\n"
"s_add_u32 s22, s66, s22\n"
"s_addc_u32 s23, s67, 0\n"
"s_add_u32 s22, s22, 0xe98a00\n"
"s_mulk_i32 s24, 0x3c00\n"
"s_addc_u32 s23, s23, 0\n"
"v_add_u32_e32 v19, s24, v19\n"
"s_add_u32 s24, s60, s26\n"
"v_lshl_or_b32 v71, s25, 5, v17\n"
"s_addc_u32 s25, s61, 0\n"
"s_add_u32 s26, s76, s26\n"
"s_addc_u32 s27, s77, 0\n"
"v_or_b32_e32 v66, s52, v0\n"
"v_or_b32_e32 v68, s53, v0\n"
"v_mov_b32_e32 v18, 0x1000\n"
"v_mul_u32_u24_e32 v13, 0x500, v13\n"
"v_or_b32_e32 v17, 0x80, v17\n"
"s_add_u32 s28, s74, s28\n"
"v_bitop3_b32 v18, s50, v18, v9 bitop3:0x36\n"
"v_or_b32_e32 v23, 0x14000, v23\n"
"v_lshlrev_b32_e32 v6, 3, v6\n"
"v_or_b32_e32 v26, v13, v0\n"
"v_or_b32_e32 v13, v17, v13\n"
"s_addc_u32 s29, s75, 0\n"
"v_or_b32_e32 v27, s50, v9\n"
"v_or_b32_e32 v28, s33, v9\n"
"v_or_b32_e32 v29, s51, v9\n"
"v_xor_b32 v72, 64, v71\n"
"v_lshrrev_b32 v206, 4, v9\n"
"s_cmp_eq_u32 s30, 0\n"
"s_cselect_b64 s[50:51], 4, 0\n"
"s_mov_b32 s52, s31\n"
"v_add_u32 v208, 0, v206\n"
"v_mul_u32_u24 v209, 205, v208\n"
"v_lshrrev_b32 v209, 14, v209\n"
"v_mul_u32_u24 v210, 80, v209\n"
"v_sub_u32 v208, v208, v210\n"
"v_lshlrev_b32 v209, 2, v209\n"
"v_xor_b32 v208, v208, v209\n"
"v_add_lshl_u32 v192, v208, v210, 4\n"
"v_add_u32 v208, 64, v206\n"
"v_mul_u32_u24 v209, 205, v208\n"
"v_lshrrev_b32 v209, 14, v209\n"
"v_mul_u32_u24 v210, 80, v209\n"
"v_sub_u32 v208, v208, v210\n"
"v_lshlrev_b32 v209, 2, v209\n"
"v_xor_b32 v208, v208, v209\n"
"v_add_lshl_u32 v193, v208, v210, 4\n"
"v_add_u32 v208, 128, v206\n"
"v_mul_u32_u24 v209, 205, v208\n"
"v_lshrrev_b32 v209, 14, v209\n"
"v_mul_u32_u24 v210, 80, v209\n"
"v_sub_u32 v208, v208, v210\n"
"v_lshlrev_b32 v209, 2, v209\n"
"v_xor_b32 v208, v208, v209\n"
"v_add_lshl_u32 v194, v208, v210, 4\n"
"v_add_u32 v208, 192, v206\n"
"v_mul_u32_u24 v209, 205, v208\n"
"v_lshrrev_b32 v209, 14, v209\n"
"v_mul_u32_u24 v210, 80, v209\n"
"v_sub_u32 v208, v208, v210\n"
"v_lshlrev_b32 v209, 2, v209\n"
"v_xor_b32 v208, v208, v209\n"
"v_add_lshl_u32 v195, v208, v210, 4\n"
"v_add_u32 v208, 256, v206\n"
"v_mul_u32_u24 v209, 205, v208\n"
"v_lshrrev_b32 v209, 14, v209\n"
"v_mul_u32_u24 v210, 80, v209\n"
"v_sub_u32 v208, v208, v210\n"
"v_lshlrev_b32 v209, 2, v209\n"
"v_xor_b32 v208, v208, v209\n"
"v_add_lshl_u32 v196, v208, v210, 4\n"
"v_lshrrev_b32 v208, 2, v206\n"
"v_mul_u32_u24 v208, 1280, v208\n"
"v_and_b32 v209, 12, v206\n"
"v_and_b32 v210, 3, v206\n"
"v_bfe_u32 v211, v206, 1, 1\n"
"v_and_b32 v212, 1, v206\n"
"v_lshl_add_u32 v212, v212, 3, v208\n"
"s_mul_i32 s46, s30, 20\n"
"v_add3_u32 v200, v210, s46, 0\n"
"v_xor_b32 v200, v200, v209\n"
"v_lshl_add_u32 v200, v200, 4, v208\n"
"v_add3_u32 v201, v210, s46, 4\n"
"v_xor_b32 v201, v201, v209\n"
"v_lshl_add_u32 v201, v201, 4, v208\n"
"v_add3_u32 v202, v211, s46, 8\n"
"v_xor_b32 v202, v202, v209\n"
"v_lshl_add_u32 v202, v202, 4, v212\n"
"v_add3_u32 v203, v210, s46, 10\n"
"v_xor_b32 v203, v203, v209\n"
"v_lshl_add_u32 v203, v203, 4, v208\n"
"v_add3_u32 v204, v210, s46, 14\n"
"v_xor_b32 v204, v204, v209\n"
"v_lshl_add_u32 v204, v204, 4, v208\n"
"v_add3_u32 v205, v211, s46, 18\n"
"v_xor_b32 v205, v205, v209\n"
"v_lshl_add_u32 v205, v205, 4, v212\n"
"s_add_u32 s53, s52, 0\n"
"s_lshr_b32 s46, s30, 1\n"
"s_and_b32 s47, s30, 1\n"
"s_mul_i32 s44, s46, 20\n"
"s_mul_i32 s45, s47, 80\n"
"s_add_u32 s44, s44, s45\n"
"s_mul_i32 s45, s53, 160\n"
"s_add_u32 s44, s44, s45\n"
"s_sub_u32 s44, s44, 11184\n"
"s_mul_i32 s46, s46, 8\n"
"s_mul_i32 s47, s47, 20\n"
"s_add_u32 s46, s46, s47\n"
"s_mul_i32 s47, s53, 40\n"
"s_add_u32 s46, s46, s47\n"
"s_sub_u32 s46, s46, 2160\n"
"s_cmp_lt_u32 s53, 70\n"
"s_cselect_b32 s44, s46, s44\n"
"s_cselect_b32 s47, 4, 40\n"
"s_lshl2_add_u32 s46, s53, s30\n"
"s_lshl_b32 s46, s46, 3\n"
"s_cmp_lt_u32 s53, 54\n"
"s_cselect_b32 s46, s46, s44\n"
"s_cselect_b64 s[44:45], s[72:73], s[68:69]\n"
"s_mul_i32 s46, s46, 1280\n"
"s_mul_i32 s47, s47, 1280\n"
"s_add_u32 s44, s44, s46\n"
"s_addc_u32 s45, s45, 0\n"
"s_add_u32 s48, s44, s47\n"
"s_addc_u32 s49, s45, 0\n"
"s_mul_i32 s46, s30, 10240\n"
"v_add_u32 v208, s46, v9\n"
"v_mov_b32 v110, 1\n"
"v_mfma_f32_16x16x32_bf16 v[128:131], a[48:51], a[64:67], v[128:131]\n"
"global_load_lds_dwordx4 v84, s[40:41] offset:0\n"
"s_waitcnt vmcnt(17)\n"
"ds_read_b128 a[0:3], v52 offset:0\n"
"v_mfma_f32_16x16x32_bf16 v[144:147], a[48:51], a[68:71], v[144:147]\n"
"ds_read_b64_tr_b16 a[16:17], v36 offset:0\n"
"ds_read_b64_tr_b16 a[18:19], v36 offset:512\n"
"v_mfma_f32_16x16x32_bf16 v[160:163], a[48:51], a[72:75], v[160:163]\n"
"ds_read_b64_tr_b16 a[20:21], v37 offset:0\n"
"v_mfma_f32_16x16x32_bf16 v[176:179], a[48:51], a[76:79], v[176:179]\n"
"global_load_lds_dwordx4 v85, s[40:41] offset:1024\n"
"s_add_u32 m0, s88, 16384\n"
"ds_read_b64_tr_b16 a[22:23], v37 offset:512\n"
"v_mfma_f32_16x16x32_bf16 v[132:135], a[52:55], a[64:67], v[132:135]\n"
"ds_read_b64_tr_b16 a[24:25], v38 offset:0\n"
"v_mfma_f32_16x16x32_bf16 v[148:151], a[52:55], a[68:71], v[148:151]\n"
"ds_read_b64_tr_b16 a[26:27], v38 offset:512\n"
"ds_read_b64_tr_b16 a[28:29], v39 offset:0\n"
"v_mfma_f32_16x16x32_bf16 v[164:167], a[52:55], a[72:75], v[164:167]\n"
"global_load_lds_dwordx4 v76, s[38:39] offset:0\n"
"s_add_u32 s36, s36, 1\n"
"s_cmp_eq_u32 s36, 10\n"
"s_cselect_b32 s36, 0, s36\n"
"ds_read_b64_tr_b16 a[30:31], v39 offset:512\n"
"v_mfma_f32_16x16x32_bf16 v[180:183], a[52:55], a[76:79], v[180:183]\n"
"v_mfma_f32_16x16x32_bf16 v[136:139], a[56:59], a[64:67], v[136:139]\n"
"v_mfma_f32_16x16x32_bf16 v[152:155], a[56:59], a[68:71], v[152:155]\n"
"global_load_lds_dwordx4 v77, s[38:39] offset:1024\n"
"s_mul_i32 s37, s36, 245760\n"
"s_add_u32 s40, s82, s37\n"
"s_addc_u32 s41, s83, 0\n"
"s_waitcnt vmcnt(18)\n"
"ds_read_b128 a[4:7], v52 offset:2048\n"
"v_mfma_f32_16x16x32_bf16 v[168:171], a[56:59], a[72:75], v[168:171]\n"
"s_waitcnt vmcnt(16)\n"
"ds_read_b128 a[8:11], v52 offset:4096\n"
"v_mfma_f32_16x16x32_bf16 v[184:187], a[56:59], a[76:79], v[184:187]\n"
"s_waitcnt vmcnt(14)\n"
"ds_read_b128 a[12:15], v52 offset:6144\n"
"v_mfma_f32_16x16x32_bf16 v[140:143], a[60:63], a[64:67], v[140:143]\n"
"global_load_lds_dwordx4 v78, s[38:39] offset:2048\n"
"v_mfma_f32_16x16x32_bf16 v[156:159], a[60:63], a[68:71], v[156:159]\n"
"v_mfma_f32_16x16x32_bf16 v[172:175], a[60:63], a[72:75], v[172:175]\n"
"v_mfma_f32_16x16x32_bf16 v[188:191], a[60:63], a[76:79], v[188:191]\n"
"global_load_lds_dwordx4 v79, s[38:39] offset:3072\n"
"s_add_u32 m0, s88, 20480\n"
"s_waitcnt lgkmcnt(0)\n"
"v_mfma_f32_16x16x32_bf16 v[112:115], a[16:19], v[56:59], v[112:115]\n"
"ds_read_b128 a[48:51], v53 offset:0\n"
"v_mfma_f32_16x16x32_bf16 v[116:119], a[20:23], v[56:59], v[116:119]\n"
"s_mov_b32 exec_lo, 0\n"
"global_load_dword v109, v1, s[66:67] offset:0 sc1\n"
"s_mov_b32 exec_lo, -1\n"
"ds_read_b64_tr_b16 a[64:65], v40 offset:0\n"
"v_mfma_f32_16x16x32_bf16 v[128:131], a[0:3], a[16:19], v[128:131]\n"
"global_load_lds_dwordx4 v80, s[38:39] offset:0\n"
"ds_read_b64_tr_b16 a[66:67], v40 offset:512\n"
"v_mfma_f32_16x16x32_bf16 v[144:147], a[0:3], a[20:23], v[144:147]\n"
"ds_read_b64_tr_b16 a[68:69], v41 offset:0\n"
"v_mfma_f32_16x16x32_bf16 v[160:163], a[0:3], a[24:27], v[160:163]\n"
"ds_read_b64_tr_b16 a[70:71], v41 offset:512\n"
"v_mfma_f32_16x16x32_bf16 v[176:179], a[0:3], a[28:31], v[176:179]\n"
"global_load_lds_dwordx4 v81, s[38:39] offset:1024\n"
"ds_read_b64_tr_b16 a[72:73], v42 offset:0\n"
"v_mfma_f32_16x16x32_bf16 v[132:135], a[4:7], a[16:19], v[132:135]\n"
"ds_read_b64_tr_b16 a[74:75], v42 offset:512\n"
"v_mfma_f32_16x16x32_bf16 v[148:151], a[4:7], a[20:23], v[148:151]\n"
"ds_read_b64_tr_b16 a[76:77], v43 offset:0\n"
"v_mfma_f32_16x16x32_bf16 v[164:167], a[4:7], a[24:27], v[164:167]\n"
"global_load_lds_dwordx4 v82, s[38:39] offset:2048\n"
"ds_read_b64_tr_b16 a[78:79], v43 offset:512\n"
"v_mfma_f32_16x16x32_bf16 v[180:183], a[4:7], a[28:31], v[180:183]\n"
"ds_read_b128 a[52:55], v53 offset:2048\n"
"ds_read_b128 a[56:59], v53 offset:4096\n"
"v_mfma_f32_16x16x32_bf16 v[136:139], a[8:11], a[16:19], v[136:139]\n"
"ds_read_b128 a[60:63], v53 offset:6144\n"
"v_mfma_f32_16x16x32_bf16 v[152:155], a[8:11], a[20:23], v[152:155]\n"
"global_load_lds_dwordx4 v83, s[38:39] offset:3072\n"
"s_lshl_b32 s37, s36, 7\n"
"s_add_u32 s38, s80, s37\n"
"s_addc_u32 s39, s81, 0\n"
"s_add_u32 m0, s89, 0\n"
"v_mfma_f32_16x16x32_bf16 v[168:171], a[8:11], a[24:27], v[168:171]\n"
"v_mfma_f32_16x16x32_bf16 v[184:187], a[8:11], a[28:31], v[184:187]\n"
"v_mfma_f32_16x16x32_bf16 v[140:143], a[12:15], a[16:19], v[140:143]\n"
"v_mfma_f32_16x16x32_bf16 v[156:159], a[12:15], a[20:23], v[156:159]\n"
"v_mfma_f32_16x16x32_bf16 v[172:175], a[12:15], a[24:27], v[172:175]\n"
"v_mfma_f32_16x16x32_bf16 v[188:191], a[12:15], a[28:31], v[188:191]\n"
"s_waitcnt vmcnt(19) lgkmcnt(0)\n"
"s_barrier\n"
"v_mfma_f32_16x16x32_bf16 v[128:131], a[48:51], a[64:67], v[128:131]\n"
"global_load_lds_dwordx4 v84, s[40:41] offset:0\n"
"s_waitcnt vmcnt(18)\n"
"ds_read_b128 a[0:3], v52 offset:8192\n"
"v_mfma_f32_16x16x32_bf16 v[144:147], a[48:51], a[68:71], v[144:147]\n"
"ds_read_b64_tr_b16 a[16:17], v36 offset:16384\n"
"ds_read_b64_tr_b16 a[18:19], v36 offset:16896\n"
"v_mfma_f32_16x16x32_bf16 v[160:163], a[48:51], a[72:75], v[160:163]\n"
"ds_read_b64_tr_b16 a[20:21], v37 offset:16384\n"
"v_mfma_f32_16x16x32_bf16 v[176:179], a[48:51], a[76:79], v[176:179]\n"
"global_load_lds_dwordx4 v85, s[40:41] offset:1024\n"
"s_add_u32 m0, s88, 0\n"
"ds_read_b64_tr_b16 a[22:23], v37 offset:16896\n"
"v_mfma_f32_16x16x32_bf16 v[132:135], a[52:55], a[64:67], v[132:135]\n"
"ds_read_b64_tr_b16 a[24:25], v38 offset:16384\n"
"v_mfma_f32_16x16x32_bf16 v[148:151], a[52:55], a[68:71], v[148:151]\n"
"ds_read_b64_tr_b16 a[26:27], v38 offset:16896\n"
"ds_read_b64_tr_b16 a[28:29], v39 offset:16384\n"
"v_mfma_f32_16x16x32_bf16 v[164:167], a[52:55], a[72:75], v[164:167]\n"
"global_load_lds_dwordx4 v76, s[38:39] offset:0\n"
"ds_read_b64_tr_b16 a[30:31], v39 offset:16896\n"
"v_mfma_f32_16x16x32_bf16 v[180:183], a[52:55], a[76:79], v[180:183]\n"
"v_mfma_f32_16x16x32_bf16 v[136:139], a[56:59], a[64:67], v[136:139]\n"
"v_mfma_f32_16x16x32_bf16 v[152:155], a[56:59], a[68:71], v[152:155]\n"
"global_load_lds_dwordx4 v77, s[38:39] offset:1024\n"
"s_waitcnt vmcnt(19)\n"
"ds_read_b128 a[4:7], v52 offset:10240\n"
"v_mfma_f32_16x16x32_bf16 v[168:171], a[56:59], a[72:75], v[168:171]\n"
"s_waitcnt vmcnt(17)\n"
"ds_read_b128 a[8:11], v52 offset:12288\n"
"v_mfma_f32_16x16x32_bf16 v[184:187], a[56:59], a[76:79], v[184:187]\n"
"s_waitcnt vmcnt(15)\n"
"ds_read_b128 a[12:15], v52 offset:14336\n"
"v_mfma_f32_16x16x32_bf16 v[140:143], a[60:63], a[64:67], v[140:143]\n"
"global_load_lds_dwordx4 v78, s[38:39] offset:2048\n"
"v_mfma_f32_16x16x32_bf16 v[156:159], a[60:63], a[68:71], v[156:159]\n"
"v_mfma_f32_16x16x32_bf16 v[172:175], a[60:63], a[72:75], v[172:175]\n"
"v_mfma_f32_16x16x32_bf16 v[188:191], a[60:63], a[76:79], v[188:191]\n"
"global_load_lds_dwordx4 v79, s[38:39] offset:3072\n"
"s_add_u32 m0, s88, 4096\n"
"s_waitcnt lgkmcnt(0)\n"
"v_mfma_f32_16x16x32_bf16 v[112:115], a[16:19], v[56:59], v[112:115]\n"
"s_waitcnt vmcnt(10)\n"
"v_cmp_eq_u32 vcc, v109, v35\n"
"ds_read_b128 a[48:51], v53 offset:8192\n"
"v_mfma_f32_16x16x32_bf16 v[116:119], a[20:23], v[56:59], v[116:119]\n"
"ds_read_b64_tr_b16 a[64:65], v40 offset:16384\n"
"ds_read_b64_tr_b16 a[66:67], v40 offset:16896\n"
"v_mfma_f32_16x16x32_bf16 v[128:131], a[0:3], a[16:19], v[128:131]\n"
"global_load_lds_dwordx4 v80, s[38:39] offset:0\n"
"ds_read_b64_tr_b16 a[68:69], v41 offset:16384\n"
"v_mfma_f32_16x16x32_bf16 v[144:147], a[0:3], a[20:23], v[144:147]\n"
"s_bitcmp1_b32 vcc_hi, 31\n"
"s_cbranch_scc1 8f\n"
"9:\n"
"ds_read_b64_tr_b16 a[70:71], v41 offset:16896\n"
"v_mfma_f32_16x16x32_bf16 v[160:163], a[0:3], a[24:27], v[160:163]\n"
"ds_read_b64_tr_b16 a[72:73], v42 offset:16384\n"
"v_mfma_f32_16x16x32_bf16 v[176:179], a[0:3], a[28:31], v[176:179]\n"
"global_load_lds_dwordx4 v81, s[38:39] offset:1024\n"
"ds_read_b64_tr_b16 a[74:75], v42 offset:16896\n"
"v_mfma_f32_16x16x32_bf16 v[132:135], a[4:7], a[16:19], v[132:135]\n"
"ds_read_b64_tr_b16 a[76:77], v43 offset:16384\n"
"v_mfma_f32_16x16x32_bf16 v[148:151], a[4:7], a[20:23], v[148:151]\n"
"ds_read_b64_tr_b16 a[78:79], v43 offset:16896\n"
"v_mfma_f32_16x16x32_bf16 v[164:167], a[4:7], a[24:27], v[164:167]\n"
"global_load_lds_dwordx4 v82, s[38:39] offset:2048\n"
"ds_read_b128 a[52:55], v53 offset:10240\n"
"v_mfma_f32_16x16x32_bf16 v[180:183], a[4:7], a[28:31], v[180:183]\n"
"ds_read_b128 a[56:59], v53 offset:12288\n"
"ds_read_b128 a[60:63], v53 offset:14336\n"
"v_mfma_f32_16x16x32_bf16 v[136:139], a[8:11], a[16:19], v[136:139]\n"
"v_mfma_f32_16x16x32_bf16 v[152:155], a[8:11], a[20:23], v[152:155]\n"
"global_load_lds_dwordx4 v83, s[38:39] offset:3072\n"
"v_mfma_f32_16x16x32_bf16 v[168:171], a[8:11], a[24:27], v[168:171]\n"
"v_mfma_f32_16x16x32_bf16 v[184:187], a[8:11], a[28:31], v[184:187]\n"
"v_mfma_f32_16x16x32_bf16 v[140:143], a[12:15], a[16:19], v[140:143]\n"
"v_mfma_f32_16x16x32_bf16 v[156:159], a[12:15], a[20:23], v[156:159]\n"
"v_mfma_f32_16x16x32_bf16 v[172:175], a[12:15], a[24:27], v[172:175]\n"
"v_mfma_f32_16x16x32_bf16 v[188:191], a[12:15], a[28:31], v[188:191]\n"
"s_waitcnt lgkmcnt(0)\n"
"s_barrier\n"
"v_mfma_f32_16x16x32_bf16 v[128:131], a[48:51], a[64:67], v[128:131]\n"
"global_load_dwordx4 a[96:99], v192, s[44:45] offset:0\n"
"ds_read_b128 a[0:3], v52 offset:16384\n"
"v_mfma_f32_16x16x32_bf16 v[144:147], a[48:51], a[68:71], v[144:147]\n"
"ds_read_b64_tr_b16 a[16:17], v36 offset:32768\n"
"ds_read_b64_tr_b16 a[18:19], v36 offset:33280\n"
"v_mfma_f32_16x16x32_bf16 v[160:163], a[48:51], a[72:75], v[160:163]\n"
"ds_read_b64_tr_b16 a[20:21], v37 offset:32768\n"
"v_mfma_f32_16x16x32_bf16 v[176:179], a[48:51], a[76:79], v[176:179]\n"
"global_load_dwordx4 a[100:103], v193, s[44:45] offset:0\n"
"ds_read_b64_tr_b16 a[22:23], v37 offset:33280\n"
"v_mfma_f32_16x16x32_bf16 v[132:135], a[52:55], a[64:67], v[132:135]\n"
"ds_read_b64_tr_b16 a[24:25], v38 offset:32768\n"
"v_mfma_f32_16x16x32_bf16 v[148:151], a[52:55], a[68:71], v[148:151]\n"
"ds_read_b64_tr_b16 a[26:27], v38 offset:33280\n"
"ds_read_b64_tr_b16 a[28:29], v39 offset:32768\n"
"v_mfma_f32_16x16x32_bf16 v[164:167], a[52:55], a[72:75], v[164:167]\n"
"global_load_dwordx4 a[104:107], v194, s[44:45] offset:0\n"
"ds_read_b64_tr_b16 a[30:31], v39 offset:33280\n"
"v_mfma_f32_16x16x32_bf16 v[180:183], a[52:55], a[76:79], v[180:183]\n"
"ds_read_b128 a[4:7], v52 offset:18432\n"
"v_mfma_f32_16x16x32_bf16 v[136:139], a[56:59], a[64:67], v[136:139]\n"
"v_mfma_f32_16x16x32_bf16 v[152:155], a[56:59], a[68:71], v[152:155]\n"
"global_load_dwordx4 a[108:111], v195, s[44:45] offset:0\n"
"v_mfma_f32_16x16x32_bf16 v[168:171], a[56:59], a[72:75], v[168:171]\n"
"s_waitcnt vmcnt(16)\n"
"ds_read_b128 a[8:11], v52 offset:20480\n"
"v_mfma_f32_16x16x32_bf16 v[184:187], a[56:59], a[76:79], v[184:187]\n"
"s_waitcnt vmcnt(14)\n"
"ds_read_b128 a[12:15], v52 offset:22528\n"
"v_mfma_f32_16x16x32_bf16 v[140:143], a[60:63], a[64:67], v[140:143]\n"
"global_load_dwordx4 a[112:115], v196, s[44:45] offset:0\n"
"v_mfma_f32_16x16x32_bf16 v[156:159], a[60:63], a[68:71], v[156:159]\n"
"v_mfma_f32_16x16x32_bf16 v[172:175], a[60:63], a[72:75], v[172:175]\n"
"v_mfma_f32_16x16x32_bf16 v[188:191], a[60:63], a[76:79], v[188:191]\n"
"global_load_dwordx4 a[116:119], v192, s[48:49] offset:0\n"
"s_waitcnt lgkmcnt(0)\n"
"v_mfma_f32_16x16x32_bf16 v[112:115], a[16:19], v[56:59], v[112:115]\n"
"ds_read_b128 a[48:51], v53 offset:16384\n"
"v_mfma_f32_16x16x32_bf16 v[116:119], a[20:23], v[56:59], v[116:119]\n"
"ds_read_b64_tr_b16 a[64:65], v40 offset:32768\n"
"ds_read_b64_tr_b16 a[66:67], v40 offset:33280\n"
"v_mfma_f32_16x16x32_bf16 v[128:131], a[0:3], a[16:19], v[128:131]\n"
"global_load_dwordx4 a[120:123], v193, s[48:49] offset:0\n"
"ds_read_b64_tr_b16 a[68:69], v41 offset:32768\n"
"v_mfma_f32_16x16x32_bf16 v[144:147], a[0:3], a[20:23], v[144:147]\n"
"ds_read_b64_tr_b16 a[70:71], v41 offset:33280\n"
"v_mfma_f32_16x16x32_bf16 v[160:163], a[0:3], a[24:27], v[160:163]\n"
"ds_read_b64_tr_b16 a[72:73], v42 offset:32768\n"
"v_mfma_f32_16x16x32_bf16 v[176:179], a[0:3], a[28:31], v[176:179]\n"
"global_load_dwordx4 a[124:127], v194, s[48:49] offset:0\n"
"ds_read_b64_tr_b16 a[74:75], v42 offset:33280\n"
"v_mfma_f32_16x16x32_bf16 v[132:135], a[4:7], a[16:19], v[132:135]\n"
"ds_read_b64_tr_b16 a[76:77], v43 offset:32768\n"
"v_mfma_f32_16x16x32_bf16 v[148:151], a[4:7], a[20:23], v[148:151]\n"
"ds_read_b64_tr_b16 a[78:79], v43 offset:33280\n"
"v_mfma_f32_16x16x32_bf16 v[164:167], a[4:7], a[24:27], v[164:167]\n"
"global_load_dwordx4 a[128:131], v195, s[48:49] offset:0\n"
"ds_read_b128 a[52:55], v53 offset:18432\n"
"v_mfma_f32_16x16x32_bf16 v[180:183], a[4:7], a[28:31], v[180:183]\n"
"ds_read_b128 a[56:59], v53 offset:20480\n"
"ds_read_b128 a[60:63], v53 offset:22528\n"
"v_mfma_f32_16x16x32_bf16 v[136:139], a[8:11], a[16:19], v[136:139]\n"
"v_mfma_f32_16x16x32_bf16 v[152:155], a[8:11], a[20:23], v[152:155]\n"
"global_load_dwordx4 a[132:135], v196, s[48:49] offset:0\n"
"v_mfma_f32_16x16x32_bf16 v[168:171], a[8:11], a[24:27], v[168:171]\n"
"v_mfma_f32_16x16x32_bf16 v[184:187], a[8:11], a[28:31], v[184:187]\n"
"v_mfma_f32_16x16x32_bf16 v[140:143], a[12:15], a[16:19], v[140:143]\n"
"global_load_dword v34, v14, s[66:67] offset:0 sc1\n"
"v_mfma_f32_16x16x32_bf16 v[156:159], a[12:15], a[20:23], v[156:159]\n"
"v_mfma_f32_16x16x32_bf16 v[172:175], a[12:15], a[24:27], v[172:175]\n"
"v_mfma_f32_16x16x32_bf16 v[188:191], a[12:15], a[28:31], v[188:191]\n"
"global_load_dword v33, v16, s[66:67] offset:0 sc1\n"
"s_lshl_b32 m0, s30, 10\n"
"s_add_u32 m0, m0, 147456\n"
"s_waitcnt vmcnt(20) lgkmcnt(0)\n"
"s_barrier\n"
"v_mfma_f32_16x16x32_bf16 v[128:131], a[48:51], a[64:67], v[128:131]\n"
"s_waitcnt vmcnt(18)\n"
"ds_read_b128 a[0:3], v52 offset:0\n"
"v_mfma_f32_16x16x32_bf16 v[144:147], a[48:51], a[68:71], v[144:147]\n"
"ds_read_b64_tr_b16 a[16:17], v36 offset:0\n"
"ds_read_b64_tr_b16 a[18:19], v36 offset:512\n"
"v_mfma_f32_16x16x32_bf16 v[160:163], a[48:51], a[72:75], v[160:163]\n"
"global_load_lds_dwordx4 v9, s[2:3] sc1\n"
"ds_read_b64_tr_b16 a[20:21], v37 offset:0\n"
"v_mfma_f32_16x16x32_bf16 v[176:179], a[48:51], a[76:79], v[176:179]\n"
"ds_read_b64_tr_b16 a[22:23], v37 offset:512\n"
"v_mfma_f32_16x16x32_bf16 v[132:135], a[52:55], a[64:67], v[132:135]\n"
"ds_read_b64_tr_b16 a[24:25], v38 offset:0\n"
"v_mfma_f32_16x16x32_bf16 v[148:151], a[52:55], a[68:71], v[148:151]\n"
"ds_read_b64_tr_b16 a[26:27], v38 offset:512\n"
"ds_read_b64_tr_b16 a[28:29], v39 offset:0\n"
"v_mfma_f32_16x16x32_bf16 v[164:167], a[52:55], a[72:75], v[164:167]\n"
"ds_read_b64_tr_b16 a[30:31], v39 offset:512\n"
"v_mfma_f32_16x16x32_bf16 v[180:183], a[52:55], a[76:79], v[180:183]\n"
"v_mfma_f32_16x16x32_bf16 v[136:139], a[56:59], a[64:67], v[136:139]\n"
"v_mfma_f32_16x16x32_bf16 v[152:155], a[56:59], a[68:71], v[152:155]\n"
"s_waitcnt vmcnt(17)\n"
"ds_read_b128 a[4:7], v52 offset:2048\n"
"v_mfma_f32_16x16x32_bf16 v[168:171], a[56:59], a[72:75], v[168:171]\n"
"s_waitcnt vmcnt(15)\n"
"ds_read_b128 a[8:11], v52 offset:4096\n"
"v_mfma_f32_16x16x32_bf16 v[184:187], a[56:59], a[76:79], v[184:187]\n"
"s_waitcnt vmcnt(13)\n"
"ds_read_b128 a[12:15], v52 offset:6144\n"
"v_mfma_f32_16x16x32_bf16 v[140:143], a[60:63], a[64:67], v[140:143]\n"
"v_mfma_f32_16x16x32_bf16 v[156:159], a[60:63], a[68:71], v[156:159]\n"
"v_mfma_f32_16x16x32_bf16 v[172:175], a[60:63], a[72:75], v[172:175]\n"
"v_mfma_f32_16x16x32_bf16 v[188:191], a[60:63], a[76:79], v[188:191]\n"
"s_waitcnt lgkmcnt(0)\n"
"v_mfma_f32_16x16x32_bf16 v[112:115], a[16:19], v[56:59], v[112:115]\n"
"ds_read_b128 a[48:51], v53 offset:0\n"
"v_mfma_f32_16x16x32_bf16 v[116:119], a[20:23], v[56:59], v[116:119]\n"
"ds_read_b64_tr_b16 a[64:65], v40 offset:0\n"
"ds_read_b64_tr_b16 a[66:67], v40 offset:512\n"
"v_mfma_f32_16x16x32_bf16 v[128:131], a[0:3], a[16:19], v[128:131]\n"
"ds_read_b64_tr_b16 a[68:69], v41 offset:0\n"
"v_mfma_f32_16x16x32_bf16 v[144:147], a[0:3], a[20:23], v[144:147]\n"
"ds_read_b64_tr_b16 a[70:71], v41 offset:512\n"
"v_mfma_f32_16x16x32_bf16 v[160:163], a[0:3], a[24:27], v[160:163]\n"
"ds_read_b64_tr_b16 a[72:73], v42 offset:0\n"
"v_mfma_f32_16x16x32_bf16 v[176:179], a[0:3], a[28:31], v[176:179]\n"
"ds_read_b64_tr_b16 a[74:75], v42 offset:512\n"
"ds_read_b64_tr_b16 a[76:77], v43 offset:0\n"
"v_mfma_f32_16x16x32_bf16 v[132:135], a[4:7], a[16:19], v[132:135]\n"
"ds_read_b64_tr_b16 a[78:79], v43 offset:512\n"
"v_mfma_f32_16x16x32_bf16 v[148:151], a[4:7], a[20:23], v[148:151]\n"
"ds_read_b128 a[52:55], v53 offset:2048\n"
"v_mfma_f32_16x16x32_bf16 v[164:167], a[4:7], a[24:27], v[164:167]\n"
"ds_read_b128 a[56:59], v53 offset:4096\n"
"v_mfma_f32_16x16x32_bf16 v[180:183], a[4:7], a[28:31], v[180:183]\n"
"ds_read_b128 a[60:63], v53 offset:6144\n"
"v_mfma_f32_16x16x32_bf16 v[136:139], a[8:11], a[16:19], v[136:139]\n"
"s_mov_b32 exec_lo, 0x10001\n"
"s_mov_b32 exec_hi, 0x10001\n"
"ds_write_b128 v66, v[112:115] offset:0\n"
"ds_write_b128 v66, v[116:119] offset:64\n"
"s_mov_b64 exec, -1\n"
"v_mfma_f32_16x16x32_bf16 v[152:155], a[8:11], a[20:23], v[152:155]\n"
"v_mfma_f32_16x16x32_bf16 v[168:171], a[8:11], a[24:27], v[168:171]\n"
"v_mfma_f32_16x16x32_bf16 v[184:187], a[8:11], a[28:31], v[184:187]\n"
"v_mfma_f32_16x16x32_bf16 v[140:143], a[12:15], a[16:19], v[140:143]\n"
"v_mfma_f32_16x16x32_bf16 v[156:159], a[12:15], a[20:23], v[156:159]\n"
"v_mfma_f32_16x16x32_bf16 v[172:175], a[12:15], a[24:27], v[172:175]\n"
"v_mfma_f32_16x16x32_bf16 v[188:191], a[12:15], a[28:31], v[188:191]\n"
"s_waitcnt vmcnt(0) lgkmcnt(0)\n"
"s_barrier\n"
"v_mfma_f32_16x16x32_bf16 v[128:131], a[48:51], a[64:67], v[128:131]\n"
"ds_read2_b32 v[36:37], v69 offset0:0 offset1:64\n"
"ds_read2_b32 v[38:39], v69 offset0:16 offset1:80\n"
"v_mfma_f32_16x16x32_bf16 v[144:147], a[48:51], a[68:71], v[144:147]\n"
"ds_read2_b32 v[40:41], v70 offset0:0 offset1:64\n"
"ds_read2_b32 v[42:43], v70 offset0:16 offset1:80\n"
"v_mfma_f32_16x16x32_bf16 v[160:163], a[48:51], a[72:75], v[160:163]\n"
"ds_read_b128 v[76:79], v65 offset:0\n"
"v_mfma_f32_16x16x32_bf16 v[176:179], a[48:51], a[76:79], v[176:179]\n"
"ds_read_b128 v[92:95], v65 offset:256\n"
"v_mfma_f32_16x16x32_bf16 v[132:135], a[52:55], a[64:67], v[132:135]\n"
"ds_read_b128 v[80:83], v65 offset:64\n"
"v_mfma_f32_16x16x32_bf16 v[148:151], a[52:55], a[68:71], v[148:151]\n"
"ds_read_b128 v[96:99], v65 offset:320\n"
"v_mfma_f32_16x16x32_bf16 v[164:167], a[52:55], a[72:75], v[164:167]\n"
"ds_read_b128 v[84:87], v65 offset:128\n"
"v_mfma_f32_16x16x32_bf16 v[180:183], a[52:55], a[76:79], v[180:183]\n"
"ds_read_b128 v[100:103], v65 offset:384\n"
"v_mfma_f32_16x16x32_bf16 v[136:139], a[56:59], a[64:67], v[136:139]\n"
"ds_read_b128 v[88:91], v65 offset:192\n"
"v_mfma_f32_16x16x32_bf16 v[152:155], a[56:59], a[68:71], v[152:155]\n"
"ds_read_b128 v[104:107], v65 offset:448\n"
"s_waitcnt lgkmcnt(11)\n"
"v_add_f32 v36, v36, v37\n"
"v_mfma_f32_16x16x32_bf16 v[168:171], a[56:59], a[72:75], v[168:171]\n"
"s_waitcnt lgkmcnt(10)\n"
"v_add_f32 v38, v38, v39\n"
"s_waitcnt lgkmcnt(9)\n"
"v_add_f32 v40, v40, v41\n"
"v_mfma_f32_16x16x32_bf16 v[184:187], a[56:59], a[76:79], v[184:187]\n"
"s_waitcnt lgkmcnt(8)\n"
"v_add_f32_e64 v42, v42, v43\n"
"v_mfma_f32_16x16x32_bf16 v[140:143], a[60:63], a[64:67], v[140:143]\n"
"v_mfma_f32_16x16x32_bf16 v[156:159], a[60:63], a[68:71], v[156:159]\n"
"v_mfma_f32_16x16x32_bf16 v[172:175], a[60:63], a[72:75], v[172:175]\n"
"v_mfma_f32_16x16x32_bf16 v[188:191], a[60:63], a[76:79], v[188:191]\n"
"s_waitcnt lgkmcnt(7)\n"
"v_pk_fma_f32 v[128:129], v[76:77], v[36:37], v[128:129] op_sel_hi:[1,0,1] neg_lo:[0,1,0] neg_hi:[0,1,0]\n"
"v_pk_fma_f32 v[130:131], v[78:79], v[36:37], v[130:131] op_sel_hi:[1,0,1] neg_lo:[0,1,0] neg_hi:[0,1,0]\n"
"s_waitcnt lgkmcnt(5)\n"
"v_pk_fma_f32 v[132:133], v[80:81], v[36:37], v[132:133] op_sel_hi:[1,0,1] neg_lo:[0,1,0] neg_hi:[0,1,0]\n"
"v_pk_fma_f32 v[134:135], v[82:83], v[36:37], v[134:135] op_sel_hi:[1,0,1] neg_lo:[0,1,0] neg_hi:[0,1,0]\n"
"v_pk_fma_f32 v[144:145], v[76:77], v[38:39], v[144:145] op_sel_hi:[1,0,1] neg_lo:[0,1,0] neg_hi:[0,1,0]\n"
"v_pk_fma_f32 v[146:147], v[78:79], v[38:39], v[146:147] op_sel_hi:[1,0,1] neg_lo:[0,1,0] neg_hi:[0,1,0]\n"
"v_pk_fma_f32 v[148:149], v[80:81], v[38:39], v[148:149] op_sel_hi:[1,0,1] neg_lo:[0,1,0] neg_hi:[0,1,0]\n"
"v_pk_fma_f32 v[150:151], v[82:83], v[38:39], v[150:151] op_sel_hi:[1,0,1] neg_lo:[0,1,0] neg_hi:[0,1,0]\n"
"v_pk_mul_f32 v[128:129], v[128:129], v[92:93]\n"
"v_pk_mul_f32 v[130:131], v[130:131], v[94:95]\n"
"s_waitcnt lgkmcnt(4)\n"
"s_nop 0\n"
"v_pk_mul_f32 v[132:133], v[132:133], v[96:97]\n"
"v_pk_mul_f32 v[134:135], v[134:135], v[98:99]\n"
"v_pk_mul_f32 v[144:145], v[144:145], v[92:93]\n"
"v_pk_mul_f32 v[146:147], v[146:147], v[94:95]\n"
"v_pk_mul_f32 v[148:149], v[148:149], v[96:97]\n"
"v_pk_mul_f32 v[150:151], v[150:151], v[98:99]\n"
"v_cvt_pk_bf16_f32 v128, v128, v129\n"
"v_cvt_pk_bf16_f32 v129, v130, v131\n"
"v_cvt_pk_bf16_f32 v130, v132, v133\n"
"v_cvt_pk_bf16_f32 v131, v134, v135\n"
"v_cvt_pk_bf16_f32 v144, v144, v145\n"
"v_cvt_pk_bf16_f32 v145, v146, v147\n"
"v_cvt_pk_bf16_f32 v146, v148, v149\n"
"v_cvt_pk_bf16_f32 v147, v150, v151\n"
"global_store_dwordx4 v60, v[128:131], s[6:7] sc1\n"
"global_store_dwordx4 v61, v[144:147], s[6:7] sc1\n"
"s_waitcnt lgkmcnt(3)\n"
"v_pk_fma_f32 v[136:137], v[84:85], v[36:37], v[136:137] op_sel_hi:[1,0,1] neg_lo:[0,1,0] neg_hi:[0,1,0]\n"
"v_pk_fma_f32 v[138:139], v[86:87], v[36:37], v[138:139] op_sel_hi:[1,0,1] neg_lo:[0,1,0] neg_hi:[0,1,0]\n"
"s_waitcnt lgkmcnt(1)\n"
"v_pk_fma_f32 v[140:141], v[88:89], v[36:37], v[140:141] op_sel_hi:[1,0,1] neg_lo:[0,1,0] neg_hi:[0,1,0]\n"
"v_pk_fma_f32 v[142:143], v[90:91], v[36:37], v[142:143] op_sel_hi:[1,0,1] neg_lo:[0,1,0] neg_hi:[0,1,0]\n"
"v_pk_fma_f32 v[152:153], v[84:85], v[38:39], v[152:153] op_sel_hi:[1,0,1] neg_lo:[0,1,0] neg_hi:[0,1,0]\n"
"v_pk_fma_f32 v[154:155], v[86:87], v[38:39], v[154:155] op_sel_hi:[1,0,1] neg_lo:[0,1,0] neg_hi:[0,1,0]\n"
"v_pk_fma_f32 v[156:157], v[88:89], v[38:39], v[156:157] op_sel_hi:[1,0,1] neg_lo:[0,1,0] neg_hi:[0,1,0]\n"
"v_pk_fma_f32 v[158:159], v[90:91], v[38:39], v[158:159] op_sel_hi:[1,0,1] neg_lo:[0,1,0] neg_hi:[0,1,0]\n"
"v_pk_mul_f32 v[136:137], v[136:137], v[100:101]\n"
"v_pk_mul_f32 v[138:139], v[138:139], v[102:103]\n"
"s_waitcnt lgkmcnt(0)\n"
"s_nop 0\n"
"v_pk_mul_f32 v[140:141], v[140:141], v[104:105]\n"
"v_pk_mul_f32 v[142:143], v[142:143], v[106:107]\n"
"v_pk_mul_f32 v[152:153], v[152:153], v[100:101]\n"
"v_pk_mul_f32 v[154:155], v[154:155], v[102:103]\n"
"v_pk_mul_f32 v[156:157], v[156:157], v[104:105]\n"
"v_pk_mul_f32 v[158:159], v[158:159], v[106:107]\n"
"v_cvt_pk_bf16_f32 v136, v136, v137\n"
"v_cvt_pk_bf16_f32 v137, v138, v139\n"
"v_cvt_pk_bf16_f32 v138, v140, v141\n"
"v_cvt_pk_bf16_f32 v139, v142, v143\n"
"v_cvt_pk_bf16_f32 v152, v152, v153\n"
"v_cvt_pk_bf16_f32 v153, v154, v155\n"
"v_cvt_pk_bf16_f32 v154, v156, v157\n"
"v_cvt_pk_bf16_f32 v155, v158, v159\n"
"global_store_dwordx4 v60, v[136:139], s[8:9] sc1\n"
"global_store_dwordx4 v61, v[152:155], s[8:9] sc1\n"
"v_pk_fma_f32 v[160:161], v[76:77], v[40:41], v[160:161] op_sel_hi:[1,0,1] neg_lo:[0,1,0] neg_hi:[0,1,0]\n"
"v_pk_fma_f32 v[162:163], v[78:79], v[40:41], v[162:163] op_sel_hi:[1,0,1] neg_lo:[0,1,0] neg_hi:[0,1,0]\n"
"v_pk_fma_f32 v[164:165], v[80:81], v[40:41], v[164:165] op_sel_hi:[1,0,1] neg_lo:[0,1,0] neg_hi:[0,1,0]\n"
"v_pk_fma_f32 v[166:167], v[82:83], v[40:41], v[166:167] op_sel_hi:[1,0,1] neg_lo:[0,1,0] neg_hi:[0,1,0]\n"
"v_pk_fma_f32 v[176:177], v[76:77], v[42:43], v[176:177] op_sel_hi:[1,0,1] neg_lo:[0,1,0] neg_hi:[0,1,0]\n"
"v_pk_fma_f32 v[178:179], v[78:79], v[42:43], v[178:179] op_sel_hi:[1,0,1] neg_lo:[0,1,0] neg_hi:[0,1,0]\n"
"v_pk_fma_f32 v[180:181], v[80:81], v[42:43], v[180:181] op_sel_hi:[1,0,1] neg_lo:[0,1,0] neg_hi:[0,1,0]\n"
"v_pk_fma_f32 v[182:183], v[82:83], v[42:43], v[182:183] op_sel_hi:[1,0,1] neg_lo:[0,1,0] neg_hi:[0,1,0]\n"
"v_pk_mul_f32 v[160:161], v[160:161], v[92:93]\n"
"v_pk_mul_f32 v[162:163], v[162:163], v[94:95]\n"
"v_pk_mul_f32 v[164:165], v[164:165], v[96:97]\n"
"v_pk_mul_f32 v[166:167], v[166:167], v[98:99]\n"
"v_pk_mul_f32 v[176:177], v[176:177], v[92:93]\n"
"v_pk_mul_f32 v[178:179], v[178:179], v[94:95]\n"
"v_pk_mul_f32 v[180:181], v[180:181], v[96:97]\n"
"v_pk_mul_f32 v[182:183], v[182:183], v[98:99]\n"
"v_cvt_pk_bf16_f32 v128, v160, v161\n"
"v_cvt_pk_bf16_f32 v129, v162, v163\n"
"v_cvt_pk_bf16_f32 v130, v164, v165\n"
"v_cvt_pk_bf16_f32 v131, v166, v167\n"
"v_cvt_pk_bf16_f32 v132, v176, v177\n"
"v_cvt_pk_bf16_f32 v133, v178, v179\n"
"v_cvt_pk_bf16_f32 v134, v180, v181\n"
"v_cvt_pk_bf16_f32 v135, v182, v183\n"
"global_store_dwordx4 v62, v[128:131], s[6:7] sc1\n"
"global_store_dwordx4 v63, v[132:135], s[6:7] sc1\n"
"v_pk_fma_f32 v[168:169], v[84:85], v[40:41], v[168:169] op_sel_hi:[1,0,1] neg_lo:[0,1,0] neg_hi:[0,1,0]\n"
"v_pk_fma_f32 v[170:171], v[86:87], v[40:41], v[170:171] op_sel_hi:[1,0,1] neg_lo:[0,1,0] neg_hi:[0,1,0]\n"
"v_pk_fma_f32 v[172:173], v[88:89], v[40:41], v[172:173] op_sel_hi:[1,0,1] neg_lo:[0,1,0] neg_hi:[0,1,0]\n"
"v_pk_fma_f32 v[174:175], v[90:91], v[40:41], v[174:175] op_sel_hi:[1,0,1] neg_lo:[0,1,0] neg_hi:[0,1,0]\n"
"v_pk_fma_f32 v[184:185], v[84:85], v[42:43], v[184:185] op_sel_hi:[1,0,1] neg_lo:[0,1,0] neg_hi:[0,1,0]\n"
"v_pk_fma_f32 v[186:187], v[86:87], v[42:43], v[186:187] op_sel_hi:[1,0,1] neg_lo:[0,1,0] neg_hi:[0,1,0]\n"
"v_pk_fma_f32 v[188:189], v[88:89], v[42:43], v[188:189] op_sel_hi:[1,0,1] neg_lo:[0,1,0] neg_hi:[0,1,0]\n"
"v_pk_fma_f32 v[190:191], v[90:91], v[42:43], v[190:191] op_sel_hi:[1,0,1] neg_lo:[0,1,0] neg_hi:[0,1,0]\n"
"v_pk_mul_f32 v[168:169], v[168:169], v[100:101]\n"
"v_pk_mul_f32 v[170:171], v[170:171], v[102:103]\n"
"v_pk_mul_f32 v[172:173], v[172:173], v[104:105]\n"
"v_pk_mul_f32 v[174:175], v[174:175], v[106:107]\n"
"v_pk_mul_f32 v[184:185], v[184:185], v[100:101]\n"
"v_pk_mul_f32 v[186:187], v[186:187], v[102:103]\n"
"v_pk_mul_f32 v[188:189], v[188:189], v[104:105]\n"
"v_pk_mul_f32 v[190:191], v[190:191], v[106:107]\n"
"v_cvt_pk_bf16_f32 v136, v168, v169\n"
"v_cvt_pk_bf16_f32 v137, v170, v171\n"
"v_cvt_pk_bf16_f32 v138, v172, v173\n"
"v_cvt_pk_bf16_f32 v139, v174, v175\n"
"v_cvt_pk_bf16_f32 v140, v184, v185\n"
"v_cvt_pk_bf16_f32 v141, v186, v187\n"
"v_cvt_pk_bf16_f32 v142, v188, v189\n"
"v_cvt_pk_bf16_f32 v143, v190, v191\n"
"global_store_dwordx4 v62, v[136:139], s[8:9] sc1\n"
"global_store_dwordx4 v63, v[140:143], s[8:9] sc1\n"
"ds_write_b128 v208, a[96:99] offset:0\n"
"ds_write_b128 v208, a[100:103] offset:1024\n"
"ds_write_b128 v208, a[104:107] offset:2048\n"
"ds_write_b128 v208, a[108:111] offset:3072\n"
"ds_write_b128 v208, a[112:115] offset:4096\n"
"ds_write_b128 v208, a[116:119] offset:5120\n"
"ds_write_b128 v208, a[120:123] offset:6144\n"
"ds_write_b128 v208, a[124:127] offset:7168\n"
"ds_write_b128 v208, a[128:131] offset:8192\n"
"ds_write_b128 v208, a[132:135] offset:9216\n"
"s_waitcnt vmcnt(0) lgkmcnt(0)\n"
"s_barrier\n"
"s_mov_b64 exec, s[50:51]\n"
"global_atomic_add v15, v110, s[66:67]\n"
"s_mov_b64 exec, -1\n"
"ds_read_b64_tr_b16 v[216:217], v200 offset:0\n"
"ds_read_b64_tr_b16 v[218:219], v200 offset:20480\n"
"s_add_u32 s53, s52, 0\n"
"s_cmp_lt_u32 s53, 54\n"
"s_cselect_b32 s47, 54, 20\n"
"s_cselect_b32 s46, 0, 378\n"
"s_add_u32 s46, s46, s53\n"
"s_mul_i32 s44, s30, s47\n"
"s_lshl1_add_u32 s46, s44, s46\n"
"s_mul_i32 s46, s46, 5120\n"
"s_add_u32 s46, s46, 0x1118800\n"
"s_mul_i32 s47, s47, 5120\n"
"s_mov_b64 s[42:43], s[66:67]\n"
"s_add_u32 s42, s42, s46\n"
"s_addc_u32 s43, s43, 0\n"
"ds_read_b64_tr_b16 v[220:221], v200 offset:8\n"
"ds_read_b64_tr_b16 v[222:223], v200 offset:20488\n"
"ds_read_b64_tr_b16 v[224:225], v201 offset:0\n"
"ds_read_b64_tr_b16 v[226:227], v201 offset:20480\n"
"ds_read_b64_tr_b16 v[228:229], v201 offset:8\n"
"ds_read_b64_tr_b16 v[230:231], v201 offset:20488\n"
"ds_read_b64_tr_b16 v[232:233], v202 offset:0\n"
"ds_read_b64_tr_b16 v[234:235], v202 offset:20480\n"
"ds_read_b64_tr_b16 v[236:237], v203 offset:0\n"
"ds_read_b64_tr_b16 v[238:239], v203 offset:20480\n"
"ds_read_b64_tr_b16 v[240:241], v203 offset:8\n"
"ds_read_b64_tr_b16 v[242:243], v203 offset:20488\n"
"ds_read_b64_tr_b16 v[244:245], v204 offset:0\n"
"ds_read_b64_tr_b16 v[246:247], v204 offset:20480\n"
"ds_read_b64_tr_b16 v[248:249], v204 offset:8\n"
"ds_read_b64_tr_b16 v[250:251], v204 offset:20488\n"
"ds_read_b64_tr_b16 v[252:253], v205 offset:0\n"
"ds_read_b64_tr_b16 v[254:255], v205 offset:20480\n"
"s_waitcnt lgkmcnt(15)\n"
"global_store_dwordx4 v9, v[216:219], s[42:43] offset:-2048 sc1\n"
"global_store_dwordx4 v9, v[220:223], s[42:43] offset:-1024 sc1\n"
"s_waitcnt lgkmcnt(14)\n"
"global_store_dwordx4 v9, v[224:227], s[42:43] offset:0 sc1\n"
"s_waitcnt lgkmcnt(12)\n"
"global_store_dwordx4 v9, v[228:231], s[42:43] offset:1024 sc1\n"
"s_waitcnt lgkmcnt(10)\n"
"global_store_dwordx4 v9, v[232:235], s[42:43] offset:2048 sc1\n"
"s_add_u32 s42, s42, s47\n"
"s_addc_u32 s43, s43, 0\n"
"s_waitcnt lgkmcnt(8)\n"
"global_store_dwordx4 v9, v[236:239], s[42:43] offset:-2048 sc1\n"
"s_waitcnt lgkmcnt(6)\n"
"global_store_dwordx4 v9, v[240:243], s[42:43] offset:-1024 sc1\n"
"s_waitcnt lgkmcnt(4)\n"
"global_store_dwordx4 v9, v[244:247], s[42:43] offset:0 sc1\n"
"s_waitcnt lgkmcnt(2)\n"
"global_store_dwordx4 v9, v[248:251], s[42:43] offset:1024 sc1\n"
"s_waitcnt lgkmcnt(0)\n"
"global_store_dwordx4 v9, v[252:255], s[42:43] offset:2048 sc1\n"
"s_branch 50b\n"
"8:\n"
"s_mov_b32 s46, 0\n"
"81:\n"
"s_sleep 4\n"
"s_mov_b32 exec_lo, 0\n"
"global_load_dword v109, v1, s[66:67] offset:0 sc1\n"
"s_mov_b32 exec_lo, -1\n"
"s_add_u32 s46, s46, 1\n"
"s_cmp_gt_u32 s46, 0x100000\n"
"s_cbranch_scc1 9b\n"
"s_waitcnt vmcnt(0)\n"
"v_cmp_eq_u32 vcc, v109, v35\n"
"s_nop 4\n"
"s_bitcmp1_b32 vcc_hi, 31\n"
"s_cbranch_scc1 81b\n"
"s_branch 9b\n"
"70:\n"
"v_bfe_u32 v57, v0, 4, 2\n"
"v_and_b32 v58, 15, v0\n"
"v_mul_u32_u24 v57, 80, v57\n"
"v_add_lshl_u32 v58, v57, v58, 4\n"
"s_add_u32 s38, s60, s80\n"
"s_addc_u32 s39, s61, 0\n"
"global_load_dword v35, v1, s[66:67] sc1\n"
"global_load_dwordx4 v[176:179], v58, s[38:39] offset:0\n"
"global_load_dwordx4 v[180:183], v58, s[38:39] offset:256\n"
"global_load_dwordx4 v[184:187], v58, s[38:39] offset:512\n"
"global_load_dwordx4 v[188:191], v58, s[38:39] offset:768\n"
"global_load_dwordx4 v[192:195], v58, s[38:39] offset:1024\n"
"s_add_u32 s38, s38, 5120\n"
"s_addc_u32 s39, s39, 0\n"
"global_load_dwordx4 v[196:199], v58, s[38:39] offset:0\n"
"s_mul_hi_u32 s0, s87, 0xcccccccd\n"
"s_lshr_b32 s0, s0, 2\n"
"s_mul_i32 s0, s0, 5\n"
"s_sub_i32 s0, s87, s0\n"
"s_cmp_lt_u32 s87, 20\n"
"s_cselect_b32 s3, s0, 5\n"
"s_and_b32 s0, s30, 1\n"
"s_lshl_b32 s1, s0, 2\n"
"v_bfe_u32 v2, v0, 4, 2\n"
"v_bfe_u32 v3, v0, 1, 3\n"
"v_bitop3_b32 v2, s1, v3, v2 bitop3:0x36\n"
"s_mul_i32 s1, s30, 0x6000\n"
"v_and_b32_e32 v3, 15, v0\n"
"v_lshl_or_b32 v3, v3, 7, s1\n"
"v_lshlrev_b32_e32 v2, 4, v2\n"
"v_or_b32_e32 v52, v2, v3\n"
"v_bitop3_b32 v53, v2, 64, v3 bitop3:0x36\n"
"v_lshrrev_b32_e32 v2, 1, v0\n"
"global_load_dwordx4 v[200:203], v58, s[38:39] offset:256\n"
"v_lshlrev_b32_e32 v4, 5, v0\n"
"v_and_b32_e32 v2, 4, v2\n"
"v_bfe_u32 v3, v0, 4, 1\n"
"v_and_b32_e32 v4, 0x180, v4\n"
"v_lshl_or_b32 v2, v3, 3, v2\n"
"v_lshl_or_b32 v3, v3, 10, v4\n"
"v_lshlrev_b32_e32 v4, 7, v0\n"
"s_lshl_b32 s0, s0, 13\n"
"v_and_b32_e32 v4, 0x1000, v4\n"
"v_or3_b32 v3, v4, s0, v3\n"
"s_mov_b32 s0, 0x76543210\n"
"s_mov_b32 s1, 0xfedcba98\n"
"s_mov_b32 s12, 0\n"
"s_cmp_eq_u32 s3, 0\n"
"s_cmov_b32 s0, 0xec97db86\n"
"s_cmov_b32 s1, 0xf542a031\n"
"s_cmov_b32 s12, 0x2a\n"
"s_cmp_eq_u32 s3, 1\n"
"s_cmov_b32 s0, 0xfd86ec75\n"
"s_cmov_b32 s1, 0x4a319b20\n"
"s_cmov_b32 s12, 0x26\n"
"global_load_dwordx4 v[204:207], v58, s[38:39] offset:512\n"
"s_cmp_eq_u32 s3, 2\n"
"s_cmov_b32 s0, 0xfca5eb94\n"
"s_cmov_b32 s1, 0xd0728361\n"
"s_cmov_b32 s12, 0x34\n"
"s_cmp_eq_u32 s3, 3\n"
"s_cmov_b32 s0, 0xecb9fda8\n"
"s_cmov_b32 s1, 0x76412530\n"
"s_cmov_b32 s12, 0x5a\n"
"s_cmp_eq_u32 s3, 4\n"
"s_cmov_b32 s0, 0xfda8ec97\n"
"s_cmov_b32 s1, 0x6053b142\n"
"s_cmov_b32 s12, 0x2a\n"
"s_and_b32 s3, s30, 2\n"
"s_lshl_b32 s4, s3, 2\n"
"v_and_b32_e32 v4, 3, v0\n"
"s_lshl_b32 s3, s3, 4\n"
"v_bitop3_b32 v5, s4, v2, v4 bitop3:0x36\n"
"v_lshlrev_b32_e32 v4, 2, v4\n"
"global_load_dwordx4 v[208:211], v58, s[38:39] offset:768\n"
"s_lshr_b64 s[4:5], s[0:1], s3\n"
"v_lshrrev_b32_e64 v6, v4, s4\n"
"v_bitop3_b32 v6, v6, v2, 15 bitop3:0x6c\n"
"s_or_b32 s4, s3, 16\n"
"v_lshlrev_b32_e32 v6, 3, v6\n"
"s_mov_b32 s6, 0x1a000\n"
"s_lshr_b64 s[4:5], s[0:1], s4\n"
"v_or_b32_e32 v8, v6, v3\n"
"v_bitop3_b32 v40, v6, s6, v3 bitop3:0x36\n"
"v_lshrrev_b32_e64 v6, v4, s4\n"
"v_lshlrev_b32_e32 v5, 3, v5\n"
"v_bitop3_b32 v6, v6, v2, 15 bitop3:0x6c\n"
"s_xor_b32 s4, s3, 32\n"
"v_or_b32_e32 v7, v3, v5\n"
"v_lshlrev_b32_e32 v6, 3, v6\n"
"s_lshr_b64 s[4:5], s[0:1], s4\n"
"s_xor_b32 s3, s3, 48\n"
"v_or_b32_e32 v44, 0x18800, v7\n"
"global_load_dwordx4 v[212:215], v58, s[38:39] offset:1024\n"
"s_add_u32 s38, s38, 5120\n"
"s_addc_u32 s39, s39, 0\n"
"v_or_b32_e32 v7, v6, v3\n"
"v_bitop3_b32 v41, v6, s6, v3 bitop3:0x36\n"
"v_lshrrev_b32_e64 v6, v4, s4\n"
"s_lshr_b64 s[4:5], s[0:1], s3\n"
"s_mov_b32 s3, 0x1a800\n"
"v_bitop3_b32 v48, v3, s3, v5 bitop3:0x36\n"
"s_mov_b32 s3, 0x18820\n"
"v_bitop3_b32 v45, v3, s3, v5 bitop3:0x36\n"
"s_mov_b32 s3, 0x1a820\n"
"v_lshrrev_b32_e64 v4, v4, s4\n"
"v_bitop3_b32 v49, v3, s3, v5 bitop3:0x36\n"
"s_mov_b32 s3, 0x18840\n"
"v_bitop3_b32 v6, v6, v2, 15 bitop3:0x6c\n"
"v_bitop3_b32 v2, v4, v2, 15 bitop3:0x6c\n"
"v_bitop3_b32 v46, v3, s3, v5 bitop3:0x36\n"
"s_mov_b32 s3, 0x1a840\n"
"v_lshlrev_b32_e32 v6, 3, v6\n"
"v_bitop3_b32 v50, v3, s3, v5 bitop3:0x36\n"
"global_load_dwordx4 v[216:219], v58, s[38:39] offset:0\n"
"s_mov_b32 s3, 0x18860\n"
"v_lshlrev_b32_e32 v2, 3, v2\n"
"v_or_b32_e32 v37, 0x18000, v7\n"
"v_or_b32_e32 v7, v6, v3\n"
"v_bitop3_b32 v47, v3, s3, v5 bitop3:0x36\n"
"s_mov_b32 s3, 0x1a860\n"
"v_bitop3_b32 v43, v2, s6, v3 bitop3:0x36\n"
"v_or_b32_e32 v2, v2, v3\n"
"s_nop 0\n"
"v_or_b32_e32 v36, 0x18000, v8\n"
"v_or_b32_e32 v38, 0x18000, v7\n"
"v_bitop3_b32 v42, v6, s6, v3 bitop3:0x36\n"
"v_bitop3_b32 v51, v3, s3, v5 bitop3:0x36\n"
"v_or_b32_e32 v39, 0x18000, v2\n"
"s_and_b32 s3, s2, 3\n"
"s_lshl_b32 s3, s3, 3\n"
"s_bfe_u32 s4, s2, 0x10001\n"
"s_lshl_b32 s9, s2, 2\n"
"s_sub_i32 s10, s3, s4\n"
"global_load_dwordx4 v[220:223], v58, s[38:39] offset:256\n"
"s_lshr_b32 s14, s2, 3\n"
"s_and_b32 s3, s9, 4\n"
"s_lshr_b32 s18, s2, 5\n"
"s_add_i32 s3, s3, s14\n"
"s_add_i32 s10, s10, s18\n"
"s_and_b32 s3, s3, 32\n"
"s_or_b32 s4, s10, s3\n"
"s_lshl_b32 s3, s2, 1\n"
"s_and_b32 s3, s3, 12\n"
"s_bfe_u32 s7, s2, 0x20003\n"
"s_add_i32 s6, s3, s14\n"
"s_and_b32 s3, s2, 4\n"
"s_lshr_b32 s13, s30, 1\n"
"s_sub_i32 s11, s6, 28\n"
"s_or_b32 s8, s7, s3\n"
"s_lshl_b32 s5, s8, 2\n"
"s_and_b32 s22, s30, 1\n"
"s_lshl_b32 s3, s11, 11\n"
"global_load_dwordx4 v[224:227], v58, s[38:39] offset:512\n"
"s_lshl_b32 s16, s13, 10\n"
"s_add_i32 s5, s5, s30\n"
"s_add_i32 s3, s3, s16\n"
"s_lshl_b32 s16, s22, 7\n"
"s_lshl_b32 s15, s10, 3\n"
"s_or_b32 s3, s3, s16\n"
"s_lshl_b32 s16, s5, 10\n"
"s_cmp_gt_u32 s4, 31\n"
"s_cselect_b32 s3, s3, s16\n"
"s_addk_i32 s6, 0xd4\n"
"s_or_b32 s15, s15, s8\n"
"s_cmp_gt_u32 s4, 31\n"
"s_cselect_b32 s6, s6, s15\n"
"s_add_i32 s8, s15, 0x1fffff38\n"
"s_cmp_gt_u32 s4, 31\n"
"s_cselect_b32 s8, s11, s8\n"
"s_addk_i32 s15, 0xff58\n"
"s_cmp_gt_u32 s4, 31\n"
"s_mul_hi_u32 s16, s10, 0xcccccccd\n"
"s_cselect_b32 s31, s11, s15\n"
"global_load_dwordx4 v[228:231], v58, s[38:39] offset:768\n"
"s_lshr_b32 s11, s16, 2\n"
"s_lshr_b32 s15, s16, 3\n"
"s_sub_i32 s11, s10, s11\n"
"s_mul_i32 s15, s15, 10\n"
"s_and_b32 s11, s11, 1\n"
"s_sub_i32 s16, s10, s15\n"
"s_or_b32 s17, s11, 10\n"
"s_lshl_b32 s15, s16, 4\n"
"s_cmp_lt_u32 s10, 10\n"
"s_cselect_b64 vcc, -1, 0\n"
"s_movk_i32 s19, 0xa00\n"
"s_and_b64 s[20:21], vcc, exec\n"
"s_cselect_b32 s54, 0x500, s19\n"
"s_mov_b32 s19, 0x58000\n"
"s_cselect_b32 s21, s19, 0x2d8000\n"
"s_lshl_b32 s55, s54, 6\n"
"s_and_b32 s25, s30, 2\n"
"v_mov_b32_e32 v2, 0x3ea52b5b\n"
"global_load_dwordx4 v[232:235], v58, s[38:39] offset:1024\n"
"s_add_u32 s38, s38, 5120\n"
"s_addc_u32 s39, s39, 0\n"
"s_cmp_lt_u32 s10, 20\n"
"v_cndmask_b32_e32 v2, 1.0, v2, vcc\n"
"s_cselect_b64 vcc, -1, 0\n"
"s_xor_b32 s20, s30, 2\n"
"s_lshr_b32 s58, s20, 1\n"
"s_lshl_b32 s19, s13, 3\n"
"s_lshl_b32 s23, s58, 3\n"
"s_sub_i32 s56, s17, s19\n"
"s_lshl_b32 s57, s25, 4\n"
"s_xor_b32 s19, s30, 1\n"
"s_sub_i32 s59, s17, s23\n"
"s_cmp_gt_u32 s20, 1\n"
"s_cselect_b32 s62, s1, s0\n"
"s_xor_b32 s24, s30, 3\n"
"s_lshr_b32 s63, s24, 1\n"
"s_lshl_b32 s23, s63, 3\n"
"s_sub_i32 s64, s17, s23\n"
"s_cmp_gt_u32 s24, 1\n"
"s_cselect_b32 s65, s1, s0\n"
"global_load_dwordx4 v[236:239], v58, s[38:39] offset:0\n"
"s_and_b32 s17, s9, 28\n"
"s_mul_i32 s9, s5, s54\n"
"s_or_b32 s23, s17, s7\n"
"s_lshr_b32 s7, s2, 6\n"
"s_lshl_b32 s78, s25, 8\n"
"s_lshl_b32 s33, s30, 10\n"
"s_lshl_b32 s34, s30, 8\n"
"s_lshl_b32 s9, s9, 1\n"
"s_add_i32 s17, s17, s7\n"
"s_or_b32 s35, s78, 0x25000\n"
"s_add_i32 s51, s33, 0x24000\n"
"s_add_i32 s52, s34, 0x25000\n"
"s_xor_b32 s53, s78, 0x25200\n"
"s_add_i32 s9, s9, s21\n"
"s_cmp_eq_u32 s11, 0\n"
"s_cselect_b32 s50, -1, 0xffff0000\n"
"s_lshr_b32 s11, s2, 1\n"
"s_lshl_b32 s21, s22, 1\n"
"global_load_dwordx4 v[240:243], v58, s[38:39] offset:256\n"
"s_and_b32 s11, s11, 28\n"
"s_add_i32 s27, s21, s13\n"
"s_add_i32 s27, s27, s11\n"
"s_lshl_b32 s11, s23, 6\n"
"s_lshl_b32 s21, s30, 4\n"
"s_add_i32 s11, s11, s21\n"
"s_lshl_b32 s21, s2, 15\n"
"s_lshl_b32 s26, s30, 13\n"
"s_add_i32 s21, s26, s21\n"
"s_mul_i32 s26, s5, 0x36000\n"
"s_lshl_b32 s28, s10, 13\n"
"s_add_i32 s29, s28, s26\n"
"s_mul_i32 s26, s30, 40\n"
"s_add_i32 s8, s26, s8\n"
"s_mul_i32 s26, s11, 0x500\n"
"s_mul_i32 s28, s18, 0xa0\n"
"s_lshl_b32 s5, s5, 5\n"
"s_add_i32 s26, s26, s28\n"
"global_load_dwordx4 v[244:247], v58, s[38:39] offset:512\n"
"s_add_i32 s21, s21, 0x13fc000\n"
"s_add_i32 s29, s29, 0x7d8000\n"
"s_addk_i32 s8, 0x400\n"
"s_add_i32 s5, s5, s10\n"
"s_cmp_gt_u32 s4, 26\n"
"s_cselect_b32 s4, s21, s29\n"
"s_cselect_b32 s8, s8, s5\n"
"s_add_u32 s4, s66, s4\n"
"s_addc_u32 s5, s67, 0\n"
"s_lshl_b32 s21, s27, 5\n"
"s_mul_i32 s11, s7, 40\n"
"s_add_i32 s86, s7, 0xfffffee0\n"
"s_lshl_b32 s87, s23, 5\n"
"s_lshl_b32 s7, s22, 4\n"
"s_add_i32 s21, s21, s17\n"
"s_or_b32 s90, s87, s7\n"
"s_lshl_b32 s7, s21, 4\n"
"s_add_i32 s91, s7, 0x2f000\n"
"global_load_dwordx4 v[248:251], v58, s[38:39] offset:768\n"
"s_lshl_b32 s7, s8, 3\n"
"s_lshl_b32 s2, s2, 3\n"
"s_mul_i32 s29, s30, 13\n"
"s_add_i32 s79, s11, 0x400\n"
"s_add_i32 s92, s7, 0xa000\n"
"s_lshl_b32 s93, s6, 7\n"
"s_add_i32 s94, s2, 0x8000\n"
"s_add_i32 s3, s3, 0x50000\n"
"s_or_b32 s95, s22, 52\n"
"s_lshr_b32 s96, s29, 1\n"
"s_add_u32 s2, s66, s3\n"
"s_addc_u32 s3, s67, 0\n"
"s_add_u32 s6, s66, s9\n"
"s_addc_u32 s7, s67, 0\n"
"s_add_i32 s9, s9, s54\n"
"s_add_u32 s8, s66, s9\n"
"s_addc_u32 s9, s67, 0\n"
"s_min_u32 s10, s10, 26\n"
"global_load_dwordx4 v[252:255], v58, s[38:39] offset:1024\n"
"s_add_u32 s38, s38, 5120\n"
"s_addc_u32 s39, s39, 0\n"
"s_lshl_b32 s10, s10, 7\n"
"v_bfe_u32 v3, v0, 4, 2\n"
"s_add_u32 s10, s70, s10\n"
"v_lshl_or_b32 v4, s58, 2, v3\n"
"s_addc_u32 s11, s71, 0\n"
"v_lshrrev_b32_e64 v4, v4, s12\n"
"s_cmp_gt_u32 s30, 1\n"
"v_lshlrev_b32_e32 v4, 9, v4\n"
"s_cselect_b32 s54, s1, s0\n"
"v_and_b32_e32 v4, 0x200, v4\n"
"v_mov_b32_e32 v5, 0x400\n"
"v_cmp_gt_u32_e64 s[0:1], s59, v3\n"
"v_lshlrev_b32_e32 v10, 2, v3\n"
"s_nop 0\n"
"v_lshl_or_b32 v6, s13, 2, v3\n"
"v_cndmask_b32_e64 v4, v5, v4, s[0:1]\n"
"v_cmp_gt_u32_e64 s[0:1], s56, v3\n"
"v_lshrrev_b32_e64 v11, v10, s54\n"
"v_lshrrev_b32_e64 v6, v6, s12\n"
"v_cndmask_b32_e64 v8, 4, 0, s[0:1]\n"
"v_and_or_b32 v11, v11, 15, s15\n"
"v_lshlrev_b32_e32 v6, 9, v6\n"
"v_or_b32_e32 v8, v8, v3\n"
"v_mul_lo_u16_e32 v11, 0xcd, v11\n"
"v_and_b32_e32 v6, 0x200, v6\n"
"v_lshlrev_b32_e32 v8, 2, v8\n"
"v_lshrrev_b16_e32 v11, 10, v11\n"
"v_and_b32_e32 v13, 15, v0\n"
"v_cndmask_b32_e64 v7, v5, v6, s[0:1]\n"
"v_lshrrev_b32_e64 v8, v8, s54\n"
"v_mul_u32_u24_e32 v11, s55, v11\n"
"v_lshlrev_b32_e32 v14, 4, v13\n"
"v_or3_b32 v7, v11, v7, v14\n"
"v_lshl_or_b32 v11, s16, 6, v13\n"
"v_and_or_b32 v8, v8, 15, s15\n"
"v_lshl_or_b32 v12, s63, 2, v3\n"
"v_or_b32_e32 v15, s57, v11\n"
"v_mul_lo_u16_e32 v8, 0xcd, v8\n"
"v_or_b32_e32 v6, 0x100, v6\n"
"v_lshrrev_b32_e64 v12, v12, s12\n"
"v_mul_u32_u24_e32 v16, 0xccd, v15\n"
"s_mov_b32 s12, 0x28000\n"
"v_mov_b32_e32 v18, 20\n"
"v_lshrrev_b16_e32 v8, 10, v8\n"
"v_cndmask_b32_e64 v6, v5, v6, s[0:1]\n"
"v_mov_b32_e32 v9, 0x500\n"
"s_movk_i32 s0, 0x140\n"
"v_mul_u32_u24_sdwa v17, v16, s12 dst_sel:DWORD dst_unused:UNUSED_PAD src0_sel:WORD_1 src1_sel:DWORD\n"
"v_mul_lo_u16_sdwa v16, v16, v18 dst_sel:DWORD dst_unused:UNUSED_PAD src0_sel:WORD_1 src1_sel:DWORD\n"
"v_mul_u32_u24_e32 v8, s55, v8\n"
"v_mad_u32_u24 v9, v3, s0, v9\n"
"v_sub_u16_e32 v16, v15, v16\n"
"v_or3_b32 v6, v8, v6, v14\n"
"v_or_b32_e32 v8, 16, v15\n"
"v_lshlrev_b16_e32 v16, 4, v16\n"
"v_or_b32_e32 v17, v9, v17\n"
"v_mul_u32_u24_e32 v15, 0xccd, v8\n"
"v_add_u32_e32 v16, v17, v16\n"
"v_mul_lo_u16_sdwa v17, v15, v18 dst_sel:DWORD dst_unused:UNUSED_PAD src0_sel:WORD_1 src1_sel:DWORD\n"
"v_sub_u16_e32 v8, v8, v17\n"
"v_mul_u32_u24_sdwa v15, v15, s12 dst_sel:DWORD dst_unused:UNUSED_PAD src0_sel:WORD_1 src1_sel:DWORD\n"
"v_lshlrev_b16_e32 v8, 4, v8\n"
"v_or_b32_e32 v15, v9, v15\n"
"v_lshrrev_b32_e64 v10, v10, s62\n"
"v_add_u32_e32 v8, v15, v8\n"
"v_cndmask_b32_e32 v61, v8, v6, vcc\n"
"v_and_or_b32 v6, v10, 15, s15\n"
"v_mul_lo_u16_e32 v6, 0xcd, v6\n"
"v_lshrrev_b16_e32 v6, 10, v6\n"
"v_mul_u32_u24_e32 v6, s55, v6\n"
"v_or3_b32 v4, v6, v4, v14\n"
"v_bitop3_b32 v6, s57, 32, v11 bitop3:0x36\n"
"v_cndmask_b32_e32 v60, v16, v7, vcc\n"
"v_mul_u32_u24_e32 v7, 0xccd, v6\n"
"v_mul_lo_u16_sdwa v8, v7, v18 dst_sel:DWORD dst_unused:UNUSED_PAD src0_sel:WORD_1 src1_sel:DWORD\n"
"v_sub_u16_e32 v6, v6, v8\n"
"v_mul_u32_u24_sdwa v7, v7, s12 dst_sel:DWORD dst_unused:UNUSED_PAD src0_sel:WORD_1 src1_sel:DWORD\n"
"v_lshlrev_b16_e32 v6, 4, v6\n"
"v_or_b32_e32 v7, v9, v7\n"
"v_add_u32_e32 v6, v7, v6\n"
"v_cndmask_b32_e32 v62, v6, v4, vcc\n"
"v_lshlrev_b32_e32 v4, 9, v12\n"
"v_and_b32_e32 v4, 0x200, v4\n"
"v_or_b32_e32 v4, 0x100, v4\n"
"v_cmp_gt_u32_e64 s[0:1], s64, v3\n"
"v_mov_b32_e32 v15, s95\n"
"s_cmp_gt_u32 s30, 1\n"
"s_cselect_b32 s58, 0, s50\n"
"v_cndmask_b32_e64 v4, v5, v4, s[0:1]\n"
"v_cndmask_b32_e64 v5, 4, 0, s[0:1]\n"
"v_or_b32_e32 v5, v5, v3\n"
"v_lshlrev_b32_e32 v5, 2, v5\n"
"v_lshrrev_b32_e64 v5, v5, s65\n"
"v_and_or_b32 v5, v5, 15, s15\n"
"v_mul_lo_u16_e32 v5, 0xcd, v5\n"
"v_lshrrev_b16_e32 v5, 10, v5\n"
"v_mul_u32_u24_e32 v5, s55, v5\n"
"v_or3_b32 v4, v5, v4, v14\n"
"v_bitop3_b32 v5, s57, 48, v11 bitop3:0x36\n"
"v_mul_u32_u24_e32 v6, 0xccd, v5\n"
"v_mul_lo_u16_sdwa v7, v6, v18 dst_sel:DWORD dst_unused:UNUSED_PAD src0_sel:WORD_1 src1_sel:DWORD\n"
"v_sub_u16_e32 v5, v5, v7\n"
"v_mul_u32_u24_sdwa v6, v6, s12 dst_sel:DWORD dst_unused:UNUSED_PAD src0_sel:WORD_1 src1_sel:DWORD\n"
"v_or_b32_e32 v6, v9, v6\n"
"v_lshlrev_b16_e32 v5, 4, v5\n"
"v_add_u32_e32 v5, v6, v5\n"
"v_cndmask_b32_e32 v63, v5, v4, vcc\n"
"v_lshrrev_b32_e32 v4, 1, v0\n"
"v_and_b32_e32 v5, 3, v0\n"
"v_and_or_b32 v4, v4, 12, v5\n"
"v_bfe_u32 v5, v0, 5, 1\n"
"v_bfe_u32 v6, v0, 2, 1\n"
"v_lshlrev_b32_e32 v4, 4, v4\n"
"v_lshlrev_b32_e32 v7, 9, v5\n"
"v_lshlrev_b32_e32 v8, 8, v6\n"
"v_or3_b32 v7, v7, v8, v4\n"
"v_lshlrev_b32_e32 v6, 3, v6\n"
"v_mul_u32_u24_e32 v8, 0xa00, v5\n"
"v_or3_b32 v4, v4, v6, v8\n"
"v_and_b32_e32 v6, 63, v0\n"
"v_lshlrev_b32_e32 v9, 4, v6\n"
"v_and_b32_e32 v10, 0x100, v9\n"
"v_bfe_u32 v11, v0, 4, 1\n"
"v_or3_b32 v8, v14, v10, v8\n"
"v_mul_u32_u24_e32 v10, 0x500, v5\n"
"v_lshlrev_b32_e32 v12, 3, v11\n"
"v_or3_b32 v10, v12, v10, v14\n"
"v_lshlrev_b32_e32 v11, 9, v11\n"
"v_lshlrev_b32_e32 v12, 8, v5\n"
"v_or3_b32 v11, v11, v12, v14\n"
"v_lshlrev_b32_e32 v12, 2, v13\n"
"v_mov_b32_e32 v14, 0x25200\n"
"v_bitop3_b32 v70, s78, v12, v14 bitop3:0xde\n"
"v_or_b32_e32 v64, s51, v12\n"
"v_or_b32_e32 v69, s35, v12\n"
"v_and_b32_e32 v12, 31, v0\n"
"s_mul_i32 s0, s30, 5\n"
"v_min_u32_e32 v14, 26, v12\n"
"v_add3_u32 v14, s0, 32, v14\n"
"v_cmp_eq_u32_e32 vcc, 21, v12\n"
"v_cmp_gt_u32_e64 s[0:1], 21, v12\n"
"v_mul_u32_u24_e32 v5, 20, v5\n"
"v_cndmask_b32_e32 v14, v14, v15, vcc\n"
"v_add_u32_e32 v15, s96, v12\n"
"v_cmp_gt_u32_e32 vcc, 7, v12\n"
"v_mov_b32_e32 v19, 0x3f803f80\n"
"v_add_lshl_u32 v5, v5, v12, 4\n"
"v_cndmask_b32_e32 v15, 26, v15, vcc\n"
"v_subrev_co_u32_e32 v16, vcc, 8, v12\n"
"v_add_u32_e32 v16, s29, v16\n"
"v_cndmask_b32_e64 v14, v14, v16, s[0:1]\n"
"s_movk_i32 s0, 0x48\n"
"v_add_u32_e32 v16, 0x1fffffe0, v14\n"
"v_lshl_add_u32 v17, v14, 2, s86\n"
"v_cmp_gt_u32_e64 s[0:1], s0, v14\n"
"v_add_u32_e32 v15, s87, v15\n"
"s_mul_i32 s16, s18, 54\n"
"v_cndmask_b32_e64 v16, v17, v16, s[0:1]\n"
"v_and_b32_e32 v17, 15, v14\n"
"v_cmp_gt_u32_e64 s[0:1], 48, v14\n"
"v_lshl_add_u32 v24, s30, 9, v8\n"
"v_lshl_add_u32 v8, s19, 9, v8\n"
"v_cndmask_b32_e64 v14, v16, v17, s[0:1]\n"
"v_add_u32_e32 v14, s79, v14\n"
"v_cndmask_b32_e32 v14, v14, v15, vcc\n"
"v_or_b32_e32 v15, s90, v13\n"
"v_mov_b32_e32 v16, 0x2f000\n"
"v_lshl_add_u32 v15, v15, 4, v16\n"
"v_mov_b32_e32 v16, 0xa000\n"
"v_lshl_add_u32 v14, v14, 3, v16\n"
"v_cmp_gt_u32_e32 vcc, 32, v6\n"
"v_mov_b32_e32 v16, s94\n"
"v_mov_b32_e32 v17, s93\n"
"v_cndmask_b32_e32 v14, v14, v15, vcc\n"
"v_and_b32_e32 v15, 1, v0\n"
"v_cmp_eq_u32_e32 vcc, 0, v15\n"
"s_mul_i32 s1, s19, 0x3c00\n"
"s_mul_i32 s18, s18, 0x19000\n"
"v_cndmask_b32_e32 v15, v16, v17, vcc\n"
"v_mov_b32_e32 v16, s92\n"
"v_mov_b32_e32 v17, s91\n"
"v_cndmask_b32_e32 v16, v16, v17, vcc\n"
"v_and_b32_e32 v17, 2, v0\n"
"v_cmp_eq_u32_e32 vcc, 0, v17\n"
"v_lshlrev_b32_e32 v17, 3, v3\n"
"v_mul_u32_u24_e32 v3, 0x1400, v3\n"
"v_cndmask_b32_e32 v15, v15, v16, vcc\n"
"v_mov_b32_e32 v16, 0x8000\n"
"v_lshl_add_u32 v16, v0, 3, v16\n"
"v_and_b32_e32 v0, 48, v0\n"
"v_or_b32_e32 v67, s35, v0\n"
"s_cmp_gt_u32 s30, 1\n"
"s_cselect_b32 s35, s50, 0\n"
"s_lshl_b32 s0, s14, 10\n"
"v_cmp_eq_u32_e32 vcc, 20, v12\n"
"s_add_i32 s34, s0, s34\n"
"s_mul_i32 s0, s30, 0x3c00\n"
"v_cndmask_b32_e32 v12, 0, v19, vcc\n"
"v_or_b32_e32 v19, 0x19000, v9\n"
"v_add_u32_e32 v20, s0, v19\n"
"s_mul_i32 s0, s22, 0x2800\n"
"v_or_b32_e32 v23, s0, v9\n"
"s_lshl_b32 s0, s17, 5\n"
"s_add_i32 s0, s27, s0\n"
"s_mulk_i32 s0, 0xa00\n"
"s_lshl_b32 s50, s13, 12\n"
"s_add_i32 s0, s0, 0x58000\n"
"s_add_u32 s0, s66, s0\n"
"v_add_u32_e32 v22, s1, v19\n"
"s_addc_u32 s1, s67, 0\n"
"s_lshl_b32 s12, s17, 1\n"
"s_add_i32 s12, s12, s13\n"
"s_mul_i32 s12, s12, 0x14000\n"
"s_add_i32 s12, s12, 0x2d8a00\n"
"s_add_u32 s12, s66, s12\n"
"s_mul_i32 s14, s23, 54\n"
"s_addc_u32 s13, s67, 0\n"
"s_add_i32 s14, s14, s29\n"
"s_lshl_b32 s14, s14, 12\n"
"s_add_i32 s14, s14, 0x7d8000\n"
"s_add_u32 s14, s66, s14\n"
"s_addc_u32 s15, s67, 0\n"
"s_add_i32 s16, s16, s29\n"
"v_lshl_or_b32 v3, v13, 3, v3\n"
"s_mulk_i32 s16, 0x1400\n"
"v_add_u32_e32 v3, 0x800, v3\n"
"s_add_i32 s16, s16, 0x1118800\n"
"v_lshl_add_u32 v25, s30, 7, v3\n"
"v_lshl_add_u32 v3, s19, 7, v3\n"
"s_add_u32 s16, s66, s16\n"
"s_mul_i32 s19, s30, 0x5000\n"
"s_addc_u32 s17, s67, 0\n"
"s_add_i32 s18, s19, s18\n"
"s_mulk_i32 s20, 0x3c00\n"
"s_add_i32 s18, s18, 0x1334000\n"
"v_add_u32_e32 v21, s20, v19\n"
"s_add_u32 s18, s66, s18\n"
"s_mul_i32 s20, s21, 0xa00\n"
"s_addc_u32 s19, s67, 0\n"
"s_add_i32 s20, s20, 0xe98000\n"
"s_add_u32 s20, s66, s20\n"
"s_addc_u32 s21, s67, 0\n"
"s_lshl_b32 s23, s23, 1\n"
"v_or_b32_e32 v65, s51, v0\n"
"s_mul_i32 s51, s22, 0xa000\n"
"s_or_b32 s22, s23, s22\n"
"s_mul_i32 s22, s22, 0xa000\n"
"s_add_u32 s22, s66, s22\n"
"s_addc_u32 s23, s67, 0\n"
"s_add_u32 s22, s22, 0xe98a00\n"
"s_mulk_i32 s24, 0x3c00\n"
"s_addc_u32 s23, s23, 0\n"
"v_add_u32_e32 v19, s24, v19\n"
"s_add_u32 s24, s60, s26\n"
"v_lshl_or_b32 v71, s25, 5, v17\n"
"s_addc_u32 s25, s61, 0\n"
"s_add_u32 s26, s76, s26\n"
"s_addc_u32 s27, s77, 0\n"
"v_or_b32_e32 v66, s52, v0\n"
"v_or_b32_e32 v68, s53, v0\n"
"v_mov_b32_e32 v18, 0x1000\n"
"v_mul_u32_u24_e32 v13, 0x500, v13\n"
"v_or_b32_e32 v17, 0x80, v17\n"
"s_add_u32 s28, s74, s28\n"
"v_bitop3_b32 v18, s50, v18, v9 bitop3:0x36\n"
"v_or_b32_e32 v23, 0x14000, v23\n"
"v_lshlrev_b32_e32 v6, 3, v6\n"
"v_or_b32_e32 v26, v13, v0\n"
"v_or_b32_e32 v13, v17, v13\n"
"s_addc_u32 s29, s75, 0\n"
"v_or_b32_e32 v27, s50, v9\n"
"v_or_b32_e32 v28, s33, v9\n"
"v_or_b32_e32 v29, s51, v9\n"
"global_load_dword v34, v14, s[66:67] offset:0 sc1\n"
"global_load_dword v33, v16, s[66:67] offset:0 sc1\n"
"v_lshrrev_b32 v50, 4, v9\n"
"s_cmp_eq_u32 s30, 0\n"
"s_cselect_b64 s[50:51], 4, 0\n"
"s_mov_b32 s52, s31\n"
"s_and_b32 s46, s52, 3\n"
"s_mul_i32 s46, s46, 20\n"
"v_add_u32 v52, 0, v50\n"
"v_mul_u32_u24 v53, 205, v52\n"
"v_lshrrev_b32 v53, 14, v53\n"
"v_mul_u32_u24 v54, 80, v53\n"
"v_sub_u32 v52, v52, v54\n"
"v_lshlrev_b32 v53, 2, v53\n"
"v_xor_b32 v52, v52, v53\n"
"v_add_lshl_u32 v36, v52, v54, 4\n"
"v_subrev_u32 v52, s46, v52\n"
"v_cmp_gt_u32 s[36:37], 20, v52\n"
"v_add_u32 v52, 64, v50\n"
"v_mul_u32_u24 v53, 205, v52\n"
"v_lshrrev_b32 v53, 14, v53\n"
"v_mul_u32_u24 v54, 80, v53\n"
"v_sub_u32 v52, v52, v54\n"
"v_lshlrev_b32 v53, 2, v53\n"
"v_xor_b32 v52, v52, v53\n"
"v_add_lshl_u32 v37, v52, v54, 4\n"
"v_subrev_u32 v52, s46, v52\n"
"v_cmp_gt_u32 s[40:41], 20, v52\n"
"v_add_u32 v52, 128, v50\n"
"v_mul_u32_u24 v53, 205, v52\n"
"v_lshrrev_b32 v53, 14, v53\n"
"v_mul_u32_u24 v54, 80, v53\n"
"v_sub_u32 v52, v52, v54\n"
"v_lshlrev_b32 v53, 2, v53\n"
"v_xor_b32 v52, v52, v53\n"
"v_add_lshl_u32 v38, v52, v54, 4\n"
"v_subrev_u32 v52, s46, v52\n"
"v_cmp_gt_u32 s[82:83], 20, v52\n"
"v_add_u32 v52, 192, v50\n"
"v_mul_u32_u24 v53, 205, v52\n"
"v_lshrrev_b32 v53, 14, v53\n"
"v_mul_u32_u24 v54, 80, v53\n"
"v_sub_u32 v52, v52, v54\n"
"v_lshlrev_b32 v53, 2, v53\n"
"v_xor_b32 v52, v52, v53\n"
"v_add_lshl_u32 v39, v52, v54, 4\n"
"v_subrev_u32 v52, s46, v52\n"
"v_cmp_gt_u32 s[84:85], 20, v52\n"
"v_add_u32 v52, 256, v50\n"
"v_mul_u32_u24 v53, 205, v52\n"
"v_lshrrev_b32 v53, 14, v53\n"
"v_mul_u32_u24 v54, 80, v53\n"
"v_sub_u32 v52, v52, v54\n"
"v_lshlrev_b32 v53, 2, v53\n"
"v_xor_b32 v52, v52, v53\n"
"v_add_lshl_u32 v40, v52, v54, 4\n"
"v_subrev_u32 v52, s46, v52\n"
"v_cmp_gt_u32 s[88:89], 20, v52\n"
"v_lshrrev_b32 v52, 2, v50\n"
"v_mul_u32_u24 v52, 1280, v52\n"
"v_and_b32 v53, 12, v50\n"
"v_and_b32 v54, 3, v50\n"
"v_bfe_u32 v55, v50, 1, 1\n"
"v_and_b32 v56, 1, v50\n"
"v_lshl_add_u32 v56, v56, 3, v52\n"
"s_mul_i32 s46, s30, 20\n"
"v_add3_u32 v44, v54, s46, 0\n"
"v_xor_b32 v44, v44, v53\n"
"v_lshl_add_u32 v44, v44, 4, v52\n"
"v_add3_u32 v45, v54, s46, 4\n"
"v_xor_b32 v45, v45, v53\n"
"v_lshl_add_u32 v45, v45, 4, v52\n"
"v_add3_u32 v46, v55, s46, 8\n"
"v_xor_b32 v46, v46, v53\n"
"v_lshl_add_u32 v46, v46, 4, v56\n"
"v_add3_u32 v47, v54, s46, 10\n"
"v_xor_b32 v47, v47, v53\n"
"v_lshl_add_u32 v47, v47, 4, v52\n"
"v_add3_u32 v48, v54, s46, 14\n"
"v_xor_b32 v48, v48, v53\n"
"v_lshl_add_u32 v48, v48, 4, v52\n"
"v_add3_u32 v49, v55, s46, 18\n"
"v_xor_b32 v49, v49, v53\n"
"v_lshl_add_u32 v49, v49, 4, v56\n"
"v_mov_b32 v52, 0x3f803f80\n"
"v_and_b32 v53, 15, v50\n"
"s_waitcnt vmcnt(21)\n"
"v_dot2_f32_bf16 v60, v176, v52, 0\n"
"v_dot2_f32_bf16 v61, v176, v176, 0\n"
"v_dot2_f32_bf16 v62, v177, v52, 0\n"
"v_dot2_f32_bf16 v63, v177, v177, 0\n"
"global_load_dwordx4 v[96:99], v58, s[38:39] offset:0\n"
"v_dot2_f32_bf16 v64, v178, v52, 0\n"
"v_dot2_f32_bf16 v65, v178, v178, 0\n"
"v_dot2_f32_bf16 v66, v179, v52, 0\n"
"v_dot2_f32_bf16 v67, v179, v179, 0\n"
"s_waitcnt vmcnt(21)\n"
"v_dot2_f32_bf16 v60, v180, v52, v60\n"
"v_dot2_f32_bf16 v61, v180, v180, v61\n"
"v_dot2_f32_bf16 v62, v181, v52, v62\n"
"v_dot2_f32_bf16 v63, v181, v181, v63\n"
"global_load_dwordx4 v[100:103], v58, s[38:39] offset:256\n"
"v_dot2_f32_bf16 v64, v182, v52, v64\n"
"v_dot2_f32_bf16 v65, v182, v182, v65\n"
"v_dot2_f32_bf16 v66, v183, v52, v66\n"
"v_dot2_f32_bf16 v67, v183, v183, v67\n"
"s_waitcnt vmcnt(21)\n"
"v_dot2_f32_bf16 v60, v184, v52, v60\n"
"v_dot2_f32_bf16 v61, v184, v184, v61\n"
"v_dot2_f32_bf16 v62, v185, v52, v62\n"
"v_dot2_f32_bf16 v63, v185, v185, v63\n"
"global_load_dwordx4 v[104:107], v58, s[38:39] offset:512\n"
"v_dot2_f32_bf16 v64, v186, v52, v64\n"
"v_dot2_f32_bf16 v65, v186, v186, v65\n"
"v_dot2_f32_bf16 v66, v187, v52, v66\n"
"v_dot2_f32_bf16 v67, v187, v187, v67\n"
"s_waitcnt vmcnt(21)\n"
"v_dot2_f32_bf16 v60, v188, v52, v60\n"
"v_dot2_f32_bf16 v61, v188, v188, v61\n"
"v_dot2_f32_bf16 v62, v189, v52, v62\n"
"v_dot2_f32_bf16 v63, v189, v189, v63\n"
"global_load_dwordx4 v[108:111], v58, s[38:39] offset:768\n"
"v_dot2_f32_bf16 v64, v190, v52, v64\n"
"v_dot2_f32_bf16 v65, v190, v190, v65\n"
"v_dot2_f32_bf16 v66, v191, v52, v66\n"
"v_dot2_f32_bf16 v67, v191, v191, v67\n"
"s_waitcnt vmcnt(21)\n"
"v_dot2_f32_bf16 v60, v192, v52, v60\n"
"v_dot2_f32_bf16 v61, v192, v192, v61\n"
"v_dot2_f32_bf16 v62, v193, v52, v62\n"
"v_dot2_f32_bf16 v63, v193, v193, v63\n"
"global_load_dwordx4 v[112:115], v58, s[38:39] offset:1024\n"
"s_add_u32 s38, s38, 5120\n"
"s_addc_u32 s39, s39, 0\n"
"v_dot2_f32_bf16 v64, v194, v52, v64\n"
"v_dot2_f32_bf16 v65, v194, v194, v65\n"
"v_dot2_f32_bf16 v66, v195, v52, v66\n"
"v_dot2_f32_bf16 v67, v195, v195, v67\n"
"v_cmp_eq_u32 vcc, 0, v53\n"
"s_nop 1\n"
"v_pk_add_f32 v[60:61], v[60:61], v[62:63]\n"
"v_pk_add_f32 v[64:65], v[64:65], v[66:67]\n"
"v_pk_add_f32 v[60:61], v[60:61], v[64:65]\n"
"s_nop 1\n"
"v_add_f32_dpp v60, v60, v60 quad_perm:[1,0,3,2] row_mask:0xf bank_mask:0xf\n"
"v_add_f32_dpp v61, v61, v61 quad_perm:[1,0,3,2] row_mask:0xf bank_mask:0xf\n"
"s_nop 0\n"
"v_add_f32_dpp v60, v60, v60 quad_perm:[2,3,0,1] row_mask:0xf bank_mask:0xf\n"
"v_add_f32_dpp v61, v61, v61 quad_perm:[2,3,0,1] row_mask:0xf bank_mask:0xf\n"
"s_nop 0\n"
"v_add_f32_dpp v60, v60, v60 row_ror:4 row_mask:0xf bank_mask:0xf\n"
"v_add_f32_dpp v61, v61, v61 row_ror:4 row_mask:0xf bank_mask:0xf\n"
"s_nop 0\n"
"v_add_f32_dpp v60, v60, v60 row_ror:8 row_mask:0xf bank_mask:0xf\n"
"v_add_f32_dpp v61, v61, v61 row_ror:8 row_mask:0xf bank_mask:0xf\n"
"v_cndmask_b32 v68, v68, v60, vcc\n"
"v_cndmask_b32 v69, v69, v61, vcc\n"
"s_waitcnt vmcnt(21)\n"
"v_dot2_f32_bf16 v60, v196, v52, 0\n"
"v_dot2_f32_bf16 v61, v196, v196, 0\n"
"v_dot2_f32_bf16 v62, v197, v52, 0\n"
"v_dot2_f32_bf16 v63, v197, v197, 0\n"
"global_load_dwordx4 v[116:119], v58, s[38:39] offset:0\n"
"v_dot2_f32_bf16 v64, v198, v52, 0\n"
"v_dot2_f32_bf16 v65, v198, v198, 0\n"
"v_dot2_f32_bf16 v66, v199, v52, 0\n"
"v_dot2_f32_bf16 v67, v199, v199, 0\n"
"s_waitcnt vmcnt(21)\n"
"v_dot2_f32_bf16 v60, v200, v52, v60\n"
"v_dot2_f32_bf16 v61, v200, v200, v61\n"
"v_dot2_f32_bf16 v62, v201, v52, v62\n"
"v_dot2_f32_bf16 v63, v201, v201, v63\n"
"global_load_dwordx4 v[120:123], v58, s[38:39] offset:256\n"
"v_dot2_f32_bf16 v64, v202, v52, v64\n"
"v_dot2_f32_bf16 v65, v202, v202, v65\n"
"v_dot2_f32_bf16 v66, v203, v52, v66\n"
"v_dot2_f32_bf16 v67, v203, v203, v67\n"
"s_waitcnt vmcnt(21)\n"
"v_dot2_f32_bf16 v60, v204, v52, v60\n"
"v_dot2_f32_bf16 v61, v204, v204, v61\n"
"v_dot2_f32_bf16 v62, v205, v52, v62\n"
"v_dot2_f32_bf16 v63, v205, v205, v63\n"
"global_load_dwordx4 v[124:127], v58, s[38:39] offset:512\n"
"v_dot2_f32_bf16 v64, v206, v52, v64\n"
"v_dot2_f32_bf16 v65, v206, v206, v65\n"
"v_dot2_f32_bf16 v66, v207, v52, v66\n"
"v_dot2_f32_bf16 v67, v207, v207, v67\n"
"s_waitcnt vmcnt(21)\n"
"v_dot2_f32_bf16 v60, v208, v52, v60\n"
"v_dot2_f32_bf16 v61, v208, v208, v61\n"
"v_dot2_f32_bf16 v62, v209, v52, v62\n"
"v_dot2_f32_bf16 v63, v209, v209, v63\n"
"global_load_dwordx4 v[128:131], v58, s[38:39] offset:768\n"
"v_dot2_f32_bf16 v64, v210, v52, v64\n"
"v_dot2_f32_bf16 v65, v210, v210, v65\n"
"v_dot2_f32_bf16 v66, v211, v52, v66\n"
"v_dot2_f32_bf16 v67, v211, v211, v67\n"
"s_waitcnt vmcnt(21)\n"
"v_dot2_f32_bf16 v60, v212, v52, v60\n"
"v_dot2_f32_bf16 v61, v212, v212, v61\n"
"v_dot2_f32_bf16 v62, v213, v52, v62\n"
"v_dot2_f32_bf16 v63, v213, v213, v63\n"
"global_load_dwordx4 v[132:135], v58, s[38:39] offset:1024\n"
"s_add_u32 s38, s38, 5120\n"
"s_addc_u32 s39, s39, 0\n"
"v_dot2_f32_bf16 v64, v214, v52, v64\n"
"v_dot2_f32_bf16 v65, v214, v214, v65\n"
"v_dot2_f32_bf16 v66, v215, v52, v66\n"
"v_dot2_f32_bf16 v67, v215, v215, v67\n"
"v_cmp_eq_u32 vcc, 1, v53\n"
"s_nop 1\n"
"v_pk_add_f32 v[60:61], v[60:61], v[62:63]\n"
"v_pk_add_f32 v[64:65], v[64:65], v[66:67]\n"
"v_pk_add_f32 v[60:61], v[60:61], v[64:65]\n"
"s_nop 1\n"
"v_add_f32_dpp v60, v60, v60 quad_perm:[1,0,3,2] row_mask:0xf bank_mask:0xf\n"
"v_add_f32_dpp v61, v61, v61 quad_perm:[1,0,3,2] row_mask:0xf bank_mask:0xf\n"
"s_nop 0\n"
"v_add_f32_dpp v60, v60, v60 quad_perm:[2,3,0,1] row_mask:0xf bank_mask:0xf\n"
"v_add_f32_dpp v61, v61, v61 quad_perm:[2,3,0,1] row_mask:0xf bank_mask:0xf\n"
"s_nop 0\n"
"v_add_f32_dpp v60, v60, v60 row_ror:4 row_mask:0xf bank_mask:0xf\n"
"v_add_f32_dpp v61, v61, v61 row_ror:4 row_mask:0xf bank_mask:0xf\n"
"s_nop 0\n"
"v_add_f32_dpp v60, v60, v60 row_ror:8 row_mask:0xf bank_mask:0xf\n"
"v_add_f32_dpp v61, v61, v61 row_ror:8 row_mask:0xf bank_mask:0xf\n"
"v_cndmask_b32 v68, v68, v60, vcc\n"
"v_cndmask_b32 v69, v69, v61, vcc\n"
"s_waitcnt vmcnt(21)\n"
"v_dot2_f32_bf16 v60, v216, v52, 0\n"
"v_dot2_f32_bf16 v61, v216, v216, 0\n"
"v_dot2_f32_bf16 v62, v217, v52, 0\n"
"v_dot2_f32_bf16 v63, v217, v217, 0\n"
"global_load_dwordx4 v[136:139], v58, s[38:39] offset:0\n"
"v_dot2_f32_bf16 v64, v218, v52, 0\n"
"v_dot2_f32_bf16 v65, v218, v218, 0\n"
"v_dot2_f32_bf16 v66, v219, v52, 0\n"
"v_dot2_f32_bf16 v67, v219, v219, 0\n"
"s_waitcnt vmcnt(21)\n"
"v_dot2_f32_bf16 v60, v220, v52, v60\n"
"v_dot2_f32_bf16 v61, v220, v220, v61\n"
"v_dot2_f32_bf16 v62, v221, v52, v62\n"
"v_dot2_f32_bf16 v63, v221, v221, v63\n"
"global_load_dwordx4 v[140:143], v58, s[38:39] offset:256\n"
"v_dot2_f32_bf16 v64, v222, v52, v64\n"
"v_dot2_f32_bf16 v65, v222, v222, v65\n"
"v_dot2_f32_bf16 v66, v223, v52, v66\n"
"v_dot2_f32_bf16 v67, v223, v223, v67\n"
"s_waitcnt vmcnt(21)\n"
"v_dot2_f32_bf16 v60, v224, v52, v60\n"
"v_dot2_f32_bf16 v61, v224, v224, v61\n"
"v_dot2_f32_bf16 v62, v225, v52, v62\n"
"v_dot2_f32_bf16 v63, v225, v225, v63\n"
"global_load_dwordx4 v[144:147], v58, s[38:39] offset:512\n"
"v_dot2_f32_bf16 v64, v226, v52, v64\n"
"v_dot2_f32_bf16 v65, v226, v226, v65\n"
"v_dot2_f32_bf16 v66, v227, v52, v66\n"
"v_dot2_f32_bf16 v67, v227, v227, v67\n"
"s_waitcnt vmcnt(21)\n"
"v_dot2_f32_bf16 v60, v228, v52, v60\n"
"v_dot2_f32_bf16 v61, v228, v228, v61\n"
"v_dot2_f32_bf16 v62, v229, v52, v62\n"
"v_dot2_f32_bf16 v63, v229, v229, v63\n"
"global_load_dwordx4 v[148:151], v58, s[38:39] offset:768\n"
"v_dot2_f32_bf16 v64, v230, v52, v64\n"
"v_dot2_f32_bf16 v65, v230, v230, v65\n"
"v_dot2_f32_bf16 v66, v231, v52, v66\n"
"v_dot2_f32_bf16 v67, v231, v231, v67\n"
"s_waitcnt vmcnt(21)\n"
"v_dot2_f32_bf16 v60, v232, v52, v60\n"
"v_dot2_f32_bf16 v61, v232, v232, v61\n"
"v_dot2_f32_bf16 v62, v233, v52, v62\n"
"v_dot2_f32_bf16 v63, v233, v233, v63\n"
"global_load_dwordx4 v[152:155], v58, s[38:39] offset:1024\n"
"s_add_u32 s38, s38, 5120\n"
"s_addc_u32 s39, s39, 0\n"
"v_dot2_f32_bf16 v64, v234, v52, v64\n"
"v_dot2_f32_bf16 v65, v234, v234, v65\n"
"v_dot2_f32_bf16 v66, v235, v52, v66\n"
"v_dot2_f32_bf16 v67, v235, v235, v67\n"
"v_cmp_eq_u32 vcc, 2, v53\n"
"s_nop 1\n"
"v_pk_add_f32 v[60:61], v[60:61], v[62:63]\n"
"v_pk_add_f32 v[64:65], v[64:65], v[66:67]\n"
"v_pk_add_f32 v[60:61], v[60:61], v[64:65]\n"
"s_nop 1\n"
"v_add_f32_dpp v60, v60, v60 quad_perm:[1,0,3,2] row_mask:0xf bank_mask:0xf\n"
"v_add_f32_dpp v61, v61, v61 quad_perm:[1,0,3,2] row_mask:0xf bank_mask:0xf\n"
"s_nop 0\n"
"v_add_f32_dpp v60, v60, v60 quad_perm:[2,3,0,1] row_mask:0xf bank_mask:0xf\n"
"v_add_f32_dpp v61, v61, v61 quad_perm:[2,3,0,1] row_mask:0xf bank_mask:0xf\n"
"s_nop 0\n"
"v_add_f32_dpp v60, v60, v60 row_ror:4 row_mask:0xf bank_mask:0xf\n"
"v_add_f32_dpp v61, v61, v61 row_ror:4 row_mask:0xf bank_mask:0xf\n"
"s_nop 0\n"
"v_add_f32_dpp v60, v60, v60 row_ror:8 row_mask:0xf bank_mask:0xf\n"
"v_add_f32_dpp v61, v61, v61 row_ror:8 row_mask:0xf bank_mask:0xf\n"
"v_cndmask_b32 v68, v68, v60, vcc\n"
"v_cndmask_b32 v69, v69, v61, vcc\n"
"s_waitcnt vmcnt(21)\n"
"v_dot2_f32_bf16 v60, v236, v52, 0\n"
"v_dot2_f32_bf16 v61, v236, v236, 0\n"
"v_dot2_f32_bf16 v62, v237, v52, 0\n"
"v_dot2_f32_bf16 v63, v237, v237, 0\n"
"global_load_dwordx4 v[156:159], v58, s[38:39] offset:0\n"
"v_dot2_f32_bf16 v64, v238, v52, 0\n"
"v_dot2_f32_bf16 v65, v238, v238, 0\n"
"v_dot2_f32_bf16 v66, v239, v52, 0\n"
"v_dot2_f32_bf16 v67, v239, v239, 0\n"
"s_waitcnt vmcnt(21)\n"
"v_dot2_f32_bf16 v60, v240, v52, v60\n"
"v_dot2_f32_bf16 v61, v240, v240, v61\n"
"v_dot2_f32_bf16 v62, v241, v52, v62\n"
"v_dot2_f32_bf16 v63, v241, v241, v63\n"
"global_load_dwordx4 v[160:163], v58, s[38:39] offset:256\n"
"v_dot2_f32_bf16 v64, v242, v52, v64\n"
"v_dot2_f32_bf16 v65, v242, v242, v65\n"
"v_dot2_f32_bf16 v66, v243, v52, v66\n"
"v_dot2_f32_bf16 v67, v243, v243, v67\n"
"s_waitcnt vmcnt(21)\n"
"v_dot2_f32_bf16 v60, v244, v52, v60\n"
"v_dot2_f32_bf16 v61, v244, v244, v61\n"
"v_dot2_f32_bf16 v62, v245, v52, v62\n"
"v_dot2_f32_bf16 v63, v245, v245, v63\n"
"global_load_dwordx4 v[164:167], v58, s[38:39] offset:512\n"
"v_dot2_f32_bf16 v64, v246, v52, v64\n"
"v_dot2_f32_bf16 v65, v246, v246, v65\n"
"v_dot2_f32_bf16 v66, v247, v52, v66\n"
"v_dot2_f32_bf16 v67, v247, v247, v67\n"
"s_waitcnt vmcnt(21)\n"
"v_dot2_f32_bf16 v60, v248, v52, v60\n"
"v_dot2_f32_bf16 v61, v248, v248, v61\n"
"v_dot2_f32_bf16 v62, v249, v52, v62\n"
"v_dot2_f32_bf16 v63, v249, v249, v63\n"
"global_load_dwordx4 v[168:171], v58, s[38:39] offset:768\n"
"v_dot2_f32_bf16 v64, v250, v52, v64\n"
"v_dot2_f32_bf16 v65, v250, v250, v65\n"
"v_dot2_f32_bf16 v66, v251, v52, v66\n"
"v_dot2_f32_bf16 v67, v251, v251, v67\n"
"s_waitcnt vmcnt(21)\n"
"v_dot2_f32_bf16 v60, v252, v52, v60\n"
"v_dot2_f32_bf16 v61, v252, v252, v61\n"
"v_dot2_f32_bf16 v62, v253, v52, v62\n"
"v_dot2_f32_bf16 v63, v253, v253, v63\n"
"global_load_dwordx4 v[172:175], v58, s[38:39] offset:1024\n"
"v_dot2_f32_bf16 v64, v254, v52, v64\n"
"v_dot2_f32_bf16 v65, v254, v254, v65\n"
"v_dot2_f32_bf16 v66, v255, v52, v66\n"
"v_dot2_f32_bf16 v67, v255, v255, v67\n"
"v_cmp_eq_u32 vcc, 3, v53\n"
"s_nop 1\n"
"v_pk_add_f32 v[60:61], v[60:61], v[62:63]\n"
"v_pk_add_f32 v[64:65], v[64:65], v[66:67]\n"
"v_pk_add_f32 v[60:61], v[60:61], v[64:65]\n"
"s_nop 1\n"
"v_add_f32_dpp v60, v60, v60 quad_perm:[1,0,3,2] row_mask:0xf bank_mask:0xf\n"
"v_add_f32_dpp v61, v61, v61 quad_perm:[1,0,3,2] row_mask:0xf bank_mask:0xf\n"
"s_nop 0\n"
"v_add_f32_dpp v60, v60, v60 quad_perm:[2,3,0,1] row_mask:0xf bank_mask:0xf\n"
"v_add_f32_dpp v61, v61, v61 quad_perm:[2,3,0,1] row_mask:0xf bank_mask:0xf\n"
"s_nop 0\n"
"v_add_f32_dpp v60, v60, v60 row_ror:4 row_mask:0xf bank_mask:0xf\n"
"v_add_f32_dpp v61, v61, v61 row_ror:4 row_mask:0xf bank_mask:0xf\n"
"s_nop 0\n"
"v_add_f32_dpp v60, v60, v60 row_ror:8 row_mask:0xf bank_mask:0xf\n"
"v_add_f32_dpp v61, v61, v61 row_ror:8 row_mask:0xf bank_mask:0xf\n"
"v_cndmask_b32 v68, v68, v60, vcc\n"
"v_cndmask_b32 v69, v69, v61, vcc\n"
"s_waitcnt vmcnt(19)\n"
"s_nop 0\n"
"v_dot2_f32_bf16 v60, v96, v52, 0\n"
"v_dot2_f32_bf16 v61, v96, v96, 0\n"
"v_dot2_f32_bf16 v62, v97, v52, 0\n"
"v_dot2_f32_bf16 v63, v97, v97, 0\n"
"v_dot2_f32_bf16 v64, v98, v52, 0\n"
"v_dot2_f32_bf16 v65, v98, v98, 0\n"
"v_dot2_f32_bf16 v66, v99, v52, 0\n"
"v_dot2_f32_bf16 v67, v99, v99, 0\n"
"s_waitcnt vmcnt(18)\n"
"s_nop 0\n"
"v_dot2_f32_bf16 v60, v100, v52, v60\n"
"v_dot2_f32_bf16 v61, v100, v100, v61\n"
"v_dot2_f32_bf16 v62, v101, v52, v62\n"
"v_dot2_f32_bf16 v63, v101, v101, v63\n"
"v_dot2_f32_bf16 v64, v102, v52, v64\n"
"v_dot2_f32_bf16 v65, v102, v102, v65\n"
"v_dot2_f32_bf16 v66, v103, v52, v66\n"
"v_dot2_f32_bf16 v67, v103, v103, v67\n"
"s_waitcnt vmcnt(17)\n"
"s_nop 0\n"
"v_dot2_f32_bf16 v60, v104, v52, v60\n"
"v_dot2_f32_bf16 v61, v104, v104, v61\n"
"v_dot2_f32_bf16 v62, v105, v52, v62\n"
"v_dot2_f32_bf16 v63, v105, v105, v63\n"
"v_dot2_f32_bf16 v64, v106, v52, v64\n"
"v_dot2_f32_bf16 v65, v106, v106, v65\n"
"v_dot2_f32_bf16 v66, v107, v52, v66\n"
"v_dot2_f32_bf16 v67, v107, v107, v67\n"
"s_waitcnt vmcnt(16)\n"
"s_nop 0\n"
"v_dot2_f32_bf16 v60, v108, v52, v60\n"
"v_dot2_f32_bf16 v61, v108, v108, v61\n"
"v_dot2_f32_bf16 v62, v109, v52, v62\n"
"v_dot2_f32_bf16 v63, v109, v109, v63\n"
"v_dot2_f32_bf16 v64, v110, v52, v64\n"
"v_dot2_f32_bf16 v65, v110, v110, v65\n"
"v_dot2_f32_bf16 v66, v111, v52, v66\n"
"v_dot2_f32_bf16 v67, v111, v111, v67\n"
"s_waitcnt vmcnt(15)\n"
"s_nop 0\n"
"v_dot2_f32_bf16 v60, v112, v52, v60\n"
"v_dot2_f32_bf16 v61, v112, v112, v61\n"
"v_dot2_f32_bf16 v62, v113, v52, v62\n"
"v_dot2_f32_bf16 v63, v113, v113, v63\n"
"v_dot2_f32_bf16 v64, v114, v52, v64\n"
"v_dot2_f32_bf16 v65, v114, v114, v65\n"
"v_dot2_f32_bf16 v66, v115, v52, v66\n"
"v_dot2_f32_bf16 v67, v115, v115, v67\n"
"v_cmp_eq_u32 vcc, 4, v53\n"
"s_nop 1\n"
"v_pk_add_f32 v[60:61], v[60:61], v[62:63]\n"
"v_pk_add_f32 v[64:65], v[64:65], v[66:67]\n"
"v_pk_add_f32 v[60:61], v[60:61], v[64:65]\n"
"s_nop 1\n"
"v_add_f32_dpp v60, v60, v60 quad_perm:[1,0,3,2] row_mask:0xf bank_mask:0xf\n"
"v_add_f32_dpp v61, v61, v61 quad_perm:[1,0,3,2] row_mask:0xf bank_mask:0xf\n"
"s_nop 0\n"
"v_add_f32_dpp v60, v60, v60 quad_perm:[2,3,0,1] row_mask:0xf bank_mask:0xf\n"
"v_add_f32_dpp v61, v61, v61 quad_perm:[2,3,0,1] row_mask:0xf bank_mask:0xf\n"
"s_nop 0\n"
"v_add_f32_dpp v60, v60, v60 row_ror:4 row_mask:0xf bank_mask:0xf\n"
"v_add_f32_dpp v61, v61, v61 row_ror:4 row_mask:0xf bank_mask:0xf\n"
"s_nop 0\n"
"v_add_f32_dpp v60, v60, v60 row_ror:8 row_mask:0xf bank_mask:0xf\n"
"v_add_f32_dpp v61, v61, v61 row_ror:8 row_mask:0xf bank_mask:0xf\n"
"v_cndmask_b32 v68, v68, v60, vcc\n"
"v_cndmask_b32 v69, v69, v61, vcc\n"
"s_waitcnt vmcnt(14)\n"
"s_nop 0\n"
"v_dot2_f32_bf16 v60, v116, v52, 0\n"
"v_dot2_f32_bf16 v61, v116, v116, 0\n"
"v_dot2_f32_bf16 v62, v117, v52, 0\n"
"v_dot2_f32_bf16 v63, v117, v117, 0\n"
"v_dot2_f32_bf16 v64, v118, v52, 0\n"
"v_dot2_f32_bf16 v65, v118, v118, 0\n"
"v_dot2_f32_bf16 v66, v119, v52, 0\n"
"v_dot2_f32_bf16 v67, v119, v119, 0\n"
"s_waitcnt vmcnt(13)\n"
"s_nop 0\n"
"v_dot2_f32_bf16 v60, v120, v52, v60\n"
"v_dot2_f32_bf16 v61, v120, v120, v61\n"
"v_dot2_f32_bf16 v62, v121, v52, v62\n"
"v_dot2_f32_bf16 v63, v121, v121, v63\n"
"v_dot2_f32_bf16 v64, v122, v52, v64\n"
"v_dot2_f32_bf16 v65, v122, v122, v65\n"
"v_dot2_f32_bf16 v66, v123, v52, v66\n"
"v_dot2_f32_bf16 v67, v123, v123, v67\n"
"s_waitcnt vmcnt(12)\n"
"s_nop 0\n"
"v_dot2_f32_bf16 v60, v124, v52, v60\n"
"v_dot2_f32_bf16 v61, v124, v124, v61\n"
"v_dot2_f32_bf16 v62, v125, v52, v62\n"
"v_dot2_f32_bf16 v63, v125, v125, v63\n"
"v_dot2_f32_bf16 v64, v126, v52, v64\n"
"v_dot2_f32_bf16 v65, v126, v126, v65\n"
"v_dot2_f32_bf16 v66, v127, v52, v66\n"
"v_dot2_f32_bf16 v67, v127, v127, v67\n"
"s_waitcnt vmcnt(11)\n"
"s_nop 0\n"
"v_dot2_f32_bf16 v60, v128, v52, v60\n"
"v_dot2_f32_bf16 v61, v128, v128, v61\n"
"v_dot2_f32_bf16 v62, v129, v52, v62\n"
"v_dot2_f32_bf16 v63, v129, v129, v63\n"
"v_dot2_f32_bf16 v64, v130, v52, v64\n"
"v_dot2_f32_bf16 v65, v130, v130, v65\n"
"v_dot2_f32_bf16 v66, v131, v52, v66\n"
"v_dot2_f32_bf16 v67, v131, v131, v67\n"
"s_waitcnt vmcnt(10)\n"
"s_nop 0\n"
"v_dot2_f32_bf16 v60, v132, v52, v60\n"
"v_dot2_f32_bf16 v61, v132, v132, v61\n"
"v_dot2_f32_bf16 v62, v133, v52, v62\n"
"v_dot2_f32_bf16 v63, v133, v133, v63\n"
"v_dot2_f32_bf16 v64, v134, v52, v64\n"
"v_dot2_f32_bf16 v65, v134, v134, v65\n"
"v_dot2_f32_bf16 v66, v135, v52, v66\n"
"v_dot2_f32_bf16 v67, v135, v135, v67\n"
"v_cmp_eq_u32 vcc, 5, v53\n"
"s_nop 1\n"
"v_pk_add_f32 v[60:61], v[60:61], v[62:63]\n"
"v_pk_add_f32 v[64:65], v[64:65], v[66:67]\n"
"v_pk_add_f32 v[60:61], v[60:61], v[64:65]\n"
"s_nop 1\n"
"v_add_f32_dpp v60, v60, v60 quad_perm:[1,0,3,2] row_mask:0xf bank_mask:0xf\n"
"v_add_f32_dpp v61, v61, v61 quad_perm:[1,0,3,2] row_mask:0xf bank_mask:0xf\n"
"s_nop 0\n"
"v_add_f32_dpp v60, v60, v60 quad_perm:[2,3,0,1] row_mask:0xf bank_mask:0xf\n"
"v_add_f32_dpp v61, v61, v61 quad_perm:[2,3,0,1] row_mask:0xf bank_mask:0xf\n"
"s_nop 0\n"
"v_add_f32_dpp v60, v60, v60 row_ror:4 row_mask:0xf bank_mask:0xf\n"
"v_add_f32_dpp v61, v61, v61 row_ror:4 row_mask:0xf bank_mask:0xf\n"
"s_nop 0\n"
"v_add_f32_dpp v60, v60, v60 row_ror:8 row_mask:0xf bank_mask:0xf\n"
"v_add_f32_dpp v61, v61, v61 row_ror:8 row_mask:0xf bank_mask:0xf\n"
"v_cndmask_b32 v68, v68, v60, vcc\n"
"v_cndmask_b32 v69, v69, v61, vcc\n"
"s_waitcnt vmcnt(9)\n"
"s_nop 0\n"
"v_dot2_f32_bf16 v60, v136, v52, 0\n"
"v_dot2_f32_bf16 v61, v136, v136, 0\n"
"v_dot2_f32_bf16 v62, v137, v52, 0\n"
"v_dot2_f32_bf16 v63, v137, v137, 0\n"
"v_dot2_f32_bf16 v64, v138, v52, 0\n"
"v_dot2_f32_bf16 v65, v138, v138, 0\n"
"v_dot2_f32_bf16 v66, v139, v52, 0\n"
"v_dot2_f32_bf16 v67, v139, v139, 0\n"
"s_waitcnt vmcnt(8)\n"
"s_nop 0\n"
"v_dot2_f32_bf16 v60, v140, v52, v60\n"
"v_dot2_f32_bf16 v61, v140, v140, v61\n"
"v_dot2_f32_bf16 v62, v141, v52, v62\n"
"v_dot2_f32_bf16 v63, v141, v141, v63\n"
"v_dot2_f32_bf16 v64, v142, v52, v64\n"
"v_dot2_f32_bf16 v65, v142, v142, v65\n"
"v_dot2_f32_bf16 v66, v143, v52, v66\n"
"v_dot2_f32_bf16 v67, v143, v143, v67\n"
"s_waitcnt vmcnt(7)\n"
"s_nop 0\n"
"v_dot2_f32_bf16 v60, v144, v52, v60\n"
"v_dot2_f32_bf16 v61, v144, v144, v61\n"
"v_dot2_f32_bf16 v62, v145, v52, v62\n"
"v_dot2_f32_bf16 v63, v145, v145, v63\n"
"v_dot2_f32_bf16 v64, v146, v52, v64\n"
"v_dot2_f32_bf16 v65, v146, v146, v65\n"
"v_dot2_f32_bf16 v66, v147, v52, v66\n"
"v_dot2_f32_bf16 v67, v147, v147, v67\n"
"s_waitcnt vmcnt(6)\n"
"s_nop 0\n"
"v_dot2_f32_bf16 v60, v148, v52, v60\n"
"v_dot2_f32_bf16 v61, v148, v148, v61\n"
"v_dot2_f32_bf16 v62, v149, v52, v62\n"
"v_dot2_f32_bf16 v63, v149, v149, v63\n"
"v_dot2_f32_bf16 v64, v150, v52, v64\n"
"v_dot2_f32_bf16 v65, v150, v150, v65\n"
"v_dot2_f32_bf16 v66, v151, v52, v66\n"
"v_dot2_f32_bf16 v67, v151, v151, v67\n"
"s_waitcnt vmcnt(5)\n"
"s_nop 0\n"
"v_dot2_f32_bf16 v60, v152, v52, v60\n"
"v_dot2_f32_bf16 v61, v152, v152, v61\n"
"v_dot2_f32_bf16 v62, v153, v52, v62\n"
"v_dot2_f32_bf16 v63, v153, v153, v63\n"
"v_dot2_f32_bf16 v64, v154, v52, v64\n"
"v_dot2_f32_bf16 v65, v154, v154, v65\n"
"v_dot2_f32_bf16 v66, v155, v52, v66\n"
"v_dot2_f32_bf16 v67, v155, v155, v67\n"
"v_cmp_eq_u32 vcc, 6, v53\n"
"s_nop 1\n"
"v_pk_add_f32 v[60:61], v[60:61], v[62:63]\n"
"v_pk_add_f32 v[64:65], v[64:65], v[66:67]\n"
"v_pk_add_f32 v[60:61], v[60:61], v[64:65]\n"
"s_nop 1\n"
"v_add_f32_dpp v60, v60, v60 quad_perm:[1,0,3,2] row_mask:0xf bank_mask:0xf\n"
"v_add_f32_dpp v61, v61, v61 quad_perm:[1,0,3,2] row_mask:0xf bank_mask:0xf\n"
"s_nop 0\n"
"v_add_f32_dpp v60, v60, v60 quad_perm:[2,3,0,1] row_mask:0xf bank_mask:0xf\n"
"v_add_f32_dpp v61, v61, v61 quad_perm:[2,3,0,1] row_mask:0xf bank_mask:0xf\n"
"s_nop 0\n"
"v_add_f32_dpp v60, v60, v60 row_ror:4 row_mask:0xf bank_mask:0xf\n"
"v_add_f32_dpp v61, v61, v61 row_ror:4 row_mask:0xf bank_mask:0xf\n"
"s_nop 0\n"
"v_add_f32_dpp v60, v60, v60 row_ror:8 row_mask:0xf bank_mask:0xf\n"
"v_add_f32_dpp v61, v61, v61 row_ror:8 row_mask:0xf bank_mask:0xf\n"
"v_cndmask_b32 v68, v68, v60, vcc\n"
"v_cndmask_b32 v69, v69, v61, vcc\n"
"s_waitcnt vmcnt(4)\n"
"s_nop 0\n"
"v_dot2_f32_bf16 v60, v156, v52, 0\n"
"v_dot2_f32_bf16 v61, v156, v156, 0\n"
"v_dot2_f32_bf16 v62, v157, v52, 0\n"
"v_dot2_f32_bf16 v63, v157, v157, 0\n"
"v_dot2_f32_bf16 v64, v158, v52, 0\n"
"v_dot2_f32_bf16 v65, v158, v158, 0\n"
"v_dot2_f32_bf16 v66, v159, v52, 0\n"
"v_dot2_f32_bf16 v67, v159, v159, 0\n"
"s_waitcnt vmcnt(3)\n"
"s_nop 0\n"
"v_dot2_f32_bf16 v60, v160, v52, v60\n"
"v_dot2_f32_bf16 v61, v160, v160, v61\n"
"v_dot2_f32_bf16 v62, v161, v52, v62\n"
"v_dot2_f32_bf16 v63, v161, v161, v63\n"
"v_dot2_f32_bf16 v64, v162, v52, v64\n"
"v_dot2_f32_bf16 v65, v162, v162, v65\n"
"v_dot2_f32_bf16 v66, v163, v52, v66\n"
"v_dot2_f32_bf16 v67, v163, v163, v67\n"
"s_waitcnt vmcnt(2)\n"
"s_nop 0\n"
"v_dot2_f32_bf16 v60, v164, v52, v60\n"
"v_dot2_f32_bf16 v61, v164, v164, v61\n"
"v_dot2_f32_bf16 v62, v165, v52, v62\n"
"v_dot2_f32_bf16 v63, v165, v165, v63\n"
"v_dot2_f32_bf16 v64, v166, v52, v64\n"
"v_dot2_f32_bf16 v65, v166, v166, v65\n"
"v_dot2_f32_bf16 v66, v167, v52, v66\n"
"v_dot2_f32_bf16 v67, v167, v167, v67\n"
"s_waitcnt vmcnt(1)\n"
"s_nop 0\n"
"v_dot2_f32_bf16 v60, v168, v52, v60\n"
"v_dot2_f32_bf16 v61, v168, v168, v61\n"
"v_dot2_f32_bf16 v62, v169, v52, v62\n"
"v_dot2_f32_bf16 v63, v169, v169, v63\n"
"v_dot2_f32_bf16 v64, v170, v52, v64\n"
"v_dot2_f32_bf16 v65, v170, v170, v65\n"
"v_dot2_f32_bf16 v66, v171, v52, v66\n"
"v_dot2_f32_bf16 v67, v171, v171, v67\n"
"s_waitcnt vmcnt(0)\n"
"s_nop 0\n"
"v_dot2_f32_bf16 v60, v172, v52, v60\n"
"v_dot2_f32_bf16 v61, v172, v172, v61\n"
"v_dot2_f32_bf16 v62, v173, v52, v62\n"
"v_dot2_f32_bf16 v63, v173, v173, v63\n"
"v_dot2_f32_bf16 v64, v174, v52, v64\n"
"v_dot2_f32_bf16 v65, v174, v174, v65\n"
"v_dot2_f32_bf16 v66, v175, v52, v66\n"
"v_dot2_f32_bf16 v67, v175, v175, v67\n"
"v_cmp_eq_u32 vcc, 7, v53\n"
"s_nop 1\n"
"v_pk_add_f32 v[60:61], v[60:61], v[62:63]\n"
"v_pk_add_f32 v[64:65], v[64:65], v[66:67]\n"
"v_pk_add_f32 v[60:61], v[60:61], v[64:65]\n"
"s_nop 1\n"
"v_add_f32_dpp v60, v60, v60 quad_perm:[1,0,3,2] row_mask:0xf bank_mask:0xf\n"
"v_add_f32_dpp v61, v61, v61 quad_perm:[1,0,3,2] row_mask:0xf bank_mask:0xf\n"
"s_nop 0\n"
"v_add_f32_dpp v60, v60, v60 quad_perm:[2,3,0,1] row_mask:0xf bank_mask:0xf\n"
"v_add_f32_dpp v61, v61, v61 quad_perm:[2,3,0,1] row_mask:0xf bank_mask:0xf\n"
"s_nop 0\n"
"v_add_f32_dpp v60, v60, v60 row_ror:4 row_mask:0xf bank_mask:0xf\n"
"v_add_f32_dpp v61, v61, v61 row_ror:4 row_mask:0xf bank_mask:0xf\n"
"s_nop 0\n"
"v_add_f32_dpp v60, v60, v60 row_ror:8 row_mask:0xf bank_mask:0xf\n"
"v_add_f32_dpp v61, v61, v61 row_ror:8 row_mask:0xf bank_mask:0xf\n"
"v_cndmask_b32 v68, v68, v60, vcc\n"
"v_cndmask_b32 v69, v69, v61, vcc\n"
"v_mov_b32 v52, 0x3acccccd\n"
"v_mul_f32 v68, v68, v52\n"
"v_mul_f32 v70, v68, v68\n"
"v_fma_f32 v69, v69, v52, -v70\n"
"v_max_f32 v69, 0, v69\n"
"v_add_f32 v70, 0x3727c5ac, v69\n"
"v_rsq_f32 v70, v70\n"
"v_lshrrev_b32 v54, 4, v50\n"
"v_mul_f32 v69, v69, v70\n"
"v_mul_f32 v69, v69, v70\n"
"v_add_f32 v69, 0x3727c5ac, v69\n"
"v_rsq_f32 v69, v69\n"
"v_lshl_add_u32 v53, v53, 2, v54\n"
"v_lshlrev_b32 v53, 2, v53\n"
"v_mul_f32 v69, v69, v70\n"
"s_mov_b32 exec_lo, 0xff00ff\n"
"s_mov_b32 exec_hi, 0xff00ff\n"
"global_store_dword v53, v68, s[2:3] offset:0 sc1\n"
"global_store_dword v53, v70, s[2:3] offset:256 sc1\n"
"global_store_dword v53, v69, s[2:3] offset:512 sc1\n"
"s_mov_b64 exec, -1\n"
"v_mov_b32 v110, 1\n"
"s_add_u32 s53, s52, 0\n"
"s_lshr_b32 s46, s30, 1\n"
"s_and_b32 s47, s30, 1\n"
"s_mul_i32 s44, s46, 20\n"
"s_mul_i32 s45, s47, 80\n"
"s_add_u32 s44, s44, s45\n"
"s_mul_i32 s45, s53, 160\n"
"s_add_u32 s44, s44, s45\n"
"s_sub_u32 s44, s44, 11184\n"
"s_mul_i32 s46, s46, 8\n"
"s_mul_i32 s47, s47, 20\n"
"s_add_u32 s46, s46, s47\n"
"s_mul_i32 s47, s53, 40\n"
"s_add_u32 s46, s46, s47\n"
"s_sub_u32 s46, s46, 2160\n"
"s_cmp_lt_u32 s53, 70\n"
"s_cselect_b32 s44, s46, s44\n"
"s_cselect_b32 s47, 4, 40\n"
"s_lshl2_add_u32 s46, s53, s30\n"
"s_lshl_b32 s46, s46, 3\n"
"s_cmp_lt_u32 s53, 54\n"
"s_cselect_b32 s46, s46, s44\n"
"s_cselect_b64 s[44:45], s[72:73], s[68:69]\n"
"s_mul_i32 s46, s46, 1280\n"
"s_mul_i32 s47, s47, 1280\n"
"s_add_u32 s44, s44, s46\n"
"s_addc_u32 s45, s45, 0\n"
"s_add_u32 s48, s44, s47\n"
"s_addc_u32 s49, s45, 0\n"
"s_mul_i32 s46, s30, 10240\n"
"s_add_u32 m0, s46, 0\n"
"s_nop 0\n"
"global_load_lds_dwordx4 v36, s[44:45] offset:0\n"
"s_add_u32 m0, s46, 1024\n"
"s_nop 0\n"
"global_load_lds_dwordx4 v37, s[44:45] offset:0\n"
"s_add_u32 m0, s46, 2048\n"
"s_nop 0\n"
"global_load_lds_dwordx4 v38, s[44:45] offset:0\n"
"s_add_u32 m0, s46, 3072\n"
"s_nop 0\n"
"global_load_lds_dwordx4 v39, s[44:45] offset:0\n"
"s_add_u32 m0, s46, 4096\n"
"s_nop 0\n"
"global_load_lds_dwordx4 v40, s[44:45] offset:0\n"
"s_add_u32 m0, s46, 5120\n"
"s_nop 0\n"
"global_load_lds_dwordx4 v36, s[48:49] offset:0\n"
"s_add_u32 m0, s46, 6144\n"
"s_nop 0\n"
"global_load_lds_dwordx4 v37, s[48:49] offset:0\n"
"s_add_u32 m0, s46, 7168\n"
"s_nop 0\n"
"global_load_lds_dwordx4 v38, s[48:49] offset:0\n"
"s_add_u32 m0, s46, 8192\n"
"s_nop 0\n"
"global_load_lds_dwordx4 v39, s[48:49] offset:0\n"
"s_add_u32 m0, s46, 9216\n"
"s_nop 0\n"
"global_load_lds_dwordx4 v40, s[48:49] offset:0\n"
"s_waitcnt vmcnt(10)\n"
"s_barrier\n"
"s_mov_b64 exec, s[50:51]\n"
"global_atomic_add v15, v110, s[66:67]\n"
"s_mov_b64 exec, -1\n"
"s_add_u32 s53, s52, 16\n"
"s_lshr_b32 s46, s30, 1\n"
"s_and_b32 s47, s30, 1\n"
"s_mul_i32 s44, s46, 20\n"
"s_mul_i32 s45, s47, 80\n"
"s_add_u32 s44, s44, s45\n"
"s_mul_i32 s45, s53, 160\n"
"s_add_u32 s44, s44, s45\n"
"s_sub_u32 s44, s44, 11184\n"
"s_mul_i32 s46, s46, 8\n"
"s_mul_i32 s47, s47, 20\n"
"s_add_u32 s46, s46, s47\n"
"s_mul_i32 s47, s53, 40\n"
"s_add_u32 s46, s46, s47\n"
"s_sub_u32 s46, s46, 2160\n"
"s_cmp_lt_u32 s53, 70\n"
"s_cselect_b32 s44, s46, s44\n"
"s_cselect_b32 s47, 4, 40\n"
"s_lshl2_add_u32 s46, s53, s30\n"
"s_lshl_b32 s46, s46, 3\n"
"s_cmp_lt_u32 s53, 54\n"
"s_cselect_b32 s46, s46, s44\n"
"s_cselect_b64 s[44:45], s[72:73], s[68:69]\n"
"s_mul_i32 s46, s46, 1280\n"
"s_mul_i32 s47, s47, 1280\n"
"s_add_u32 s44, s44, s46\n"
"s_addc_u32 s45, s45, 0\n"
"s_add_u32 s48, s44, s47\n"
"s_addc_u32 s49, s45, 0\n"
"s_mul_i32 s46, s30, 10240\n"
"s_add_u32 m0, s46, 40960\n"
"s_nop 0\n"
"global_load_lds_dwordx4 v36, s[44:45] offset:0\n"
"s_add_u32 m0, s46, 41984\n"
"s_nop 0\n"
"global_load_lds_dwordx4 v37, s[44:45] offset:0\n"
"s_add_u32 m0, s46, 43008\n"
"s_nop 0\n"
"global_load_lds_dwordx4 v38, s[44:45] offset:0\n"
"s_add_u32 m0, s46, 44032\n"
"s_nop 0\n"
"global_load_lds_dwordx4 v39, s[44:45] offset:0\n"
"s_add_u32 m0, s46, 45056\n"
"s_nop 0\n"
"global_load_lds_dwordx4 v40, s[44:45] offset:0\n"
"s_add_u32 m0, s46, 46080\n"
"s_nop 0\n"
"global_load_lds_dwordx4 v36, s[48:49] offset:0\n"
"s_add_u32 m0, s46, 47104\n"
"s_nop 0\n"
"global_load_lds_dwordx4 v37, s[48:49] offset:0\n"
"s_add_u32 m0, s46, 48128\n"
"s_nop 0\n"
"global_load_lds_dwordx4 v38, s[48:49] offset:0\n"
"s_add_u32 m0, s46, 49152\n"
"s_nop 0\n"
"global_load_lds_dwordx4 v39, s[48:49] offset:0\n"
"s_add_u32 m0, s46, 50176\n"
"s_nop 0\n"
"global_load_lds_dwordx4 v40, s[48:49] offset:0\n"
"s_cmp_gt_u32 s52, 7\n"
"s_cbranch_scc1 71f\n"
"s_lshr_b32 s52, s52, 2\n"
"s_add_u32 s53, s52, 72\n"
"s_lshr_b32 s46, s30, 1\n"
"s_and_b32 s47, s30, 1\n"
"s_mul_i32 s44, s46, 20\n"
"s_mul_i32 s45, s47, 80\n"
"s_add_u32 s44, s44, s45\n"
"s_mul_i32 s45, s53, 160\n"
"s_add_u32 s44, s44, s45\n"
"s_sub_u32 s44, s44, 11184\n"
"s_mul_i32 s46, s46, 8\n"
"s_mul_i32 s47, s47, 20\n"
"s_add_u32 s46, s46, s47\n"
"s_mul_i32 s47, s53, 40\n"
"s_add_u32 s46, s46, s47\n"
"s_sub_u32 s46, s46, 2160\n"
"s_cmp_lt_u32 s53, 70\n"
"s_cselect_b32 s44, s46, s44\n"
"s_cselect_b32 s47, 4, 40\n"
"s_lshl2_add_u32 s46, s53, s30\n"
"s_lshl_b32 s46, s46, 3\n"
"s_cmp_lt_u32 s53, 54\n"
"s_cselect_b32 s46, s46, s44\n"
"s_cselect_b64 s[44:45], s[72:73], s[68:69]\n"
"s_mul_i32 s46, s46, 1280\n"
"s_mul_i32 s47, s47, 1280\n"
"s_add_u32 s44, s44, s46\n"
"s_addc_u32 s45, s45, 0\n"
"s_add_u32 s48, s44, s47\n"
"s_addc_u32 s49, s45, 0\n"
"s_mul_i32 s46, s30, 10240\n"
"s_add_u32 m0, s46, 122880\n"
"s_mov_b64 exec, s[36:37]\n"
"global_load_lds_dwordx4 v36, s[44:45] offset:0\n"
"s_add_u32 m0, s46, 123904\n"
"s_mov_b64 exec, s[40:41]\n"
"global_load_lds_dwordx4 v37, s[44:45] offset:0\n"
"s_add_u32 m0, s46, 124928\n"
"s_mov_b64 exec, s[82:83]\n"
"global_load_lds_dwordx4 v38, s[44:45] offset:0\n"
"s_add_u32 m0, s46, 125952\n"
"s_mov_b64 exec, s[84:85]\n"
"global_load_lds_dwordx4 v39, s[44:45] offset:0\n"
"s_add_u32 m0, s46, 126976\n"
"s_mov_b64 exec, s[88:89]\n"
"global_load_lds_dwordx4 v40, s[44:45] offset:0\n"
"s_add_u32 m0, s46, 128000\n"
"s_mov_b64 exec, s[36:37]\n"
"global_load_lds_dwordx4 v36, s[48:49] offset:0\n"
"s_add_u32 m0, s46, 129024\n"
"s_mov_b64 exec, s[40:41]\n"
"global_load_lds_dwordx4 v37, s[48:49] offset:0\n"
"s_add_u32 m0, s46, 130048\n"
"s_mov_b64 exec, s[82:83]\n"
"global_load_lds_dwordx4 v38, s[48:49] offset:0\n"
"s_add_u32 m0, s46, 131072\n"
"s_mov_b64 exec, s[84:85]\n"
"global_load_lds_dwordx4 v39, s[48:49] offset:0\n"
"s_add_u32 m0, s46, 132096\n"
"s_mov_b64 exec, s[88:89]\n"
"global_load_lds_dwordx4 v40, s[48:49] offset:0\n"
"s_mov_b64 exec, -1\n"
"s_mov_b32 s52, s31\n"
"71:\n"
"s_add_u32 s53, s52, 32\n"
"s_lshr_b32 s46, s30, 1\n"
"s_and_b32 s47, s30, 1\n"
"s_mul_i32 s44, s46, 20\n"
"s_mul_i32 s45, s47, 80\n"
"s_add_u32 s44, s44, s45\n"
"s_mul_i32 s45, s53, 160\n"
"s_add_u32 s44, s44, s45\n"
"s_sub_u32 s44, s44, 11184\n"
"s_mul_i32 s46, s46, 8\n"
"s_mul_i32 s47, s47, 20\n"
"s_add_u32 s46, s46, s47\n"
"s_mul_i32 s47, s53, 40\n"
"s_add_u32 s46, s46, s47\n"
"s_sub_u32 s46, s46, 2160\n"
"s_cmp_lt_u32 s53, 70\n"
"s_cselect_b32 s44, s46, s44\n"
"s_cselect_b32 s47, 4, 40\n"
"s_lshl2_add_u32 s46, s53, s30\n"
"s_lshl_b32 s46, s46, 3\n"
"s_cmp_lt_u32 s53, 54\n"
"s_cselect_b32 s46, s46, s44\n"
"s_cselect_b64 s[44:45], s[72:73], s[68:69]\n"
"s_mul_i32 s46, s46, 1280\n"
"s_mul_i32 s47, s47, 1280\n"
"s_add_u32 s44, s44, s46\n"
"s_addc_u32 s45, s45, 0\n"
"s_add_u32 s48, s44, s47\n"
"s_addc_u32 s49, s45, 0\n"
"s_mul_i32 s46, s30, 10240\n"
"s_add_u32 m0, s46, 81920\n"
"s_nop 0\n"
"global_load_lds_dwordx4 v36, s[44:45] offset:0\n"
"s_add_u32 m0, s46, 82944\n"
"s_nop 0\n"
"global_load_lds_dwordx4 v37, s[44:45] offset:0\n"
"s_add_u32 m0, s46, 83968\n"
"s_nop 0\n"
"global_load_lds_dwordx4 v38, s[44:45] offset:0\n"
"s_add_u32 m0, s46, 84992\n"
"s_nop 0\n"
"global_load_lds_dwordx4 v39, s[44:45] offset:0\n"
"s_add_u32 m0, s46, 86016\n"
"s_nop 0\n"
"global_load_lds_dwordx4 v40, s[44:45] offset:0\n"
"s_add_u32 m0, s46, 87040\n"
"s_nop 0\n"
"global_load_lds_dwordx4 v36, s[48:49] offset:0\n"
"s_add_u32 m0, s46, 88064\n"
"s_nop 0\n"
"global_load_lds_dwordx4 v37, s[48:49] offset:0\n"
"s_add_u32 m0, s46, 89088\n"
"s_nop 0\n"
"global_load_lds_dwordx4 v38, s[48:49] offset:0\n"
"s_add_u32 m0, s46, 90112\n"
"s_nop 0\n"
"global_load_lds_dwordx4 v39, s[48:49] offset:0\n"
"s_add_u32 m0, s46, 91136\n"
"s_nop 0\n"
"global_load_lds_dwordx4 v40, s[48:49] offset:0\n"
"s_waitcnt vmcnt(20)\n"
"s_barrier\n"
"ds_read_b64_tr_b16 v[128:129], v44 offset:0\n"
"ds_read_b64_tr_b16 v[130:131], v44 offset:20480\n"
"s_add_u32 s53, s52, 0\n"
"s_cmp_lt_u32 s53, 54\n"
"s_cselect_b32 s47, 54, 20\n"
"s_cselect_b32 s46, 0, 378\n"
"s_add_u32 s46, s46, s53\n"
"s_mul_i32 s44, s30, s47\n"
"s_lshl1_add_u32 s46, s44, s46\n"
"s_mul_i32 s46, s46, 5120\n"
"s_add_u32 s46, s46, 0x1118800\n"
"s_mul_i32 s47, s47, 5120\n"
"s_mov_b64 s[42:43], s[66:67]\n"
"s_add_u32 s42, s42, s46\n"
"s_addc_u32 s43, s43, 0\n"
"ds_read_b64_tr_b16 v[132:133], v44 offset:8\n"
"ds_read_b64_tr_b16 v[134:135], v44 offset:20488\n"
"ds_read_b64_tr_b16 v[136:137], v45 offset:0\n"
"ds_read_b64_tr_b16 v[138:139], v45 offset:20480\n"
"ds_read_b64_tr_b16 v[140:141], v45 offset:8\n"
"ds_read_b64_tr_b16 v[142:143], v45 offset:20488\n"
"ds_read_b64_tr_b16 v[144:145], v46 offset:0\n"
"ds_read_b64_tr_b16 v[146:147], v46 offset:20480\n"
"ds_read_b64_tr_b16 v[148:149], v47 offset:0\n"
"ds_read_b64_tr_b16 v[150:151], v47 offset:20480\n"
"ds_read_b64_tr_b16 v[152:153], v47 offset:8\n"
"ds_read_b64_tr_b16 v[154:155], v47 offset:20488\n"
"ds_read_b64_tr_b16 v[156:157], v48 offset:0\n"
"ds_read_b64_tr_b16 v[158:159], v48 offset:20480\n"
"ds_read_b64_tr_b16 v[160:161], v48 offset:8\n"
"ds_read_b64_tr_b16 v[162:163], v48 offset:20488\n"
"ds_read_b64_tr_b16 v[164:165], v49 offset:0\n"
"ds_read_b64_tr_b16 v[166:167], v49 offset:20480\n"
"s_waitcnt lgkmcnt(15)\n"
"global_store_dwordx4 v9, v[128:131], s[42:43] offset:-2048 sc1\n"
"global_store_dwordx4 v9, v[132:135], s[42:43] offset:-1024 sc1\n"
"s_waitcnt lgkmcnt(14)\n"
"global_store_dwordx4 v9, v[136:139], s[42:43] offset:0 sc1\n"
"s_waitcnt lgkmcnt(12)\n"
"global_store_dwordx4 v9, v[140:143], s[42:43] offset:1024 sc1\n"
"s_waitcnt lgkmcnt(10)\n"
"global_store_dwordx4 v9, v[144:147], s[42:43] offset:2048 sc1\n"
"s_add_u32 s42, s42, s47\n"
"s_addc_u32 s43, s43, 0\n"
"s_waitcnt lgkmcnt(8)\n"
"global_store_dwordx4 v9, v[148:151], s[42:43] offset:-2048 sc1\n"
"s_waitcnt lgkmcnt(6)\n"
"global_store_dwordx4 v9, v[152:155], s[42:43] offset:-1024 sc1\n"
"s_waitcnt lgkmcnt(4)\n"
"global_store_dwordx4 v9, v[156:159], s[42:43] offset:0 sc1\n"
"s_waitcnt lgkmcnt(2)\n"
"global_store_dwordx4 v9, v[160:163], s[42:43] offset:1024 sc1\n"
"s_waitcnt lgkmcnt(0)\n"
"global_store_dwordx4 v9, v[164:167], s[42:43] offset:2048 sc1\n"
"s_waitcnt vmcnt(20)\n"
"s_barrier\n"
"s_mov_b64 exec, s[50:51]\n"
"global_atomic_add v15, v110, s[66:67]\n"
"s_mov_b64 exec, -1\n"
"ds_read_b64_tr_b16 v[168:169], v44 offset:40960\n"
"ds_read_b64_tr_b16 v[170:171], v44 offset:61440\n"
"s_add_u32 s53, s52, 16\n"
"s_cmp_lt_u32 s53, 54\n"
"s_cselect_b32 s47, 54, 20\n"
"s_cselect_b32 s46, 0, 378\n"
"s_add_u32 s46, s46, s53\n"
"s_mul_i32 s44, s30, s47\n"
"s_lshl1_add_u32 s46, s44, s46\n"
"s_mul_i32 s46, s46, 5120\n"
"s_add_u32 s46, s46, 0x1118800\n"
"s_mul_i32 s47, s47, 5120\n"
"s_mov_b64 s[42:43], s[66:67]\n"
"s_add_u32 s42, s42, s46\n"
"s_addc_u32 s43, s43, 0\n"
"ds_read_b64_tr_b16 v[172:173], v44 offset:40968\n"
"ds_read_b64_tr_b16 v[174:175], v44 offset:61448\n"
"ds_read_b64_tr_b16 v[176:177], v45 offset:40960\n"
"ds_read_b64_tr_b16 v[178:179], v45 offset:61440\n"
"ds_read_b64_tr_b16 v[180:181], v45 offset:40968\n"
"ds_read_b64_tr_b16 v[182:183], v45 offset:61448\n"
"ds_read_b64_tr_b16 v[184:185], v46 offset:40960\n"
"ds_read_b64_tr_b16 v[186:187], v46 offset:61440\n"
"ds_read_b64_tr_b16 v[188:189], v47 offset:40960\n"
"ds_read_b64_tr_b16 v[190:191], v47 offset:61440\n"
"ds_read_b64_tr_b16 v[192:193], v47 offset:40968\n"
"ds_read_b64_tr_b16 v[194:195], v47 offset:61448\n"
"ds_read_b64_tr_b16 v[196:197], v48 offset:40960\n"
"ds_read_b64_tr_b16 v[198:199], v48 offset:61440\n"
"ds_read_b64_tr_b16 v[200:201], v48 offset:40968\n"
"ds_read_b64_tr_b16 v[202:203], v48 offset:61448\n"
"ds_read_b64_tr_b16 v[204:205], v49 offset:40960\n"
"ds_read_b64_tr_b16 v[206:207], v49 offset:61440\n"
"s_waitcnt lgkmcnt(15)\n"
"global_store_dwordx4 v9, v[168:171], s[42:43] offset:-2048 sc1\n"
"global_store_dwordx4 v9, v[172:175], s[42:43] offset:-1024 sc1\n"
"s_waitcnt lgkmcnt(14)\n"
"global_store_dwordx4 v9, v[176:179], s[42:43] offset:0 sc1\n"
"s_waitcnt lgkmcnt(12)\n"
"global_store_dwordx4 v9, v[180:183], s[42:43] offset:1024 sc1\n"
"s_waitcnt lgkmcnt(10)\n"
"global_store_dwordx4 v9, v[184:187], s[42:43] offset:2048 sc1\n"
"s_add_u32 s42, s42, s47\n"
"s_addc_u32 s43, s43, 0\n"
"s_waitcnt lgkmcnt(8)\n"
"global_store_dwordx4 v9, v[188:191], s[42:43] offset:-2048 sc1\n"
"s_waitcnt lgkmcnt(6)\n"
"global_store_dwordx4 v9, v[192:195], s[42:43] offset:-1024 sc1\n"
"s_waitcnt lgkmcnt(4)\n"
"global_store_dwordx4 v9, v[196:199], s[42:43] offset:0 sc1\n"
"s_waitcnt lgkmcnt(2)\n"
"global_store_dwordx4 v9, v[200:203], s[42:43] offset:1024 sc1\n"
"s_waitcnt lgkmcnt(0)\n"
"s_nop 0\n"
"global_store_dwordx4 v9, v[204:207], s[42:43] offset:2048 sc1\n"
"v_add_u32 v44, 0x14000, v44\n"
"v_add_u32 v45, 0x14000, v45\n"
"v_add_u32 v46, 0x14000, v46\n"
"v_add_u32 v47, 0x14000, v47\n"
"v_add_u32 v48, 0x14000, v48\n"
"v_add_u32 v49, 0x14000, v49\n"
"s_waitcnt vmcnt(20)\n"
"s_barrier\n"
"ds_read_b64_tr_b16 v[208:209], v44 offset:0\n"
"ds_read_b64_tr_b16 v[210:211], v44 offset:20480\n"
"s_add_u32 s53, s52, 32\n"
"s_cmp_lt_u32 s53, 54\n"
"s_cselect_b32 s47, 54, 20\n"
"s_cselect_b32 s46, 0, 378\n"
"s_add_u32 s46, s46, s53\n"
"s_mul_i32 s44, s30, s47\n"
"s_lshl1_add_u32 s46, s44, s46\n"
"s_mul_i32 s46, s46, 5120\n"
"s_add_u32 s46, s46, 0x1118800\n"
"s_mul_i32 s47, s47, 5120\n"
"s_mov_b64 s[42:43], s[66:67]\n"
"s_add_u32 s42, s42, s46\n"
"s_addc_u32 s43, s43, 0\n"
"ds_read_b64_tr_b16 v[212:213], v44 offset:8\n"
"ds_read_b64_tr_b16 v[214:215], v44 offset:20488\n"
"ds_read_b64_tr_b16 v[216:217], v45 offset:0\n"
"ds_read_b64_tr_b16 v[218:219], v45 offset:20480\n"
"ds_read_b64_tr_b16 v[220:221], v45 offset:8\n"
"ds_read_b64_tr_b16 v[222:223], v45 offset:20488\n"
"ds_read_b64_tr_b16 v[224:225], v46 offset:0\n"
"ds_read_b64_tr_b16 v[226:227], v46 offset:20480\n"
"ds_read_b64_tr_b16 v[228:229], v47 offset:0\n"
"ds_read_b64_tr_b16 v[230:231], v47 offset:20480\n"
"ds_read_b64_tr_b16 v[232:233], v47 offset:8\n"
"ds_read_b64_tr_b16 v[234:235], v47 offset:20488\n"
"ds_read_b64_tr_b16 v[236:237], v48 offset:0\n"
"ds_read_b64_tr_b16 v[238:239], v48 offset:20480\n"
"ds_read_b64_tr_b16 v[240:241], v48 offset:8\n"
"ds_read_b64_tr_b16 v[242:243], v48 offset:20488\n"
"ds_read_b64_tr_b16 v[244:245], v49 offset:0\n"
"ds_read_b64_tr_b16 v[246:247], v49 offset:20480\n"
"s_waitcnt lgkmcnt(15)\n"
"global_store_dwordx4 v9, v[208:211], s[42:43] offset:-2048 sc1\n"
"global_store_dwordx4 v9, v[212:215], s[42:43] offset:-1024 sc1\n"
"s_waitcnt lgkmcnt(14)\n"
"global_store_dwordx4 v9, v[216:219], s[42:43] offset:0 sc1\n"
"s_waitcnt lgkmcnt(12)\n"
"global_store_dwordx4 v9, v[220:223], s[42:43] offset:1024 sc1\n"
"s_waitcnt lgkmcnt(10)\n"
"global_store_dwordx4 v9, v[224:227], s[42:43] offset:2048 sc1\n"
"s_add_u32 s42, s42, s47\n"
"s_addc_u32 s43, s43, 0\n"
"s_waitcnt lgkmcnt(8)\n"
"global_store_dwordx4 v9, v[228:231], s[42:43] offset:-2048 sc1\n"
"s_waitcnt lgkmcnt(6)\n"
"global_store_dwordx4 v9, v[232:235], s[42:43] offset:-1024 sc1\n"
"s_waitcnt lgkmcnt(4)\n"
"global_store_dwordx4 v9, v[236:239], s[42:43] offset:0 sc1\n"
"s_waitcnt lgkmcnt(2)\n"
"global_store_dwordx4 v9, v[240:243], s[42:43] offset:1024 sc1\n"
"s_waitcnt lgkmcnt(0)\n"
"global_store_dwordx4 v9, v[244:247], s[42:43] offset:2048 sc1\n"
"s_and_b32 s46, s52, 11\n"
"s_cmp_lg_u32 s46, s30\n"
"s_cbranch_scc1 72f\n"
"s_lshr_b32 s52, s52, 2\n"
"ds_read_b64_tr_b16 v[60:61], v44 offset:40960\n"
"ds_read_b64_tr_b16 v[62:63], v44 offset:61440\n"
"s_add_u32 s53, s52, 72\n"
"s_cmp_lt_u32 s53, 54\n"
"s_cselect_b32 s47, 54, 20\n"
"s_cselect_b32 s46, 0, 378\n"
"s_add_u32 s46, s46, s53\n"
"s_mul_i32 s44, s30, s47\n"
"s_lshl1_add_u32 s46, s44, s46\n"
"s_mul_i32 s46, s46, 5120\n"
"s_add_u32 s46, s46, 0x1118800\n"
"s_mul_i32 s47, s47, 5120\n"
"s_mov_b64 s[42:43], s[66:67]\n"
"s_add_u32 s42, s42, s46\n"
"s_addc_u32 s43, s43, 0\n"
"ds_read_b64_tr_b16 v[64:65], v44 offset:40968\n"
"ds_read_b64_tr_b16 v[66:67], v44 offset:61448\n"
"ds_read_b64_tr_b16 v[68:69], v45 offset:40960\n"
"ds_read_b64_tr_b16 v[70:71], v45 offset:61440\n"
"ds_read_b64_tr_b16 v[72:73], v45 offset:40968\n"
"ds_read_b64_tr_b16 v[74:75], v45 offset:61448\n"
"ds_read_b64_tr_b16 v[76:77], v46 offset:40960\n"
"ds_read_b64_tr_b16 v[78:79], v46 offset:61440\n"
"ds_read_b64_tr_b16 v[80:81], v47 offset:40960\n"
"ds_read_b64_tr_b16 v[82:83], v47 offset:61440\n"
"ds_read_b64_tr_b16 v[84:85], v47 offset:40968\n"
"ds_read_b64_tr_b16 v[86:87], v47 offset:61448\n"
"ds_read_b64_tr_b16 v[88:89], v48 offset:40960\n"
"ds_read_b64_tr_b16 v[90:91], v48 offset:61440\n"
"ds_read_b64_tr_b16 v[92:93], v48 offset:40968\n"
"ds_read_b64_tr_b16 v[94:95], v48 offset:61448\n"
"ds_read_b64_tr_b16 v[96:97], v49 offset:40960\n"
"ds_read_b64_tr_b16 v[98:99], v49 offset:61440\n"
"s_waitcnt lgkmcnt(15)\n"
"global_store_dwordx4 v9, v[60:63], s[42:43] offset:-2048 sc1\n"
"global_store_dwordx4 v9, v[64:67], s[42:43] offset:-1024 sc1\n"
"s_waitcnt lgkmcnt(14)\n"
"global_store_dwordx4 v9, v[68:71], s[42:43] offset:0 sc1\n"
"s_waitcnt lgkmcnt(12)\n"
"global_store_dwordx4 v9, v[72:75], s[42:43] offset:1024 sc1\n"
"s_waitcnt lgkmcnt(10)\n"
"global_store_dwordx4 v9, v[76:79], s[42:43] offset:2048 sc1\n"
"s_add_u32 s42, s42, s47\n"
"s_addc_u32 s43, s43, 0\n"
"s_waitcnt lgkmcnt(8)\n"
"global_store_dwordx4 v9, v[80:83], s[42:43] offset:-2048 sc1\n"
"s_waitcnt lgkmcnt(6)\n"
"global_store_dwordx4 v9, v[84:87], s[42:43] offset:-1024 sc1\n"
"s_waitcnt lgkmcnt(4)\n"
"global_store_dwordx4 v9, v[88:91], s[42:43] offset:0 sc1\n"
"s_waitcnt lgkmcnt(2)\n"
"global_store_dwordx4 v9, v[92:95], s[42:43] offset:1024 sc1\n"
"s_waitcnt lgkmcnt(0)\n"
"global_store_dwordx4 v9, v[96:99], s[42:43] offset:2048 sc1\n"
"72:\n"
"s_branch 50b\n"
"99:\n"
        : "+{s[0:1]}"(ka), "+{s2}"(bid), "+{v0}"(tid) :
        : "v1", "v2", "v3", "v4", "v5", "v6", "v7", "v8", "v9", "v10", "v11", "v12", "v13", "v14", "v15", "v16", "v17", "v18", "v19", "v20", "v21", "v22", "v23", "v24", "v25", "v26", "v27", "v28", "v29", "v30", "v31", "v32", "v33", "v34", "v35", "v36", "v37", "v38", "v39", "v40", "v41", "v42", "v43", "v44", "v45", "v46", "v47", "v48", "v49", "v50", "v51", "v52", "v53", "v54", "v55", "v56", "v57", "v58", "v59", "v60", "v61", "v62", "v63", "v64", "v65", "v66", "v67", "v68", "v69", "v70", "v71", "v72", "v73", "v74", "v75", "v76", "v77", "v78", "v79", "v80", "v81", "v82", "v83", "v84", "v85", "v86", "v87", "v88", "v89", "v90", "v91", "v92", "v93", "v94", "v95", "v96", "v97", "v98", "v99", "v100", "v101", "v102", "v103", "v104", "v105", "v106", "v107", "v108", "v109", "v110", "v111", "v112", "v113", "v114", "v115", "v116", "v117", "v118", "v119", "v120", "v121", "v122", "v123", "v124", "v125", "v126", "v127", "v128", "v129", "v130", "v131", "v132", "v133", "v134", "v135", "v136", "v137", "v138", "v139", "v140", "v141", "v142", "v143", "v144", "v145", "v146", "v147", "v148", "v149", "v150", "v151", "v152", "v153", "v154", "v155", "v156", "v157", "v158", "v159", "v160", "v161", "v162", "v163", "v164", "v165", "v166", "v167", "v168", "v169", "v170", "v171", "v172", "v173", "v174", "v175", "v176", "v177", "v178", "v179", "v180", "v181", "v182", "v183", "v184", "v185", "v186", "v187", "v188", "v189", "v190", "v191", "v192", "v193", "v194", "v195", "v196", "v197", "v198", "v199", "v200", "v201", "v202", "v203", "v204", "v205", "v206", "v207", "v208", "v209", "v210", "v211", "v212", "v213", "v214", "v215", "v216", "v217", "v218", "v219", "v220", "v221", "v222", "v223", "v224", "v225", "v226", "v227", "v228", "v229", "v230", "v231", "v232", "v233", "v234", "v235", "v236", "v237", "v238", "v239", "v240", "v241", "v242", "v243", "v244", "v245", "v246", "v247", "v248", "v249", "v250", "v251", "v252", "v253", "v254", "v255", "a0", "a1", "a2", "a3", "a4", "a5", "a6", "a7", "a8", "a9", "a10", "a11", "a12", "a13", "a14", "a15", "a16", "a17", "a18", "a19", "a20", "a21", "a22", "a23", "a24", "a25", "a26", "a27", "a28", "a29", "a30", "a31", "a32", "a33", "a34", "a35", "a36", "a37", "a38", "a39", "a40", "a41", "a42", "a43", "a44", "a45", "a46", "a47", "a48", "a49", "a50", "a51", "a52", "a53", "a54", "a55", "a56", "a57", "a58", "a59", "a60", "a61", "a62", "a63", "a64", "a65", "a66", "a67", "a68", "a69", "a70", "a71", "a72", "a73", "a74", "a75", "a76", "a77", "a78", "a79", "a80", "a81", "a82", "a83", "a84", "a85", "a86", "a87", "a88", "a89", "a90", "a91", "a92", "a93", "a94", "a95", "a96", "a97", "a98", "a99", "a100", "a101", "a102", "a103", "a104", "a105", "a106", "a107", "a108", "a109", "a110", "a111", "a112", "a113", "a114", "a115", "a116", "a117", "a118", "a119", "a120", "a121", "a122", "a123", "a124", "a125", "a126", "a127", "a128", "a129", "a130", "a131", "a132", "a133", "a134", "a135", "a136", "a137", "a138", "a139", "a140", "a141", "a142", "a143", "a144", "a145", "a146", "a147", "a148", "a149", "a150", "a151", "a152", "a153", "a154", "a155", "a156", "a157", "a158", "a159", "a160", "a161", "a162", "a163", "a164", "a165", "a166", "a167", "a168", "a169", "a170", "a171", "a172", "a173", "a174", "a175", "a176", "a177", "a178", "a179", "a180", "a181", "a182", "a183", "a184", "a185", "a186", "a187", "a188", "a189", "a190", "a191", "a192", "a193", "a194", "a195", "a196", "a197", "a198", "a199", "a200", "a201", "a202", "a203", "a204", "a205", "a206", "a207", "a208", "a209", "a210", "a211", "a212", "a213", "a214", "a215", "a216", "a217", "a218", "a219", "a220", "a221", "a222", "a223", "a224", "a225", "a226", "a227", "a228", "a229", "a230", "a231", "a232", "a233", "a234", "a235", "a236", "a237", "a238", "a239", "a240", "a241", "a242", "a243", "a244", "a245", "a246", "a247", "a248", "a249", "a250", "a251", "a252", "a253", "a254", "a255", "s3", "s4", "s5", "s6", "s7", "s8", "s9", "s10", "s11", "s12", "s13", "s14", "s15", "s16", "s17", "s18", "s19", "s20", "s21", "s22", "s23", "s24", "s25", "s26", "s27", "s28", "s29", "s30", "s31", "s33", "s34", "s35", "s36", "s37", "s38", "s39", "s40", "s41", "s42", "s43", "s44", "s45", "s46", "s47", "s48", "s49", "s50", "s51", "s52", "s53", "s54", "s55", "s56", "s57", "s58", "s59", "s60", "s61", "s62", "s63", "s64", "s65", "s66", "s67", "s68", "s69", "s70", "s71", "s72", "s73", "s74", "s75", "s76", "s77", "s78", "s79", "s80", "s81", "s82", "s83", "s84", "s85", "s86", "s87", "s88", "s89", "s90", "s91", "s92", "s93", "s94", "s95", "s96", "vcc", "scc", "memory");
}

extern "C" void kernel_launch(void* const* d_in, const int* in_sizes, int n_in, void* d_out, int out_size, void* d_ws, size_t ws_size, hipStream_t stream) {
    (void)hipFuncSetAttribute((const void*)fused, hipFuncAttributeMaxDynamicSharedMemorySize, 163840);
    fused<<<256, 256, 163840, stream>>>(d_in[0], d_in[1], d_in[4], d_ws, d_in[2], d_in[5], d_in[6], d_in[7], d_out);
}
```
